```python
import torch

def _softmax(x, axis=-1):
    return torch.softmax(x, dim=axis)
def _gelu(x):
    return torch.nn.functional.gelu(x, approximate="tanh")
def _silu(x):
    return torch.nn.functional.silu(x)
def _sigmoid(x):
    return torch.sigmoid(x)
def _layernorm(x, axis=-1, eps=1e-5):
    m = x.mean(dim=axis, keepdim=True)
    v = x.var(dim=axis, keepdim=True, unbiased=False)
    return (x - m) / torch.sqrt(v + eps)
def _scatter_add(tgt, idx, src, axis=0):
    return tgt.clone().index_add_(axis, idx, src)
def _segment_sum(src, seg, n):
    out = torch.zeros((n,) + src.shape[1:], dtype=src.dtype)
    return out.index_add_(0, seg, src)
def _pad(x, pw):
    flat = tuple(v for pair in reversed(pw) for v in pair)
    return torch.nn.functional.pad(x, flat)

kernel_name = 'parallel_sum__compressed_kv_cla__mixer_channel_mlp__prompt'

def setup_inputs():

    _w = (lambda a: (lambda t: t.to(torch.bfloat16) if t.is_floating_point() else t)(torch.as_tensor(a)))
    def _A_setup():
        B, H, Sq, Sk, D, M = 1, 32, 2048, 2048, 128, 512
        Dc = 4 * D
        return {
            "x": _w((lambda *s: torch.randn(*s, dtype=torch.bfloat16) * 0.05)(B, Sq, M)),
            "c_kv": _w((lambda *s: torch.randn(*s, dtype=torch.bfloat16) * 0.4)(B, Sk, Dc)),
            "w_q": _w((lambda *s: torch.randn(*s, dtype=torch.bfloat16) * 3.2)(M, H, D) * (M ** -0.5)),
            "w_uk": _w((lambda *s: torch.randn(*s, dtype=torch.bfloat16) * 3.2)(Dc, H, D) * (Dc ** -0.5)),
            "w_uv": _w((lambda *s: torch.randn(*s, dtype=torch.bfloat16) * 0.4)(Dc, H, D) * (Dc ** -0.5)),
            "w_o": _w((lambda *s: torch.randn(*s, dtype=torch.bfloat16) * 0.4)(H, D, M) * ((H * D) ** -0.5)),
        }
    _A_in = _A_setup()

    def _B_setup():
        B, S, D, F = 1, 2048, 512, 1376
        return {
            "x": _w((lambda *s: torch.randn(*s, dtype=torch.bfloat16) * 0.05)(B, S, D)),
            "w1": _w((lambda *s: torch.randn(*s, dtype=torch.bfloat16) * 0.8)(D, F) * (D ** -0.5)),
            "b1": _w((lambda *s: torch.randn(*s, dtype=torch.bfloat16) * 0.4)(F)),
            "w2": _w((lambda *s: torch.randn(*s, dtype=torch.bfloat16) * 0.05)(F, D) * (F ** -0.5)),
            "b2": _w((lambda *s: torch.randn(*s, dtype=torch.bfloat16) * 0.05)(D)),
        }
    _B_in = _B_setup()

    return {
        "A_x": _A_in["x"],
        "A_c_kv": _A_in["c_kv"],
        "A_w_q": _A_in["w_q"],
        "A_w_uk": _A_in["w_uk"],
        "A_w_uv": _A_in["w_uv"],
        "A_w_o": _A_in["w_o"],
        "B_x": _B_in["x"],
        "B_w1": _B_in["w1"],
        "B_b1": _B_in["b1"],
        "B_w2": _B_in["w2"],
        "B_b2": _B_in["b2"],
    }

def reference(A_x, A_c_kv, A_w_q, A_w_uk, A_w_uv, A_w_o, B_x, B_w1, B_b1, B_w2, B_b2):
    def _A_ref(x, c_kv, w_q, w_uk, w_uv, w_o):
        B, Sq, M = x.shape[0], x.shape[1], x.shape[2]
        Sk = c_kv.shape[1]
        H, D = w_q.shape[1], w_q.shape[2]
        q = torch.einsum("bqm,mhd->bhqd", x, w_q)
        k = torch.einsum("bkc,chd->bhkd", c_kv, w_uk)
        v = torch.einsum("bkc,chd->bhkd", c_kv, w_uv)
        scores = torch.einsum("bhqd,bhkd->bhqk", q, k) * (D ** -0.5)
        off = Sk - Sq
        qi = torch.arange(Sq)[:, None]
        kj = torch.arange(Sk)[None, :]
        cmask = kj <= (qi + off)
        scores = torch.where(cmask[None, None], scores, -1e9)
        p = _softmax(scores, axis=-1)
        ctx = torch.einsum("bhqk,bhkd->bhqd", p, v)
        return torch.einsum("bhqd,hdm->bqm", ctx, w_o)

    def _B_ref(x, w1, b1, w2, b2):
        y = _layernorm(x, axis=-1)
        h = _gelu(torch.einsum("bsd,df->bsf", y, w1) + b1)
        return torch.einsum("bsf,fd->bsd", h, w2) + b2

    x = A_x
    xn = _layernorm(x, axis=-1)
    return x + _A_ref(xn, A_c_kv, A_w_q, A_w_uk, A_w_uv, A_w_o) + _B_ref(xn, B_w1, B_b1, B_w2, B_b2)
```

```cpp
#include <hip/hip_runtime.h>
#include <hip/hip_bf16.h>
typedef unsigned short u16;
__device__ inline float bf2f(u16 b){ return __uint_as_float(((unsigned)b)<<16);}
__device__ inline u16 f2bf(float f){ unsigned u=__float_as_uint(f); u += 0x7FFFu + ((u>>16)&1u); return (u16)(u>>16);}
constexpr int S=2048, M=512, H=32, D=128, F=1376;

__global__ void ln_kernel(const u16* x, u16* xn, u16* y){
  int row=blockIdx.x, lane=threadIdx.x;
  float v[8]; float s=0;
  for(int i=0;i<8;i++){ v[i]=bf2f(x[row*M+lane*8+i]); s+=v[i]; }
  for(int o=32;o>=1;o>>=1) s+=__shfl_xor(s,o);
  float mean=s/M; float q=0; for(int i=0;i<8;i++){ float d=v[i]-mean; q+=d*d; }
  for(int o=32;o>=1;o>>=1) q+=__shfl_xor(q,o);
  float rstd=rsqrtf(q/M+1e-5f);
  s=0; for(int i=0;i<8;i++){ u16 b=f2bf((v[i]-mean)*rstd); xn[row*M+lane*8+i]=b; v[i]=bf2f(b); s+=v[i]; }
  for(int o=32;o>=1;o>>=1) s+=__shfl_xor(s,o);
  mean=s/M; q=0; for(int i=0;i<8;i++){ float d=v[i]-mean; q+=d*d; }
  for(int o=32;o>=1;o>>=1) q+=__shfl_xor(q,o);
  rstd=rsqrtf(q/M+1e-5f);
  for(int i=0;i<8;i++) y[row*M+lane*8+i]=f2bf((v[i]-mean)*rstd);
}
__global__ void gemm_naive(const u16* A,const u16* B,u16* C,int Mm,int N,int K,const u16* bias,int mode){
  __shared__ float As[16][17], Bs[16][17];
  int tx=threadIdx.x, ty=threadIdx.y; int n=blockIdx.x*16+tx, m=blockIdx.y*16+ty; float acc=0;
  for(int k0=0;k0<K;k0+=16){ As[ty][tx]=bf2f(A[m*K+k0+tx]); Bs[ty][tx]=bf2f(B[(k0+ty)*N+n]); __syncthreads();
    for(int k=0;k<16;k++) acc+=As[ty][k]*Bs[k][tx]; __syncthreads(); }
  if(mode==1){ float v=acc+bf2f(bias[n]); float t=tanhf(0.7978845608028654f*(v+0.044715f*v*v*v)); acc=0.5f*v*(1.f+t);}
  C[m*N+n]=f2bf(acc);
}
__global__ void attn_naive(const u16* Q,const u16* K,const u16* V,u16* ctx){
  __shared__ float p[S]; __shared__ float qs[D];
  int q=blockIdx.x, h=blockIdx.y, t=threadIdx.x;
  qs[t]=bf2f(Q[q*(H*D)+h*D+t]); __syncthreads();
  float mx=-1e30f;
  for(int k=t;k<=q;k+=128){ float s=0; const u16* kr=K+(size_t)k*(H*D)+h*D; for(int d=0;d<D;d++) s+=qs[d]*bf2f(kr[d]); s*=0.08838834764831845f; p[k]=s; mx=fmaxf(mx,s);}
  __shared__ float red[128]; red[t]=mx; __syncthreads(); for(int o=64;o>=1;o>>=1){ if(t<o) red[t]=fmaxf(red[t],red[t+o]); __syncthreads(); }
  mx=red[0]; __syncthreads(); float sum=0; for(int k=t;k<=q;k+=128){ float e=__expf(p[k]-mx); p[k]=e; sum+=e; }
  red[t]=sum; __syncthreads(); for(int o=64;o>=1;o>>=1){ if(t<o) red[t]+=red[t+o]; __syncthreads(); }
  sum=red[0]; float acc=0; for(int k=0;k<=q;k++) acc+=p[k]*bf2f(V[(size_t)k*(H*D)+h*D+t]);
  ctx[q*(H*D)+h*D+t]=f2bf(acc/sum);
}
__global__ void final_naive(const u16* x,const u16* ctx,const u16* wo,const u16* hh,const u16* w2,const u16* b2,u16* out){
  __shared__ float As[16][17], Bs[16][17];
  int tx=threadIdx.x, ty=threadIdx.y; int n=blockIdx.x*16+tx, m=blockIdx.y*16+ty; float acc=0, acc2=0;
  for(int k0=0;k0<H*D;k0+=16){ As[ty][tx]=bf2f(ctx[m*(H*D)+k0+tx]); Bs[ty][tx]=bf2f(wo[(k0+ty)*M+n]); __syncthreads();
    for(int k=0;k<16;k++) acc+=As[ty][k]*Bs[k][tx]; __syncthreads(); }
  for(int k0=0;k0<F;k0+=16){ As[ty][tx]=bf2f(hh[m*F+k0+tx]); Bs[ty][tx]=bf2f(w2[(k0+ty)*M+n]); __syncthreads();
    for(int k=0;k<16;k++) acc2+=As[ty][k]*Bs[k][tx]; __syncthreads(); }
  out[m*M+n]=f2bf(bf2f(x[m*M+n])+acc+acc2+bf2f(b2[n]));
}
extern "C" void kernel_launch(void* const* d_in, const int* in_sizes, int n_in, void* d_out, int out_size, void* d_ws, size_t ws_size, hipStream_t stream){
  const u16* x=(const u16*)d_in[0]; const u16* ckv=(const u16*)d_in[1]; const u16* wq=(const u16*)d_in[2]; const u16* wuk=(const u16*)d_in[3];
  const u16* wuv=(const u16*)d_in[4]; const u16* wo=(const u16*)d_in[5]; const u16* w1=(const u16*)d_in[7]; const u16* b1=(const u16*)d_in[8];
  const u16* w2=(const u16*)d_in[9]; const u16* b2=(const u16*)d_in[10];
  u16* ws=(u16*)d_ws; u16* xn=ws; u16* y=xn+S*M; u16* Q=y+S*M; u16* K=Q+(size_t)S*H*D; u16* V=K+(size_t)S*H*D; u16* ctx=V+(size_t)S*H*D; u16* hh=ctx+(size_t)S*H*D;
  ln_kernel<<<S,64,0,stream>>>(x,xn,y);
  dim3 b(16,16);
  gemm_naive<<<dim3(H*D/16,S/16),b,0,stream>>>(xn,wq,Q,S,H*D,M,nullptr,0);
  gemm_naive<<<dim3(H*D/16,S/16),b,0,stream>>>(ckv,wuk,K,S,H*D,M,nullptr,0);
  gemm_naive<<<dim3(H*D/16,S/16),b,0,stream>>>(ckv,wuv,V,S,H*D,M,nullptr,0);
  gemm_naive<<<dim3(F/16,S/16),b,0,stream>>>(y,w1,hh,S,F,M,b1,1);
  attn_naive<<<dim3(S,H),128,0,stream>>>(Q,K,V,ctx);
  final_naive<<<dim3(M/16,S/16),b,0,stream>>>(x,ctx,wo,hh,w2,b2,(u16*)d_out);
}
```

```cpp
#include <hip/hip_runtime.h>
#include <utility>
typedef unsigned short u16;
typedef float f32x2 __attribute__((ext_vector_type(2)));
typedef float f32x4 __attribute__((ext_vector_type(4)));
typedef __bf16 bf16x2 __attribute__((ext_vector_type(2)));
typedef unsigned u32x4 __attribute__((ext_vector_type(4)));
#define DI __device__ __forceinline__
constexpr size_t WS_FLAG = 0, WS_DBG = 131072, WS_XN = 1u << 20, WS_Y = WS_XN + (2u << 20), WS_K = WS_Y + (2u << 20), WS_V = WS_K + (16u << 20),
                 WS_A = WS_V + (16u << 20), WS_P = WS_A + (size_t)16 * 171 * 8192, WS_END = WS_P + (16u << 20);
DI unsigned rfl(unsigned x) { return __builtin_amdgcn_readfirstlane(x); }
DI unsigned lane_id() { unsigned l; asm volatile("v_mbcnt_lo_u32_b32 %0, -1, 0\n\tv_mbcnt_hi_u32_b32 %0, -1, %0" : "=v"(l)); return l; }
DI unsigned cvtpk(float lo, float hi) { f32x2 x = {lo, hi}; return __builtin_bit_cast(unsigned, __builtin_convertvector(x, bf16x2)); }
DI float bf_lo(unsigned u) { return __uint_as_float(u << 16); }
DI float bf_hi(unsigned u) { return __uint_as_float(u & 0xffff0000u); }
DI __amdgpu_buffer_rsrc_t mkrsrc(const void* p, unsigned bytes) { return __builtin_amdgcn_make_buffer_rsrc((void*)p, (short)0, (int)bytes, 0x00020000); }
template <int I> DI float accr() { float r; asm volatile("v_accvgpr_read_b32 %0, a[%c1]" : "=v"(r) : "i"(I)); return r; }
template <int T0, int T1> DI u32x4 pack2(float sc) {
  u32x4 w;
  w[0] = cvtpk(accr<T0>() * sc, accr<T0 + 1>() * sc); w[1] = cvtpk(accr<T0 + 2>() * sc, accr<T0 + 3>() * sc);
  w[2] = cvtpk(accr<T1>() * sc, accr<T1 + 1>() * sc); w[3] = cvtpk(accr<T1 + 2>() * sc, accr<T1 + 3>() * sc);
  return w;
}
template <int T0, int T1> DI u32x4 pack2n() {
  u32x4 w;
  w[0] = cvtpk(accr<T0>(), accr<T0 + 1>()); w[1] = cvtpk(accr<T0 + 2>(), accr<T0 + 3>());
  w[2] = cvtpk(accr<T1>(), accr<T1 + 1>()); w[3] = cvtpk(accr<T1 + 2>(), accr<T1 + 3>());
  return w;
}
__shared__ __attribute__((aligned(16))) char smem[163840];

#define DBGW 0
#define STAMP(k)
struct Params { const u16 *x, *ckv, *wq, *wuk, *wuv, *wo, *w1, *b1, *w2, *b2; u16* out; char* ws; };

typedef unsigned u32x2 __attribute__((ext_vector_type(2)));
constexpr unsigned FLAG0 = 256;
typedef __attribute__((address_space(1))) unsigned long long gu64;
DI u32x4 ldu(__amdgpu_buffer_rsrc_t rs, unsigned off) { return __builtin_amdgcn_raw_buffer_load_b128(rs, 0, off, 16); }
DI void slow_rows(const u16* wq, char* ws, unsigned head, unsigned blk, unsigned wid) {
  const __amdgpu_buffer_rsrc_t rs = mkrsrc(ws, (unsigned)WS_END);
  const __amdgpu_buffer_rsrc_t rq = mkrsrc(wq + head * 128, 512 * 8192 - head * 256);
  const unsigned l = lane_id(), rh = wid & 1, kp = wid >> 1, row = blk * 128 + rh * 64 + l;
  asm volatile("s_waitcnt vmcnt(0)" ::: "memory");
  float q[128];
#pragma unroll
  for (int d = 0; d < 128; ++d) q[d] = 0.f;
#pragma unroll 1
  for (unsigned k = 0; k < 512; ++k) {
    const unsigned xw = __builtin_amdgcn_raw_buffer_load_b32(rs, ((row >> 2) * 256 + (row & 3)) * 16, (unsigned)WS_XN + (k >> 3) * 64 + (k & 6) * 2, 16);
    const float xv = (k & 1) ? bf_hi(xw) : bf_lo(xw);
#pragma unroll
    for (int c = 0; c < 16; ++c) {
      const u32x4 w = __builtin_amdgcn_raw_buffer_load_b128(rq, 0, k * 8192 + c * 16, 0);
#pragma unroll
      for (int i = 0; i < 4; ++i) { q[c * 8 + 2 * i] += xv * bf_lo(w[i]); q[c * 8 + 2 * i + 1] += xv * bf_hi(w[i]); }
    }
  }
#pragma unroll
  for (int d = 0; d < 128; d += 2) { const unsigned u = cvtpk(q[d] * 0.12751743082f, q[d + 1] * 0.12751743082f); q[d] = bf_lo(u); q[d + 1] = bf_hi(u); }
  float acc[64], m = -1e30f, lsum = 0.f;
#pragma unroll
  for (int d = 0; d < 64; ++d) acc[d] = 0.f;
  const unsigned nkey = blk * 128 + rh * 64 + 64, kb = (unsigned)WS_K + head * (512u << 10), vb = (unsigned)WS_V + head * (512u << 10);
#pragma unroll 1
  for (unsigned key = 0; key < nkey; ++key) {
    float sc = 0.f;
#pragma unroll
    for (int c = 0; c < 16; ++c) {
      const u32x4 w = ldu(rs, kb + ((key >> 4) * 4 + (c >> 2)) * 1024 + ((c & 3) * 16 + (key & 15)) * 16);
      const int d0 = 32 * (c >> 2) + 4 * (c & 3);
      sc += q[d0] * bf_lo(w[0]) + q[d0 + 1] * bf_hi(w[0]) + q[d0 + 2] * bf_lo(w[1]) + q[d0 + 3] * bf_hi(w[1]);
      sc += q[d0 + 16] * bf_lo(w[2]) + q[d0 + 17] * bf_hi(w[2]) + q[d0 + 18] * bf_lo(w[3]) + q[d0 + 19] * bf_hi(w[3]);
    }
    const bool vis = key <= row;
    const float mn = vis ? fmaxf(m, sc) : m;
    const float al = __builtin_amdgcn_exp2f(m - mn), pp = vis ? __builtin_amdgcn_exp2f(sc - mn) : 0.f;
    m = mn; lsum = lsum * al + pp;
    const unsigned kk = key & 31, vo = vb + (key >> 5) * 8192 + kp * 4096 + ((kk >> 2) & 3) * 256 + (kk >> 4) * 8 + (kk & 2) * 2;
#pragma unroll
    for (int d = 0; d < 64; ++d) {
      const unsigned w = __builtin_amdgcn_raw_buffer_load_b32(rs, 0, vo + (d >> 4) * 1024 + (d & 15) * 16, 16);
      acc[d] = acc[d] * al + pp * ((kk & 1) ? bf_hi(w) : bf_lo(w));
    }
  }
  const float inv = 1.f / lsum;
  const unsigned ob = (unsigned)WS_A + (blk * 171 + head * 4 + kp * 2) * 8192 + rh * 4096 + (l >> 4) * 1024 + (l & 15) * 16;
#pragma unroll
  for (int c = 0; c < 8; ++c) {
    const int d0 = 32 * (c >> 2) + 4 * (c & 3);
    u32x4 w;
    w[0] = cvtpk(acc[d0] * inv, acc[d0 + 1] * inv); w[1] = cvtpk(acc[d0 + 2] * inv, acc[d0 + 3] * inv);
    w[2] = cvtpk(acc[d0 + 16] * inv, acc[d0 + 17] * inv); w[3] = cvtpk(acc[d0 + 18] * inv, acc[d0 + 19] * inv);
    __builtin_amdgcn_raw_buffer_store_b128(w, rs, ob + (c >> 2) * 8192 + (c & 3) * 256, 0, 16);
  }
  asm volatile("s_waitcnt vmcnt(0)" ::: "memory");
}

__global__ __launch_bounds__(256, 1) void k_main(Params p) {
  const unsigned bid = blockIdx.x, wid = rfl(threadIdx.x >> 6);
  const unsigned xcd = bid & 7, idx = bid >> 3, head = xcd * 4 + (idx >> 3), j = idx & 7;
  asm volatile("" :: "s"((unsigned)(size_t)smem));
  const unsigned long long tok = *(const __attribute__((address_space(4))) unsigned long long*)p.ws + 0x9E3779B97F4A7C15ull;
  STAMP(0);
  const __amdgpu_buffer_rsrc_t srdWS = mkrsrc(p.ws, (unsigned)WS_END);
  const __amdgpu_buffer_rsrc_t srdC = mkrsrc(p.ckv + (size_t)j * 256 * 512, 256 * 1024);
  const __amdgpu_buffer_rsrc_t srdW = mkrsrc(((wid >> 1) ? p.wuv : p.wuk) + head * 128 + (wid & 1) * 64, 511 * 8192 + 128);
  const unsigned rot = (j + head) & 3;
  const unsigned oFl = FLAG0 + bid * 64 + wid * 8;
  {
    const unsigned l = lane_id();
    const unsigned e = l >> 5, g1 = (l >> 4) & 1, q = (l >> 2) & 3;
    const unsigned wcol = (2 * e + (((l >> 1) & 1) ^ g1)) * 32 + (l & 1) * 16;
    const unsigned voffC = (wid * 16 + (l >> 4)) * 1024 + ((l & 15) ^ (l >> 4)) * 16;
    const unsigned voffW = (8 * g1 + q) * 8192 + wcol;
    asm volatile(
  "s_nop 4\n"
  "s_getpc_b64 s[68:69]\n"
  "KCODE_BEGIN:\n"
  "s_and_b32 s69, s69, 0xffff\n"
  "s_mov_b32 s70, KCODE_END-KCODE_BEGIN\n"
  "s_mov_b32 s71, 0x20000\n"
  "s_lshl_b32 s64, %[wid], 12\n"
  "s_mul_i32 s66, %[wid], 24576\n"
  "s_lshl_b32 s67, %[rot], 20\n"
  "s_lshl_b32 s72, %[rot], 8\n"
  "s_add_u32 m0, s66, 65536\n"
  "s_add_u32 s65, s67, 0\n"
  "buffer_load_dwordx4 %[voffW], %[srdW], s65 offen nt lds\n"
  "s_add_u32 m0, s66, 66560\n"
  "s_add_u32 s65, s67, 131072\n"
  "buffer_load_dwordx4 %[voffW], %[srdW], s65 offen nt lds\n"
  "s_add_u32 m0, s66, 67584\n"
  "s_add_u32 s65, s67, 32768\n"
  "buffer_load_dwordx4 %[voffW], %[srdW], s65 offen nt lds\n"
  "s_add_u32 m0, s66, 68608\n"
  "s_add_u32 s65, s67, 163840\n"
  "buffer_load_dwordx4 %[voffW], %[srdW], s65 offen nt lds\n"
  "s_add_u32 m0, s64, 0\n"
  "s_add_u32 s65, s72, 0\n"
  "buffer_load_dwordx4 %[voffC], %[srdC], s65 offen lds\n"
  "v_xor_b32 v16, 64, %[voffC]\n"
  "s_add_u32 m0, s64, 1024\n"
  "s_add_u32 s65, s72, 4096\n"
  "buffer_load_dwordx4 v16, %[srdC], s65 offen lds\n"
  "v_xor_b32 v16, 128, %[voffC]\n"
  "s_add_u32 m0, s64, 2048\n"
  "s_add_u32 s65, s72, 8192\n"
  "buffer_load_dwordx4 v16, %[srdC], s65 offen lds\n"
  "v_xor_b32 v16, 192, %[voffC]\n"
  "s_add_u32 m0, s64, 3072\n"
  "s_add_u32 s65, s72, 12288\n"
  "buffer_load_dwordx4 v16, %[srdC], s65 offen lds\n"
  "s_mov_b32 m0, 64512\n"
  "s_nop 0\n"
  "buffer_load_dword %[tI], s[68:71], 0 offen lds\n"
  "s_add_u32 m0, s66, 69632\n"
  "s_add_u32 s65, s67, 262144\n"
  "buffer_load_dwordx4 %[voffW], %[srdW], s65 offen nt lds\n"
  "s_add_u32 m0, s66, 70656\n"
  "s_add_u32 s65, s67, 393216\n"
  "buffer_load_dwordx4 %[voffW], %[srdW], s65 offen nt lds\n"
  "s_add_u32 m0, s66, 71680\n"
  "s_add_u32 s65, s67, 294912\n"
  "buffer_load_dwordx4 %[voffW], %[srdW], s65 offen nt lds\n"
  "s_add_u32 m0, s66, 72704\n"
  "s_add_u32 s65, s67, 425984\n"
  "buffer_load_dwordx4 %[voffW], %[srdW], s65 offen nt lds\n"
  "s_add_u32 m0, s66, 73728\n"
  "s_add_u32 s65, s67, 524288\n"
  "buffer_load_dwordx4 %[voffW], %[srdW], s65 offen nt lds\n"
  "s_add_u32 m0, s66, 74752\n"
  "s_add_u32 s65, s67, 655360\n"
  "buffer_load_dwordx4 %[voffW], %[srdW], s65 offen nt lds\n"
  "s_add_u32 m0, s66, 75776\n"
  "s_add_u32 s65, s67, 557056\n"
  "buffer_load_dwordx4 %[voffW], %[srdW], s65 offen nt lds\n"
  "s_add_u32 m0, s66, 76800\n"
  "s_add_u32 s65, s67, 688128\n"
  "buffer_load_dwordx4 %[voffW], %[srdW], s65 offen nt lds\n"
    :: [srdC] "s"(srdC), [srdW] "s"(srdW), [voffC] "v"(voffC), [voffW] "v"(voffW), [wid] "s"(rfl(wid)), [tI] "v"(l < 6 ? ((idx * 4 + wid) * 6 + l) * 128 : 0x7f000000u), [rot] "s"(rfl(rot))
    : "v16", "s64", "s65", "s66", "s67", "s68", "s69", "s70", "s71", "s72", "vcc", "scc", "memory");
  }
  STAMP(1);
  const unsigned blkA = 15 - j, blkB = j;
  const unsigned oK = (unsigned)WS_K + head * (512u << 10) + wid * 8192;
  {
    const unsigned l = lane_id();
    const unsigned e = l >> 5, g1 = (l >> 4) & 1, q = (l >> 2) & 3, c = l & 15, g = l >> 4;
    const unsigned wcol = (2 * e + (((l >> 1) & 1) ^ g1)) * 32 + (l & 1) * 16;
    const unsigned voffC = (wid * 16 + (l >> 4)) * 1024 + ((l & 15) ^ (l >> 4)) * 16;
    const unsigned voffW = (8 * g1 + q) * 8192 + wcol;
    const unsigned bC = c * 256 + (g ^ c) * 16;
    const unsigned TRU = e * 512 + g1 * 256 + q * 64 + g1 * 32 + (l & 3) * 8, TRB = TRU + e * 512;
    const bool upon = idx < 30;
    const unsigned rbU = 2 * xcd + (idx >= 15 ? 1u : 0u), fb = upon ? (idx >= 15 ? idx - 15 : idx) : 14u;
    const __amdgpu_buffer_rsrc_t srdW1 = mkrsrc(p.w1 + fb * 96, upon ? 512 * 2752 - fb * 192 : 0u);
    const __amdgpu_buffer_rsrc_t srdB1 = mkrsrc(p.b1 + fb * 96, 2752 - fb * 192);
    const __amdgpu_buffer_rsrc_t srdWq = mkrsrc(p.wq + head * 128, 512 * 8192 - head * 256);
    const unsigned voffW1 = (32 * wid + 16 * e + 8 * g1 + q) * 2752 + (((l >> 1) & 1) ^ g1) * 32 + (l & 1) * 16;
    const unsigned voffWq = (4 * (wid >> 1) + 8 * g1 + q) * 8192 + (wid & 1) * 128 + wcol;
    const unsigned blk = (wid >> 1) ? blkB : blkA;
    const unsigned prow = l < 16 ? rbU * 128 + wid * 32 + 4 * (l & 7) : blk * 128 + (wid & 1) * 64 + 4 * (l & 15);
    const unsigned pollLN = FLAG0 + (prow >> 3) * 64 + ((prow >> 2) & 1) * 8;
    const unsigned pollKV = FLAG0 + 16384 + (xcd + 8 * ((idx & 24) + ((l >> 2) & 7))) * 64 + (l & 3) * 8;
    const unsigned oKVo = (unsigned)((wid >> 1) ? WS_V : WS_K) + head * (512u << 10) + j * 65536 + (wid & 1) * ((wid >> 1) ? 4096 : 2048);
    const unsigned oY = upon ? (unsigned)WS_Y + (rbU * 8 + wid * 2) * 16384 : 0x7f000000u;
    const unsigned oX = (unsigned)WS_XN + (blk * 8 + (wid & 1) * 4) * 16384;
    const unsigned oH = (unsigned)WS_A + (rbU * 171 + 128 + fb * 3) * 8192 + wid * 2048;
    const unsigned nH = upon ? (fb == 14 ? 1u : 3u) : 0u;
    const unsigned row = bid * 8 + (wid & 1) * 4;
    const unsigned tWq = (j * 64 + wid * 16) * 8192, rotU = (fb + 2 * (rbU & 1)) & 3;
    const __amdgpu_buffer_rsrc_t srdW2t = mkrsrc((const char*)p.w2 + (bid * 4 + wid) * 1376, 1376);
    const __amdgpu_buffer_rsrc_t srdW1t = mkrsrc((const char*)p.w1 + (bid * 4 + wid) * 1376, 1376), srdCt = mkrsrc((const char*)p.ckv + (bid * 4 + wid) * 2048, 2048);
    const __amdgpu_buffer_rsrc_t srdX = mkrsrc((const char*)p.x + (bid * 8 + (wid & 1) * 4) * 1024, wid < 2 ? 4096u : 0u);
    const unsigned oLN = (unsigned)WS_XN + row * 1024, voffX = (c >> 2) * 4096 + g * 64 + (c & 3) * 16;
    asm volatile(
  "s_nop 4\n"
  "s_lshl_b32 s72, %[wid], 12\n"
  "s_mul_i32 s73, %[wid], 24576\n"
  "v_mov_b32 v16, %[voffC]\n"
  "v_xor_b32 v17, 64, %[voffC]\n"
  "v_add_u32 v17, 4096, v17\n"
  "v_xor_b32 v18, 128, %[voffC]\n"
  "v_add_u32 v18, 8192, v18\n"
  "v_xor_b32 v19, 192, %[voffC]\n"
  "v_add_u32 v19, 12288, v19\n"
  "v_xor_b32 v20, 0, %[bC]\n"
  "v_xor_b32 v21, 64, %[bC]\n"
  "v_xor_b32 v22, 128, %[bC]\n"
  "v_xor_b32 v23, 192, %[bC]\n"
  "s_add_u32 s65, s73, 0x10000\n"
  "v_add_u32 v24, s65, %[TRB]\n"
  "v_xor_b32 v25, 32, v24\n"
  "s_mul_i32 s74, %[wid], 6144\n"
  "s_lshl_b32 s75, %[wid], 11\n"
  "s_lshl_b32 s76, %[wid], 13\n"
  "s_mov_b32 s66, %[oKVo]\n"
  "s_mov_b32 s80, %[oLN]\n"
  "s_add_u32 s81, %[oLN], 0x200000\n"
  "s_mov_b32 s67, 1024\n"
  "s_mov_b32 s68, 5120\n"
  "s_cmp_gt_u32 %[wid], 1\n"
  "s_cselect_b32 s67, s67, 4096\n"
  "s_cselect_b32 s69, s68, 3072\n"
  "s_cselect_b32 s68, s68, 0xffffd400\n"
  "s_mov_b64 s[84:85], %[tok]\n"
  "v_mov_b32 v26, s84\n"
  "v_mov_b32 v27, s85\n"
  "v_and_b32 v228, 48, %[L16]\n"
  "v_lshlrev_b32 v228, 6, v228\n"
  "v_lshrrev_b32 v229, 2, %[L16]\n"
  "v_and_b32 v229, 0xf0, v229\n"
  "v_or_b32 v228, v228, v229\n"
  "v_lshlrev_b32 v230, 3, %[L16]\n"
  "v_lshrrev_b32 v231, 5, %[L16]\n"
  "v_lshlrev_b32 v231, 13, v231\n"
  "v_and_b32 v229, 0x80, v230\n"
  "v_or_b32 v231, v231, v229\n"
  "v_and_b32 v231, 0x1ffff, v231\n"
  "s_add_u32 s82, %[rot], 0\n"
  "s_and_b32 s82, s82, 3\n"
  "s_lshl_b32 s82, s82, 20\n"
  "s_add_u32 m0, s73, 77824\n"
  "s_add_u32 s64, s82, 786432\n"
  "buffer_load_dwordx4 %[voffW], %[srdW], s64 offen nt lds\n"
  "s_add_u32 m0, s73, 78848\n"
  "s_add_u32 s64, s82, 917504\n"
  "buffer_load_dwordx4 %[voffW], %[srdW], s64 offen nt lds\n"
  "s_add_u32 m0, s73, 79872\n"
  "s_add_u32 s64, s82, 819200\n"
  "buffer_load_dwordx4 %[voffW], %[srdW], s64 offen nt lds\n"
  "s_add_u32 m0, s73, 80896\n"
  "s_add_u32 s64, s82, 950272\n"
  "buffer_load_dwordx4 %[voffW], %[srdW], s64 offen nt lds\n"
  "buffer_load_dwordx4 v[196:199], v228, %[srdX], 0 offen offset:0\n"
  "buffer_load_dwordx4 v[200:203], v228, %[srdX], 0 offen offset:256\n"
  "buffer_load_dwordx4 v[204:207], v228, %[srdX], 0 offen offset:512\n"
  "buffer_load_dwordx4 v[208:211], v228, %[srdX], 0 offen offset:768\n"
  "buffer_load_dword v232, v230, %[srdCt], 0 offen\n"
  "buffer_load_dword v232, v230, %[srdW1t], 0 offen\n"
  "buffer_load_dword v232, v231, %[srdWq], %[tWq] offen\n"
  "buffer_load_dword v232, v230, %[srdW2t], 0 offen\n"
  "s_waitcnt vmcnt(21)\n"
  "s_barrier\n"
  "ds_read_b64_tr_b16 a[0:1], v24 offset:0\n"
  "ds_read_b64_tr_b16 a[2:3], v24 offset:2048\n"
  "ds_read_b64_tr_b16 a[4:5], v25 offset:0\n"
  "ds_read_b64_tr_b16 a[6:7], v25 offset:2048\n"
  "ds_read_b64_tr_b16 a[8:9], v24 offset:512\n"
  "ds_read_b64_tr_b16 a[10:11], v24 offset:2560\n"
  "ds_read_b64_tr_b16 a[12:13], v25 offset:512\n"
  "ds_read_b64_tr_b16 a[14:15], v25 offset:2560\n"
  "ds_read_b128 v[36:39], v20 offset:0\n"
  "ds_read_b128 v[40:43], v20 offset:4096\n"
  "ds_read_b128 v[44:47], v20 offset:8192\n"
  "ds_read_b128 v[48:51], v20 offset:12288\n"
  "s_cmp_gt_u32 %[wid], 1\n"
  "s_cbranch_scc1 KVV_%=\n"
  "s_waitcnt vmcnt(16) lgkmcnt(0)\n"
  "v_mfma_f32_16x16x32_bf16 v[68:71], a[0:3], v[36:39], 0\n"
  "ds_read_b64_tr_b16 a[16:17], v24 offset:4096\n"
  "ds_read_b64_tr_b16 a[18:19], v24 offset:6144\n"
  "v_mfma_f32_16x16x32_bf16 v[72:75], a[0:3], v[40:43], 0\n"
  "ds_read_b64_tr_b16 a[20:21], v25 offset:4096\n"
  "v_mfma_f32_16x16x32_bf16 v[76:79], a[0:3], v[44:47], 0\n"
  "ds_read_b64_tr_b16 a[22:23], v25 offset:6144\n"
  "s_add_u32 s82, %[rot], 1\n"
  "s_and_b32 s82, s82, 3\n"
  "s_lshl_b32 s82, s82, 20\n"
  "s_add_u32 m0, s73, 81920\n"
  "s_add_u32 s64, s82, 0\n"
  "buffer_load_dwordx4 %[voffW], %[srdW], s64 offen lds\n"
  "v_mfma_f32_16x16x32_bf16 v[80:83], a[0:3], v[48:51], 0\n"
  "ds_read_b64_tr_b16 a[24:25], v24 offset:4608\n"
  "s_add_u32 s83, %[rot], 1\n"
  "s_and_b32 s83, s83, 3\n"
  "s_lshl_b32 s83, s83, 8\n"
  "s_add_u32 m0, s72, 16384\n"
  "v_mfma_f32_16x16x32_bf16 v[84:87], a[4:7], v[36:39], 0\n"
  "ds_read_b64_tr_b16 a[26:27], v24 offset:6656\n"
  "s_add_u32 s64, s83, 0\n"
  "v_mfma_f32_16x16x32_bf16 v[88:91], a[4:7], v[40:43], 0\n"
  "ds_read_b64_tr_b16 a[28:29], v25 offset:4608\n"
  "ds_read_b64_tr_b16 a[30:31], v25 offset:6656\n"
  "buffer_load_dwordx4 v16, %[srdC], s64 offen lds\n"
  "v_mfma_f32_16x16x32_bf16 v[92:95], a[4:7], v[44:47], 0\n"
  "ds_read_b128 v[52:55], v21 offset:0\n"
  "s_add_u32 m0, s72, 17408\n"
  "v_mfma_f32_16x16x32_bf16 v[96:99], a[4:7], v[48:51], 0\n"
  "ds_read_b128 v[56:59], v21 offset:4096\n"
  "s_add_u32 s64, s83, 0\n"
  "v_mfma_f32_16x16x32_bf16 v[100:103], a[8:11], v[36:39], 0\n"
  "ds_read_b128 v[60:63], v21 offset:8192\n"
  "buffer_load_dwordx4 v17, %[srdC], s64 offen lds\n"
  "v_mfma_f32_16x16x32_bf16 v[104:107], a[8:11], v[40:43], 0\n"
  "ds_read_b128 v[64:67], v21 offset:12288\n"
  "s_add_u32 m0, s72, 18432\n"
  "v_mfma_f32_16x16x32_bf16 v[108:111], a[8:11], v[44:47], 0\n"
  "s_add_u32 s64, s83, 0\n"
  "v_mfma_f32_16x16x32_bf16 v[112:115], a[8:11], v[48:51], 0\n"
  "buffer_load_dwordx4 v18, %[srdC], s64 offen lds\n"
  "v_mfma_f32_16x16x32_bf16 v[116:119], a[12:15], v[36:39], 0\n"
  "s_add_u32 m0, s72, 19456\n"
  "v_mfma_f32_16x16x32_bf16 v[120:123], a[12:15], v[40:43], 0\n"
  "s_add_u32 s64, s83, 0\n"
  "v_mfma_f32_16x16x32_bf16 v[124:127], a[12:15], v[44:47], 0\n"
  "buffer_load_dwordx4 v19, %[srdC], s64 offen lds\n"
  "v_mfma_f32_16x16x32_bf16 v[128:131], a[12:15], v[48:51], 0\n"
  "s_add_u32 m0, s73, 82944\n"
  "s_waitcnt vmcnt(17) lgkmcnt(0)\n"
  "v_mfma_f32_16x16x32_bf16 v[68:71], a[16:19], v[52:55], v[68:71]\n"
  "ds_read_b64_tr_b16 a[32:33], v24 offset:8192\n"
  "ds_read_b64_tr_b16 a[34:35], v24 offset:10240\n"
  "s_add_u32 s64, s82, 131072\n"
  "v_mfma_f32_16x16x32_bf16 v[72:75], a[16:19], v[56:59], v[72:75]\n"
  "ds_read_b64_tr_b16 a[36:37], v25 offset:8192\n"
  "buffer_load_dwordx4 %[voffW], %[srdW], s64 offen lds\n"
  "v_mfma_f32_16x16x32_bf16 v[76:79], a[16:19], v[60:63], v[76:79]\n"
  "ds_read_b64_tr_b16 a[38:39], v25 offset:10240\n"
  "s_add_u32 m0, s73, 83968\n"
  "v_mfma_f32_16x16x32_bf16 v[80:83], a[16:19], v[64:67], v[80:83]\n"
  "ds_read_b64_tr_b16 a[40:41], v24 offset:8704\n"
  "s_add_u32 s64, s82, 32768\n"
  "v_mfma_f32_16x16x32_bf16 v[84:87], a[20:23], v[52:55], v[84:87]\n"
  "ds_read_b64_tr_b16 a[42:43], v24 offset:10752\n"
  "buffer_load_dwordx4 %[voffW], %[srdW], s64 offen lds\n"
  "v_mfma_f32_16x16x32_bf16 v[88:91], a[20:23], v[56:59], v[88:91]\n"
  "ds_read_b64_tr_b16 a[44:45], v25 offset:8704\n"
  "ds_read_b64_tr_b16 a[46:47], v25 offset:10752\n"
  "s_add_u32 m0, s73, 84992\n"
  "v_mfma_f32_16x16x32_bf16 v[92:95], a[20:23], v[60:63], v[92:95]\n"
  "ds_read_b128 v[36:39], v22 offset:0\n"
  "s_add_u32 s64, s82, 163840\n"
  "v_mfma_f32_16x16x32_bf16 v[96:99], a[20:23], v[64:67], v[96:99]\n"
  "ds_read_b128 v[40:43], v22 offset:4096\n"
  "buffer_load_dwordx4 %[voffW], %[srdW], s64 offen lds\n"
  "v_mfma_f32_16x16x32_bf16 v[100:103], a[24:27], v[52:55], v[100:103]\n"
  "ds_read_b128 v[44:47], v22 offset:8192\n"
  "s_add_u32 m0, s73, 86016\n"
  "v_mfma_f32_16x16x32_bf16 v[104:107], a[24:27], v[56:59], v[104:107]\n"
  "ds_read_b128 v[48:51], v22 offset:12288\n"
  "s_add_u32 s64, s82, 262144\n"
  "v_mfma_f32_16x16x32_bf16 v[108:111], a[24:27], v[60:63], v[108:111]\n"
  "buffer_load_dwordx4 %[voffW], %[srdW], s64 offen lds\n"
  "v_mfma_f32_16x16x32_bf16 v[112:115], a[24:27], v[64:67], v[112:115]\n"
  "s_add_u32 m0, s73, 87040\n"
  "v_mfma_f32_16x16x32_bf16 v[116:119], a[28:31], v[52:55], v[116:119]\n"
  "s_add_u32 s64, s82, 393216\n"
  "v_mfma_f32_16x16x32_bf16 v[120:123], a[28:31], v[56:59], v[120:123]\n"
  "buffer_load_dwordx4 %[voffW], %[srdW], s64 offen lds\n"
  "v_mfma_f32_16x16x32_bf16 v[124:127], a[28:31], v[60:63], v[124:127]\n"
  "s_add_u32 m0, s73, 88064\n"
  "v_mfma_f32_16x16x32_bf16 v[128:131], a[28:31], v[64:67], v[128:131]\n"
  "s_add_u32 s64, s82, 294912\n"
  "s_waitcnt vmcnt(18) lgkmcnt(0)\n"
  "v_mfma_f32_16x16x32_bf16 v[68:71], a[32:35], v[36:39], v[68:71]\n"
  "ds_read_b64_tr_b16 a[48:49], v24 offset:12288\n"
  "ds_read_b64_tr_b16 a[50:51], v24 offset:14336\n"
  "buffer_load_dwordx4 %[voffW], %[srdW], s64 offen lds\n"
  "v_mfma_f32_16x16x32_bf16 v[72:75], a[32:35], v[40:43], v[72:75]\n"
  "ds_read_b64_tr_b16 a[52:53], v25 offset:12288\n"
  "s_add_u32 m0, s73, 89088\n"
  "v_mfma_f32_16x16x32_bf16 v[76:79], a[32:35], v[44:47], v[76:79]\n"
  "ds_read_b64_tr_b16 a[54:55], v25 offset:14336\n"
  "s_add_u32 s64, s82, 425984\n"
  "v_mfma_f32_16x16x32_bf16 v[80:83], a[32:35], v[48:51], v[80:83]\n"
  "ds_read_b64_tr_b16 a[56:57], v24 offset:12800\n"
  "buffer_load_dwordx4 %[voffW], %[srdW], s64 offen lds\n"
  "v_mfma_f32_16x16x32_bf16 v[84:87], a[36:39], v[36:39], v[84:87]\n"
  "ds_read_b64_tr_b16 a[58:59], v24 offset:14848\n"
  "s_add_u32 m0, s73, 65536\n"
  "v_mfma_f32_16x16x32_bf16 v[88:91], a[36:39], v[40:43], v[88:91]\n"
  "ds_read_b64_tr_b16 a[60:61], v25 offset:12800\n"
  "ds_read_b64_tr_b16 a[62:63], v25 offset:14848\n"
  "s_add_u32 s64, s82, 524288\n"
  "v_mfma_f32_16x16x32_bf16 v[92:95], a[36:39], v[44:47], v[92:95]\n"
  "ds_read_b128 v[52:55], v23 offset:0\n"
  "buffer_load_dwordx4 %[voffW], %[srdW], s64 offen lds\n"
  "v_mfma_f32_16x16x32_bf16 v[96:99], a[36:39], v[48:51], v[96:99]\n"
  "ds_read_b128 v[56:59], v23 offset:4096\n"
  "s_add_u32 m0, s73, 66560\n"
  "v_mfma_f32_16x16x32_bf16 v[100:103], a[40:43], v[36:39], v[100:103]\n"
  "ds_read_b128 v[60:63], v23 offset:8192\n"
  "s_add_u32 s64, s82, 655360\n"
  "v_mfma_f32_16x16x32_bf16 v[104:107], a[40:43], v[40:43], v[104:107]\n"
  "ds_read_b128 v[64:67], v23 offset:12288\n"
  "buffer_load_dwordx4 %[voffW], %[srdW], s64 offen lds\n"
  "v_mfma_f32_16x16x32_bf16 v[108:111], a[40:43], v[44:47], v[108:111]\n"
  "s_add_u32 m0, s73, 67584\n"
  "v_mfma_f32_16x16x32_bf16 v[112:115], a[40:43], v[48:51], v[112:115]\n"
  "s_add_u32 s64, s82, 557056\n"
  "v_mfma_f32_16x16x32_bf16 v[116:119], a[44:47], v[36:39], v[116:119]\n"
  "buffer_load_dwordx4 %[voffW], %[srdW], s64 offen lds\n"
  "v_mfma_f32_16x16x32_bf16 v[120:123], a[44:47], v[40:43], v[120:123]\n"
  "s_add_u32 m0, s73, 68608\n"
  "v_mfma_f32_16x16x32_bf16 v[124:127], a[44:47], v[44:47], v[124:127]\n"
  "s_add_u32 s64, s82, 688128\n"
  "v_mfma_f32_16x16x32_bf16 v[128:131], a[44:47], v[48:51], v[128:131]\n"
  "buffer_load_dwordx4 %[voffW], %[srdW], s64 offen lds\n"
  "s_waitcnt vmcnt(8) lgkmcnt(0)\n"
  "s_barrier\n"
  "v_mfma_f32_16x16x32_bf16 v[68:71], a[48:51], v[52:55], v[68:71]\n"
  "ds_read_b64_tr_b16 a[64:65], v24 offset:16384\n"
  "ds_read_b64_tr_b16 a[66:67], v24 offset:18432\n"
  "s_add_u32 m0, s73, 69632\n"
  "v_mfma_f32_16x16x32_bf16 v[72:75], a[48:51], v[56:59], v[72:75]\n"
  "ds_read_b64_tr_b16 a[68:69], v25 offset:16384\n"
  "s_add_u32 s64, s82, 786432\n"
  "v_mfma_f32_16x16x32_bf16 v[76:79], a[48:51], v[60:63], v[76:79]\n"
  "ds_read_b64_tr_b16 a[70:71], v25 offset:18432\n"
  "buffer_load_dwordx4 %[voffW], %[srdW], s64 offen lds\n"
  "v_mfma_f32_16x16x32_bf16 v[80:83], a[48:51], v[64:67], v[80:83]\n"
  "ds_read_b64_tr_b16 a[72:73], v24 offset:16896\n"
  "s_add_u32 m0, s73, 70656\n"
  "v_mov_b32 v212, 0x3f803f80\n"
  "v_mfma_f32_16x16x32_bf16 v[84:87], a[52:55], v[52:55], v[84:87]\n"
  "ds_read_b64_tr_b16 a[74:75], v24 offset:18944\n"
  "s_add_u32 s64, s82, 917504\n"
  "v_mfma_f32_16x16x32_bf16 v[88:91], a[52:55], v[56:59], v[88:91]\n"
  "ds_read_b64_tr_b16 a[76:77], v25 offset:16896\n"
  "ds_read_b64_tr_b16 a[78:79], v25 offset:18944\n"
  "buffer_load_dwordx4 %[voffW], %[srdW], s64 offen lds\n"
  "v_mov_b32 v213, 0\n"
  "v_mfma_f32_16x16x32_bf16 v[92:95], a[52:55], v[60:63], v[92:95]\n"
  "ds_read_b128 v[36:39], v20 offset:16384\n"
  "s_add_u32 m0, s73, 71680\n"
  "v_mfma_f32_16x16x32_bf16 v[96:99], a[52:55], v[64:67], v[96:99]\n"
  "ds_read_b128 v[40:43], v20 offset:20480\n"
  "s_add_u32 s64, s82, 819200\n"
  "v_mov_b32 v214, 0\n"
  "v_mfma_f32_16x16x32_bf16 v[100:103], a[56:59], v[52:55], v[100:103]\n"
  "ds_read_b128 v[44:47], v20 offset:24576\n"
  "buffer_load_dwordx4 %[voffW], %[srdW], s64 offen lds\n"
  "v_mfma_f32_16x16x32_bf16 v[104:107], a[56:59], v[56:59], v[104:107]\n"
  "ds_read_b128 v[48:51], v20 offset:28672\n"
  "s_add_u32 m0, s73, 72704\n"
  "v_dot2c_f32_bf16 v213, v196, v212\n"
  "v_mfma_f32_16x16x32_bf16 v[108:111], a[56:59], v[60:63], v[108:111]\n"
  "s_add_u32 s64, s82, 950272\n"
  "v_dot2c_f32_bf16 v214, v196, v196\n"
  "v_mfma_f32_16x16x32_bf16 v[112:115], a[56:59], v[64:67], v[112:115]\n"
  "buffer_load_dwordx4 %[voffW], %[srdW], s64 offen lds\n"
  "s_add_u32 s83, %[rot], 2\n"
  "s_and_b32 s83, s83, 3\n"
  "s_lshl_b32 s83, s83, 8\n"
  "v_dot2c_f32_bf16 v213, v197, v212\n"
  "v_mfma_f32_16x16x32_bf16 v[116:119], a[60:63], v[52:55], v[116:119]\n"
  "s_add_u32 m0, s72, 32768\n"
  "v_dot2c_f32_bf16 v214, v197, v197\n"
  "v_mfma_f32_16x16x32_bf16 v[120:123], a[60:63], v[56:59], v[120:123]\n"
  "s_add_u32 s64, s83, 0\n"
  "v_dot2c_f32_bf16 v213, v198, v212\n"
  "v_dot2c_f32_bf16 v214, v198, v198\n"
  "v_mfma_f32_16x16x32_bf16 v[124:127], a[60:63], v[60:63], v[124:127]\n"
  "buffer_load_dwordx4 v16, %[srdC], s64 offen lds\n"
  "v_dot2c_f32_bf16 v213, v199, v212\n"
  "v_mfma_f32_16x16x32_bf16 v[128:131], a[60:63], v[64:67], v[128:131]\n"
  "s_add_u32 m0, s72, 33792\n"
  "v_dot2c_f32_bf16 v214, v199, v199\n"
  "v_dot2c_f32_bf16 v213, v200, v212\n"
  "s_waitcnt vmcnt(9) lgkmcnt(0)\n"
  "v_mfma_f32_16x16x32_bf16 v[68:71], a[64:67], v[36:39], v[68:71]\n"
  "ds_read_b64_tr_b16 a[80:81], v24 offset:20480\n"
  "ds_read_b64_tr_b16 a[82:83], v24 offset:22528\n"
  "s_add_u32 s64, s83, 0\n"
  "v_mfma_f32_16x16x32_bf16 v[72:75], a[64:67], v[40:43], v[72:75]\n"
  "ds_read_b64_tr_b16 a[84:85], v25 offset:20480\n"
  "buffer_load_dwordx4 v17, %[srdC], s64 offen lds\n"
  "v_dot2c_f32_bf16 v214, v200, v200\n"
  "v_mfma_f32_16x16x32_bf16 v[76:79], a[64:67], v[44:47], v[76:79]\n"
  "ds_read_b64_tr_b16 a[86:87], v25 offset:22528\n"
  "s_add_u32 m0, s72, 34816\n"
  "v_dot2c_f32_bf16 v213, v201, v212\n"
  "v_mfma_f32_16x16x32_bf16 v[80:83], a[64:67], v[48:51], v[80:83]\n"
  "ds_read_b64_tr_b16 a[88:89], v24 offset:20992\n"
  "s_add_u32 s64, s83, 0\n"
  "v_mfma_f32_16x16x32_bf16 v[84:87], a[68:71], v[36:39], v[84:87]\n"
  "ds_read_b64_tr_b16 a[90:91], v24 offset:23040\n"
  "buffer_load_dwordx4 v18, %[srdC], s64 offen lds\n"
  "v_dot2c_f32_bf16 v214, v201, v201\n"
  "v_mfma_f32_16x16x32_bf16 v[88:91], a[68:71], v[40:43], v[88:91]\n"
  "ds_read_b64_tr_b16 a[92:93], v25 offset:20992\n"
  "ds_read_b64_tr_b16 a[94:95], v25 offset:23040\n"
  "s_add_u32 m0, s72, 35840\n"
  "v_mfma_f32_16x16x32_bf16 v[92:95], a[68:71], v[44:47], v[92:95]\n"
  "ds_read_b128 v[52:55], v21 offset:16384\n"
  "s_add_u32 s64, s83, 0\n"
  "v_dot2c_f32_bf16 v213, v202, v212\n"
  "v_mfma_f32_16x16x32_bf16 v[96:99], a[68:71], v[48:51], v[96:99]\n"
  "ds_read_b128 v[56:59], v21 offset:20480\n"
  "buffer_load_dwordx4 v19, %[srdC], s64 offen lds\n"
  "s_add_u32 s82, %[rot], 2\n"
  "s_and_b32 s82, s82, 3\n"
  "s_lshl_b32 s82, s82, 20\n"
  "v_mfma_f32_16x16x32_bf16 v[100:103], a[72:75], v[36:39], v[100:103]\n"
  "ds_read_b128 v[60:63], v21 offset:24576\n"
  "s_add_u32 m0, s73, 73728\n"
  "v_dot2c_f32_bf16 v214, v202, v202\n"
  "v_mfma_f32_16x16x32_bf16 v[104:107], a[72:75], v[40:43], v[104:107]\n"
  "ds_read_b128 v[64:67], v21 offset:28672\n"
  "s_add_u32 s64, s82, 0\n"
  "v_mfma_f32_16x16x32_bf16 v[108:111], a[72:75], v[44:47], v[108:111]\n"
  "buffer_load_dwordx4 %[voffW], %[srdW], s64 offen lds\n"
  "v_dot2c_f32_bf16 v213, v203, v212\n"
  "v_dot2c_f32_bf16 v214, v203, v203\n"
  "v_mfma_f32_16x16x32_bf16 v[112:115], a[72:75], v[48:51], v[112:115]\n"
  "s_add_u32 m0, s73, 74752\n"
  "v_dot2c_f32_bf16 v213, v204, v212\n"
  "v_mfma_f32_16x16x32_bf16 v[116:119], a[76:79], v[36:39], v[116:119]\n"
  "s_add_u32 s64, s82, 131072\n"
  "v_dot2c_f32_bf16 v214, v204, v204\n"
  "v_dot2c_f32_bf16 v213, v205, v212\n"
  "v_mfma_f32_16x16x32_bf16 v[120:123], a[76:79], v[40:43], v[120:123]\n"
  "buffer_load_dwordx4 %[voffW], %[srdW], s64 offen lds\n"
  "v_dot2c_f32_bf16 v214, v205, v205\n"
  "v_mfma_f32_16x16x32_bf16 v[124:127], a[76:79], v[44:47], v[124:127]\n"
  "s_add_u32 m0, s73, 75776\n"
  "v_dot2c_f32_bf16 v213, v206, v212\n"
  "v_dot2c_f32_bf16 v214, v206, v206\n"
  "v_mfma_f32_16x16x32_bf16 v[128:131], a[76:79], v[48:51], v[128:131]\n"
  "s_add_u32 s64, s82, 32768\n"
  "v_dot2c_f32_bf16 v213, v207, v212\n"
  "s_waitcnt vmcnt(10) lgkmcnt(0)\n"
  "v_mfma_f32_16x16x32_bf16 v[68:71], a[80:83], v[52:55], v[68:71]\n"
  "ds_read_b64_tr_b16 a[96:97], v24 offset:0\n"
  "ds_read_b64_tr_b16 a[98:99], v24 offset:2048\n"
  "buffer_load_dwordx4 %[voffW], %[srdW], s64 offen lds\n"
  "v_dot2c_f32_bf16 v214, v207, v207\n"
  "v_mfma_f32_16x16x32_bf16 v[72:75], a[80:83], v[56:59], v[72:75]\n"
  "ds_read_b64_tr_b16 a[100:101], v25 offset:0\n"
  "s_add_u32 m0, s73, 76800\n"
  "v_dot2c_f32_bf16 v213, v208, v212\n"
  "v_mfma_f32_16x16x32_bf16 v[76:79], a[80:83], v[60:63], v[76:79]\n"
  "ds_read_b64_tr_b16 a[102:103], v25 offset:2048\n"
  "s_add_u32 s64, s82, 163840\n"
  "v_mfma_f32_16x16x32_bf16 v[80:83], a[80:83], v[64:67], v[80:83]\n"
  "ds_read_b64_tr_b16 a[104:105], v24 offset:512\n"
  "buffer_load_dwordx4 %[voffW], %[srdW], s64 offen lds\n"
  "v_dot2c_f32_bf16 v214, v208, v208\n"
  "v_mfma_f32_16x16x32_bf16 v[84:87], a[84:87], v[52:55], v[84:87]\n"
  "ds_read_b64_tr_b16 a[106:107], v24 offset:2560\n"
  "s_add_u32 m0, s73, 77824\n"
  "v_mfma_f32_16x16x32_bf16 v[88:91], a[84:87], v[56:59], v[88:91]\n"
  "ds_read_b64_tr_b16 a[108:109], v25 offset:512\n"
  "ds_read_b64_tr_b16 a[110:111], v25 offset:2560\n"
  "s_add_u32 s64, s82, 262144\n"
  "v_dot2c_f32_bf16 v213, v209, v212\n"
  "v_mfma_f32_16x16x32_bf16 v[92:95], a[84:87], v[60:63], v[92:95]\n"
  "ds_read_b128 v[36:39], v22 offset:16384\n"
  "buffer_load_dwordx4 %[voffW], %[srdW], s64 offen lds\n"
  "v_mfma_f32_16x16x32_bf16 v[96:99], a[84:87], v[64:67], v[96:99]\n"
  "ds_read_b128 v[40:43], v22 offset:20480\n"
  "s_add_u32 m0, s73, 78848\n"
  "v_dot2c_f32_bf16 v214, v209, v209\n"
  "v_mfma_f32_16x16x32_bf16 v[100:103], a[88:91], v[52:55], v[100:103]\n"
  "ds_read_b128 v[44:47], v22 offset:24576\n"
  "s_add_u32 s64, s82, 393216\n"
  "v_mfma_f32_16x16x32_bf16 v[104:107], a[88:91], v[56:59], v[104:107]\n"
  "ds_read_b128 v[48:51], v22 offset:28672\n"
  "buffer_load_dwordx4 %[voffW], %[srdW], s64 offen lds\n"
  "v_dot2c_f32_bf16 v213, v210, v212\n"
  "v_mfma_f32_16x16x32_bf16 v[108:111], a[88:91], v[60:63], v[108:111]\n"
  "s_add_u32 m0, s73, 79872\n"
  "v_dot2c_f32_bf16 v214, v210, v210\n"
  "v_mfma_f32_16x16x32_bf16 v[112:115], a[88:91], v[64:67], v[112:115]\n"
  "s_add_u32 s64, s82, 294912\n"
  "v_dot2c_f32_bf16 v213, v211, v212\n"
  "v_dot2c_f32_bf16 v214, v211, v211\n"
  "v_mfma_f32_16x16x32_bf16 v[116:119], a[92:95], v[52:55], v[116:119]\n"
  "buffer_load_dwordx4 %[voffW], %[srdW], s64 offen lds\n"
  "v_add_f32_dpp v213, v213, v213 row_ror:4 row_mask:0xf bank_mask:0xf\n"
  "v_mfma_f32_16x16x32_bf16 v[120:123], a[92:95], v[56:59], v[120:123]\n"
  "s_add_u32 m0, s73, 80896\n"
  "v_add_f32_dpp v214, v214, v214 row_ror:4 row_mask:0xf bank_mask:0xf\n"
  "v_add_f32_dpp v213, v213, v213 row_ror:8 row_mask:0xf bank_mask:0xf\n"
  "v_mfma_f32_16x16x32_bf16 v[124:127], a[92:95], v[60:63], v[124:127]\n"
  "s_add_u32 s64, s82, 425984\n"
  "v_add_f32_dpp v214, v214, v214 row_ror:8 row_mask:0xf bank_mask:0xf\n"
  "v_mfma_f32_16x16x32_bf16 v[128:131], a[92:95], v[64:67], v[128:131]\n"
  "buffer_load_dwordx4 %[voffW], %[srdW], s64 offen lds\n"
  "v_mov_b32 v215, v213\n"
  "v_mov_b32 v216, v214\n"
  "s_waitcnt vmcnt(12) lgkmcnt(0)\n"
  "v_mfma_f32_16x16x32_bf16 v[68:71], a[96:99], v[36:39], v[68:71]\n"
  "ds_read_b64_tr_b16 a[112:113], v24 offset:4096\n"
  "ds_read_b64_tr_b16 a[114:115], v24 offset:6144\n"
  "s_add_u32 m0, s73, 81920\n"
  "v_mfma_f32_16x16x32_bf16 v[72:75], a[96:99], v[40:43], v[72:75]\n"
  "ds_read_b64_tr_b16 a[116:117], v25 offset:4096\n"
  "s_add_u32 s64, s82, 524288\n"
  "v_permlane16_swap_b32 v213, v215\n"
  "v_mfma_f32_16x16x32_bf16 v[76:79], a[96:99], v[44:47], v[76:79]\n"
  "ds_read_b64_tr_b16 a[118:119], v25 offset:6144\n"
  "buffer_load_dwordx4 %[voffW], %[srdW], s64 offen lds\n"
  "v_mfma_f32_16x16x32_bf16 v[80:83], a[96:99], v[48:51], v[80:83]\n"
  "ds_read_b64_tr_b16 a[120:121], v24 offset:4608\n"
  "s_add_u32 m0, s73, 82944\n"
  "v_permlane16_swap_b32 v214, v216\n"
  "v_mfma_f32_16x16x32_bf16 v[84:87], a[100:103], v[36:39], v[84:87]\n"
  "ds_read_b64_tr_b16 a[122:123], v24 offset:6656\n"
  "s_add_u32 s64, s82, 655360\n"
  "v_add_f32 v213, v213, v215\n"
  "v_mfma_f32_16x16x32_bf16 v[88:91], a[100:103], v[40:43], v[88:91]\n"
  "ds_read_b64_tr_b16 a[124:125], v25 offset:4608\n"
  "ds_read_b64_tr_b16 a[126:127], v25 offset:6656\n"
  "buffer_load_dwordx4 %[voffW], %[srdW], s64 offen lds\n"
  "v_mfma_f32_16x16x32_bf16 v[92:95], a[100:103], v[44:47], v[92:95]\n"
  "ds_read_b128 v[52:55], v23 offset:16384\n"
  "s_add_u32 m0, s73, 83968\n"
  "v_add_f32 v214, v214, v216\n"
  "v_mfma_f32_16x16x32_bf16 v[96:99], a[100:103], v[48:51], v[96:99]\n"
  "ds_read_b128 v[56:59], v23 offset:20480\n"
  "s_add_u32 s64, s82, 557056\n"
  "v_mfma_f32_16x16x32_bf16 v[100:103], a[104:107], v[36:39], v[100:103]\n"
  "ds_read_b128 v[60:63], v23 offset:24576\n"
  "buffer_load_dwordx4 %[voffW], %[srdW], s64 offen lds\n"
  "v_mov_b32 v215, v213\n"
  "v_mfma_f32_16x16x32_bf16 v[104:107], a[104:107], v[40:43], v[104:107]\n"
  "ds_read_b128 v[64:67], v23 offset:28672\n"
  "s_add_u32 m0, s73, 84992\n"
  "v_mfma_f32_16x16x32_bf16 v[108:111], a[104:107], v[44:47], v[108:111]\n"
  "s_add_u32 s64, s82, 688128\n"
  "v_mov_b32 v216, v214\n"
  "v_permlane32_swap_b32 v213, v215\n"
  "v_mfma_f32_16x16x32_bf16 v[112:115], a[104:107], v[48:51], v[112:115]\n"
  "buffer_load_dwordx4 %[voffW], %[srdW], s64 offen lds\n"
  "v_permlane32_swap_b32 v214, v216\n"
  "v_mfma_f32_16x16x32_bf16 v[116:119], a[108:111], v[36:39], v[116:119]\n"
  "s_add_u32 m0, s73, 86016\n"
  "v_add_f32 v213, v213, v215\n"
  "v_add_f32 v214, v214, v216\n"
  "v_mfma_f32_16x16x32_bf16 v[120:123], a[108:111], v[40:43], v[120:123]\n"
  "s_add_u32 s64, s82, 786432\n"
  "v_mul_f32 v213, 0x3b000000, v213\n"
  "v_mfma_f32_16x16x32_bf16 v[124:127], a[108:111], v[44:47], v[124:127]\n"
  "buffer_load_dwordx4 %[voffW], %[srdW], s64 offen lds\n"
  "v_mul_f32 v214, 0x3b000000, v214\n"
  "v_fma_f32 v214, -v213, v213, v214\n"
  "v_mfma_f32_16x16x32_bf16 v[128:131], a[108:111], v[48:51], v[128:131]\n"
  "s_add_u32 m0, s73, 87040\n"
  "v_max_f32 v214, 0, v214\n"
  "s_waitcnt vmcnt(9) lgkmcnt(0)\n"
  "s_barrier\n"
  "v_mfma_f32_16x16x32_bf16 v[68:71], a[112:115], v[52:55], v[68:71]\n"
  "ds_read_b64_tr_b16 a[128:129], v24 offset:8192\n"
  "ds_read_b64_tr_b16 a[130:131], v24 offset:10240\n"
  "s_add_u32 s64, s82, 917504\n"
  "v_add_f32 v214, 0x3727c5ac, v214\n"
  "v_mfma_f32_16x16x32_bf16 v[72:75], a[112:115], v[56:59], v[72:75]\n"
  "ds_read_b64_tr_b16 a[132:133], v25 offset:8192\n"
  "buffer_load_dwordx4 %[voffW], %[srdW], s64 offen lds\n"
  "v_mfma_f32_16x16x32_bf16 v[76:79], a[112:115], v[60:63], v[76:79]\n"
  "ds_read_b64_tr_b16 a[134:135], v25 offset:10240\n"
  "s_add_u32 m0, s73, 88064\n"
  "v_mfma_f32_16x16x32_bf16 v[80:83], a[112:115], v[64:67], v[80:83]\n"
  "ds_read_b64_tr_b16 a[136:137], v24 offset:8704\n"
  "s_add_u32 s64, s82, 819200\n"
  "v_mfma_f32_16x16x32_bf16 v[84:87], a[116:119], v[52:55], v[84:87]\n"
  "ds_read_b64_tr_b16 a[138:139], v24 offset:10752\n"
  "buffer_load_dwordx4 %[voffW], %[srdW], s64 offen lds\n"
  "v_rsq_f32 v214, v214\n"
  "v_mfma_f32_16x16x32_bf16 v[88:91], a[116:119], v[56:59], v[88:91]\n"
  "ds_read_b64_tr_b16 a[140:141], v25 offset:8704\n"
  "ds_read_b64_tr_b16 a[142:143], v25 offset:10752\n"
  "s_add_u32 m0, s73, 89088\n"
  "v_mfma_f32_16x16x32_bf16 v[92:95], a[116:119], v[60:63], v[92:95]\n"
  "ds_read_b128 v[36:39], v20 offset:32768\n"
  "s_add_u32 s64, s82, 950272\n"
  "v_mul_f32 v213, v213, v214\n"
  "v_mfma_f32_16x16x32_bf16 v[96:99], a[116:119], v[64:67], v[96:99]\n"
  "ds_read_b128 v[40:43], v20 offset:36864\n"
  "buffer_load_dwordx4 %[voffW], %[srdW], s64 offen lds\n"
  "s_add_u32 s83, %[rot], 3\n"
  "s_and_b32 s83, s83, 3\n"
  "s_lshl_b32 s83, s83, 8\n"
  "v_lshlrev_b32 v220, 16, v196\n"
  "v_mfma_f32_16x16x32_bf16 v[100:103], a[120:123], v[52:55], v[100:103]\n"
  "ds_read_b128 v[44:47], v20 offset:40960\n"
  "s_add_u32 m0, s72, 49152\n"
  "v_mfma_f32_16x16x32_bf16 v[104:107], a[120:123], v[56:59], v[104:107]\n"
  "ds_read_b128 v[48:51], v20 offset:45056\n"
  "s_add_u32 s64, s83, 0\n"
  "v_and_b32 v221, 0xffff0000, v196\n"
  "v_mfma_f32_16x16x32_bf16 v[108:111], a[120:123], v[60:63], v[108:111]\n"
  "buffer_load_dwordx4 v16, %[srdC], s64 offen lds\n"
  "v_fma_f32 v220, v220, v214, -v213\n"
  "v_mfma_f32_16x16x32_bf16 v[112:115], a[120:123], v[64:67], v[112:115]\n"
  "s_add_u32 m0, s72, 50176\n"
  "v_fma_f32 v221, v221, v214, -v213\n"
  "v_cvt_pk_bf16_f32 v196, v220, v221\n"
  "v_mfma_f32_16x16x32_bf16 v[116:119], a[124:127], v[52:55], v[116:119]\n"
  "s_add_u32 s64, s83, 0\n"
  "v_lshlrev_b32 v222, 16, v197\n"
  "v_mfma_f32_16x16x32_bf16 v[120:123], a[124:127], v[56:59], v[120:123]\n"
  "buffer_load_dwordx4 v17, %[srdC], s64 offen lds\n"
  "v_and_b32 v223, 0xffff0000, v197\n"
  "v_fma_f32 v222, v222, v214, -v213\n"
  "v_mfma_f32_16x16x32_bf16 v[124:127], a[124:127], v[60:63], v[124:127]\n"
  "s_add_u32 m0, s72, 51200\n"
  "v_fma_f32 v223, v223, v214, -v213\n"
  "v_mfma_f32_16x16x32_bf16 v[128:131], a[124:127], v[64:67], v[128:131]\n"
  "s_add_u32 s64, s83, 0\n"
  "v_cvt_pk_bf16_f32 v197, v222, v223\n"
  "v_lshlrev_b32 v224, 16, v198\n"
  "s_waitcnt vmcnt(10) lgkmcnt(0)\n"
  "v_mfma_f32_16x16x32_bf16 v[68:71], a[128:131], v[36:39], v[68:71]\n"
  "ds_read_b64_tr_b16 a[144:145], v24 offset:12288\n"
  "ds_read_b64_tr_b16 a[146:147], v24 offset:14336\n"
  "buffer_load_dwordx4 v18, %[srdC], s64 offen lds\n"
  "v_mfma_f32_16x16x32_bf16 v[72:75], a[128:131], v[40:43], v[72:75]\n"
  "ds_read_b64_tr_b16 a[148:149], v25 offset:12288\n"
  "s_add_u32 m0, s72, 52224\n"
  "v_and_b32 v225, 0xffff0000, v198\n"
  "v_mfma_f32_16x16x32_bf16 v[76:79], a[128:131], v[44:47], v[76:79]\n"
  "ds_read_b64_tr_b16 a[150:151], v25 offset:14336\n"
  "s_add_u32 s64, s83, 0\n"
  "v_mfma_f32_16x16x32_bf16 v[80:83], a[128:131], v[48:51], v[80:83]\n"
  "ds_read_b64_tr_b16 a[152:153], v24 offset:12800\n"
  "buffer_load_dwordx4 v19, %[srdC], s64 offen lds\n"
  "s_add_u32 s82, %[rot], 3\n"
  "s_and_b32 s82, s82, 3\n"
  "s_lshl_b32 s82, s82, 20\n"
  "v_fma_f32 v224, v224, v214, -v213\n"
  "v_mfma_f32_16x16x32_bf16 v[84:87], a[132:135], v[36:39], v[84:87]\n"
  "ds_read_b64_tr_b16 a[154:155], v24 offset:14848\n"
  "s_add_u32 m0, s73, 65536\n"
  "v_mfma_f32_16x16x32_bf16 v[88:91], a[132:135], v[40:43], v[88:91]\n"
  "ds_read_b64_tr_b16 a[156:157], v25 offset:12800\n"
  "ds_read_b64_tr_b16 a[158:159], v25 offset:14848\n"
  "s_add_u32 s64, s82, 0\n"
  "v_fma_f32 v225, v225, v214, -v213\n"
  "v_mfma_f32_16x16x32_bf16 v[92:95], a[132:135], v[44:47], v[92:95]\n"
  "ds_read_b128 v[52:55], v21 offset:32768\n"
  "buffer_load_dwordx4 %[voffW], %[srdW], s64 offen lds\n"
  "v_mfma_f32_16x16x32_bf16 v[96:99], a[132:135], v[48:51], v[96:99]\n"
  "ds_read_b128 v[56:59], v21 offset:36864\n"
  "s_add_u32 m0, s73, 66560\n"
  "v_cvt_pk_bf16_f32 v198, v224, v225\n"
  "v_mfma_f32_16x16x32_bf16 v[100:103], a[136:139], v[36:39], v[100:103]\n"
  "ds_read_b128 v[60:63], v21 offset:40960\n"
  "s_add_u32 s64, s82, 131072\n"
  "v_mfma_f32_16x16x32_bf16 v[104:107], a[136:139], v[40:43], v[104:107]\n"
  "ds_read_b128 v[64:67], v21 offset:45056\n"
  "buffer_load_dwordx4 %[voffW], %[srdW], s64 offen lds\n"
  "v_lshlrev_b32 v226, 16, v199\n"
  "v_mfma_f32_16x16x32_bf16 v[108:111], a[136:139], v[44:47], v[108:111]\n"
  "s_add_u32 m0, s73, 67584\n"
  "v_and_b32 v227, 0xffff0000, v199\n"
  "v_mfma_f32_16x16x32_bf16 v[112:115], a[136:139], v[48:51], v[112:115]\n"
  "s_add_u32 s64, s82, 32768\n"
  "v_fma_f32 v226, v226, v214, -v213\n"
  "v_fma_f32 v227, v227, v214, -v213\n"
  "v_mfma_f32_16x16x32_bf16 v[116:119], a[140:143], v[36:39], v[116:119]\n"
  "buffer_load_dwordx4 %[voffW], %[srdW], s64 offen lds\n"
  "v_cvt_pk_bf16_f32 v199, v226, v227\n"
  "v_mfma_f32_16x16x32_bf16 v[120:123], a[140:143], v[40:43], v[120:123]\n"
  "s_add_u32 m0, s73, 68608\n"
  "buffer_store_dwordx4 v[196:199], %[L16], %[srdWS], s80 offen offset:0 sc1\n"
  "v_lshlrev_b32 v220, 16, v200\n"
  "v_mfma_f32_16x16x32_bf16 v[124:127], a[140:143], v[44:47], v[124:127]\n"
  "s_add_u32 s64, s82, 163840\n"
  "v_and_b32 v221, 0xffff0000, v200\n"
  "v_mfma_f32_16x16x32_bf16 v[128:131], a[140:143], v[48:51], v[128:131]\n"
  "buffer_load_dwordx4 %[voffW], %[srdW], s64 offen lds\n"
  "v_fma_f32 v220, v220, v214, -v213\n"
  "v_fma_f32 v221, v221, v214, -v213\n"
  "s_waitcnt vmcnt(13) lgkmcnt(0)\n"
  "v_mfma_f32_16x16x32_bf16 v[68:71], a[144:147], v[52:55], v[68:71]\n"
  "ds_read_b64_tr_b16 a[160:161], v24 offset:16384\n"
  "ds_read_b64_tr_b16 a[162:163], v24 offset:18432\n"
  "s_add_u32 m0, s73, 69632\n"
  "v_cvt_pk_bf16_f32 v200, v220, v221\n"
  "v_mfma_f32_16x16x32_bf16 v[72:75], a[144:147], v[56:59], v[72:75]\n"
  "ds_read_b64_tr_b16 a[164:165], v25 offset:16384\n"
  "s_add_u32 s64, s82, 262144\n"
  "v_mfma_f32_16x16x32_bf16 v[76:79], a[144:147], v[60:63], v[76:79]\n"
  "ds_read_b64_tr_b16 a[166:167], v25 offset:18432\n"
  "buffer_load_dwordx4 %[voffW], %[srdW], s64 offen lds\n"
  "v_lshlrev_b32 v222, 16, v201\n"
  "v_mfma_f32_16x16x32_bf16 v[80:83], a[144:147], v[64:67], v[80:83]\n"
  "ds_read_b64_tr_b16 a[168:169], v24 offset:16896\n"
  "s_add_u32 m0, s73, 70656\n"
  "v_mfma_f32_16x16x32_bf16 v[84:87], a[148:151], v[52:55], v[84:87]\n"
  "ds_read_b64_tr_b16 a[170:171], v24 offset:18944\n"
  "s_add_u32 s64, s82, 393216\n"
  "v_and_b32 v223, 0xffff0000, v201\n"
  "v_mfma_f32_16x16x32_bf16 v[88:91], a[148:151], v[56:59], v[88:91]\n"
  "ds_read_b64_tr_b16 a[172:173], v25 offset:16896\n"
  "ds_read_b64_tr_b16 a[174:175], v25 offset:18944\n"
  "buffer_load_dwordx4 %[voffW], %[srdW], s64 offen lds\n"
  "v_mfma_f32_16x16x32_bf16 v[92:95], a[148:151], v[60:63], v[92:95]\n"
  "ds_read_b128 v[36:39], v22 offset:32768\n"
  "s_add_u32 m0, s73, 71680\n"
  "v_fma_f32 v222, v222, v214, -v213\n"
  "v_mfma_f32_16x16x32_bf16 v[96:99], a[148:151], v[64:67], v[96:99]\n"
  "ds_read_b128 v[40:43], v22 offset:36864\n"
  "s_add_u32 s64, s82, 294912\n"
  "v_mfma_f32_16x16x32_bf16 v[100:103], a[152:155], v[52:55], v[100:103]\n"
  "ds_read_b128 v[44:47], v22 offset:40960\n"
  "buffer_load_dwordx4 %[voffW], %[srdW], s64 offen lds\n"
  "v_fma_f32 v223, v223, v214, -v213\n"
  "v_mfma_f32_16x16x32_bf16 v[104:107], a[152:155], v[56:59], v[104:107]\n"
  "ds_read_b128 v[48:51], v22 offset:45056\n"
  "s_add_u32 m0, s73, 72704\n"
  "v_mfma_f32_16x16x32_bf16 v[108:111], a[152:155], v[60:63], v[108:111]\n"
  "s_add_u32 s64, s82, 425984\n"
  "v_cvt_pk_bf16_f32 v201, v222, v223\n"
  "v_lshlrev_b32 v224, 16, v202\n"
  "v_mfma_f32_16x16x32_bf16 v[112:115], a[152:155], v[64:67], v[112:115]\n"
  "buffer_load_dwordx4 %[voffW], %[srdW], s64 offen lds\n"
  "v_and_b32 v225, 0xffff0000, v202\n"
  "v_mfma_f32_16x16x32_bf16 v[116:119], a[156:159], v[52:55], v[116:119]\n"
  "s_add_u32 m0, s73, 73728\n"
  "v_fma_f32 v224, v224, v214, -v213\n"
  "v_fma_f32 v225, v225, v214, -v213\n"
  "v_mfma_f32_16x16x32_bf16 v[120:123], a[156:159], v[56:59], v[120:123]\n"
  "s_add_u32 s64, s82, 524288\n"
  "v_cvt_pk_bf16_f32 v202, v224, v225\n"
  "v_mfma_f32_16x16x32_bf16 v[124:127], a[156:159], v[60:63], v[124:127]\n"
  "buffer_load_dwordx4 %[voffW], %[srdW], s64 offen lds\n"
  "v_lshlrev_b32 v226, 16, v203\n"
  "v_and_b32 v227, 0xffff0000, v203\n"
  "v_mfma_f32_16x16x32_bf16 v[128:131], a[156:159], v[64:67], v[128:131]\n"
  "s_add_u32 m0, s73, 74752\n"
  "v_fma_f32 v226, v226, v214, -v213\n"
  "s_waitcnt vmcnt(14) lgkmcnt(0)\n"
  "v_mfma_f32_16x16x32_bf16 v[68:71], a[160:163], v[36:39], v[68:71]\n"
  "ds_read_b64_tr_b16 a[176:177], v24 offset:20480\n"
  "ds_read_b64_tr_b16 a[178:179], v24 offset:22528\n"
  "s_add_u32 s64, s82, 655360\n"
  "v_fma_f32 v227, v227, v214, -v213\n"
  "v_mfma_f32_16x16x32_bf16 v[72:75], a[160:163], v[40:43], v[72:75]\n"
  "ds_read_b64_tr_b16 a[180:181], v25 offset:20480\n"
  "buffer_load_dwordx4 %[voffW], %[srdW], s64 offen lds\n"
  "v_mfma_f32_16x16x32_bf16 v[76:79], a[160:163], v[44:47], v[76:79]\n"
  "ds_read_b64_tr_b16 a[182:183], v25 offset:22528\n"
  "s_add_u32 m0, s73, 75776\n"
  "v_cvt_pk_bf16_f32 v203, v226, v227\n"
  "v_mfma_f32_16x16x32_bf16 v[80:83], a[160:163], v[48:51], v[80:83]\n"
  "ds_read_b64_tr_b16 a[184:185], v24 offset:20992\n"
  "s_add_u32 s64, s82, 557056\n"
  "v_mfma_f32_16x16x32_bf16 v[84:87], a[164:167], v[36:39], v[84:87]\n"
  "ds_read_b64_tr_b16 a[186:187], v24 offset:23040\n"
  "buffer_load_dwordx4 %[voffW], %[srdW], s64 offen lds\n"
  "buffer_store_dwordx4 v[200:203], %[L16], %[srdWS], s80 offen offset:1024 sc1\n"
  "v_mfma_f32_16x16x32_bf16 v[88:91], a[164:167], v[40:43], v[88:91]\n"
  "ds_read_b64_tr_b16 a[188:189], v25 offset:20992\n"
  "ds_read_b64_tr_b16 a[190:191], v25 offset:23040\n"
  "s_add_u32 m0, s73, 76800\n"
  "v_mfma_f32_16x16x32_bf16 v[92:95], a[164:167], v[44:47], v[92:95]\n"
  "ds_read_b128 v[52:55], v23 offset:32768\n"
  "s_add_u32 s64, s82, 688128\n"
  "v_lshlrev_b32 v220, 16, v204\n"
  "v_mfma_f32_16x16x32_bf16 v[96:99], a[164:167], v[48:51], v[96:99]\n"
  "ds_read_b128 v[56:59], v23 offset:36864\n"
  "buffer_load_dwordx4 %[voffW], %[srdW], s64 offen lds\n"
  "s_add_u32 s83, %[rot], 0\n"
  "s_and_b32 s83, s83, 3\n"
  "s_lshl_b32 s83, s83, 8\n"
  "v_mfma_f32_16x16x32_bf16 v[100:103], a[168:171], v[36:39], v[100:103]\n"
  "ds_read_b128 v[60:63], v23 offset:40960\n"
  "s_add_u32 m0, s72, 0\n"
  "v_and_b32 v221, 0xffff0000, v204\n"
  "v_mfma_f32_16x16x32_bf16 v[104:107], a[168:171], v[40:43], v[104:107]\n"
  "ds_read_b128 v[64:67], v23 offset:45056\n"
  "s_add_u32 s64, s83, 65536\n"
  "v_mfma_f32_16x16x32_bf16 v[108:111], a[168:171], v[44:47], v[108:111]\n"
  "buffer_load_dwordx4 v16, %[srdC], s64 offen lds\n"
  "v_fma_f32 v220, v220, v214, -v213\n"
  "v_fma_f32 v221, v221, v214, -v213\n"
  "v_mfma_f32_16x16x32_bf16 v[112:115], a[168:171], v[48:51], v[112:115]\n"
  "s_add_u32 m0, s72, 1024\n"
  "v_cvt_pk_bf16_f32 v204, v220, v221\n"
  "v_mfma_f32_16x16x32_bf16 v[116:119], a[172:175], v[36:39], v[116:119]\n"
  "s_add_u32 s64, s83, 65536\n"
  "v_lshlrev_b32 v222, 16, v205\n"
  "v_and_b32 v223, 0xffff0000, v205\n"
  "v_mfma_f32_16x16x32_bf16 v[120:123], a[172:175], v[40:43], v[120:123]\n"
  "buffer_load_dwordx4 v17, %[srdC], s64 offen lds\n"
  "v_fma_f32 v222, v222, v214, -v213\n"
  "v_mfma_f32_16x16x32_bf16 v[124:127], a[172:175], v[44:47], v[124:127]\n"
  "s_add_u32 m0, s72, 2048\n"
  "v_fma_f32 v223, v223, v214, -v213\n"
  "v_cvt_pk_bf16_f32 v205, v222, v223\n"
  "v_mfma_f32_16x16x32_bf16 v[128:131], a[172:175], v[48:51], v[128:131]\n"
  "s_add_u32 s64, s83, 65536\n"
  "v_lshlrev_b32 v224, 16, v206\n"
  "s_waitcnt vmcnt(11) lgkmcnt(0)\n"
  "s_barrier\n"
  "v_mfma_f32_16x16x32_bf16 v[68:71], a[176:179], v[52:55], v[68:71]\n"
  "ds_read_b64_tr_b16 a[192:193], v24 offset:0\n"
  "ds_read_b64_tr_b16 a[194:195], v24 offset:2048\n"
  "buffer_load_dwordx4 v18, %[srdC], s64 offen lds\n"
  "v_and_b32 v225, 0xffff0000, v206\n"
  "v_mfma_f32_16x16x32_bf16 v[72:75], a[176:179], v[56:59], v[72:75]\n"
  "ds_read_b64_tr_b16 a[196:197], v25 offset:0\n"
  "s_add_u32 m0, s72, 3072\n"
  "v_mfma_f32_16x16x32_bf16 v[76:79], a[176:179], v[60:63], v[76:79]\n"
  "ds_read_b64_tr_b16 a[198:199], v25 offset:2048\n"
  "s_add_u32 s64, s83, 65536\n"
  "v_fma_f32 v224, v224, v214, -v213\n"
  "v_mfma_f32_16x16x32_bf16 v[80:83], a[176:179], v[64:67], v[80:83]\n"
  "ds_read_b64_tr_b16 a[200:201], v24 offset:512\n"
  "buffer_load_dwordx4 v19, %[srdC], s64 offen lds\n"
  "v_mfma_f32_16x16x32_bf16 v[84:87], a[180:183], v[52:55], v[84:87]\n"
  "ds_read_b64_tr_b16 a[202:203], v24 offset:2560\n"
  "s_add_u32 m0, s73, 77824\n"
  "v_fma_f32 v225, v225, v214, -v213\n"
  "v_mfma_f32_16x16x32_bf16 v[88:91], a[180:183], v[56:59], v[88:91]\n"
  "ds_read_b64_tr_b16 a[204:205], v25 offset:512\n"
  "ds_read_b64_tr_b16 a[206:207], v25 offset:2560\n"
  "s_add_u32 s64, s82, 786432\n"
  "v_mfma_f32_16x16x32_bf16 v[92:95], a[180:183], v[60:63], v[92:95]\n"
  "ds_read_b128 v[36:39], v20 offset:49152\n"
  "buffer_load_dwordx4 %[voffW], %[srdW], s64 offen lds\n"
  "v_cvt_pk_bf16_f32 v206, v224, v225\n"
  "v_mfma_f32_16x16x32_bf16 v[96:99], a[180:183], v[64:67], v[96:99]\n"
  "ds_read_b128 v[40:43], v20 offset:53248\n"
  "s_add_u32 m0, s73, 78848\n"
  "v_mfma_f32_16x16x32_bf16 v[100:103], a[184:187], v[52:55], v[100:103]\n"
  "ds_read_b128 v[44:47], v20 offset:57344\n"
  "s_add_u32 s64, s82, 917504\n"
  "v_lshlrev_b32 v226, 16, v207\n"
  "v_mfma_f32_16x16x32_bf16 v[104:107], a[184:187], v[56:59], v[104:107]\n"
  "ds_read_b128 v[48:51], v20 offset:61440\n"
  "buffer_load_dwordx4 %[voffW], %[srdW], s64 offen lds\n"
  "v_mfma_f32_16x16x32_bf16 v[108:111], a[184:187], v[60:63], v[108:111]\n"
  "s_add_u32 m0, s73, 79872\n"
  "v_and_b32 v227, 0xffff0000, v207\n"
  "v_fma_f32 v226, v226, v214, -v213\n"
  "v_mfma_f32_16x16x32_bf16 v[112:115], a[184:187], v[64:67], v[112:115]\n"
  "s_add_u32 s64, s82, 819200\n"
  "v_fma_f32 v227, v227, v214, -v213\n"
  "v_mfma_f32_16x16x32_bf16 v[116:119], a[188:191], v[52:55], v[116:119]\n"
  "buffer_load_dwordx4 %[voffW], %[srdW], s64 offen lds\n"
  "v_cvt_pk_bf16_f32 v207, v226, v227\n"
  "buffer_store_dwordx4 v[204:207], %[L16], %[srdWS], s80 offen offset:2048 sc1\n"
  "v_mfma_f32_16x16x32_bf16 v[120:123], a[188:191], v[56:59], v[120:123]\n"
  "s_add_u32 m0, s73, 80896\n"
  "v_lshlrev_b32 v220, 16, v208\n"
  "v_mfma_f32_16x16x32_bf16 v[124:127], a[188:191], v[60:63], v[124:127]\n"
  "s_add_u32 s64, s82, 950272\n"
  "v_and_b32 v221, 0xffff0000, v208\n"
  "v_fma_f32 v220, v220, v214, -v213\n"
  "v_mfma_f32_16x16x32_bf16 v[128:131], a[188:191], v[64:67], v[128:131]\n"
  "buffer_load_dwordx4 %[voffW], %[srdW], s64 offen lds\n"
  "s_add_u32 s83, %[rot], 1\n"
  "s_and_b32 s83, s83, 3\n"
  "s_lshl_b32 s83, s83, 8\n"
  "v_fma_f32 v221, v221, v214, -v213\n"
  "s_waitcnt vmcnt(14) lgkmcnt(0)\n"
  "v_mfma_f32_16x16x32_bf16 v[68:71], a[192:195], v[36:39], v[68:71]\n"
  "ds_read_b64_tr_b16 a[208:209], v24 offset:4096\n"
  "ds_read_b64_tr_b16 a[210:211], v24 offset:6144\n"
  "s_add_u32 m0, s72, 16384\n"
  "v_mfma_f32_16x16x32_bf16 v[72:75], a[192:195], v[40:43], v[72:75]\n"
  "ds_read_b64_tr_b16 a[212:213], v25 offset:4096\n"
  "s_add_u32 s64, s83, 65536\n"
  "v_cvt_pk_bf16_f32 v208, v220, v221\n"
  "v_mfma_f32_16x16x32_bf16 v[76:79], a[192:195], v[44:47], v[76:79]\n"
  "ds_read_b64_tr_b16 a[214:215], v25 offset:6144\n"
  "buffer_load_dwordx4 v16, %[srdC], s64 offen lds\n"
  "v_mfma_f32_16x16x32_bf16 v[80:83], a[192:195], v[48:51], v[80:83]\n"
  "ds_read_b64_tr_b16 a[216:217], v24 offset:4608\n"
  "s_add_u32 m0, s72, 17408\n"
  "v_lshlrev_b32 v222, 16, v209\n"
  "v_mfma_f32_16x16x32_bf16 v[84:87], a[196:199], v[36:39], v[84:87]\n"
  "ds_read_b64_tr_b16 a[218:219], v24 offset:6656\n"
  "s_add_u32 s64, s83, 65536\n"
  "v_mfma_f32_16x16x32_bf16 v[88:91], a[196:199], v[40:43], v[88:91]\n"
  "ds_read_b64_tr_b16 a[220:221], v25 offset:4608\n"
  "ds_read_b64_tr_b16 a[222:223], v25 offset:6656\n"
  "buffer_load_dwordx4 v17, %[srdC], s64 offen lds\n"
  "v_and_b32 v223, 0xffff0000, v209\n"
  "v_mfma_f32_16x16x32_bf16 v[92:95], a[196:199], v[44:47], v[92:95]\n"
  "ds_read_b128 v[52:55], v21 offset:49152\n"
  "s_add_u32 m0, s72, 18432\n"
  "v_mfma_f32_16x16x32_bf16 v[96:99], a[196:199], v[48:51], v[96:99]\n"
  "ds_read_b128 v[56:59], v21 offset:53248\n"
  "s_add_u32 s64, s83, 65536\n"
  "v_fma_f32 v222, v222, v214, -v213\n"
  "v_mfma_f32_16x16x32_bf16 v[100:103], a[200:203], v[36:39], v[100:103]\n"
  "ds_read_b128 v[60:63], v21 offset:57344\n"
  "buffer_load_dwordx4 v18, %[srdC], s64 offen lds\n"
  "v_mfma_f32_16x16x32_bf16 v[104:107], a[200:203], v[40:43], v[104:107]\n"
  "ds_read_b128 v[64:67], v21 offset:61440\n"
  "s_add_u32 m0, s72, 19456\n"
  "v_fma_f32 v223, v223, v214, -v213\n"
  "v_mfma_f32_16x16x32_bf16 v[108:111], a[200:203], v[44:47], v[108:111]\n"
  "s_add_u32 s64, s83, 65536\n"
  "v_cvt_pk_bf16_f32 v209, v222, v223\n"
  "v_mfma_f32_16x16x32_bf16 v[112:115], a[200:203], v[48:51], v[112:115]\n"
  "buffer_load_dwordx4 v19, %[srdC], s64 offen lds\n"
  "s_add_u32 s83, %[rot], 2\n"
  "s_and_b32 s83, s83, 3\n"
  "s_lshl_b32 s83, s83, 8\n"
  "v_mfma_f32_16x16x32_bf16 v[116:119], a[204:207], v[36:39], v[116:119]\n"
  "s_add_u32 m0, s72, 32768\n"
  "v_lshlrev_b32 v224, 16, v210\n"
  "v_and_b32 v225, 0xffff0000, v210\n"
  "v_mfma_f32_16x16x32_bf16 v[120:123], a[204:207], v[40:43], v[120:123]\n"
  "s_add_u32 s64, s83, 65536\n"
  "v_fma_f32 v224, v224, v214, -v213\n"
  "v_fma_f32 v225, v225, v214, -v213\n"
  "v_mfma_f32_16x16x32_bf16 v[124:127], a[204:207], v[44:47], v[124:127]\n"
  "buffer_load_dwordx4 v16, %[srdC], s64 offen lds\n"
  "v_cvt_pk_bf16_f32 v210, v224, v225\n"
  "v_mfma_f32_16x16x32_bf16 v[128:131], a[204:207], v[48:51], v[128:131]\n"
  "s_add_u32 m0, s72, 33792\n"
  "v_lshlrev_b32 v226, 16, v211\n"
  "s_waitcnt vmcnt(14) lgkmcnt(0)\n"
  "v_mfma_f32_16x16x32_bf16 v[68:71], a[208:211], v[52:55], v[68:71]\n"
  "ds_read_b64_tr_b16 a[224:225], v24 offset:8192\n"
  "ds_read_b64_tr_b16 a[226:227], v24 offset:10240\n"
  "s_add_u32 s64, s83, 65536\n"
  "v_and_b32 v227, 0xffff0000, v211\n"
  "v_mfma_f32_16x16x32_bf16 v[72:75], a[208:211], v[56:59], v[72:75]\n"
  "ds_read_b64_tr_b16 a[228:229], v25 offset:8192\n"
  "buffer_load_dwordx4 v17, %[srdC], s64 offen lds\n"
  "v_mfma_f32_16x16x32_bf16 v[76:79], a[208:211], v[60:63], v[76:79]\n"
  "ds_read_b64_tr_b16 a[230:231], v25 offset:10240\n"
  "s_add_u32 m0, s72, 34816\n"
  "v_fma_f32 v226, v226, v214, -v213\n"
  "v_mfma_f32_16x16x32_bf16 v[80:83], a[208:211], v[64:67], v[80:83]\n"
  "ds_read_b64_tr_b16 a[232:233], v24 offset:8704\n"
  "s_add_u32 s64, s83, 65536\n"
  "v_mfma_f32_16x16x32_bf16 v[84:87], a[212:215], v[52:55], v[84:87]\n"
  "ds_read_b64_tr_b16 a[234:235], v24 offset:10752\n"
  "buffer_load_dwordx4 v18, %[srdC], s64 offen lds\n"
  "v_fma_f32 v227, v227, v214, -v213\n"
  "v_mfma_f32_16x16x32_bf16 v[88:91], a[212:215], v[56:59], v[88:91]\n"
  "ds_read_b64_tr_b16 a[236:237], v25 offset:8704\n"
  "ds_read_b64_tr_b16 a[238:239], v25 offset:10752\n"
  "s_add_u32 m0, s72, 35840\n"
  "v_mfma_f32_16x16x32_bf16 v[92:95], a[212:215], v[60:63], v[92:95]\n"
  "ds_read_b128 v[36:39], v22 offset:49152\n"
  "s_add_u32 s64, s83, 65536\n"
  "v_cvt_pk_bf16_f32 v211, v226, v227\n"
  "v_mfma_f32_16x16x32_bf16 v[96:99], a[212:215], v[64:67], v[96:99]\n"
  "ds_read_b128 v[40:43], v22 offset:53248\n"
  "buffer_load_dwordx4 v19, %[srdC], s64 offen lds\n"
  "v_mfma_f32_16x16x32_bf16 v[100:103], a[216:219], v[52:55], v[100:103]\n"
  "ds_read_b128 v[44:47], v22 offset:57344\n"
  "buffer_store_dwordx4 v[208:211], %[L16], %[srdWS], s80 offen offset:3072 sc1\n"
  "v_mov_b32 v213, 0\n"
  "v_mfma_f32_16x16x32_bf16 v[104:107], a[216:219], v[56:59], v[104:107]\n"
  "ds_read_b128 v[48:51], v22 offset:61440\n"
  "v_mov_b32 v214, 0\n"
  "v_mfma_f32_16x16x32_bf16 v[108:111], a[216:219], v[60:63], v[108:111]\n"
  "v_dot2c_f32_bf16 v213, v196, v212\n"
  "v_dot2c_f32_bf16 v214, v196, v196\n"
  "v_dot2c_f32_bf16 v213, v197, v212\n"
  "v_mfma_f32_16x16x32_bf16 v[112:115], a[216:219], v[64:67], v[112:115]\n"
  "v_dot2c_f32_bf16 v214, v197, v197\n"
  "v_dot2c_f32_bf16 v213, v198, v212\n"
  "v_mfma_f32_16x16x32_bf16 v[116:119], a[220:223], v[52:55], v[116:119]\n"
  "v_dot2c_f32_bf16 v214, v198, v198\n"
  "v_dot2c_f32_bf16 v213, v199, v212\n"
  "v_dot2c_f32_bf16 v214, v199, v199\n"
  "v_mfma_f32_16x16x32_bf16 v[120:123], a[220:223], v[56:59], v[120:123]\n"
  "v_dot2c_f32_bf16 v213, v200, v212\n"
  "v_dot2c_f32_bf16 v214, v200, v200\n"
  "v_mfma_f32_16x16x32_bf16 v[124:127], a[220:223], v[60:63], v[124:127]\n"
  "v_dot2c_f32_bf16 v213, v201, v212\n"
  "v_dot2c_f32_bf16 v214, v201, v201\n"
  "v_dot2c_f32_bf16 v213, v202, v212\n"
  "v_mfma_f32_16x16x32_bf16 v[128:131], a[220:223], v[64:67], v[128:131]\n"
  "v_dot2c_f32_bf16 v214, v202, v202\n"
  "v_dot2c_f32_bf16 v213, v203, v212\n"
  "s_waitcnt vmcnt(9) lgkmcnt(0)\n"
  "v_mfma_f32_16x16x32_bf16 v[68:71], a[224:227], v[36:39], v[68:71]\n"
  "ds_read_b64_tr_b16 a[240:241], v24 offset:12288\n"
  "ds_read_b64_tr_b16 a[242:243], v24 offset:14336\n"
  "v_dot2c_f32_bf16 v214, v203, v203\n"
  "v_mfma_f32_16x16x32_bf16 v[72:75], a[224:227], v[40:43], v[72:75]\n"
  "ds_read_b64_tr_b16 a[244:245], v25 offset:12288\n"
  "v_dot2c_f32_bf16 v213, v204, v212\n"
  "v_mfma_f32_16x16x32_bf16 v[76:79], a[224:227], v[44:47], v[76:79]\n"
  "ds_read_b64_tr_b16 a[246:247], v25 offset:14336\n"
  "v_dot2c_f32_bf16 v214, v204, v204\n"
  "v_dot2c_f32_bf16 v213, v205, v212\n"
  "v_mfma_f32_16x16x32_bf16 v[80:83], a[224:227], v[48:51], v[80:83]\n"
  "ds_read_b64_tr_b16 a[248:249], v24 offset:12800\n"
  "v_dot2c_f32_bf16 v214, v205, v205\n"
  "v_mfma_f32_16x16x32_bf16 v[84:87], a[228:231], v[36:39], v[84:87]\n"
  "ds_read_b64_tr_b16 a[250:251], v24 offset:14848\n"
  "v_dot2c_f32_bf16 v213, v206, v212\n"
  "v_dot2c_f32_bf16 v214, v206, v206\n"
  "v_mfma_f32_16x16x32_bf16 v[88:91], a[228:231], v[40:43], v[88:91]\n"
  "ds_read_b64_tr_b16 a[252:253], v25 offset:12800\n"
  "ds_read_b64_tr_b16 a[254:255], v25 offset:14848\n"
  "v_mfma_f32_16x16x32_bf16 v[92:95], a[228:231], v[44:47], v[92:95]\n"
  "ds_read_b128 v[52:55], v23 offset:49152\n"
  "v_dot2c_f32_bf16 v213, v207, v212\n"
  "v_dot2c_f32_bf16 v214, v207, v207\n"
  "v_mfma_f32_16x16x32_bf16 v[96:99], a[228:231], v[48:51], v[96:99]\n"
  "ds_read_b128 v[56:59], v23 offset:53248\n"
  "v_dot2c_f32_bf16 v213, v208, v212\n"
  "v_mfma_f32_16x16x32_bf16 v[100:103], a[232:235], v[36:39], v[100:103]\n"
  "ds_read_b128 v[60:63], v23 offset:57344\n"
  "v_dot2c_f32_bf16 v214, v208, v208\n"
  "v_dot2c_f32_bf16 v213, v209, v212\n"
  "v_mfma_f32_16x16x32_bf16 v[104:107], a[232:235], v[40:43], v[104:107]\n"
  "ds_read_b128 v[64:67], v23 offset:61440\n"
  "v_dot2c_f32_bf16 v214, v209, v209\n"
  "v_mfma_f32_16x16x32_bf16 v[108:111], a[232:235], v[44:47], v[108:111]\n"
  "v_dot2c_f32_bf16 v213, v210, v212\n"
  "v_dot2c_f32_bf16 v214, v210, v210\n"
  "v_dot2c_f32_bf16 v213, v211, v212\n"
  "v_mfma_f32_16x16x32_bf16 v[112:115], a[232:235], v[48:51], v[112:115]\n"
  "v_dot2c_f32_bf16 v214, v211, v211\n"
  "s_nop 0\n"
  "v_add_f32_dpp v213, v213, v213 row_ror:4 row_mask:0xf bank_mask:0xf\n"
  "v_mfma_f32_16x16x32_bf16 v[116:119], a[236:239], v[36:39], v[116:119]\n"
  "v_add_f32_dpp v214, v214, v214 row_ror:4 row_mask:0xf bank_mask:0xf\n"
  "v_add_f32_dpp v213, v213, v213 row_ror:8 row_mask:0xf bank_mask:0xf\n"
  "s_nop 0\n"
  "v_add_f32_dpp v214, v214, v214 row_ror:8 row_mask:0xf bank_mask:0xf\n"
  "v_mfma_f32_16x16x32_bf16 v[120:123], a[236:239], v[40:43], v[120:123]\n"
  "v_mov_b32 v215, v213\n"
  "v_mov_b32 v216, v214\n"
  "v_mfma_f32_16x16x32_bf16 v[124:127], a[236:239], v[44:47], v[124:127]\n"
  "v_permlane16_swap_b32 v213, v215\n"
  "v_permlane16_swap_b32 v214, v216\n"
  "v_mfma_f32_16x16x32_bf16 v[128:131], a[236:239], v[48:51], v[128:131]\n"
  "v_add_f32 v213, v213, v215\n"
  "v_add_f32 v214, v214, v216\n"
  "v_mov_b32 v215, v213\n"
  "s_waitcnt vmcnt(63) lgkmcnt(0)\n"
  "s_barrier\n"
  "v_mfma_f32_16x16x32_bf16 v[68:71], a[240:243], v[52:55], v[68:71]\n"
  "ds_read_b128 v[36:39], v20 offset:0\n"
  "v_mov_b32 v216, v214\n"
  "v_mfma_f32_16x16x32_bf16 v[72:75], a[240:243], v[56:59], v[72:75]\n"
  "v_permlane32_swap_b32 v213, v215\n"
  "v_permlane32_swap_b32 v214, v216\n"
  "v_add_f32 v213, v213, v215\n"
  "v_mfma_f32_16x16x32_bf16 v[76:79], a[240:243], v[60:63], v[76:79]\n"
  "ds_read_b128 v[40:43], v20 offset:4096\n"
  "v_add_f32 v214, v214, v216\n"
  "v_mfma_f32_16x16x32_bf16 v[80:83], a[240:243], v[64:67], v[80:83]\n"
  "v_mul_f32 v213, 0x3b000000, v213\n"
  "v_mul_f32 v214, 0x3b000000, v214\n"
  "v_fma_f32 v214, -v213, v213, v214\n"
  "v_mfma_f32_16x16x32_bf16 v[84:87], a[244:247], v[52:55], v[84:87]\n"
  "ds_read_b128 v[44:47], v20 offset:8192\n"
  "v_max_f32 v214, 0, v214\n"
  "v_mfma_f32_16x16x32_bf16 v[88:91], a[244:247], v[56:59], v[88:91]\n"
  "v_add_f32 v214, 0x3727c5ac, v214\n"
  "v_rsq_f32 v214, v214\n"
  "v_mfma_f32_16x16x32_bf16 v[92:95], a[244:247], v[60:63], v[92:95]\n"
  "ds_read_b128 v[48:51], v20 offset:12288\n"
  "v_mul_f32 v213, v213, v214\n"
  "v_mfma_f32_16x16x32_bf16 v[96:99], a[244:247], v[64:67], v[96:99]\n"
  "v_lshlrev_b32 v220, 16, v196\n"
  "v_and_b32 v221, 0xffff0000, v196\n"
  "v_mfma_f32_16x16x32_bf16 v[100:103], a[248:251], v[52:55], v[100:103]\n"
  "v_fma_f32 v220, v220, v214, -v213\n"
  "v_fma_f32 v221, v221, v214, -v213\n"
  "v_cvt_pk_bf16_f32 v196, v220, v221\n"
  "v_mfma_f32_16x16x32_bf16 v[104:107], a[248:251], v[56:59], v[104:107]\n"
  "v_lshlrev_b32 v222, 16, v197\n"
  "v_and_b32 v223, 0xffff0000, v197\n"
  "v_mfma_f32_16x16x32_bf16 v[108:111], a[248:251], v[60:63], v[108:111]\n"
  "v_fma_f32 v222, v222, v214, -v213\n"
  "v_fma_f32 v223, v223, v214, -v213\n"
  "v_cvt_pk_bf16_f32 v197, v222, v223\n"
  "v_mfma_f32_16x16x32_bf16 v[112:115], a[248:251], v[64:67], v[112:115]\n"
  "v_lshlrev_b32 v224, 16, v198\n"
  "v_and_b32 v225, 0xffff0000, v198\n"
  "v_mfma_f32_16x16x32_bf16 v[116:119], a[252:255], v[52:55], v[116:119]\n"
  "v_fma_f32 v224, v224, v214, -v213\n"
  "v_fma_f32 v225, v225, v214, -v213\n"
  "v_cvt_pk_bf16_f32 v198, v224, v225\n"
  "v_mfma_f32_16x16x32_bf16 v[120:123], a[252:255], v[56:59], v[120:123]\n"
  "s_add_u32 s83, %[rot], 3\n"
  "s_and_b32 s83, s83, 3\n"
  "s_lshl_b32 s83, s83, 8\n"
  "v_mfma_f32_16x16x32_bf16 v[124:127], a[252:255], v[60:63], v[124:127]\n"
  "s_add_u32 m0, s72, 49152\n"
  "v_lshlrev_b32 v226, 16, v199\n"
  "v_mfma_f32_16x16x32_bf16 v[128:131], a[252:255], v[64:67], v[128:131]\n"
  "s_add_u32 s64, s83, 65536\n"
  "v_and_b32 v227, 0xffff0000, v199\n"
  "v_fma_f32 v226, v226, v214, -v213\n"
  "s_waitcnt lgkmcnt(0)\n"
  "v_mfma_f32_16x16x32_bf16 v[132:135], a[0:3], v[36:39], 0\n"
  "ds_read_b128 v[52:55], v21 offset:0\n"
  "buffer_load_dwordx4 v16, %[srdC], s64 offen lds\n"
  "v_mfma_f32_16x16x32_bf16 v[136:139], a[0:3], v[40:43], 0\n"
  "v_fma_f32 v227, v227, v214, -v213\n"
  "v_cvt_pk_bf16_f32 v199, v226, v227\n"
  "buffer_store_dwordx4 v[196:199], %[L16], %[srdWS], s81 offen offset:0 sc1\n"
  "v_mfma_f32_16x16x32_bf16 v[140:143], a[0:3], v[44:47], 0\n"
  "ds_read_b128 v[56:59], v21 offset:4096\n"
  "s_add_u32 m0, s72, 50176\n"
  "v_mfma_f32_16x16x32_bf16 v[144:147], a[0:3], v[48:51], 0\n"
  "s_add_u32 s64, s83, 65536\n"
  "v_lshlrev_b32 v220, 16, v200\n"
  "v_mfma_f32_16x16x32_bf16 v[148:151], a[4:7], v[36:39], 0\n"
  "ds_read_b128 v[60:63], v21 offset:8192\n"
  "buffer_load_dwordx4 v17, %[srdC], s64 offen lds\n"
  "v_and_b32 v221, 0xffff0000, v200\n"
  "v_mfma_f32_16x16x32_bf16 v[152:155], a[4:7], v[40:43], 0\n"
  "v_fma_f32 v220, v220, v214, -v213\n"
  "v_fma_f32 v221, v221, v214, -v213\n"
  "v_mfma_f32_16x16x32_bf16 v[156:159], a[4:7], v[44:47], 0\n"
  "ds_read_b128 v[64:67], v21 offset:12288\n"
  "v_cvt_pk_bf16_f32 v200, v220, v221\n"
  "v_lshlrev_b32 v222, 16, v201\n"
  "v_mfma_f32_16x16x32_bf16 v[160:163], a[4:7], v[48:51], 0\n"
  "s_add_u32 m0, s72, 51200\n"
  "v_and_b32 v223, 0xffff0000, v201\n"
  "v_mfma_f32_16x16x32_bf16 v[164:167], a[8:11], v[36:39], 0\n"
  "s_add_u32 s64, s83, 65536\n"
  "v_fma_f32 v222, v222, v214, -v213\n"
  "v_mfma_f32_16x16x32_bf16 v[168:171], a[8:11], v[40:43], 0\n"
  "buffer_load_dwordx4 v18, %[srdC], s64 offen lds\n"
  "v_fma_f32 v223, v223, v214, -v213\n"
  "v_cvt_pk_bf16_f32 v201, v222, v223\n"
  "v_mfma_f32_16x16x32_bf16 v[172:175], a[8:11], v[44:47], 0\n"
  "v_lshlrev_b32 v224, 16, v202\n"
  "v_and_b32 v225, 0xffff0000, v202\n"
  "v_mfma_f32_16x16x32_bf16 v[176:179], a[8:11], v[48:51], 0\n"
  "s_add_u32 m0, s72, 52224\n"
  "v_fma_f32 v224, v224, v214, -v213\n"
  "v_mfma_f32_16x16x32_bf16 v[180:183], a[12:15], v[36:39], 0\n"
  "s_add_u32 s64, s83, 65536\n"
  "v_cvt_pk_bf16_f32 v68, v68, v69\n"
  "v_fma_f32 v225, v225, v214, -v213\n"
  "v_mfma_f32_16x16x32_bf16 v[184:187], a[12:15], v[40:43], 0\n"
  "buffer_load_dwordx4 v19, %[srdC], s64 offen lds\n"
  "v_cvt_pk_bf16_f32 v69, v70, v71\n"
  "v_mfma_f32_16x16x32_bf16 v[188:191], a[12:15], v[44:47], 0\n"
  "v_cvt_pk_bf16_f32 v70, v84, v85\n"
  "v_cvt_pk_bf16_f32 v71, v86, v87\n"
  "v_cvt_pk_bf16_f32 v202, v224, v225\n"
  "v_mfma_f32_16x16x32_bf16 v[192:195], a[12:15], v[48:51], 0\n"
  "buffer_store_dwordx4 v[68:71], %[L16], %[srdWS], s66 offen sc1\n"
  "v_lshlrev_b32 v226, 16, v203\n"
  "s_waitcnt lgkmcnt(0)\n"
  "v_mfma_f32_16x16x32_bf16 v[132:135], a[16:19], v[52:55], v[132:135]\n"
  "ds_read_b128 v[36:39], v22 offset:0\n"
  "v_and_b32 v227, 0xffff0000, v203\n"
  "v_mfma_f32_16x16x32_bf16 v[136:139], a[16:19], v[56:59], v[136:139]\n"
  "v_fma_f32 v226, v226, v214, -v213\n"
  "v_fma_f32 v227, v227, v214, -v213\n"
  "v_cvt_pk_bf16_f32 v203, v226, v227\n"
  "v_mfma_f32_16x16x32_bf16 v[140:143], a[16:19], v[60:63], v[140:143]\n"
  "ds_read_b128 v[40:43], v22 offset:4096\n"
  "buffer_store_dwordx4 v[200:203], %[L16], %[srdWS], s81 offen offset:1024 sc1\n"
  "v_mfma_f32_16x16x32_bf16 v[144:147], a[16:19], v[64:67], v[144:147]\n"
  "v_lshlrev_b32 v220, 16, v204\n"
  "v_and_b32 v221, 0xffff0000, v204\n"
  "v_mfma_f32_16x16x32_bf16 v[148:151], a[20:23], v[52:55], v[148:151]\n"
  "ds_read_b128 v[44:47], v22 offset:8192\n"
  "v_fma_f32 v220, v220, v214, -v213\n"
  "v_fma_f32 v221, v221, v214, -v213\n"
  "v_mfma_f32_16x16x32_bf16 v[152:155], a[20:23], v[56:59], v[152:155]\n"
  "v_cvt_pk_bf16_f32 v204, v220, v221\n"
  "v_lshlrev_b32 v222, 16, v205\n"
  "v_mfma_f32_16x16x32_bf16 v[156:159], a[20:23], v[60:63], v[156:159]\n"
  "ds_read_b128 v[48:51], v22 offset:12288\n"
  "v_and_b32 v223, 0xffff0000, v205\n"
  "v_mfma_f32_16x16x32_bf16 v[160:163], a[20:23], v[64:67], v[160:163]\n"
  "v_fma_f32 v222, v222, v214, -v213\n"
  "v_fma_f32 v223, v223, v214, -v213\n"
  "v_cvt_pk_bf16_f32 v205, v222, v223\n"
  "v_mfma_f32_16x16x32_bf16 v[164:167], a[24:27], v[52:55], v[164:167]\n"
  "v_lshlrev_b32 v224, 16, v206\n"
  "v_and_b32 v225, 0xffff0000, v206\n"
  "v_mfma_f32_16x16x32_bf16 v[168:171], a[24:27], v[56:59], v[168:171]\n"
  "v_fma_f32 v224, v224, v214, -v213\n"
  "v_fma_f32 v225, v225, v214, -v213\n"
  "v_mfma_f32_16x16x32_bf16 v[172:175], a[24:27], v[60:63], v[172:175]\n"
  "v_cvt_pk_bf16_f32 v206, v224, v225\n"
  "v_lshlrev_b32 v226, 16, v207\n"
  "v_and_b32 v227, 0xffff0000, v207\n"
  "v_mfma_f32_16x16x32_bf16 v[176:179], a[24:27], v[64:67], v[176:179]\n"
  "v_fma_f32 v226, v226, v214, -v213\n"
  "v_fma_f32 v227, v227, v214, -v213\n"
  "v_mfma_f32_16x16x32_bf16 v[180:183], a[28:31], v[52:55], v[180:183]\n"
  "v_cvt_pk_bf16_f32 v207, v226, v227\n"
  "buffer_store_dwordx4 v[204:207], %[L16], %[srdWS], s81 offen offset:2048 sc1\n"
  "v_mfma_f32_16x16x32_bf16 v[184:187], a[28:31], v[56:59], v[184:187]\n"
  "v_lshlrev_b32 v220, 16, v208\n"
  "v_and_b32 v221, 0xffff0000, v208\n"
  "v_mfma_f32_16x16x32_bf16 v[188:191], a[28:31], v[60:63], v[188:191]\n"
  "v_fma_f32 v220, v220, v214, -v213\n"
  "v_fma_f32 v221, v221, v214, -v213\n"
  "v_cvt_pk_bf16_f32 v208, v220, v221\n"
  "v_mfma_f32_16x16x32_bf16 v[192:195], a[28:31], v[64:67], v[192:195]\n"
  "v_lshlrev_b32 v222, 16, v209\n"
  "v_and_b32 v223, 0xffff0000, v209\n"
  "s_waitcnt lgkmcnt(0)\n"
  "v_mfma_f32_16x16x32_bf16 v[132:135], a[32:35], v[36:39], v[132:135]\n"
  "ds_read_b128 v[52:55], v23 offset:0\n"
  "v_fma_f32 v222, v222, v214, -v213\n"
  "v_mfma_f32_16x16x32_bf16 v[136:139], a[32:35], v[40:43], v[136:139]\n"
  "v_fma_f32 v223, v223, v214, -v213\n"
  "v_cvt_pk_bf16_f32 v209, v222, v223\n"
  "v_mfma_f32_16x16x32_bf16 v[140:143], a[32:35], v[44:47], v[140:143]\n"
  "ds_read_b128 v[56:59], v23 offset:4096\n"
  "s_add_u32 s86, %[rotU], 0\n"
  "s_and_b32 s86, s86, 3\n"
  "s_mul_i32 s86, s86, 352256\n"
  "s_add_u32 m0, s74, 65536\n"
  "s_add_u32 s64, s86, 0\n"
  "buffer_load_dwordx4 %[voffW1], %[srdW1], s64 offen lds\n"
  "v_mfma_f32_16x16x32_bf16 v[144:147], a[32:35], v[48:51], v[144:147]\n"
  "v_lshlrev_b32 v224, 16, v210\n"
  "v_and_b32 v225, 0xffff0000, v210\n"
  "v_fma_f32 v224, v224, v214, -v213\n"
  "v_mfma_f32_16x16x32_bf16 v[148:151], a[36:39], v[36:39], v[148:151]\n"
  "ds_read_b128 v[60:63], v23 offset:8192\n"
  "v_fma_f32 v225, v225, v214, -v213\n"
  "v_mfma_f32_16x16x32_bf16 v[152:155], a[36:39], v[40:43], v[152:155]\n"
  "v_cvt_pk_bf16_f32 v210, v224, v225\n"
  "v_lshlrev_b32 v226, 16, v211\n"
  "v_mfma_f32_16x16x32_bf16 v[156:159], a[36:39], v[44:47], v[156:159]\n"
  "ds_read_b128 v[64:67], v23 offset:12288\n"
  "v_and_b32 v227, 0xffff0000, v211\n"
  "v_mfma_f32_16x16x32_bf16 v[160:163], a[36:39], v[48:51], v[160:163]\n"
  "v_fma_f32 v226, v226, v214, -v213\n"
  "v_fma_f32 v227, v227, v214, -v213\n"
  "v_mfma_f32_16x16x32_bf16 v[164:167], a[40:43], v[36:39], v[164:167]\n"
  "v_cvt_pk_bf16_f32 v72, v72, v73\n"
  "v_cvt_pk_bf16_f32 v73, v74, v75\n"
  "v_mfma_f32_16x16x32_bf16 v[168:171], a[40:43], v[40:43], v[168:171]\n"
  "v_cvt_pk_bf16_f32 v74, v88, v89\n"
  "v_cvt_pk_bf16_f32 v75, v90, v91\n"
  "v_mfma_f32_16x16x32_bf16 v[172:175], a[40:43], v[44:47], v[172:175]\n"
  "s_add_u32 s66, s66, s67\n"
  "buffer_store_dwordx4 v[72:75], %[L16], %[srdWS], s66 offen sc1\n"
  "v_mfma_f32_16x16x32_bf16 v[176:179], a[40:43], v[48:51], v[176:179]\n"
  "v_cvt_pk_bf16_f32 v211, v226, v227\n"
  "buffer_store_dwordx4 v[208:211], %[L16], %[srdWS], s81 offen offset:3072 sc1\n"
  "v_mfma_f32_16x16x32_bf16 v[180:183], a[44:47], v[36:39], v[180:183]\n"
  "v_mfma_f32_16x16x32_bf16 v[184:187], a[44:47], v[40:43], v[184:187]\n"
  "v_mfma_f32_16x16x32_bf16 v[188:191], a[44:47], v[44:47], v[188:191]\n"
  "v_mfma_f32_16x16x32_bf16 v[192:195], a[44:47], v[48:51], v[192:195]\n"
  "s_waitcnt vmcnt(16) lgkmcnt(0)\n"
  "s_barrier\n"
  "v_mfma_f32_16x16x32_bf16 v[132:135], a[48:51], v[52:55], v[132:135]\n"
  "ds_read_b128 v[36:39], v20 offset:16384\n"
  "v_mfma_f32_16x16x32_bf16 v[136:139], a[48:51], v[56:59], v[136:139]\n"
  "v_mfma_f32_16x16x32_bf16 v[140:143], a[48:51], v[60:63], v[140:143]\n"
  "ds_read_b128 v[40:43], v20 offset:20480\n"
  "s_add_u32 s83, %[rot], 0\n"
  "s_and_b32 s83, s83, 3\n"
  "s_lshl_b32 s83, s83, 8\n"
  "s_add_u32 m0, s72, 0\n"
  "s_add_u32 s64, s83, 131072\n"
  "buffer_load_dwordx4 v16, %[srdC], s64 offen lds\n"
  "v_mfma_f32_16x16x32_bf16 v[144:147], a[48:51], v[64:67], v[144:147]\n"
  "v_mfma_f32_16x16x32_bf16 v[148:151], a[52:55], v[52:55], v[148:151]\n"
  "ds_read_b128 v[44:47], v20 offset:24576\n"
  "s_add_u32 m0, s72, 1024\n"
  "v_mfma_f32_16x16x32_bf16 v[152:155], a[52:55], v[56:59], v[152:155]\n"
  "s_add_u32 s64, s83, 131072\n"
  "v_mfma_f32_16x16x32_bf16 v[156:159], a[52:55], v[60:63], v[156:159]\n"
  "ds_read_b128 v[48:51], v20 offset:28672\n"
  "buffer_load_dwordx4 v17, %[srdC], s64 offen lds\n"
  "v_mfma_f32_16x16x32_bf16 v[160:163], a[52:55], v[64:67], v[160:163]\n"
  "v_mfma_f32_16x16x32_bf16 v[164:167], a[56:59], v[52:55], v[164:167]\n"
  "v_mfma_f32_16x16x32_bf16 v[168:171], a[56:59], v[56:59], v[168:171]\n"
  "s_add_u32 m0, s72, 2048\n"
  "v_mfma_f32_16x16x32_bf16 v[172:175], a[56:59], v[60:63], v[172:175]\n"
  "s_add_u32 s64, s83, 131072\n"
  "v_mfma_f32_16x16x32_bf16 v[176:179], a[56:59], v[64:67], v[176:179]\n"
  "buffer_load_dwordx4 v18, %[srdC], s64 offen lds\n"
  "v_mfma_f32_16x16x32_bf16 v[180:183], a[60:63], v[52:55], v[180:183]\n"
  "v_mfma_f32_16x16x32_bf16 v[184:187], a[60:63], v[56:59], v[184:187]\n"
  "s_add_u32 m0, s72, 3072\n"
  "v_mfma_f32_16x16x32_bf16 v[188:191], a[60:63], v[60:63], v[188:191]\n"
  "s_add_u32 s64, s83, 131072\n"
  "v_mfma_f32_16x16x32_bf16 v[192:195], a[60:63], v[64:67], v[192:195]\n"
  "buffer_load_dwordx4 v19, %[srdC], s64 offen lds\n"
  "s_waitcnt lgkmcnt(0)\n"
  "v_mfma_f32_16x16x32_bf16 v[132:135], a[64:67], v[36:39], v[132:135]\n"
  "ds_read_b128 v[52:55], v21 offset:16384\n"
  "v_mfma_f32_16x16x32_bf16 v[136:139], a[64:67], v[40:43], v[136:139]\n"
  "v_mfma_f32_16x16x32_bf16 v[140:143], a[64:67], v[44:47], v[140:143]\n"
  "ds_read_b128 v[56:59], v21 offset:20480\n"
  "s_add_u32 m0, s74, 66560\n"
  "v_mfma_f32_16x16x32_bf16 v[144:147], a[64:67], v[48:51], v[144:147]\n"
  "s_add_u32 s64, s86, 64\n"
  "v_mfma_f32_16x16x32_bf16 v[148:151], a[68:71], v[36:39], v[148:151]\n"
  "ds_read_b128 v[60:63], v21 offset:24576\n"
  "buffer_load_dwordx4 %[voffW1], %[srdW1], s64 offen lds\n"
  "v_mfma_f32_16x16x32_bf16 v[152:155], a[68:71], v[40:43], v[152:155]\n"
  "v_cvt_pk_bf16_f32 v76, v76, v77\n"
  "v_cvt_pk_bf16_f32 v77, v78, v79\n"
  "v_mfma_f32_16x16x32_bf16 v[156:159], a[68:71], v[44:47], v[156:159]\n"
  "ds_read_b128 v[64:67], v21 offset:28672\n"
  "v_cvt_pk_bf16_f32 v78, v92, v93\n"
  "v_mfma_f32_16x16x32_bf16 v[160:163], a[68:71], v[48:51], v[160:163]\n"
  "v_cvt_pk_bf16_f32 v79, v94, v95\n"
  "s_add_u32 s66, s66, s67\n"
  "v_mfma_f32_16x16x32_bf16 v[164:167], a[72:75], v[36:39], v[164:167]\n"
  "buffer_store_dwordx4 v[76:79], %[L16], %[srdWS], s66 offen sc1\n"
  "v_mfma_f32_16x16x32_bf16 v[168:171], a[72:75], v[40:43], v[168:171]\n"
  "v_mfma_f32_16x16x32_bf16 v[172:175], a[72:75], v[44:47], v[172:175]\n"
  "v_mfma_f32_16x16x32_bf16 v[176:179], a[72:75], v[48:51], v[176:179]\n"
  "v_mfma_f32_16x16x32_bf16 v[180:183], a[76:79], v[36:39], v[180:183]\n"
  "v_mfma_f32_16x16x32_bf16 v[184:187], a[76:79], v[40:43], v[184:187]\n"
  "v_mfma_f32_16x16x32_bf16 v[188:191], a[76:79], v[44:47], v[188:191]\n"
  "v_mfma_f32_16x16x32_bf16 v[192:195], a[76:79], v[48:51], v[192:195]\n"
  "s_waitcnt lgkmcnt(0)\n"
  "v_mfma_f32_16x16x32_bf16 v[132:135], a[80:83], v[52:55], v[132:135]\n"
  "ds_read_b128 v[36:39], v22 offset:16384\n"
  "v_mfma_f32_16x16x32_bf16 v[136:139], a[80:83], v[56:59], v[136:139]\n"
  "v_mfma_f32_16x16x32_bf16 v[140:143], a[80:83], v[60:63], v[140:143]\n"
  "ds_read_b128 v[40:43], v22 offset:20480\n"
  "v_mfma_f32_16x16x32_bf16 v[144:147], a[80:83], v[64:67], v[144:147]\n"
  "v_mfma_f32_16x16x32_bf16 v[148:151], a[84:87], v[52:55], v[148:151]\n"
  "ds_read_b128 v[44:47], v22 offset:24576\n"
  "s_add_u32 m0, s74, 67584\n"
  "v_mfma_f32_16x16x32_bf16 v[152:155], a[84:87], v[56:59], v[152:155]\n"
  "s_add_u32 s64, s86, 128\n"
  "v_mfma_f32_16x16x32_bf16 v[156:159], a[84:87], v[60:63], v[156:159]\n"
  "ds_read_b128 v[48:51], v22 offset:28672\n"
  "buffer_load_dwordx4 %[voffW1], %[srdW1], s64 offen lds\n"
  "v_mfma_f32_16x16x32_bf16 v[160:163], a[84:87], v[64:67], v[160:163]\n"
  "v_mfma_f32_16x16x32_bf16 v[164:167], a[88:91], v[52:55], v[164:167]\n"
  "v_mfma_f32_16x16x32_bf16 v[168:171], a[88:91], v[56:59], v[168:171]\n"
  "v_mfma_f32_16x16x32_bf16 v[172:175], a[88:91], v[60:63], v[172:175]\n"
  "v_mfma_f32_16x16x32_bf16 v[176:179], a[88:91], v[64:67], v[176:179]\n"
  "v_mfma_f32_16x16x32_bf16 v[180:183], a[92:95], v[52:55], v[180:183]\n"
  "v_mfma_f32_16x16x32_bf16 v[184:187], a[92:95], v[56:59], v[184:187]\n"
  "v_mfma_f32_16x16x32_bf16 v[188:191], a[92:95], v[60:63], v[188:191]\n"
  "v_mfma_f32_16x16x32_bf16 v[192:195], a[92:95], v[64:67], v[192:195]\n"
  "s_waitcnt lgkmcnt(0)\n"
  "v_mfma_f32_16x16x32_bf16 v[132:135], a[96:99], v[36:39], v[132:135]\n"
  "ds_read_b128 v[52:55], v23 offset:16384\n"
  "v_cvt_pk_bf16_f32 v80, v80, v81\n"
  "v_mfma_f32_16x16x32_bf16 v[136:139], a[96:99], v[40:43], v[136:139]\n"
  "v_cvt_pk_bf16_f32 v81, v82, v83\n"
  "v_cvt_pk_bf16_f32 v82, v96, v97\n"
  "v_mfma_f32_16x16x32_bf16 v[140:143], a[96:99], v[44:47], v[140:143]\n"
  "ds_read_b128 v[56:59], v23 offset:20480\n"
  "v_cvt_pk_bf16_f32 v83, v98, v99\n"
  "v_mfma_f32_16x16x32_bf16 v[144:147], a[96:99], v[48:51], v[144:147]\n"
  "s_add_u32 s66, s66, s67\n"
  "buffer_store_dwordx4 v[80:83], %[L16], %[srdWS], s66 offen sc1\n"
  "v_mfma_f32_16x16x32_bf16 v[148:151], a[100:103], v[36:39], v[148:151]\n"
  "ds_read_b128 v[60:63], v23 offset:24576\n"
  "v_mfma_f32_16x16x32_bf16 v[152:155], a[100:103], v[40:43], v[152:155]\n"
  "v_mfma_f32_16x16x32_bf16 v[156:159], a[100:103], v[44:47], v[156:159]\n"
  "ds_read_b128 v[64:67], v23 offset:28672\n"
  "s_add_u32 m0, s74, 68608\n"
  "v_mfma_f32_16x16x32_bf16 v[160:163], a[100:103], v[48:51], v[160:163]\n"
  "s_add_u32 s64, s86, 11008\n"
  "v_mfma_f32_16x16x32_bf16 v[164:167], a[104:107], v[36:39], v[164:167]\n"
  "buffer_load_dwordx4 %[voffW1], %[srdW1], s64 offen lds\n"
  "v_mfma_f32_16x16x32_bf16 v[168:171], a[104:107], v[40:43], v[168:171]\n"
  "v_mfma_f32_16x16x32_bf16 v[172:175], a[104:107], v[44:47], v[172:175]\n"
  "v_mfma_f32_16x16x32_bf16 v[176:179], a[104:107], v[48:51], v[176:179]\n"
  "v_mfma_f32_16x16x32_bf16 v[180:183], a[108:111], v[36:39], v[180:183]\n"
  "v_mfma_f32_16x16x32_bf16 v[184:187], a[108:111], v[40:43], v[184:187]\n"
  "v_mfma_f32_16x16x32_bf16 v[188:191], a[108:111], v[44:47], v[188:191]\n"
  "v_mfma_f32_16x16x32_bf16 v[192:195], a[108:111], v[48:51], v[192:195]\n"
  "s_waitcnt vmcnt(21) lgkmcnt(0)\n"
  "s_barrier\n"
  "v_mfma_f32_16x16x32_bf16 v[132:135], a[112:115], v[52:55], v[132:135]\n"
  "ds_read_b128 v[36:39], v20 offset:32768\n"
  "v_mfma_f32_16x16x32_bf16 v[136:139], a[112:115], v[56:59], v[136:139]\n"
  "s_waitcnt vmcnt(9)\n"
  "v_mfma_f32_16x16x32_bf16 v[140:143], a[112:115], v[60:63], v[140:143]\n"
  "ds_read_b128 v[40:43], v20 offset:36864\n"
  "s_add_u32 s83, %[rot], 1\n"
  "s_and_b32 s83, s83, 3\n"
  "s_lshl_b32 s83, s83, 8\n"
  "s_add_u32 m0, s72, 16384\n"
  "s_add_u32 s64, s83, 131072\n"
  "buffer_load_dwordx4 v16, %[srdC], s64 offen lds\n"
  "buffer_store_dwordx2 v[26:27], off, %[srdWS], %[oFl] sc1\n"
  "v_mfma_f32_16x16x32_bf16 v[144:147], a[112:115], v[64:67], v[144:147]\n"
  "v_mfma_f32_16x16x32_bf16 v[148:151], a[116:119], v[52:55], v[148:151]\n"
  "ds_read_b128 v[44:47], v20 offset:40960\n"
  "s_add_u32 m0, s72, 17408\n"
  "v_mfma_f32_16x16x32_bf16 v[152:155], a[116:119], v[56:59], v[152:155]\n"
  "s_add_u32 s64, s83, 131072\n"
  "v_mfma_f32_16x16x32_bf16 v[156:159], a[116:119], v[60:63], v[156:159]\n"
  "ds_read_b128 v[48:51], v20 offset:45056\n"
  "buffer_load_dwordx4 v17, %[srdC], s64 offen lds\n"
  "v_mfma_f32_16x16x32_bf16 v[160:163], a[116:119], v[64:67], v[160:163]\n"
  "v_mfma_f32_16x16x32_bf16 v[164:167], a[120:123], v[52:55], v[164:167]\n"
  "v_mfma_f32_16x16x32_bf16 v[168:171], a[120:123], v[56:59], v[168:171]\n"
  "s_add_u32 m0, s74, 69632\n"
  "v_mfma_f32_16x16x32_bf16 v[172:175], a[120:123], v[60:63], v[172:175]\n"
  "s_add_u32 s64, s86, 11072\n"
  "v_mfma_f32_16x16x32_bf16 v[176:179], a[120:123], v[64:67], v[176:179]\n"
  "buffer_load_dwordx4 %[voffW1], %[srdW1], s64 offen lds\n"
  "v_mfma_f32_16x16x32_bf16 v[180:183], a[124:127], v[52:55], v[180:183]\n"
  "v_cvt_pk_bf16_f32 v100, v100, v101\n"
  "v_cvt_pk_bf16_f32 v101, v102, v103\n"
  "v_mfma_f32_16x16x32_bf16 v[184:187], a[124:127], v[56:59], v[184:187]\n"
  "v_cvt_pk_bf16_f32 v102, v116, v117\n"
  "v_cvt_pk_bf16_f32 v103, v118, v119\n"
  "v_mfma_f32_16x16x32_bf16 v[188:191], a[124:127], v[60:63], v[188:191]\n"
  "s_add_u32 m0, s72, 18432\n"
  "s_add_u32 s66, s66, s68\n"
  "v_mfma_f32_16x16x32_bf16 v[192:195], a[124:127], v[64:67], v[192:195]\n"
  "s_add_u32 s64, s83, 131072\n"
  "buffer_store_dwordx4 v[100:103], %[L16], %[srdWS], s66 offen sc1\n"
  "s_waitcnt lgkmcnt(0)\n"
  "v_mfma_f32_16x16x32_bf16 v[132:135], a[128:131], v[36:39], v[132:135]\n"
  "ds_read_b128 v[52:55], v21 offset:32768\n"
  "buffer_load_dwordx4 v18, %[srdC], s64 offen lds\n"
  "v_mfma_f32_16x16x32_bf16 v[136:139], a[128:131], v[40:43], v[136:139]\n"
  "v_mfma_f32_16x16x32_bf16 v[140:143], a[128:131], v[44:47], v[140:143]\n"
  "ds_read_b128 v[56:59], v21 offset:36864\n"
  "s_add_u32 m0, s72, 19456\n"
  "v_mfma_f32_16x16x32_bf16 v[144:147], a[128:131], v[48:51], v[144:147]\n"
  "s_add_u32 s64, s83, 131072\n"
  "v_mfma_f32_16x16x32_bf16 v[148:151], a[132:135], v[36:39], v[148:151]\n"
  "ds_read_b128 v[60:63], v21 offset:40960\n"
  "buffer_load_dwordx4 v19, %[srdC], s64 offen lds\n"
  "v_mfma_f32_16x16x32_bf16 v[152:155], a[132:135], v[40:43], v[152:155]\n"
  "v_mfma_f32_16x16x32_bf16 v[156:159], a[132:135], v[44:47], v[156:159]\n"
  "ds_read_b128 v[64:67], v21 offset:45056\n"
  "v_mfma_f32_16x16x32_bf16 v[160:163], a[132:135], v[48:51], v[160:163]\n"
  "v_mfma_f32_16x16x32_bf16 v[164:167], a[136:139], v[36:39], v[164:167]\n"
  "v_mfma_f32_16x16x32_bf16 v[168:171], a[136:139], v[40:43], v[168:171]\n"
  "v_mfma_f32_16x16x32_bf16 v[172:175], a[136:139], v[44:47], v[172:175]\n"
  "v_mfma_f32_16x16x32_bf16 v[176:179], a[136:139], v[48:51], v[176:179]\n"
  "s_add_u32 m0, s74, 70656\n"
  "v_mfma_f32_16x16x32_bf16 v[180:183], a[140:143], v[36:39], v[180:183]\n"
  "s_add_u32 s64, s86, 11136\n"
  "v_mfma_f32_16x16x32_bf16 v[184:187], a[140:143], v[40:43], v[184:187]\n"
  "buffer_load_dwordx4 %[voffW1], %[srdW1], s64 offen lds\n"
  "v_mfma_f32_16x16x32_bf16 v[188:191], a[140:143], v[44:47], v[188:191]\n"
  "v_mfma_f32_16x16x32_bf16 v[192:195], a[140:143], v[48:51], v[192:195]\n"
  "s_waitcnt lgkmcnt(0)\n"
  "v_mfma_f32_16x16x32_bf16 v[132:135], a[144:147], v[52:55], v[132:135]\n"
  "ds_read_b128 v[36:39], v22 offset:32768\n"
  "v_mfma_f32_16x16x32_bf16 v[136:139], a[144:147], v[56:59], v[136:139]\n"
  "v_mfma_f32_16x16x32_bf16 v[140:143], a[144:147], v[60:63], v[140:143]\n"
  "ds_read_b128 v[40:43], v22 offset:36864\n"
  "v_mfma_f32_16x16x32_bf16 v[144:147], a[144:147], v[64:67], v[144:147]\n"
  "v_mfma_f32_16x16x32_bf16 v[148:151], a[148:151], v[52:55], v[148:151]\n"
  "ds_read_b128 v[44:47], v22 offset:40960\n"
  "v_mfma_f32_16x16x32_bf16 v[152:155], a[148:151], v[56:59], v[152:155]\n"
  "v_mfma_f32_16x16x32_bf16 v[156:159], a[148:151], v[60:63], v[156:159]\n"
  "ds_read_b128 v[48:51], v22 offset:45056\n"
  "v_mfma_f32_16x16x32_bf16 v[160:163], a[148:151], v[64:67], v[160:163]\n"
  "v_mfma_f32_16x16x32_bf16 v[164:167], a[152:155], v[52:55], v[164:167]\n"
  "v_cvt_pk_bf16_f32 v104, v104, v105\n"
  "v_cvt_pk_bf16_f32 v105, v106, v107\n"
  "v_mfma_f32_16x16x32_bf16 v[168:171], a[152:155], v[56:59], v[168:171]\n"
  "v_cvt_pk_bf16_f32 v106, v120, v121\n"
  "v_cvt_pk_bf16_f32 v107, v122, v123\n"
  "v_mfma_f32_16x16x32_bf16 v[172:175], a[152:155], v[60:63], v[172:175]\n"
  "s_add_u32 s66, s66, s67\n"
  "buffer_store_dwordx4 v[104:107], %[L16], %[srdWS], s66 offen sc1\n"
  "v_mfma_f32_16x16x32_bf16 v[176:179], a[152:155], v[64:67], v[176:179]\n"
  "v_mfma_f32_16x16x32_bf16 v[180:183], a[156:159], v[52:55], v[180:183]\n"
  "s_add_u32 s86, %[rotU], 1\n"
  "s_and_b32 s86, s86, 3\n"
  "s_mul_i32 s86, s86, 352256\n"
  "v_mfma_f32_16x16x32_bf16 v[184:187], a[156:159], v[56:59], v[184:187]\n"
  "s_add_u32 m0, s74, 90112\n"
  "v_mfma_f32_16x16x32_bf16 v[188:191], a[156:159], v[60:63], v[188:191]\n"
  "s_add_u32 s64, s86, 0\n"
  "v_mfma_f32_16x16x32_bf16 v[192:195], a[156:159], v[64:67], v[192:195]\n"
  "buffer_load_dwordx4 %[voffW1], %[srdW1], s64 offen lds\n"
  "s_waitcnt lgkmcnt(0)\n"
  "v_mfma_f32_16x16x32_bf16 v[132:135], a[160:163], v[36:39], v[132:135]\n"
  "ds_read_b128 v[52:55], v23 offset:32768\n"
  "v_mfma_f32_16x16x32_bf16 v[136:139], a[160:163], v[40:43], v[136:139]\n"
  "v_mfma_f32_16x16x32_bf16 v[140:143], a[160:163], v[44:47], v[140:143]\n"
  "ds_read_b128 v[56:59], v23 offset:36864\n"
  "v_mfma_f32_16x16x32_bf16 v[144:147], a[160:163], v[48:51], v[144:147]\n"
  "v_mfma_f32_16x16x32_bf16 v[148:151], a[164:167], v[36:39], v[148:151]\n"
  "ds_read_b128 v[60:63], v23 offset:40960\n"
  "v_mfma_f32_16x16x32_bf16 v[152:155], a[164:167], v[40:43], v[152:155]\n"
  "v_mfma_f32_16x16x32_bf16 v[156:159], a[164:167], v[44:47], v[156:159]\n"
  "ds_read_b128 v[64:67], v23 offset:45056\n"
  "v_mfma_f32_16x16x32_bf16 v[160:163], a[164:167], v[48:51], v[160:163]\n"
  "v_mfma_f32_16x16x32_bf16 v[164:167], a[168:171], v[36:39], v[164:167]\n"
  "v_mfma_f32_16x16x32_bf16 v[168:171], a[168:171], v[40:43], v[168:171]\n"
  "v_mfma_f32_16x16x32_bf16 v[172:175], a[168:171], v[44:47], v[172:175]\n"
  "v_mfma_f32_16x16x32_bf16 v[176:179], a[168:171], v[48:51], v[176:179]\n"
  "v_mfma_f32_16x16x32_bf16 v[180:183], a[172:175], v[36:39], v[180:183]\n"
  "v_mfma_f32_16x16x32_bf16 v[184:187], a[172:175], v[40:43], v[184:187]\n"
  "v_mfma_f32_16x16x32_bf16 v[188:191], a[172:175], v[44:47], v[188:191]\n"
  "v_mfma_f32_16x16x32_bf16 v[192:195], a[172:175], v[48:51], v[192:195]\n"
  "s_add_u32 m0, s74, 91136\n"
  "s_waitcnt vmcnt(63) lgkmcnt(0)\n"
  "s_barrier\n"
  "v_mfma_f32_16x16x32_bf16 v[132:135], a[176:179], v[52:55], v[132:135]\n"
  "ds_read_b128 v[36:39], v20 offset:49152\n"
  "s_add_u32 s64, s86, 64\n"
  "v_mfma_f32_16x16x32_bf16 v[136:139], a[176:179], v[56:59], v[136:139]\n"
  "buffer_load_dwordx4 %[voffW1], %[srdW1], s64 offen lds\n"
  "v_mfma_f32_16x16x32_bf16 v[140:143], a[176:179], v[60:63], v[140:143]\n"
  "ds_read_b128 v[40:43], v20 offset:53248\n"
  "v_mfma_f32_16x16x32_bf16 v[144:147], a[176:179], v[64:67], v[144:147]\n"
  "s_add_u32 s83, %[rot], 2\n"
  "s_and_b32 s83, s83, 3\n"
  "s_lshl_b32 s83, s83, 8\n"
  "v_mfma_f32_16x16x32_bf16 v[148:151], a[180:183], v[52:55], v[148:151]\n"
  "ds_read_b128 v[44:47], v20 offset:57344\n"
  "s_add_u32 m0, s72, 32768\n"
  "v_mfma_f32_16x16x32_bf16 v[152:155], a[180:183], v[56:59], v[152:155]\n"
  "s_add_u32 s64, s83, 131072\n"
  "v_cvt_pk_bf16_f32 v108, v108, v109\n"
  "v_mfma_f32_16x16x32_bf16 v[156:159], a[180:183], v[60:63], v[156:159]\n"
  "ds_read_b128 v[48:51], v20 offset:61440\n"
  "buffer_load_dwordx4 v16, %[srdC], s64 offen lds\n"
  "v_mfma_f32_16x16x32_bf16 v[160:163], a[180:183], v[64:67], v[160:163]\n"
  "v_cvt_pk_bf16_f32 v109, v110, v111\n"
  "v_cvt_pk_bf16_f32 v110, v124, v125\n"
  "v_mfma_f32_16x16x32_bf16 v[164:167], a[184:187], v[52:55], v[164:167]\n"
  "s_add_u32 m0, s72, 33792\n"
  "v_cvt_pk_bf16_f32 v111, v126, v127\n"
  "v_mfma_f32_16x16x32_bf16 v[168:171], a[184:187], v[56:59], v[168:171]\n"
  "s_add_u32 s64, s83, 131072\n"
  "s_add_u32 s66, s66, s67\n"
  "v_mfma_f32_16x16x32_bf16 v[172:175], a[184:187], v[60:63], v[172:175]\n"
  "buffer_load_dwordx4 v17, %[srdC], s64 offen lds\n"
  "buffer_store_dwordx4 v[108:111], %[L16], %[srdWS], s66 offen sc1\n"
  "v_mfma_f32_16x16x32_bf16 v[176:179], a[184:187], v[64:67], v[176:179]\n"
  "v_mfma_f32_16x16x32_bf16 v[180:183], a[188:191], v[52:55], v[180:183]\n"
  "v_mfma_f32_16x16x32_bf16 v[184:187], a[188:191], v[56:59], v[184:187]\n"
  "s_add_u32 m0, s72, 34816\n"
  "v_mfma_f32_16x16x32_bf16 v[188:191], a[188:191], v[60:63], v[188:191]\n"
  "s_add_u32 s64, s83, 131072\n"
  "v_mfma_f32_16x16x32_bf16 v[192:195], a[188:191], v[64:67], v[192:195]\n"
  "buffer_load_dwordx4 v18, %[srdC], s64 offen lds\n"
  "s_waitcnt lgkmcnt(0)\n"
  "v_mfma_f32_16x16x32_bf16 v[132:135], a[192:195], v[36:39], v[132:135]\n"
  "ds_read_b128 v[52:55], v21 offset:49152\n"
  "v_mfma_f32_16x16x32_bf16 v[136:139], a[192:195], v[40:43], v[136:139]\n"
  "s_add_u32 m0, s72, 35840\n"
  "v_mfma_f32_16x16x32_bf16 v[140:143], a[192:195], v[44:47], v[140:143]\n"
  "ds_read_b128 v[56:59], v21 offset:53248\n"
  "s_add_u32 s64, s83, 131072\n"
  "v_mfma_f32_16x16x32_bf16 v[144:147], a[192:195], v[48:51], v[144:147]\n"
  "buffer_load_dwordx4 v19, %[srdC], s64 offen lds\n"
  "v_mfma_f32_16x16x32_bf16 v[148:151], a[196:199], v[36:39], v[148:151]\n"
  "ds_read_b128 v[60:63], v21 offset:57344\n"
  "v_mfma_f32_16x16x32_bf16 v[152:155], a[196:199], v[40:43], v[152:155]\n"
  "v_mfma_f32_16x16x32_bf16 v[156:159], a[196:199], v[44:47], v[156:159]\n"
  "ds_read_b128 v[64:67], v21 offset:61440\n"
  "s_add_u32 m0, s74, 92160\n"
  "v_mfma_f32_16x16x32_bf16 v[160:163], a[196:199], v[48:51], v[160:163]\n"
  "s_add_u32 s64, s86, 128\n"
  "v_mfma_f32_16x16x32_bf16 v[164:167], a[200:203], v[36:39], v[164:167]\n"
  "buffer_load_dwordx4 %[voffW1], %[srdW1], s64 offen lds\n"
  "v_mfma_f32_16x16x32_bf16 v[168:171], a[200:203], v[40:43], v[168:171]\n"
  "v_mfma_f32_16x16x32_bf16 v[172:175], a[200:203], v[44:47], v[172:175]\n"
  "v_mfma_f32_16x16x32_bf16 v[176:179], a[200:203], v[48:51], v[176:179]\n"
  "v_mfma_f32_16x16x32_bf16 v[180:183], a[204:207], v[36:39], v[180:183]\n"
  "v_mfma_f32_16x16x32_bf16 v[184:187], a[204:207], v[40:43], v[184:187]\n"
  "v_mfma_f32_16x16x32_bf16 v[188:191], a[204:207], v[44:47], v[188:191]\n"
  "v_mfma_f32_16x16x32_bf16 v[192:195], a[204:207], v[48:51], v[192:195]\n"
  "s_waitcnt lgkmcnt(0)\n"
  "v_mfma_f32_16x16x32_bf16 v[132:135], a[208:211], v[52:55], v[132:135]\n"
  "ds_read_b128 v[36:39], v22 offset:49152\n"
  "v_cvt_pk_bf16_f32 v112, v112, v113\n"
  "v_mfma_f32_16x16x32_bf16 v[136:139], a[208:211], v[56:59], v[136:139]\n"
  "v_cvt_pk_bf16_f32 v113, v114, v115\n"
  "v_cvt_pk_bf16_f32 v114, v128, v129\n"
  "v_mfma_f32_16x16x32_bf16 v[140:143], a[208:211], v[60:63], v[140:143]\n"
  "ds_read_b128 v[40:43], v22 offset:53248\n"
  "v_cvt_pk_bf16_f32 v115, v130, v131\n"
  "v_mfma_f32_16x16x32_bf16 v[144:147], a[208:211], v[64:67], v[144:147]\n"
  "s_add_u32 s66, s66, s67\n"
  "buffer_store_dwordx4 v[112:115], %[L16], %[srdWS], s66 offen sc1\n"
  "v_mfma_f32_16x16x32_bf16 v[148:151], a[212:215], v[52:55], v[148:151]\n"
  "ds_read_b128 v[44:47], v22 offset:57344\n"
  "v_mfma_f32_16x16x32_bf16 v[152:155], a[212:215], v[56:59], v[152:155]\n"
  "v_mfma_f32_16x16x32_bf16 v[156:159], a[212:215], v[60:63], v[156:159]\n"
  "ds_read_b128 v[48:51], v22 offset:61440\n"
  "v_mfma_f32_16x16x32_bf16 v[160:163], a[212:215], v[64:67], v[160:163]\n"
  "v_mfma_f32_16x16x32_bf16 v[164:167], a[216:219], v[52:55], v[164:167]\n"
  "s_add_u32 m0, s74, 93184\n"
  "v_mfma_f32_16x16x32_bf16 v[168:171], a[216:219], v[56:59], v[168:171]\n"
  "s_add_u32 s64, s86, 11008\n"
  "v_mfma_f32_16x16x32_bf16 v[172:175], a[216:219], v[60:63], v[172:175]\n"
  "buffer_load_dwordx4 %[voffW1], %[srdW1], s64 offen lds\n"
  "v_mfma_f32_16x16x32_bf16 v[176:179], a[216:219], v[64:67], v[176:179]\n"
  "v_mfma_f32_16x16x32_bf16 v[180:183], a[220:223], v[52:55], v[180:183]\n"
  "v_mfma_f32_16x16x32_bf16 v[184:187], a[220:223], v[56:59], v[184:187]\n"
  "v_mfma_f32_16x16x32_bf16 v[188:191], a[220:223], v[60:63], v[188:191]\n"
  "v_mfma_f32_16x16x32_bf16 v[192:195], a[220:223], v[64:67], v[192:195]\n"
  "s_waitcnt lgkmcnt(0)\n"
  "v_mfma_f32_16x16x32_bf16 v[132:135], a[224:227], v[36:39], v[132:135]\n"
  "ds_read_b128 v[52:55], v23 offset:49152\n"
  "v_mfma_f32_16x16x32_bf16 v[136:139], a[224:227], v[40:43], v[136:139]\n"
  "v_mfma_f32_16x16x32_bf16 v[140:143], a[224:227], v[44:47], v[140:143]\n"
  "ds_read_b128 v[56:59], v23 offset:53248\n"
  "v_mfma_f32_16x16x32_bf16 v[144:147], a[224:227], v[48:51], v[144:147]\n"
  "v_mfma_f32_16x16x32_bf16 v[148:151], a[228:231], v[36:39], v[148:151]\n"
  "ds_read_b128 v[60:63], v23 offset:57344\n"
  "v_mfma_f32_16x16x32_bf16 v[152:155], a[228:231], v[40:43], v[152:155]\n"
  "v_mfma_f32_16x16x32_bf16 v[156:159], a[228:231], v[44:47], v[156:159]\n"
  "ds_read_b128 v[64:67], v23 offset:61440\n"
  "v_mfma_f32_16x16x32_bf16 v[160:163], a[228:231], v[48:51], v[160:163]\n"
  "v_mfma_f32_16x16x32_bf16 v[164:167], a[232:235], v[36:39], v[164:167]\n"
  "v_mfma_f32_16x16x32_bf16 v[168:171], a[232:235], v[40:43], v[168:171]\n"
  "v_mfma_f32_16x16x32_bf16 v[172:175], a[232:235], v[44:47], v[172:175]\n"
  "s_add_u32 m0, s74, 94208\n"
  "v_mfma_f32_16x16x32_bf16 v[176:179], a[232:235], v[48:51], v[176:179]\n"
  "s_add_u32 s64, s86, 11072\n"
  "v_mfma_f32_16x16x32_bf16 v[180:183], a[236:239], v[36:39], v[180:183]\n"
  "buffer_load_dwordx4 %[voffW1], %[srdW1], s64 offen lds\n"
  "v_mfma_f32_16x16x32_bf16 v[184:187], a[236:239], v[40:43], v[184:187]\n"
  "v_mfma_f32_16x16x32_bf16 v[188:191], a[236:239], v[44:47], v[188:191]\n"
  "v_mfma_f32_16x16x32_bf16 v[192:195], a[236:239], v[48:51], v[192:195]\n"
  "s_waitcnt vmcnt(25) lgkmcnt(0)\n"
  "s_barrier\n"
  "v_mfma_f32_16x16x32_bf16 v[132:135], a[240:243], v[52:55], v[132:135]\n"
  "ds_read_b128 v[36:39], v20 offset:0\n"
  "v_mfma_f32_16x16x32_bf16 v[136:139], a[240:243], v[56:59], v[136:139]\n"
  "v_mfma_f32_16x16x32_bf16 v[140:143], a[240:243], v[60:63], v[140:143]\n"
  "ds_read_b128 v[40:43], v20 offset:4096\n"
  "s_add_u32 s83, %[rot], 3\n"
  "s_and_b32 s83, s83, 3\n"
  "s_lshl_b32 s83, s83, 8\n"
  "s_add_u32 m0, s72, 49152\n"
  "s_add_u32 s64, s83, 131072\n"
  "buffer_load_dwordx4 v16, %[srdC], s64 offen lds\n"
  "v_mfma_f32_16x16x32_bf16 v[144:147], a[240:243], v[64:67], v[144:147]\n"
  "v_mfma_f32_16x16x32_bf16 v[148:151], a[244:247], v[52:55], v[148:151]\n"
  "ds_read_b128 v[44:47], v20 offset:8192\n"
  "s_add_u32 m0, s72, 50176\n"
  "v_mfma_f32_16x16x32_bf16 v[152:155], a[244:247], v[56:59], v[152:155]\n"
  "s_add_u32 s64, s83, 131072\n"
  "v_mfma_f32_16x16x32_bf16 v[156:159], a[244:247], v[60:63], v[156:159]\n"
  "ds_read_b128 v[48:51], v20 offset:12288\n"
  "buffer_load_dwordx4 v17, %[srdC], s64 offen lds\n"
  "v_mfma_f32_16x16x32_bf16 v[160:163], a[244:247], v[64:67], v[160:163]\n"
  "v_mfma_f32_16x16x32_bf16 v[164:167], a[248:251], v[52:55], v[164:167]\n"
  "v_mfma_f32_16x16x32_bf16 v[168:171], a[248:251], v[56:59], v[168:171]\n"
  "s_add_u32 m0, s72, 51200\n"
  "v_mfma_f32_16x16x32_bf16 v[172:175], a[248:251], v[60:63], v[172:175]\n"
  "s_add_u32 s64, s83, 131072\n"
  "v_mfma_f32_16x16x32_bf16 v[176:179], a[248:251], v[64:67], v[176:179]\n"
  "buffer_load_dwordx4 v18, %[srdC], s64 offen lds\n"
  "v_mfma_f32_16x16x32_bf16 v[180:183], a[252:255], v[52:55], v[180:183]\n"
  "v_mfma_f32_16x16x32_bf16 v[184:187], a[252:255], v[56:59], v[184:187]\n"
  "v_mfma_f32_16x16x32_bf16 v[188:191], a[252:255], v[60:63], v[188:191]\n"
  "s_add_u32 m0, s74, 95232\n"
  "v_mfma_f32_16x16x32_bf16 v[192:195], a[252:255], v[64:67], v[192:195]\n"
  "s_add_u32 s64, s86, 11136\n"
  "s_waitcnt lgkmcnt(0)\n"
  "v_mfma_f32_16x16x32_bf16 v[68:71], a[0:3], v[36:39], 0\n"
  "ds_read_b128 v[52:55], v21 offset:0\n"
  "buffer_load_dwordx4 %[voffW1], %[srdW1], s64 offen lds\n"
  "v_mfma_f32_16x16x32_bf16 v[72:75], a[0:3], v[40:43], 0\n"
  "v_mfma_f32_16x16x32_bf16 v[76:79], a[0:3], v[44:47], 0\n"
  "ds_read_b128 v[56:59], v21 offset:4096\n"
  "s_add_u32 m0, s72, 52224\n"
  "v_mfma_f32_16x16x32_bf16 v[80:83], a[0:3], v[48:51], 0\n"
  "s_add_u32 s64, s83, 131072\n"
  "v_mfma_f32_16x16x32_bf16 v[84:87], a[4:7], v[36:39], 0\n"
  "ds_read_b128 v[60:63], v21 offset:8192\n"
  "buffer_load_dwordx4 v19, %[srdC], s64 offen lds\n"
  "v_mfma_f32_16x16x32_bf16 v[88:91], a[4:7], v[40:43], 0\n"
  "v_mfma_f32_16x16x32_bf16 v[92:95], a[4:7], v[44:47], 0\n"
  "ds_read_b128 v[64:67], v21 offset:12288\n"
  "v_mfma_f32_16x16x32_bf16 v[96:99], a[4:7], v[48:51], 0\n"
  "v_mfma_f32_16x16x32_bf16 v[100:103], a[8:11], v[36:39], 0\n"
  "v_mfma_f32_16x16x32_bf16 v[104:107], a[8:11], v[40:43], 0\n"
  "v_mfma_f32_16x16x32_bf16 v[108:111], a[8:11], v[44:47], 0\n"
  "v_mfma_f32_16x16x32_bf16 v[112:115], a[8:11], v[48:51], 0\n"
  "v_mfma_f32_16x16x32_bf16 v[116:119], a[12:15], v[36:39], 0\n"
  "v_cvt_pk_bf16_f32 v132, v132, v133\n"
  "v_cvt_pk_bf16_f32 v133, v134, v135\n"
  "v_mfma_f32_16x16x32_bf16 v[120:123], a[12:15], v[40:43], 0\n"
  "v_cvt_pk_bf16_f32 v134, v148, v149\n"
  "v_cvt_pk_bf16_f32 v135, v150, v151\n"
  "v_mfma_f32_16x16x32_bf16 v[124:127], a[12:15], v[44:47], 0\n"
  "s_add_u32 s66, s66, s69\n"
  "buffer_store_dwordx4 v[132:135], %[L16], %[srdWS], s66 offen sc1\n"
  "v_mfma_f32_16x16x32_bf16 v[128:131], a[12:15], v[48:51], 0\n"
  "s_add_u32 s86, %[rotU], 2\n"
  "s_and_b32 s86, s86, 3\n"
  "s_mul_i32 s86, s86, 352256\n"
  "s_waitcnt lgkmcnt(0)\n"
  "v_mfma_f32_16x16x32_bf16 v[68:71], a[16:19], v[52:55], v[68:71]\n"
  "ds_read_b128 v[36:39], v22 offset:0\n"
  "s_add_u32 m0, s74, 114688\n"
  "v_mfma_f32_16x16x32_bf16 v[72:75], a[16:19], v[56:59], v[72:75]\n"
  "s_add_u32 s64, s86, 0\n"
  "v_mfma_f32_16x16x32_bf16 v[76:79], a[16:19], v[60:63], v[76:79]\n"
  "ds_read_b128 v[40:43], v22 offset:4096\n"
  "buffer_load_dwordx4 %[voffW1], %[srdW1], s64 offen lds\n"
  "v_mfma_f32_16x16x32_bf16 v[80:83], a[16:19], v[64:67], v[80:83]\n"
  "v_mfma_f32_16x16x32_bf16 v[84:87], a[20:23], v[52:55], v[84:87]\n"
  "ds_read_b128 v[44:47], v22 offset:8192\n"
  "v_mfma_f32_16x16x32_bf16 v[88:91], a[20:23], v[56:59], v[88:91]\n"
  "v_mfma_f32_16x16x32_bf16 v[92:95], a[20:23], v[60:63], v[92:95]\n"
  "ds_read_b128 v[48:51], v22 offset:12288\n"
  "v_mfma_f32_16x16x32_bf16 v[96:99], a[20:23], v[64:67], v[96:99]\n"
  "v_mfma_f32_16x16x32_bf16 v[100:103], a[24:27], v[52:55], v[100:103]\n"
  "v_mfma_f32_16x16x32_bf16 v[104:107], a[24:27], v[56:59], v[104:107]\n"
  "v_mfma_f32_16x16x32_bf16 v[108:111], a[24:27], v[60:63], v[108:111]\n"
  "v_mfma_f32_16x16x32_bf16 v[112:115], a[24:27], v[64:67], v[112:115]\n"
  "v_mfma_f32_16x16x32_bf16 v[116:119], a[28:31], v[52:55], v[116:119]\n"
  "v_mfma_f32_16x16x32_bf16 v[120:123], a[28:31], v[56:59], v[120:123]\n"
  "v_mfma_f32_16x16x32_bf16 v[124:127], a[28:31], v[60:63], v[124:127]\n"
  "v_mfma_f32_16x16x32_bf16 v[128:131], a[28:31], v[64:67], v[128:131]\n"
  "s_waitcnt lgkmcnt(0)\n"
  "v_mfma_f32_16x16x32_bf16 v[68:71], a[32:35], v[36:39], v[68:71]\n"
  "ds_read_b128 v[52:55], v23 offset:0\n"
  "v_mfma_f32_16x16x32_bf16 v[72:75], a[32:35], v[40:43], v[72:75]\n"
  "v_mfma_f32_16x16x32_bf16 v[76:79], a[32:35], v[44:47], v[76:79]\n"
  "ds_read_b128 v[56:59], v23 offset:4096\n"
  "s_add_u32 m0, s74, 115712\n"
  "v_mfma_f32_16x16x32_bf16 v[80:83], a[32:35], v[48:51], v[80:83]\n"
  "s_add_u32 s64, s86, 64\n"
  "v_mfma_f32_16x16x32_bf16 v[84:87], a[36:39], v[36:39], v[84:87]\n"
  "ds_read_b128 v[60:63], v23 offset:8192\n"
  "buffer_load_dwordx4 %[voffW1], %[srdW1], s64 offen lds\n"
  "v_mfma_f32_16x16x32_bf16 v[88:91], a[36:39], v[40:43], v[88:91]\n"
  "v_mfma_f32_16x16x32_bf16 v[92:95], a[36:39], v[44:47], v[92:95]\n"
  "ds_read_b128 v[64:67], v23 offset:12288\n"
  "v_mfma_f32_16x16x32_bf16 v[96:99], a[36:39], v[48:51], v[96:99]\n"
  "v_mfma_f32_16x16x32_bf16 v[100:103], a[40:43], v[36:39], v[100:103]\n"
  "v_cvt_pk_bf16_f32 v136, v136, v137\n"
  "v_cvt_pk_bf16_f32 v137, v138, v139\n"
  "v_mfma_f32_16x16x32_bf16 v[104:107], a[40:43], v[40:43], v[104:107]\n"
  "v_cvt_pk_bf16_f32 v138, v152, v153\n"
  "v_cvt_pk_bf16_f32 v139, v154, v155\n"
  "v_mfma_f32_16x16x32_bf16 v[108:111], a[40:43], v[44:47], v[108:111]\n"
  "s_add_u32 s66, s66, s67\n"
  "buffer_store_dwordx4 v[136:139], %[L16], %[srdWS], s66 offen sc1\n"
  "v_mfma_f32_16x16x32_bf16 v[112:115], a[40:43], v[48:51], v[112:115]\n"
  "v_mfma_f32_16x16x32_bf16 v[116:119], a[44:47], v[36:39], v[116:119]\n"
  "v_mfma_f32_16x16x32_bf16 v[120:123], a[44:47], v[40:43], v[120:123]\n"
  "v_mfma_f32_16x16x32_bf16 v[124:127], a[44:47], v[44:47], v[124:127]\n"
  "v_mfma_f32_16x16x32_bf16 v[128:131], a[44:47], v[48:51], v[128:131]\n"
  "s_waitcnt vmcnt(22) lgkmcnt(0)\n"
  "s_barrier\n"
  "v_mfma_f32_16x16x32_bf16 v[68:71], a[48:51], v[52:55], v[68:71]\n"
  "ds_read_b128 v[36:39], v20 offset:16384\n"
  "v_mfma_f32_16x16x32_bf16 v[72:75], a[48:51], v[56:59], v[72:75]\n"
  "v_mfma_f32_16x16x32_bf16 v[76:79], a[48:51], v[60:63], v[76:79]\n"
  "ds_read_b128 v[40:43], v20 offset:20480\n"
  "s_add_u32 s83, %[rot], 0\n"
  "s_and_b32 s83, s83, 3\n"
  "s_lshl_b32 s83, s83, 8\n"
  "s_add_u32 m0, s72, 0\n"
  "s_add_u32 s64, s83, 196608\n"
  "buffer_load_dwordx4 v16, %[srdC], s64 offen lds\n"
  "v_mfma_f32_16x16x32_bf16 v[80:83], a[48:51], v[64:67], v[80:83]\n"
  "v_mfma_f32_16x16x32_bf16 v[84:87], a[52:55], v[52:55], v[84:87]\n"
  "ds_read_b128 v[44:47], v20 offset:24576\n"
  "v_mfma_f32_16x16x32_bf16 v[88:91], a[52:55], v[56:59], v[88:91]\n"
  "s_add_u32 m0, s74, 116736\n"
  "v_mfma_f32_16x16x32_bf16 v[92:95], a[52:55], v[60:63], v[92:95]\n"
  "ds_read_b128 v[48:51], v20 offset:28672\n"
  "s_add_u32 s64, s86, 128\n"
  "v_mfma_f32_16x16x32_bf16 v[96:99], a[52:55], v[64:67], v[96:99]\n"
  "buffer_load_dwordx4 %[voffW1], %[srdW1], s64 offen lds\n"
  "v_mfma_f32_16x16x32_bf16 v[100:103], a[56:59], v[52:55], v[100:103]\n"
  "v_mfma_f32_16x16x32_bf16 v[104:107], a[56:59], v[56:59], v[104:107]\n"
  "s_add_u32 m0, s72, 1024\n"
  "v_mfma_f32_16x16x32_bf16 v[108:111], a[56:59], v[60:63], v[108:111]\n"
  "s_add_u32 s64, s83, 196608\n"
  "v_mfma_f32_16x16x32_bf16 v[112:115], a[56:59], v[64:67], v[112:115]\n"
  "buffer_load_dwordx4 v17, %[srdC], s64 offen lds\n"
  "v_mfma_f32_16x16x32_bf16 v[116:119], a[60:63], v[52:55], v[116:119]\n"
  "v_mfma_f32_16x16x32_bf16 v[120:123], a[60:63], v[56:59], v[120:123]\n"
  "v_mfma_f32_16x16x32_bf16 v[124:127], a[60:63], v[60:63], v[124:127]\n"
  "s_add_u32 m0, s72, 2048\n"
  "v_mfma_f32_16x16x32_bf16 v[128:131], a[60:63], v[64:67], v[128:131]\n"
  "s_add_u32 s64, s83, 196608\n"
  "s_waitcnt lgkmcnt(0)\n"
  "v_mfma_f32_16x16x32_bf16 v[68:71], a[64:67], v[36:39], v[68:71]\n"
  "ds_read_b128 v[52:55], v21 offset:16384\n"
  "buffer_load_dwordx4 v18, %[srdC], s64 offen lds\n"
  "v_mfma_f32_16x16x32_bf16 v[72:75], a[64:67], v[40:43], v[72:75]\n"
  "v_mfma_f32_16x16x32_bf16 v[76:79], a[64:67], v[44:47], v[76:79]\n"
  "ds_read_b128 v[56:59], v21 offset:20480\n"
  "s_add_u32 m0, s72, 3072\n"
  "v_mfma_f32_16x16x32_bf16 v[80:83], a[64:67], v[48:51], v[80:83]\n"
  "s_add_u32 s64, s83, 196608\n"
  "v_mfma_f32_16x16x32_bf16 v[84:87], a[68:71], v[36:39], v[84:87]\n"
  "ds_read_b128 v[60:63], v21 offset:24576\n"
  "buffer_load_dwordx4 v19, %[srdC], s64 offen lds\n"
  "v_mfma_f32_16x16x32_bf16 v[88:91], a[68:71], v[40:43], v[88:91]\n"
  "v_cvt_pk_bf16_f32 v140, v140, v141\n"
  "v_cvt_pk_bf16_f32 v141, v142, v143\n"
  "v_mfma_f32_16x16x32_bf16 v[92:95], a[68:71], v[44:47], v[92:95]\n"
  "ds_read_b128 v[64:67], v21 offset:28672\n"
  "v_cvt_pk_bf16_f32 v142, v156, v157\n"
  "v_mfma_f32_16x16x32_bf16 v[96:99], a[68:71], v[48:51], v[96:99]\n"
  "s_add_u32 m0, s74, 117760\n"
  "v_cvt_pk_bf16_f32 v143, v158, v159\n"
  "v_mfma_f32_16x16x32_bf16 v[100:103], a[72:75], v[36:39], v[100:103]\n"
  "s_add_u32 s64, s86, 11008\n"
  "s_add_u32 s66, s66, s67\n"
  "v_mfma_f32_16x16x32_bf16 v[104:107], a[72:75], v[40:43], v[104:107]\n"
  "buffer_load_dwordx4 %[voffW1], %[srdW1], s64 offen lds\n"
  "buffer_store_dwordx4 v[140:143], %[L16], %[srdWS], s66 offen sc1\n"
  "v_mfma_f32_16x16x32_bf16 v[108:111], a[72:75], v[44:47], v[108:111]\n"
  "v_mfma_f32_16x16x32_bf16 v[112:115], a[72:75], v[48:51], v[112:115]\n"
  "v_mfma_f32_16x16x32_bf16 v[116:119], a[76:79], v[36:39], v[116:119]\n"
  "v_mfma_f32_16x16x32_bf16 v[120:123], a[76:79], v[40:43], v[120:123]\n"
  "v_mfma_f32_16x16x32_bf16 v[124:127], a[76:79], v[44:47], v[124:127]\n"
  "v_mfma_f32_16x16x32_bf16 v[128:131], a[76:79], v[48:51], v[128:131]\n"
  "s_waitcnt lgkmcnt(0)\n"
  "v_mfma_f32_16x16x32_bf16 v[68:71], a[80:83], v[52:55], v[68:71]\n"
  "ds_read_b128 v[36:39], v22 offset:16384\n"
  "v_mfma_f32_16x16x32_bf16 v[72:75], a[80:83], v[56:59], v[72:75]\n"
  "v_mfma_f32_16x16x32_bf16 v[76:79], a[80:83], v[60:63], v[76:79]\n"
  "ds_read_b128 v[40:43], v22 offset:20480\n"
  "v_mfma_f32_16x16x32_bf16 v[80:83], a[80:83], v[64:67], v[80:83]\n"
  "v_mfma_f32_16x16x32_bf16 v[84:87], a[84:87], v[52:55], v[84:87]\n"
  "ds_read_b128 v[44:47], v22 offset:24576\n"
  "v_mfma_f32_16x16x32_bf16 v[88:91], a[84:87], v[56:59], v[88:91]\n"
  "v_mfma_f32_16x16x32_bf16 v[92:95], a[84:87], v[60:63], v[92:95]\n"
  "ds_read_b128 v[48:51], v22 offset:28672\n"
  "v_mfma_f32_16x16x32_bf16 v[96:99], a[84:87], v[64:67], v[96:99]\n"
  "v_mfma_f32_16x16x32_bf16 v[100:103], a[88:91], v[52:55], v[100:103]\n"
  "buffer_load_dwordx2 v[28:29], %[pollLN], %[srdWS], 0 offen sc1\n"
  "v_mfma_f32_16x16x32_bf16 v[104:107], a[88:91], v[56:59], v[104:107]\n"
  "s_add_u32 m0, s74, 118784\n"
  "v_mfma_f32_16x16x32_bf16 v[108:111], a[88:91], v[60:63], v[108:111]\n"
  "s_add_u32 s64, s86, 11072\n"
  "v_mfma_f32_16x16x32_bf16 v[112:115], a[88:91], v[64:67], v[112:115]\n"
  "buffer_load_dwordx4 %[voffW1], %[srdW1], s64 offen lds\n"
  "v_mfma_f32_16x16x32_bf16 v[116:119], a[92:95], v[52:55], v[116:119]\n"
  "v_mfma_f32_16x16x32_bf16 v[120:123], a[92:95], v[56:59], v[120:123]\n"
  "v_mfma_f32_16x16x32_bf16 v[124:127], a[92:95], v[60:63], v[124:127]\n"
  "v_mfma_f32_16x16x32_bf16 v[128:131], a[92:95], v[64:67], v[128:131]\n"
  "s_waitcnt lgkmcnt(0)\n"
  "v_mfma_f32_16x16x32_bf16 v[68:71], a[96:99], v[36:39], v[68:71]\n"
  "ds_read_b128 v[52:55], v23 offset:16384\n"
  "v_cvt_pk_bf16_f32 v144, v144, v145\n"
  "v_mfma_f32_16x16x32_bf16 v[72:75], a[96:99], v[40:43], v[72:75]\n"
  "v_cvt_pk_bf16_f32 v145, v146, v147\n"
  "v_cvt_pk_bf16_f32 v146, v160, v161\n"
  "v_mfma_f32_16x16x32_bf16 v[76:79], a[96:99], v[44:47], v[76:79]\n"
  "ds_read_b128 v[56:59], v23 offset:20480\n"
  "v_cvt_pk_bf16_f32 v147, v162, v163\n"
  "v_mfma_f32_16x16x32_bf16 v[80:83], a[96:99], v[48:51], v[80:83]\n"
  "s_add_u32 s66, s66, s67\n"
  "buffer_store_dwordx4 v[144:147], %[L16], %[srdWS], s66 offen sc1\n"
  "v_mfma_f32_16x16x32_bf16 v[84:87], a[100:103], v[36:39], v[84:87]\n"
  "ds_read_b128 v[60:63], v23 offset:24576\n"
  "v_mfma_f32_16x16x32_bf16 v[88:91], a[100:103], v[40:43], v[88:91]\n"
  "v_mfma_f32_16x16x32_bf16 v[92:95], a[100:103], v[44:47], v[92:95]\n"
  "ds_read_b128 v[64:67], v23 offset:28672\n"
  "v_mfma_f32_16x16x32_bf16 v[96:99], a[100:103], v[48:51], v[96:99]\n"
  "v_mfma_f32_16x16x32_bf16 v[100:103], a[104:107], v[36:39], v[100:103]\n"
  "v_mfma_f32_16x16x32_bf16 v[104:107], a[104:107], v[40:43], v[104:107]\n"
  "v_mfma_f32_16x16x32_bf16 v[108:111], a[104:107], v[44:47], v[108:111]\n"
  "v_mfma_f32_16x16x32_bf16 v[112:115], a[104:107], v[48:51], v[112:115]\n"
  "s_add_u32 m0, s74, 119808\n"
  "v_mfma_f32_16x16x32_bf16 v[116:119], a[108:111], v[36:39], v[116:119]\n"
  "s_add_u32 s64, s86, 11136\n"
  "v_mfma_f32_16x16x32_bf16 v[120:123], a[108:111], v[40:43], v[120:123]\n"
  "buffer_load_dwordx4 %[voffW1], %[srdW1], s64 offen lds\n"
  "v_mfma_f32_16x16x32_bf16 v[124:127], a[108:111], v[44:47], v[124:127]\n"
  "v_mfma_f32_16x16x32_bf16 v[128:131], a[108:111], v[48:51], v[128:131]\n"
  "s_waitcnt vmcnt(24) lgkmcnt(0)\n"
  "s_barrier\n"
  "v_mfma_f32_16x16x32_bf16 v[68:71], a[112:115], v[52:55], v[68:71]\n"
  "ds_read_b128 v[36:39], v20 offset:32768\n"
  "v_mfma_f32_16x16x32_bf16 v[72:75], a[112:115], v[56:59], v[72:75]\n"
  "v_mfma_f32_16x16x32_bf16 v[76:79], a[112:115], v[60:63], v[76:79]\n"
  "ds_read_b128 v[40:43], v20 offset:36864\n"
  "s_add_u32 s83, %[rot], 1\n"
  "s_and_b32 s83, s83, 3\n"
  "s_lshl_b32 s83, s83, 8\n"
  "s_add_u32 m0, s72, 16384\n"
  "s_add_u32 s64, s83, 196608\n"
  "buffer_load_dwordx4 v16, %[srdC], s64 offen lds\n"
  "v_mfma_f32_16x16x32_bf16 v[80:83], a[112:115], v[64:67], v[80:83]\n"
  "v_mfma_f32_16x16x32_bf16 v[84:87], a[116:119], v[52:55], v[84:87]\n"
  "ds_read_b128 v[44:47], v20 offset:40960\n"
  "s_add_u32 m0, s72, 17408\n"
  "v_mfma_f32_16x16x32_bf16 v[88:91], a[116:119], v[56:59], v[88:91]\n"
  "s_add_u32 s64, s83, 196608\n"
  "v_mfma_f32_16x16x32_bf16 v[92:95], a[116:119], v[60:63], v[92:95]\n"
  "ds_read_b128 v[48:51], v20 offset:45056\n"
  "buffer_load_dwordx4 v17, %[srdC], s64 offen lds\n"
  "v_mfma_f32_16x16x32_bf16 v[96:99], a[116:119], v[64:67], v[96:99]\n"
  "v_mfma_f32_16x16x32_bf16 v[100:103], a[120:123], v[52:55], v[100:103]\n"
  "v_mfma_f32_16x16x32_bf16 v[104:107], a[120:123], v[56:59], v[104:107]\n"
  "s_add_u32 m0, s72, 18432\n"
  "v_mfma_f32_16x16x32_bf16 v[108:111], a[120:123], v[60:63], v[108:111]\n"
  "s_add_u32 s64, s83, 196608\n"
  "v_mfma_f32_16x16x32_bf16 v[112:115], a[120:123], v[64:67], v[112:115]\n"
  "buffer_load_dwordx4 v18, %[srdC], s64 offen lds\n"
  "v_mfma_f32_16x16x32_bf16 v[116:119], a[124:127], v[52:55], v[116:119]\n"
  "v_cvt_pk_bf16_f32 v164, v164, v165\n"
  "v_cvt_pk_bf16_f32 v165, v166, v167\n"
  "v_mfma_f32_16x16x32_bf16 v[120:123], a[124:127], v[56:59], v[120:123]\n"
  "v_cvt_pk_bf16_f32 v166, v180, v181\n"
  "v_cvt_pk_bf16_f32 v167, v182, v183\n"
  "v_mfma_f32_16x16x32_bf16 v[124:127], a[124:127], v[60:63], v[124:127]\n"
  "s_add_u32 m0, s72, 19456\n"
  "s_add_u32 s66, s66, s68\n"
  "v_mfma_f32_16x16x32_bf16 v[128:131], a[124:127], v[64:67], v[128:131]\n"
  "s_add_u32 s64, s83, 196608\n"
  "buffer_store_dwordx4 v[164:167], %[L16], %[srdWS], s66 offen sc1\n"
  "s_waitcnt lgkmcnt(0)\n"
  "v_mfma_f32_16x16x32_bf16 v[68:71], a[128:131], v[36:39], v[68:71]\n"
  "ds_read_b128 v[52:55], v21 offset:32768\n"
  "buffer_load_dwordx4 v19, %[srdC], s64 offen lds\n"
  "v_mfma_f32_16x16x32_bf16 v[72:75], a[128:131], v[40:43], v[72:75]\n"
  "s_add_u32 s86, %[rotU], 3\n"
  "s_and_b32 s86, s86, 3\n"
  "s_mul_i32 s86, s86, 352256\n"
  "v_mfma_f32_16x16x32_bf16 v[76:79], a[128:131], v[44:47], v[76:79]\n"
  "ds_read_b128 v[56:59], v21 offset:36864\n"
  "s_add_u32 m0, s74, 139264\n"
  "v_mfma_f32_16x16x32_bf16 v[80:83], a[128:131], v[48:51], v[80:83]\n"
  "s_add_u32 s64, s86, 0\n"
  "v_mfma_f32_16x16x32_bf16 v[84:87], a[132:135], v[36:39], v[84:87]\n"
  "ds_read_b128 v[60:63], v21 offset:40960\n"
  "buffer_load_dwordx4 %[voffW1], %[srdW1], s64 offen lds\n"
  "v_mfma_f32_16x16x32_bf16 v[88:91], a[132:135], v[40:43], v[88:91]\n"
  "v_mfma_f32_16x16x32_bf16 v[92:95], a[132:135], v[44:47], v[92:95]\n"
  "ds_read_b128 v[64:67], v21 offset:45056\n"
  "v_mfma_f32_16x16x32_bf16 v[96:99], a[132:135], v[48:51], v[96:99]\n"
  "v_mfma_f32_16x16x32_bf16 v[100:103], a[136:139], v[36:39], v[100:103]\n"
  "v_mfma_f32_16x16x32_bf16 v[104:107], a[136:139], v[40:43], v[104:107]\n"
  "v_mfma_f32_16x16x32_bf16 v[108:111], a[136:139], v[44:47], v[108:111]\n"
  "v_mfma_f32_16x16x32_bf16 v[112:115], a[136:139], v[48:51], v[112:115]\n"
  "v_mfma_f32_16x16x32_bf16 v[116:119], a[140:143], v[36:39], v[116:119]\n"
  "v_mfma_f32_16x16x32_bf16 v[120:123], a[140:143], v[40:43], v[120:123]\n"
  "v_mfma_f32_16x16x32_bf16 v[124:127], a[140:143], v[44:47], v[124:127]\n"
  "v_mfma_f32_16x16x32_bf16 v[128:131], a[140:143], v[48:51], v[128:131]\n"
  "s_waitcnt lgkmcnt(0)\n"
  "v_mfma_f32_16x16x32_bf16 v[68:71], a[144:147], v[52:55], v[68:71]\n"
  "ds_read_b128 v[36:39], v22 offset:32768\n"
  "v_mfma_f32_16x16x32_bf16 v[72:75], a[144:147], v[56:59], v[72:75]\n"
  "v_mfma_f32_16x16x32_bf16 v[76:79], a[144:147], v[60:63], v[76:79]\n"
  "ds_read_b128 v[40:43], v22 offset:36864\n"
  "v_mfma_f32_16x16x32_bf16 v[80:83], a[144:147], v[64:67], v[80:83]\n"
  "s_waitcnt vmcnt(9)\n"
  "v_cmp_eq_u64 vcc, %[tok], v[28:29]\n"
  "s_nop 0\n"
  "s_cmp_eq_u64 vcc, -1\n"
  "s_cbranch_scc0 SPINLNK_%=\n"
  "BACKLNK_%=:\n"
  "v_mfma_f32_16x16x32_bf16 v[84:87], a[148:151], v[52:55], v[84:87]\n"
  "ds_read_b128 v[44:47], v22 offset:40960\n"
  "s_add_u32 m0, s74, 140288\n"
  "v_mfma_f32_16x16x32_bf16 v[88:91], a[148:151], v[56:59], v[88:91]\n"
  "s_add_u32 s64, s86, 64\n"
  "v_mfma_f32_16x16x32_bf16 v[92:95], a[148:151], v[60:63], v[92:95]\n"
  "ds_read_b128 v[48:51], v22 offset:45056\n"
  "buffer_load_dwordx4 %[voffW1], %[srdW1], s64 offen lds\n"
  "v_mfma_f32_16x16x32_bf16 v[96:99], a[148:151], v[64:67], v[96:99]\n"
  "v_mfma_f32_16x16x32_bf16 v[100:103], a[152:155], v[52:55], v[100:103]\n"
  "v_cvt_pk_bf16_f32 v168, v168, v169\n"
  "v_cvt_pk_bf16_f32 v169, v170, v171\n"
  "v_mfma_f32_16x16x32_bf16 v[104:107], a[152:155], v[56:59], v[104:107]\n"
  "v_cvt_pk_bf16_f32 v170, v184, v185\n"
  "v_cvt_pk_bf16_f32 v171, v186, v187\n"
  "v_mfma_f32_16x16x32_bf16 v[108:111], a[152:155], v[60:63], v[108:111]\n"
  "s_add_u32 s66, s66, s67\n"
  "buffer_store_dwordx4 v[168:171], %[L16], %[srdWS], s66 offen sc1\n"
  "v_mfma_f32_16x16x32_bf16 v[112:115], a[152:155], v[64:67], v[112:115]\n"
  "v_mfma_f32_16x16x32_bf16 v[116:119], a[156:159], v[52:55], v[116:119]\n"
  "v_mfma_f32_16x16x32_bf16 v[120:123], a[156:159], v[56:59], v[120:123]\n"
  "v_mfma_f32_16x16x32_bf16 v[124:127], a[156:159], v[60:63], v[124:127]\n"
  "v_mfma_f32_16x16x32_bf16 v[128:131], a[156:159], v[64:67], v[128:131]\n"
  "s_waitcnt lgkmcnt(0)\n"
  "v_mfma_f32_16x16x32_bf16 v[68:71], a[160:163], v[36:39], v[68:71]\n"
  "ds_read_b128 v[52:55], v23 offset:32768\n"
  "v_mfma_f32_16x16x32_bf16 v[72:75], a[160:163], v[40:43], v[72:75]\n"
  "v_mfma_f32_16x16x32_bf16 v[76:79], a[160:163], v[44:47], v[76:79]\n"
  "ds_read_b128 v[56:59], v23 offset:36864\n"
  "v_mfma_f32_16x16x32_bf16 v[80:83], a[160:163], v[48:51], v[80:83]\n"
  "v_mfma_f32_16x16x32_bf16 v[84:87], a[164:167], v[36:39], v[84:87]\n"
  "ds_read_b128 v[60:63], v23 offset:40960\n"
  "v_mfma_f32_16x16x32_bf16 v[88:91], a[164:167], v[40:43], v[88:91]\n"
  "v_mfma_f32_16x16x32_bf16 v[92:95], a[164:167], v[44:47], v[92:95]\n"
  "ds_read_b128 v[64:67], v23 offset:45056\n"
  "s_add_u32 m0, s74, 141312\n"
  "v_mfma_f32_16x16x32_bf16 v[96:99], a[164:167], v[48:51], v[96:99]\n"
  "s_add_u32 s64, s86, 128\n"
  "v_mfma_f32_16x16x32_bf16 v[100:103], a[168:171], v[36:39], v[100:103]\n"
  "buffer_load_dwordx4 %[voffW1], %[srdW1], s64 offen lds\n"
  "v_mfma_f32_16x16x32_bf16 v[104:107], a[168:171], v[40:43], v[104:107]\n"
  "v_mfma_f32_16x16x32_bf16 v[108:111], a[168:171], v[44:47], v[108:111]\n"
  "v_mfma_f32_16x16x32_bf16 v[112:115], a[168:171], v[48:51], v[112:115]\n"
  "v_mfma_f32_16x16x32_bf16 v[116:119], a[172:175], v[36:39], v[116:119]\n"
  "v_mfma_f32_16x16x32_bf16 v[120:123], a[172:175], v[40:43], v[120:123]\n"
  "v_mfma_f32_16x16x32_bf16 v[124:127], a[172:175], v[44:47], v[124:127]\n"
  "v_mfma_f32_16x16x32_bf16 v[128:131], a[172:175], v[48:51], v[128:131]\n"
  "s_waitcnt vmcnt(63) lgkmcnt(0)\n"
  "s_barrier\n"
  "v_mfma_f32_16x16x32_bf16 v[68:71], a[176:179], v[52:55], v[68:71]\n"
  "ds_read_b128 v[36:39], v20 offset:49152\n"
  "v_mfma_f32_16x16x32_bf16 v[72:75], a[176:179], v[56:59], v[72:75]\n"
  "v_mfma_f32_16x16x32_bf16 v[76:79], a[176:179], v[60:63], v[76:79]\n"
  "ds_read_b128 v[40:43], v20 offset:53248\n"
  "s_add_u32 s83, %[rot], 2\n"
  "s_and_b32 s83, s83, 3\n"
  "s_lshl_b32 s83, s83, 8\n"
  "s_add_u32 m0, s72, 32768\n"
  "s_add_u32 s64, s83, 196608\n"
  "buffer_load_dwordx4 v16, %[srdC], s64 offen lds\n"
  "v_mfma_f32_16x16x32_bf16 v[80:83], a[176:179], v[64:67], v[80:83]\n"
  "v_mfma_f32_16x16x32_bf16 v[84:87], a[180:183], v[52:55], v[84:87]\n"
  "ds_read_b128 v[44:47], v20 offset:57344\n"
  "v_cvt_pk_bf16_f32 v172, v172, v173\n"
  "v_mfma_f32_16x16x32_bf16 v[88:91], a[180:183], v[56:59], v[88:91]\n"
  "s_add_u32 m0, s72, 33792\n"
  "v_cvt_pk_bf16_f32 v173, v174, v175\n"
  "v_mfma_f32_16x16x32_bf16 v[92:95], a[180:183], v[60:63], v[92:95]\n"
  "ds_read_b128 v[48:51], v20 offset:61440\n"
  "s_add_u32 s64, s83, 196608\n"
  "v_mfma_f32_16x16x32_bf16 v[96:99], a[180:183], v[64:67], v[96:99]\n"
  "buffer_load_dwordx4 v17, %[srdC], s64 offen lds\n"
  "v_cvt_pk_bf16_f32 v174, v188, v189\n"
  "v_mfma_f32_16x16x32_bf16 v[100:103], a[184:187], v[52:55], v[100:103]\n"
  "v_cvt_pk_bf16_f32 v175, v190, v191\n"
  "s_add_u32 s66, s66, s67\n"
  "v_mfma_f32_16x16x32_bf16 v[104:107], a[184:187], v[56:59], v[104:107]\n"
  "s_add_u32 m0, s74, 142336\n"
  "buffer_store_dwordx4 v[172:175], %[L16], %[srdWS], s66 offen sc1\n"
  "v_mfma_f32_16x16x32_bf16 v[108:111], a[184:187], v[60:63], v[108:111]\n"
  "s_add_u32 s64, s86, 11008\n"
  "v_mfma_f32_16x16x32_bf16 v[112:115], a[184:187], v[64:67], v[112:115]\n"
  "buffer_load_dwordx4 %[voffW1], %[srdW1], s64 offen lds\n"
  "v_mfma_f32_16x16x32_bf16 v[116:119], a[188:191], v[52:55], v[116:119]\n"
  "v_mfma_f32_16x16x32_bf16 v[120:123], a[188:191], v[56:59], v[120:123]\n"
  "v_mfma_f32_16x16x32_bf16 v[124:127], a[188:191], v[60:63], v[124:127]\n"
  "s_add_u32 m0, s72, 34816\n"
  "v_mfma_f32_16x16x32_bf16 v[128:131], a[188:191], v[64:67], v[128:131]\n"
  "s_add_u32 s64, s83, 196608\n"
  "s_waitcnt lgkmcnt(0)\n"
  "v_mfma_f32_16x16x32_bf16 v[68:71], a[192:195], v[36:39], v[68:71]\n"
  "ds_read_b128 v[52:55], v21 offset:49152\n"
  "buffer_load_dwordx4 v18, %[srdC], s64 offen lds\n"
  "v_mfma_f32_16x16x32_bf16 v[72:75], a[192:195], v[40:43], v[72:75]\n"
  "v_mfma_f32_16x16x32_bf16 v[76:79], a[192:195], v[44:47], v[76:79]\n"
  "ds_read_b128 v[56:59], v21 offset:53248\n"
  "s_add_u32 m0, s72, 35840\n"
  "v_mfma_f32_16x16x32_bf16 v[80:83], a[192:195], v[48:51], v[80:83]\n"
  "s_add_u32 s64, s83, 196608\n"
  "v_mfma_f32_16x16x32_bf16 v[84:87], a[196:199], v[36:39], v[84:87]\n"
  "ds_read_b128 v[60:63], v21 offset:57344\n"
  "buffer_load_dwordx4 v19, %[srdC], s64 offen lds\n"
  "v_mfma_f32_16x16x32_bf16 v[88:91], a[196:199], v[40:43], v[88:91]\n"
  "v_mfma_f32_16x16x32_bf16 v[92:95], a[196:199], v[44:47], v[92:95]\n"
  "ds_read_b128 v[64:67], v21 offset:61440\n"
  "v_mfma_f32_16x16x32_bf16 v[96:99], a[196:199], v[48:51], v[96:99]\n"
  "v_mfma_f32_16x16x32_bf16 v[100:103], a[200:203], v[36:39], v[100:103]\n"
  "v_mfma_f32_16x16x32_bf16 v[104:107], a[200:203], v[40:43], v[104:107]\n"
  "v_mfma_f32_16x16x32_bf16 v[108:111], a[200:203], v[44:47], v[108:111]\n"
  "v_mfma_f32_16x16x32_bf16 v[112:115], a[200:203], v[48:51], v[112:115]\n"
  "s_add_u32 m0, s74, 143360\n"
  "v_mfma_f32_16x16x32_bf16 v[116:119], a[204:207], v[36:39], v[116:119]\n"
  "s_add_u32 s64, s86, 11072\n"
  "v_mfma_f32_16x16x32_bf16 v[120:123], a[204:207], v[40:43], v[120:123]\n"
  "buffer_load_dwordx4 %[voffW1], %[srdW1], s64 offen lds\n"
  "v_mfma_f32_16x16x32_bf16 v[124:127], a[204:207], v[44:47], v[124:127]\n"
  "v_mfma_f32_16x16x32_bf16 v[128:131], a[204:207], v[48:51], v[128:131]\n"
  "s_waitcnt lgkmcnt(0)\n"
  "v_mfma_f32_16x16x32_bf16 v[68:71], a[208:211], v[52:55], v[68:71]\n"
  "ds_read_b128 v[36:39], v22 offset:49152\n"
  "v_cvt_pk_bf16_f32 v176, v176, v177\n"
  "v_mfma_f32_16x16x32_bf16 v[72:75], a[208:211], v[56:59], v[72:75]\n"
  "v_cvt_pk_bf16_f32 v177, v178, v179\n"
  "v_cvt_pk_bf16_f32 v178, v192, v193\n"
  "v_mfma_f32_16x16x32_bf16 v[76:79], a[208:211], v[60:63], v[76:79]\n"
  "ds_read_b128 v[40:43], v22 offset:53248\n"
  "v_cvt_pk_bf16_f32 v179, v194, v195\n"
  "v_mfma_f32_16x16x32_bf16 v[80:83], a[208:211], v[64:67], v[80:83]\n"
  "s_add_u32 s66, s66, s67\n"
  "buffer_store_dwordx4 v[176:179], %[L16], %[srdWS], s66 offen sc1\n"
  "v_mfma_f32_16x16x32_bf16 v[84:87], a[212:215], v[52:55], v[84:87]\n"
  "ds_read_b128 v[44:47], v22 offset:57344\n"
  "v_mfma_f32_16x16x32_bf16 v[88:91], a[212:215], v[56:59], v[88:91]\n"
  "v_mfma_f32_16x16x32_bf16 v[92:95], a[212:215], v[60:63], v[92:95]\n"
  "ds_read_b128 v[48:51], v22 offset:61440\n"
  "v_mfma_f32_16x16x32_bf16 v[96:99], a[212:215], v[64:67], v[96:99]\n"
  "v_mfma_f32_16x16x32_bf16 v[100:103], a[216:219], v[52:55], v[100:103]\n"
  "v_mfma_f32_16x16x32_bf16 v[104:107], a[216:219], v[56:59], v[104:107]\n"
  "v_mfma_f32_16x16x32_bf16 v[108:111], a[216:219], v[60:63], v[108:111]\n"
  "v_mfma_f32_16x16x32_bf16 v[112:115], a[216:219], v[64:67], v[112:115]\n"
  "v_mfma_f32_16x16x32_bf16 v[116:119], a[220:223], v[52:55], v[116:119]\n"
  "v_mfma_f32_16x16x32_bf16 v[120:123], a[220:223], v[56:59], v[120:123]\n"
  "s_add_u32 m0, s74, 144384\n"
  "v_mfma_f32_16x16x32_bf16 v[124:127], a[220:223], v[60:63], v[124:127]\n"
  "s_add_u32 s64, s86, 11136\n"
  "v_mfma_f32_16x16x32_bf16 v[128:131], a[220:223], v[64:67], v[128:131]\n"
  "buffer_load_dwordx4 %[voffW1], %[srdW1], s64 offen lds\n"
  "s_waitcnt lgkmcnt(0)\n"
  "v_mfma_f32_16x16x32_bf16 v[68:71], a[224:227], v[36:39], v[68:71]\n"
  "ds_read_b128 v[52:55], v23 offset:49152\n"
  "v_mfma_f32_16x16x32_bf16 v[72:75], a[224:227], v[40:43], v[72:75]\n"
  "v_mfma_f32_16x16x32_bf16 v[76:79], a[224:227], v[44:47], v[76:79]\n"
  "ds_read_b128 v[56:59], v23 offset:53248\n"
  "v_mfma_f32_16x16x32_bf16 v[80:83], a[224:227], v[48:51], v[80:83]\n"
  "v_mfma_f32_16x16x32_bf16 v[84:87], a[228:231], v[36:39], v[84:87]\n"
  "ds_read_b128 v[60:63], v23 offset:57344\n"
  "v_mfma_f32_16x16x32_bf16 v[88:91], a[228:231], v[40:43], v[88:91]\n"
  "v_mfma_f32_16x16x32_bf16 v[92:95], a[228:231], v[44:47], v[92:95]\n"
  "ds_read_b128 v[64:67], v23 offset:61440\n"
  "v_mfma_f32_16x16x32_bf16 v[96:99], a[228:231], v[48:51], v[96:99]\n"
  "v_mfma_f32_16x16x32_bf16 v[100:103], a[232:235], v[36:39], v[100:103]\n"
  "v_mfma_f32_16x16x32_bf16 v[104:107], a[232:235], v[40:43], v[104:107]\n"
  "v_mfma_f32_16x16x32_bf16 v[108:111], a[232:235], v[44:47], v[108:111]\n"
  "v_mfma_f32_16x16x32_bf16 v[112:115], a[232:235], v[48:51], v[112:115]\n"
  "v_mfma_f32_16x16x32_bf16 v[116:119], a[236:239], v[36:39], v[116:119]\n"
  "v_mfma_f32_16x16x32_bf16 v[120:123], a[236:239], v[40:43], v[120:123]\n"
  "v_mfma_f32_16x16x32_bf16 v[124:127], a[236:239], v[44:47], v[124:127]\n"
  "v_mfma_f32_16x16x32_bf16 v[128:131], a[236:239], v[48:51], v[128:131]\n"
  "s_waitcnt vmcnt(63) lgkmcnt(0)\n"
  "s_barrier\n"
  "v_mfma_f32_16x16x32_bf16 v[68:71], a[240:243], v[52:55], v[68:71]\n"
  "ds_read_b128 v[36:39], v20 offset:0\n"
  "v_mfma_f32_16x16x32_bf16 v[72:75], a[240:243], v[56:59], v[72:75]\n"
  "v_mfma_f32_16x16x32_bf16 v[76:79], a[240:243], v[60:63], v[76:79]\n"
  "ds_read_b128 v[40:43], v20 offset:4096\n"
  "s_add_u32 s83, %[rot], 3\n"
  "s_and_b32 s83, s83, 3\n"
  "s_lshl_b32 s83, s83, 8\n"
  "s_add_u32 m0, s72, 49152\n"
  "s_add_u32 s64, s83, 196608\n"
  "buffer_load_dwordx4 v16, %[srdC], s64 offen lds\n"
  "v_mfma_f32_16x16x32_bf16 v[80:83], a[240:243], v[64:67], v[80:83]\n"
  "v_mfma_f32_16x16x32_bf16 v[84:87], a[244:247], v[52:55], v[84:87]\n"
  "ds_read_b128 v[44:47], v20 offset:8192\n"
  "v_mfma_f32_16x16x32_bf16 v[88:91], a[244:247], v[56:59], v[88:91]\n"
  "s_add_u32 m0, s72, 50176\n"
  "v_mfma_f32_16x16x32_bf16 v[92:95], a[244:247], v[60:63], v[92:95]\n"
  "ds_read_b128 v[48:51], v20 offset:12288\n"
  "s_add_u32 s64, s83, 196608\n"
  "v_mfma_f32_16x16x32_bf16 v[96:99], a[244:247], v[64:67], v[96:99]\n"
  "buffer_load_dwordx4 v17, %[srdC], s64 offen lds\n"
  "v_mfma_f32_16x16x32_bf16 v[100:103], a[248:251], v[52:55], v[100:103]\n"
  "v_mfma_f32_16x16x32_bf16 v[104:107], a[248:251], v[56:59], v[104:107]\n"
  "s_add_u32 m0, s72, 51200\n"
  "v_mfma_f32_16x16x32_bf16 v[108:111], a[248:251], v[60:63], v[108:111]\n"
  "s_add_u32 s64, s83, 196608\n"
  "v_mfma_f32_16x16x32_bf16 v[112:115], a[248:251], v[64:67], v[112:115]\n"
  "buffer_load_dwordx4 v18, %[srdC], s64 offen lds\n"
  "v_mfma_f32_16x16x32_bf16 v[116:119], a[252:255], v[52:55], v[116:119]\n"
  "v_mfma_f32_16x16x32_bf16 v[120:123], a[252:255], v[56:59], v[120:123]\n"
  "v_mfma_f32_16x16x32_bf16 v[124:127], a[252:255], v[60:63], v[124:127]\n"
  "s_add_u32 m0, s72, 52224\n"
  "v_mfma_f32_16x16x32_bf16 v[128:131], a[252:255], v[64:67], v[128:131]\n"
  "s_add_u32 s64, s83, 196608\n"
  "s_waitcnt lgkmcnt(0)\n"
  "v_mfma_f32_16x16x32_bf16 v[132:135], a[0:3], v[36:39], 0\n"
  "ds_read_b128 v[52:55], v21 offset:0\n"
  "buffer_load_dwordx4 v19, %[srdC], s64 offen lds\n"
  "v_mfma_f32_16x16x32_bf16 v[136:139], a[0:3], v[40:43], 0\n"
  "v_mfma_f32_16x16x32_bf16 v[140:143], a[0:3], v[44:47], 0\n"
  "ds_read_b128 v[56:59], v21 offset:4096\n"
  "v_mfma_f32_16x16x32_bf16 v[144:147], a[0:3], v[48:51], 0\n"
  "v_mfma_f32_16x16x32_bf16 v[148:151], a[4:7], v[36:39], 0\n"
  "ds_read_b128 v[60:63], v21 offset:8192\n"
  "v_mfma_f32_16x16x32_bf16 v[152:155], a[4:7], v[40:43], 0\n"
  "v_mfma_f32_16x16x32_bf16 v[156:159], a[4:7], v[44:47], 0\n"
  "ds_read_b128 v[64:67], v21 offset:12288\n"
  "v_mfma_f32_16x16x32_bf16 v[160:163], a[4:7], v[48:51], 0\n"
  "v_mfma_f32_16x16x32_bf16 v[164:167], a[8:11], v[36:39], 0\n"
  "v_mfma_f32_16x16x32_bf16 v[168:171], a[8:11], v[40:43], 0\n"
  "v_mfma_f32_16x16x32_bf16 v[172:175], a[8:11], v[44:47], 0\n"
  "v_mfma_f32_16x16x32_bf16 v[176:179], a[8:11], v[48:51], 0\n"
  "v_mfma_f32_16x16x32_bf16 v[180:183], a[12:15], v[36:39], 0\n"
  "v_cvt_pk_bf16_f32 v68, v68, v69\n"
  "v_cvt_pk_bf16_f32 v69, v70, v71\n"
  "v_mfma_f32_16x16x32_bf16 v[184:187], a[12:15], v[40:43], 0\n"
  "v_cvt_pk_bf16_f32 v70, v84, v85\n"
  "v_cvt_pk_bf16_f32 v71, v86, v87\n"
  "v_mfma_f32_16x16x32_bf16 v[188:191], a[12:15], v[44:47], 0\n"
  "s_add_u32 s66, s66, s69\n"
  "buffer_store_dwordx4 v[68:71], %[L16], %[srdWS], s66 offen sc1\n"
  "v_mfma_f32_16x16x32_bf16 v[192:195], a[12:15], v[48:51], 0\n"
  "s_waitcnt lgkmcnt(0)\n"
  "v_mfma_f32_16x16x32_bf16 v[132:135], a[16:19], v[52:55], v[132:135]\n"
  "ds_read_b128 v[36:39], v22 offset:0\n"
  "v_mfma_f32_16x16x32_bf16 v[136:139], a[16:19], v[56:59], v[136:139]\n"
  "s_add_u32 s96, %[rot], 0\n"
  "s_and_b32 s96, s96, 3\n"
  "s_lshl_b32 s96, s96, 10\n"
  "s_add_u32 s96, s96, %[oX]\n"
  "s_add_u32 s97, s96, 0x4000\n"
  "s_add_u32 s98, s96, 0x8000\n"
  "s_add_u32 s99, s96, 0xc000\n"
  "buffer_load_dwordx4 a[0:3], %[voffX], %[srdWS], s96 offen offset:0 sc1\n"
  "v_mfma_f32_16x16x32_bf16 v[140:143], a[16:19], v[60:63], v[140:143]\n"
  "ds_read_b128 v[40:43], v22 offset:4096\n"
  "v_mfma_f32_16x16x32_bf16 v[144:147], a[16:19], v[64:67], v[144:147]\n"
  "v_mfma_f32_16x16x32_bf16 v[148:151], a[20:23], v[52:55], v[148:151]\n"
  "ds_read_b128 v[44:47], v22 offset:8192\n"
  "v_mfma_f32_16x16x32_bf16 v[152:155], a[20:23], v[56:59], v[152:155]\n"
  "v_mfma_f32_16x16x32_bf16 v[156:159], a[20:23], v[60:63], v[156:159]\n"
  "ds_read_b128 v[48:51], v22 offset:12288\n"
  "v_mfma_f32_16x16x32_bf16 v[160:163], a[20:23], v[64:67], v[160:163]\n"
  "v_mfma_f32_16x16x32_bf16 v[164:167], a[24:27], v[52:55], v[164:167]\n"
  "v_mfma_f32_16x16x32_bf16 v[168:171], a[24:27], v[56:59], v[168:171]\n"
  "v_mfma_f32_16x16x32_bf16 v[172:175], a[24:27], v[60:63], v[172:175]\n"
  "v_mfma_f32_16x16x32_bf16 v[176:179], a[24:27], v[64:67], v[176:179]\n"
  "v_mfma_f32_16x16x32_bf16 v[180:183], a[28:31], v[52:55], v[180:183]\n"
  "v_mfma_f32_16x16x32_bf16 v[184:187], a[28:31], v[56:59], v[184:187]\n"
  "v_mfma_f32_16x16x32_bf16 v[188:191], a[28:31], v[60:63], v[188:191]\n"
  "v_mfma_f32_16x16x32_bf16 v[192:195], a[28:31], v[64:67], v[192:195]\n"
  "s_waitcnt lgkmcnt(0)\n"
  "v_mfma_f32_16x16x32_bf16 v[132:135], a[32:35], v[36:39], v[132:135]\n"
  "ds_read_b128 v[52:55], v23 offset:0\n"
  "v_mfma_f32_16x16x32_bf16 v[136:139], a[32:35], v[40:43], v[136:139]\n"
  "buffer_load_dwordx4 a[4:7], %[voffX], %[srdWS], s97 offen offset:0 sc1\n"
  "v_mfma_f32_16x16x32_bf16 v[140:143], a[32:35], v[44:47], v[140:143]\n"
  "ds_read_b128 v[56:59], v23 offset:4096\n"
  "v_mfma_f32_16x16x32_bf16 v[144:147], a[32:35], v[48:51], v[144:147]\n"
  "v_mfma_f32_16x16x32_bf16 v[148:151], a[36:39], v[36:39], v[148:151]\n"
  "ds_read_b128 v[60:63], v23 offset:8192\n"
  "v_mfma_f32_16x16x32_bf16 v[152:155], a[36:39], v[40:43], v[152:155]\n"
  "v_mfma_f32_16x16x32_bf16 v[156:159], a[36:39], v[44:47], v[156:159]\n"
  "ds_read_b128 v[64:67], v23 offset:12288\n"
  "v_mfma_f32_16x16x32_bf16 v[160:163], a[36:39], v[48:51], v[160:163]\n"
  "v_mfma_f32_16x16x32_bf16 v[164:167], a[40:43], v[36:39], v[164:167]\n"
  "v_cvt_pk_bf16_f32 v72, v72, v73\n"
  "v_cvt_pk_bf16_f32 v73, v74, v75\n"
  "v_mfma_f32_16x16x32_bf16 v[168:171], a[40:43], v[40:43], v[168:171]\n"
  "v_cvt_pk_bf16_f32 v74, v88, v89\n"
  "v_cvt_pk_bf16_f32 v75, v90, v91\n"
  "v_mfma_f32_16x16x32_bf16 v[172:175], a[40:43], v[44:47], v[172:175]\n"
  "s_add_u32 s66, s66, s67\n"
  "buffer_store_dwordx4 v[72:75], %[L16], %[srdWS], s66 offen sc1\n"
  "v_mfma_f32_16x16x32_bf16 v[176:179], a[40:43], v[48:51], v[176:179]\n"
  "v_mfma_f32_16x16x32_bf16 v[180:183], a[44:47], v[36:39], v[180:183]\n"
  "v_mfma_f32_16x16x32_bf16 v[184:187], a[44:47], v[40:43], v[184:187]\n"
  "v_mfma_f32_16x16x32_bf16 v[188:191], a[44:47], v[44:47], v[188:191]\n"
  "v_mfma_f32_16x16x32_bf16 v[192:195], a[44:47], v[48:51], v[192:195]\n"
  "s_waitcnt vmcnt(21) lgkmcnt(0)\n"
  "s_barrier\n"
  "v_mfma_f32_16x16x32_bf16 v[132:135], a[48:51], v[52:55], v[132:135]\n"
  "ds_read_b128 v[36:39], v20 offset:16384\n"
  "v_mfma_f32_16x16x32_bf16 v[136:139], a[48:51], v[56:59], v[136:139]\n"
  "buffer_load_dwordx4 a[8:11], %[voffX], %[srdWS], s98 offen offset:0 sc1\n"
  "v_mfma_f32_16x16x32_bf16 v[140:143], a[48:51], v[60:63], v[140:143]\n"
  "ds_read_b128 v[40:43], v20 offset:20480\n"
  "v_mfma_f32_16x16x32_bf16 v[144:147], a[48:51], v[64:67], v[144:147]\n"
  "v_mfma_f32_16x16x32_bf16 v[148:151], a[52:55], v[52:55], v[148:151]\n"
  "ds_read_b128 v[44:47], v20 offset:24576\n"
  "v_mfma_f32_16x16x32_bf16 v[152:155], a[52:55], v[56:59], v[152:155]\n"
  "v_mfma_f32_16x16x32_bf16 v[156:159], a[52:55], v[60:63], v[156:159]\n"
  "ds_read_b128 v[48:51], v20 offset:28672\n"
  "v_mfma_f32_16x16x32_bf16 v[160:163], a[52:55], v[64:67], v[160:163]\n"
  "v_mfma_f32_16x16x32_bf16 v[164:167], a[56:59], v[52:55], v[164:167]\n"
  "v_mfma_f32_16x16x32_bf16 v[168:171], a[56:59], v[56:59], v[168:171]\n"
  "v_mfma_f32_16x16x32_bf16 v[172:175], a[56:59], v[60:63], v[172:175]\n"
  "v_mfma_f32_16x16x32_bf16 v[176:179], a[56:59], v[64:67], v[176:179]\n"
  "v_mfma_f32_16x16x32_bf16 v[180:183], a[60:63], v[52:55], v[180:183]\n"
  "v_mfma_f32_16x16x32_bf16 v[184:187], a[60:63], v[56:59], v[184:187]\n"
  "v_mfma_f32_16x16x32_bf16 v[188:191], a[60:63], v[60:63], v[188:191]\n"
  "v_mfma_f32_16x16x32_bf16 v[192:195], a[60:63], v[64:67], v[192:195]\n"
  "s_waitcnt lgkmcnt(0)\n"
  "v_mfma_f32_16x16x32_bf16 v[132:135], a[64:67], v[36:39], v[132:135]\n"
  "ds_read_b128 v[52:55], v21 offset:16384\n"
  "v_mfma_f32_16x16x32_bf16 v[136:139], a[64:67], v[40:43], v[136:139]\n"
  "buffer_load_dwordx4 a[12:15], %[voffX], %[srdWS], s99 offen offset:0 sc1\n"
  "v_mfma_f32_16x16x32_bf16 v[140:143], a[64:67], v[44:47], v[140:143]\n"
  "ds_read_b128 v[56:59], v21 offset:20480\n"
  "v_mfma_f32_16x16x32_bf16 v[144:147], a[64:67], v[48:51], v[144:147]\n"
  "v_mfma_f32_16x16x32_bf16 v[148:151], a[68:71], v[36:39], v[148:151]\n"
  "ds_read_b128 v[60:63], v21 offset:24576\n"
  "v_cvt_pk_bf16_f32 v76, v76, v77\n"
  "v_mfma_f32_16x16x32_bf16 v[152:155], a[68:71], v[40:43], v[152:155]\n"
  "v_cvt_pk_bf16_f32 v77, v78, v79\n"
  "v_cvt_pk_bf16_f32 v78, v92, v93\n"
  "v_mfma_f32_16x16x32_bf16 v[156:159], a[68:71], v[44:47], v[156:159]\n"
  "ds_read_b128 v[64:67], v21 offset:28672\n"
  "v_cvt_pk_bf16_f32 v79, v94, v95\n"
  "v_mfma_f32_16x16x32_bf16 v[160:163], a[68:71], v[48:51], v[160:163]\n"
  "s_add_u32 s66, s66, s67\n"
  "buffer_store_dwordx4 v[76:79], %[L16], %[srdWS], s66 offen sc1\n"
  "v_mfma_f32_16x16x32_bf16 v[164:167], a[72:75], v[36:39], v[164:167]\n"
  "v_mfma_f32_16x16x32_bf16 v[168:171], a[72:75], v[40:43], v[168:171]\n"
  "v_mfma_f32_16x16x32_bf16 v[172:175], a[72:75], v[44:47], v[172:175]\n"
  "v_mfma_f32_16x16x32_bf16 v[176:179], a[72:75], v[48:51], v[176:179]\n"
  "v_mfma_f32_16x16x32_bf16 v[180:183], a[76:79], v[36:39], v[180:183]\n"
  "v_mfma_f32_16x16x32_bf16 v[184:187], a[76:79], v[40:43], v[184:187]\n"
  "v_mfma_f32_16x16x32_bf16 v[188:191], a[76:79], v[44:47], v[188:191]\n"
  "v_mfma_f32_16x16x32_bf16 v[192:195], a[76:79], v[48:51], v[192:195]\n"
  "s_waitcnt lgkmcnt(0)\n"
  "v_mfma_f32_16x16x32_bf16 v[132:135], a[80:83], v[52:55], v[132:135]\n"
  "ds_read_b128 v[36:39], v22 offset:16384\n"
  "v_mfma_f32_16x16x32_bf16 v[136:139], a[80:83], v[56:59], v[136:139]\n"
  "buffer_load_dwordx4 a[16:19], %[voffX], %[srdWS], s96 offen offset:256 sc1\n"
  "v_mfma_f32_16x16x32_bf16 v[140:143], a[80:83], v[60:63], v[140:143]\n"
  "ds_read_b128 v[40:43], v22 offset:20480\n"
  "v_mfma_f32_16x16x32_bf16 v[144:147], a[80:83], v[64:67], v[144:147]\n"
  "s_add_u32 s92, %[rotU], 0\n"
  "s_and_b32 s92, s92, 3\n"
  "s_lshl_b32 s92, s92, 10\n"
  "v_mfma_f32_16x16x32_bf16 v[148:151], a[84:87], v[52:55], v[148:151]\n"
  "ds_read_b128 v[44:47], v22 offset:24576\n"
  "s_add_u32 s92, s92, %[oY]\n"
  "v_mfma_f32_16x16x32_bf16 v[152:155], a[84:87], v[56:59], v[152:155]\n"
  "s_add_u32 s93, s92, 0x4000\n"
  "v_mfma_f32_16x16x32_bf16 v[156:159], a[84:87], v[60:63], v[156:159]\n"
  "ds_read_b128 v[48:51], v22 offset:28672\n"
  "buffer_load_dwordx4 a[64:67], %[voffX], %[srdWS], s92 offen offset:0 sc1\n"
  "v_mfma_f32_16x16x32_bf16 v[160:163], a[84:87], v[64:67], v[160:163]\n"
  "v_mfma_f32_16x16x32_bf16 v[164:167], a[88:91], v[52:55], v[164:167]\n"
  "v_mfma_f32_16x16x32_bf16 v[168:171], a[88:91], v[56:59], v[168:171]\n"
  "v_mfma_f32_16x16x32_bf16 v[172:175], a[88:91], v[60:63], v[172:175]\n"
  "buffer_load_dwordx4 a[68:71], %[voffX], %[srdWS], s93 offen offset:0 sc1\n"
  "v_mfma_f32_16x16x32_bf16 v[176:179], a[88:91], v[64:67], v[176:179]\n"
  "v_mfma_f32_16x16x32_bf16 v[180:183], a[92:95], v[52:55], v[180:183]\n"
  "v_mfma_f32_16x16x32_bf16 v[184:187], a[92:95], v[56:59], v[184:187]\n"
  "v_mfma_f32_16x16x32_bf16 v[188:191], a[92:95], v[60:63], v[188:191]\n"
  "v_mfma_f32_16x16x32_bf16 v[192:195], a[92:95], v[64:67], v[192:195]\n"
  "buffer_load_dwordx4 a[72:75], %[voffX], %[srdWS], s92 offen offset:256 sc1\n"
  "s_waitcnt lgkmcnt(0)\n"
  "v_mfma_f32_16x16x32_bf16 v[132:135], a[96:99], v[36:39], v[132:135]\n"
  "ds_read_b128 v[52:55], v23 offset:16384\n"
  "v_cvt_pk_bf16_f32 v80, v80, v81\n"
  "v_mfma_f32_16x16x32_bf16 v[136:139], a[96:99], v[40:43], v[136:139]\n"
  "v_cvt_pk_bf16_f32 v81, v82, v83\n"
  "v_cvt_pk_bf16_f32 v82, v96, v97\n"
  "v_mfma_f32_16x16x32_bf16 v[140:143], a[96:99], v[44:47], v[140:143]\n"
  "ds_read_b128 v[56:59], v23 offset:20480\n"
  "v_cvt_pk_bf16_f32 v83, v98, v99\n"
  "v_mfma_f32_16x16x32_bf16 v[144:147], a[96:99], v[48:51], v[144:147]\n"
  "buffer_load_dwordx4 a[20:23], %[voffX], %[srdWS], s97 offen offset:256 sc1\n"
  "s_add_u32 s66, s66, s67\n"
  "v_mfma_f32_16x16x32_bf16 v[148:151], a[100:103], v[36:39], v[148:151]\n"
  "ds_read_b128 v[60:63], v23 offset:24576\n"
  "buffer_store_dwordx4 v[80:83], %[L16], %[srdWS], s66 offen sc1\n"
  "v_mfma_f32_16x16x32_bf16 v[152:155], a[100:103], v[40:43], v[152:155]\n"
  "v_mfma_f32_16x16x32_bf16 v[156:159], a[100:103], v[44:47], v[156:159]\n"
  "ds_read_b128 v[64:67], v23 offset:28672\n"
  "v_mfma_f32_16x16x32_bf16 v[160:163], a[100:103], v[48:51], v[160:163]\n"
  "v_mfma_f32_16x16x32_bf16 v[164:167], a[104:107], v[36:39], v[164:167]\n"
  "buffer_load_dwordx4 a[76:79], %[voffX], %[srdWS], s93 offen offset:256 sc1\n"
  "v_mfma_f32_16x16x32_bf16 v[168:171], a[104:107], v[40:43], v[168:171]\n"
  "v_mfma_f32_16x16x32_bf16 v[172:175], a[104:107], v[44:47], v[172:175]\n"
  "v_mfma_f32_16x16x32_bf16 v[176:179], a[104:107], v[48:51], v[176:179]\n"
  "v_mfma_f32_16x16x32_bf16 v[180:183], a[108:111], v[36:39], v[180:183]\n"
  "v_mfma_f32_16x16x32_bf16 v[184:187], a[108:111], v[40:43], v[184:187]\n"
  "buffer_load_dwordx4 a[80:83], %[voffX], %[srdWS], s92 offen offset:512 sc1\n"
  "v_mfma_f32_16x16x32_bf16 v[188:191], a[108:111], v[44:47], v[188:191]\n"
  "v_mfma_f32_16x16x32_bf16 v[192:195], a[108:111], v[48:51], v[192:195]\n"
  "s_waitcnt vmcnt(22) lgkmcnt(0)\n"
  "s_barrier\n"
  "v_mfma_f32_16x16x32_bf16 v[132:135], a[112:115], v[52:55], v[132:135]\n"
  "ds_read_b128 v[36:39], v20 offset:32768\n"
  "v_mfma_f32_16x16x32_bf16 v[136:139], a[112:115], v[56:59], v[136:139]\n"
  "buffer_load_dwordx4 a[24:27], %[voffX], %[srdWS], s98 offen offset:256 sc1\n"
  "v_mfma_f32_16x16x32_bf16 v[140:143], a[112:115], v[60:63], v[140:143]\n"
  "ds_read_b128 v[40:43], v20 offset:36864\n"
  "v_mfma_f32_16x16x32_bf16 v[144:147], a[112:115], v[64:67], v[144:147]\n"
  "v_mfma_f32_16x16x32_bf16 v[148:151], a[116:119], v[52:55], v[148:151]\n"
  "ds_read_b128 v[44:47], v20 offset:40960\n"
  "v_mfma_f32_16x16x32_bf16 v[152:155], a[116:119], v[56:59], v[152:155]\n"
  "v_mfma_f32_16x16x32_bf16 v[156:159], a[116:119], v[60:63], v[156:159]\n"
  "ds_read_b128 v[48:51], v20 offset:45056\n"
  "buffer_load_dwordx4 a[84:87], %[voffX], %[srdWS], s93 offen offset:512 sc1\n"
  "v_mfma_f32_16x16x32_bf16 v[160:163], a[116:119], v[64:67], v[160:163]\n"
  "v_mfma_f32_16x16x32_bf16 v[164:167], a[120:123], v[52:55], v[164:167]\n"
  "v_mfma_f32_16x16x32_bf16 v[168:171], a[120:123], v[56:59], v[168:171]\n"
  "v_mfma_f32_16x16x32_bf16 v[172:175], a[120:123], v[60:63], v[172:175]\n"
  "buffer_load_dwordx4 a[88:91], %[voffX], %[srdWS], s92 offen offset:768 sc1\n"
  "v_mfma_f32_16x16x32_bf16 v[176:179], a[120:123], v[64:67], v[176:179]\n"
  "v_mfma_f32_16x16x32_bf16 v[180:183], a[124:127], v[52:55], v[180:183]\n"
  "v_cvt_pk_bf16_f32 v100, v100, v101\n"
  "v_cvt_pk_bf16_f32 v101, v102, v103\n"
  "v_mfma_f32_16x16x32_bf16 v[184:187], a[124:127], v[56:59], v[184:187]\n"
  "v_cvt_pk_bf16_f32 v102, v116, v117\n"
  "v_cvt_pk_bf16_f32 v103, v118, v119\n"
  "v_mfma_f32_16x16x32_bf16 v[188:191], a[124:127], v[60:63], v[188:191]\n"
  "s_add_u32 s66, s66, s68\n"
  "buffer_store_dwordx4 v[100:103], %[L16], %[srdWS], s66 offen sc1\n"
  "v_mfma_f32_16x16x32_bf16 v[192:195], a[124:127], v[64:67], v[192:195]\n"
  "buffer_load_dwordx4 a[92:95], %[voffX], %[srdWS], s93 offen offset:768 sc1\n"
  "s_waitcnt lgkmcnt(0)\n"
  "v_mfma_f32_16x16x32_bf16 v[132:135], a[128:131], v[36:39], v[132:135]\n"
  "ds_read_b128 v[52:55], v21 offset:32768\n"
  "v_mfma_f32_16x16x32_bf16 v[136:139], a[128:131], v[40:43], v[136:139]\n"
  "v_mfma_f32_16x16x32_bf16 v[140:143], a[128:131], v[44:47], v[140:143]\n"
  "ds_read_b128 v[56:59], v21 offset:36864\n"
  "v_mfma_f32_16x16x32_bf16 v[144:147], a[128:131], v[48:51], v[144:147]\n"
  "v_mfma_f32_16x16x32_bf16 v[148:151], a[132:135], v[36:39], v[148:151]\n"
  "ds_read_b128 v[60:63], v21 offset:40960\n"
  "v_lshrrev_b32 v31, 1, %[TRB]\n"
  "buffer_load_dwordx4 a[28:31], %[voffX], %[srdWS], s99 offen offset:256 sc1\n"
  "v_mfma_f32_16x16x32_bf16 v[152:155], a[132:135], v[40:43], v[152:155]\n"
  "s_add_u32 s92, %[rotU], 1\n"
  "s_and_b32 s92, s92, 3\n"
  "s_lshl_b32 s92, s92, 10\n"
  "v_mfma_f32_16x16x32_bf16 v[156:159], a[132:135], v[44:47], v[156:159]\n"
  "ds_read_b128 v[64:67], v21 offset:45056\n"
  "v_and_b32 v31, 0x200, v31\n"
  "s_add_u32 s92, s92, %[oY]\n"
  "v_mfma_f32_16x16x32_bf16 v[160:163], a[132:135], v[48:51], v[160:163]\n"
  "s_add_u32 s93, s92, 0x4000\n"
  "v_mfma_f32_16x16x32_bf16 v[164:167], a[136:139], v[36:39], v[164:167]\n"
  "v_sub_u32 v30, %[TRB], v31\n"
  "buffer_load_dwordx4 a[96:99], %[voffX], %[srdWS], s92 offen offset:0 sc1\n"
  "v_mfma_f32_16x16x32_bf16 v[168:171], a[136:139], v[40:43], v[168:171]\n"
  "v_mfma_f32_16x16x32_bf16 v[172:175], a[136:139], v[44:47], v[172:175]\n"
  "v_add_u32 v30, 0x10000, v30\n"
  "v_mfma_f32_16x16x32_bf16 v[176:179], a[136:139], v[48:51], v[176:179]\n"
  "v_mfma_f32_16x16x32_bf16 v[180:183], a[140:143], v[36:39], v[180:183]\n"
  "v_xor_b32 v31, 32, v30\n"
  "v_mfma_f32_16x16x32_bf16 v[184:187], a[140:143], v[40:43], v[184:187]\n"
  "buffer_load_dwordx4 a[100:103], %[voffX], %[srdWS], s93 offen offset:0 sc1\n"
  "v_mfma_f32_16x16x32_bf16 v[188:191], a[140:143], v[44:47], v[188:191]\n"
  "v_add_u32 v32, 0xc000, v30\n"
  "v_mfma_f32_16x16x32_bf16 v[192:195], a[140:143], v[48:51], v[192:195]\n"
  "s_waitcnt lgkmcnt(0)\n"
  "v_mfma_f32_16x16x32_bf16 v[132:135], a[144:147], v[52:55], v[132:135]\n"
  "ds_read_b128 v[36:39], v22 offset:32768\n"
  "v_xor_b32 v33, 32, v32\n"
  "v_mfma_f32_16x16x32_bf16 v[136:139], a[144:147], v[56:59], v[136:139]\n"
  "buffer_load_dwordx4 a[32:35], %[voffX], %[srdWS], s96 offen offset:512 sc1\n"
  "v_mfma_f32_16x16x32_bf16 v[140:143], a[144:147], v[60:63], v[140:143]\n"
  "ds_read_b128 v[40:43], v22 offset:36864\n"
  "v_mfma_f32_16x16x32_bf16 v[144:147], a[144:147], v[64:67], v[144:147]\n"
  "v_mfma_f32_16x16x32_bf16 v[148:151], a[148:151], v[52:55], v[148:151]\n"
  "ds_read_b128 v[44:47], v22 offset:40960\n"
  "v_mfma_f32_16x16x32_bf16 v[152:155], a[148:151], v[56:59], v[152:155]\n"
  "v_mfma_f32_16x16x32_bf16 v[156:159], a[148:151], v[60:63], v[156:159]\n"
  "ds_read_b128 v[48:51], v22 offset:45056\n"
  "buffer_load_dwordx4 a[104:107], %[voffX], %[srdWS], s92 offen offset:256 sc1\n"
  "v_mfma_f32_16x16x32_bf16 v[160:163], a[148:151], v[64:67], v[160:163]\n"
  "v_mfma_f32_16x16x32_bf16 v[164:167], a[152:155], v[52:55], v[164:167]\n"
  "v_cvt_pk_bf16_f32 v104, v104, v105\n"
  "v_cvt_pk_bf16_f32 v105, v106, v107\n"
  "v_mfma_f32_16x16x32_bf16 v[168:171], a[152:155], v[56:59], v[168:171]\n"
  "v_cvt_pk_bf16_f32 v106, v120, v121\n"
  "v_cvt_pk_bf16_f32 v107, v122, v123\n"
  "v_mfma_f32_16x16x32_bf16 v[172:175], a[152:155], v[60:63], v[172:175]\n"
  "s_add_u32 s66, s66, s67\n"
  "buffer_store_dwordx4 v[104:107], %[L16], %[srdWS], s66 offen sc1\n"
  "v_mfma_f32_16x16x32_bf16 v[176:179], a[152:155], v[64:67], v[176:179]\n"
  "buffer_load_dwordx4 a[108:111], %[voffX], %[srdWS], s93 offen offset:256 sc1\n"
  "v_mfma_f32_16x16x32_bf16 v[180:183], a[156:159], v[52:55], v[180:183]\n"
  "v_mfma_f32_16x16x32_bf16 v[184:187], a[156:159], v[56:59], v[184:187]\n"
  "v_mfma_f32_16x16x32_bf16 v[188:191], a[156:159], v[60:63], v[188:191]\n"
  "v_mfma_f32_16x16x32_bf16 v[192:195], a[156:159], v[64:67], v[192:195]\n"
  "buffer_load_dwordx4 a[112:115], %[voffX], %[srdWS], s92 offen offset:512 sc1\n"
  "s_waitcnt lgkmcnt(0)\n"
  "v_mfma_f32_16x16x32_bf16 v[132:135], a[160:163], v[36:39], v[132:135]\n"
  "ds_read_b128 v[52:55], v23 offset:32768\n"
  "v_mfma_f32_16x16x32_bf16 v[136:139], a[160:163], v[40:43], v[136:139]\n"
  "v_mfma_f32_16x16x32_bf16 v[140:143], a[160:163], v[44:47], v[140:143]\n"
  "ds_read_b128 v[56:59], v23 offset:36864\n"
  "v_mfma_f32_16x16x32_bf16 v[144:147], a[160:163], v[48:51], v[144:147]\n"
  "v_mfma_f32_16x16x32_bf16 v[148:151], a[164:167], v[36:39], v[148:151]\n"
  "ds_read_b128 v[60:63], v23 offset:40960\n"
  "buffer_load_dwordx4 a[36:39], %[voffX], %[srdWS], s97 offen offset:512 sc1\n"
  "v_mfma_f32_16x16x32_bf16 v[152:155], a[164:167], v[40:43], v[152:155]\n"
  "v_mfma_f32_16x16x32_bf16 v[156:159], a[164:167], v[44:47], v[156:159]\n"
  "ds_read_b128 v[64:67], v23 offset:45056\n"
  "v_mfma_f32_16x16x32_bf16 v[160:163], a[164:167], v[48:51], v[160:163]\n"
  "v_mfma_f32_16x16x32_bf16 v[164:167], a[168:171], v[36:39], v[164:167]\n"
  "buffer_load_dwordx4 a[116:119], %[voffX], %[srdWS], s93 offen offset:512 sc1\n"
  "v_mfma_f32_16x16x32_bf16 v[168:171], a[168:171], v[40:43], v[168:171]\n"
  "v_mfma_f32_16x16x32_bf16 v[172:175], a[168:171], v[44:47], v[172:175]\n"
  "v_mfma_f32_16x16x32_bf16 v[176:179], a[168:171], v[48:51], v[176:179]\n"
  "v_mfma_f32_16x16x32_bf16 v[180:183], a[172:175], v[36:39], v[180:183]\n"
  "v_mfma_f32_16x16x32_bf16 v[184:187], a[172:175], v[40:43], v[184:187]\n"
  "buffer_load_dwordx4 a[120:123], %[voffX], %[srdWS], s92 offen offset:768 sc1\n"
  "v_mfma_f32_16x16x32_bf16 v[188:191], a[172:175], v[44:47], v[188:191]\n"
  "v_mfma_f32_16x16x32_bf16 v[192:195], a[172:175], v[48:51], v[192:195]\n"
  "s_waitcnt vmcnt(31) lgkmcnt(0)\n"
  "s_barrier\n"
  "v_mfma_f32_16x16x32_bf16 v[132:135], a[176:179], v[52:55], v[132:135]\n"
  "ds_read_b128 v[36:39], v20 offset:49152\n"
  "v_mfma_f32_16x16x32_bf16 v[136:139], a[176:179], v[56:59], v[136:139]\n"
  "v_mfma_f32_16x16x32_bf16 v[140:143], a[176:179], v[60:63], v[140:143]\n"
  "ds_read_b128 v[40:43], v20 offset:53248\n"
  "buffer_load_dwordx4 a[40:43], %[voffX], %[srdWS], s98 offen offset:512 sc1\n"
  "v_mfma_f32_16x16x32_bf16 v[144:147], a[176:179], v[64:67], v[144:147]\n"
  "v_mfma_f32_16x16x32_bf16 v[148:151], a[180:183], v[52:55], v[148:151]\n"
  "ds_read_b128 v[44:47], v20 offset:57344\n"
  "v_cvt_pk_bf16_f32 v108, v108, v109\n"
  "v_mfma_f32_16x16x32_bf16 v[152:155], a[180:183], v[56:59], v[152:155]\n"
  "v_cvt_pk_bf16_f32 v109, v110, v111\n"
  "v_cvt_pk_bf16_f32 v110, v124, v125\n"
  "v_mfma_f32_16x16x32_bf16 v[156:159], a[180:183], v[60:63], v[156:159]\n"
  "ds_read_b128 v[48:51], v20 offset:61440\n"
  "buffer_load_dwordx4 a[124:127], %[voffX], %[srdWS], s93 offen offset:768 sc1\n"
  "v_mfma_f32_16x16x32_bf16 v[160:163], a[180:183], v[64:67], v[160:163]\n"
  "v_cvt_pk_bf16_f32 v111, v126, v127\n"
  "s_add_u32 s66, s66, s67\n"
  "v_mfma_f32_16x16x32_bf16 v[164:167], a[184:187], v[52:55], v[164:167]\n"
  "s_add_u32 s92, %[rotU], 2\n"
  "s_and_b32 s92, s92, 3\n"
  "s_lshl_b32 s92, s92, 10\n"
  "v_mfma_f32_16x16x32_bf16 v[168:171], a[184:187], v[56:59], v[168:171]\n"
  "s_add_u32 s92, s92, %[oY]\n"
  "buffer_store_dwordx4 v[108:111], %[L16], %[srdWS], s66 offen sc1\n"
  "v_mfma_f32_16x16x32_bf16 v[172:175], a[184:187], v[60:63], v[172:175]\n"
  "s_add_u32 s93, s92, 0x4000\n"
  "v_mfma_f32_16x16x32_bf16 v[176:179], a[184:187], v[64:67], v[176:179]\n"
  "buffer_load_dwordx4 a[128:131], %[voffX], %[srdWS], s92 offen offset:0 sc1\n"
  "v_mfma_f32_16x16x32_bf16 v[180:183], a[188:191], v[52:55], v[180:183]\n"
  "v_mfma_f32_16x16x32_bf16 v[184:187], a[188:191], v[56:59], v[184:187]\n"
  "v_mfma_f32_16x16x32_bf16 v[188:191], a[188:191], v[60:63], v[188:191]\n"
  "v_mfma_f32_16x16x32_bf16 v[192:195], a[188:191], v[64:67], v[192:195]\n"
  "buffer_load_dwordx4 a[132:135], %[voffX], %[srdWS], s93 offen offset:0 sc1\n"
  "s_waitcnt lgkmcnt(0)\n"
  "v_mfma_f32_16x16x32_bf16 v[132:135], a[192:195], v[36:39], v[132:135]\n"
  "ds_read_b128 v[52:55], v21 offset:49152\n"
  "v_mfma_f32_16x16x32_bf16 v[136:139], a[192:195], v[40:43], v[136:139]\n"
  "v_mfma_f32_16x16x32_bf16 v[140:143], a[192:195], v[44:47], v[140:143]\n"
  "ds_read_b128 v[56:59], v21 offset:53248\n"
  "v_mfma_f32_16x16x32_bf16 v[144:147], a[192:195], v[48:51], v[144:147]\n"
  "v_mfma_f32_16x16x32_bf16 v[148:151], a[196:199], v[36:39], v[148:151]\n"
  "ds_read_b128 v[60:63], v21 offset:57344\n"
  "buffer_load_dwordx4 a[44:47], %[voffX], %[srdWS], s99 offen offset:512 sc1\n"
  "v_mfma_f32_16x16x32_bf16 v[152:155], a[196:199], v[40:43], v[152:155]\n"
  "v_mfma_f32_16x16x32_bf16 v[156:159], a[196:199], v[44:47], v[156:159]\n"
  "ds_read_b128 v[64:67], v21 offset:61440\n"
  "v_mfma_f32_16x16x32_bf16 v[160:163], a[196:199], v[48:51], v[160:163]\n"
  "v_mfma_f32_16x16x32_bf16 v[164:167], a[200:203], v[36:39], v[164:167]\n"
  "v_mfma_f32_16x16x32_bf16 v[168:171], a[200:203], v[40:43], v[168:171]\n"
  "buffer_load_dwordx4 a[136:139], %[voffX], %[srdWS], s92 offen offset:256 sc1\n"
  "v_mfma_f32_16x16x32_bf16 v[172:175], a[200:203], v[44:47], v[172:175]\n"
  "v_mfma_f32_16x16x32_bf16 v[176:179], a[200:203], v[48:51], v[176:179]\n"
  "v_mfma_f32_16x16x32_bf16 v[180:183], a[204:207], v[36:39], v[180:183]\n"
  "v_mfma_f32_16x16x32_bf16 v[184:187], a[204:207], v[40:43], v[184:187]\n"
  "buffer_load_dwordx4 a[140:143], %[voffX], %[srdWS], s93 offen offset:256 sc1\n"
  "v_mfma_f32_16x16x32_bf16 v[188:191], a[204:207], v[44:47], v[188:191]\n"
  "v_mfma_f32_16x16x32_bf16 v[192:195], a[204:207], v[48:51], v[192:195]\n"
  "s_waitcnt lgkmcnt(0)\n"
  "v_mfma_f32_16x16x32_bf16 v[132:135], a[208:211], v[52:55], v[132:135]\n"
  "ds_read_b128 v[36:39], v22 offset:49152\n"
  "v_cvt_pk_bf16_f32 v112, v112, v113\n"
  "v_mfma_f32_16x16x32_bf16 v[136:139], a[208:211], v[56:59], v[136:139]\n"
  "v_cvt_pk_bf16_f32 v113, v114, v115\n"
  "v_cvt_pk_bf16_f32 v114, v128, v129\n"
  "v_mfma_f32_16x16x32_bf16 v[140:143], a[208:211], v[60:63], v[140:143]\n"
  "ds_read_b128 v[40:43], v22 offset:53248\n"
  "buffer_load_dwordx4 a[48:51], %[voffX], %[srdWS], s96 offen offset:768 sc1\n"
  "v_mfma_f32_16x16x32_bf16 v[144:147], a[208:211], v[64:67], v[144:147]\n"
  "v_cvt_pk_bf16_f32 v115, v130, v131\n"
  "s_add_u32 s66, s66, s67\n"
  "v_mfma_f32_16x16x32_bf16 v[148:151], a[212:215], v[52:55], v[148:151]\n"
  "ds_read_b128 v[44:47], v22 offset:57344\n"
  "buffer_store_dwordx4 v[112:115], %[L16], %[srdWS], s66 offen sc1\n"
  "v_mfma_f32_16x16x32_bf16 v[152:155], a[212:215], v[56:59], v[152:155]\n"
  "v_mfma_f32_16x16x32_bf16 v[156:159], a[212:215], v[60:63], v[156:159]\n"
  "ds_read_b128 v[48:51], v22 offset:61440\n"
  "buffer_load_dwordx4 a[144:147], %[voffX], %[srdWS], s92 offen offset:512 sc1\n"
  "v_mfma_f32_16x16x32_bf16 v[160:163], a[212:215], v[64:67], v[160:163]\n"
  "v_mfma_f32_16x16x32_bf16 v[164:167], a[216:219], v[52:55], v[164:167]\n"
  "v_mfma_f32_16x16x32_bf16 v[168:171], a[216:219], v[56:59], v[168:171]\n"
  "v_mfma_f32_16x16x32_bf16 v[172:175], a[216:219], v[60:63], v[172:175]\n"
  "v_mfma_f32_16x16x32_bf16 v[176:179], a[216:219], v[64:67], v[176:179]\n"
  "buffer_load_dwordx4 a[148:151], %[voffX], %[srdWS], s93 offen offset:512 sc1\n"
  "v_mfma_f32_16x16x32_bf16 v[180:183], a[220:223], v[52:55], v[180:183]\n"
  "v_mfma_f32_16x16x32_bf16 v[184:187], a[220:223], v[56:59], v[184:187]\n"
  "v_mfma_f32_16x16x32_bf16 v[188:191], a[220:223], v[60:63], v[188:191]\n"
  "v_mfma_f32_16x16x32_bf16 v[192:195], a[220:223], v[64:67], v[192:195]\n"
  "s_waitcnt lgkmcnt(0)\n"
  "v_mfma_f32_16x16x32_bf16 v[132:135], a[224:227], v[36:39], v[132:135]\n"
  "ds_read_b128 v[52:55], v23 offset:49152\n"
  "buffer_load_dwordx4 a[152:155], %[voffX], %[srdWS], s92 offen offset:768 sc1\n"
  "v_mfma_f32_16x16x32_bf16 v[136:139], a[224:227], v[40:43], v[136:139]\n"
  "v_mfma_f32_16x16x32_bf16 v[140:143], a[224:227], v[44:47], v[140:143]\n"
  "ds_read_b128 v[56:59], v23 offset:53248\n"
  "v_mfma_f32_16x16x32_bf16 v[144:147], a[224:227], v[48:51], v[144:147]\n"
  "v_mfma_f32_16x16x32_bf16 v[148:151], a[228:231], v[36:39], v[148:151]\n"
  "ds_read_b128 v[60:63], v23 offset:57344\n"
  "buffer_load_dwordx4 a[52:55], %[voffX], %[srdWS], s97 offen offset:768 sc1\n"
  "v_mfma_f32_16x16x32_bf16 v[152:155], a[228:231], v[40:43], v[152:155]\n"
  "v_mfma_f32_16x16x32_bf16 v[156:159], a[228:231], v[44:47], v[156:159]\n"
  "ds_read_b128 v[64:67], v23 offset:61440\n"
  "v_mfma_f32_16x16x32_bf16 v[160:163], a[228:231], v[48:51], v[160:163]\n"
  "v_mfma_f32_16x16x32_bf16 v[164:167], a[232:235], v[36:39], v[164:167]\n"
  "v_mfma_f32_16x16x32_bf16 v[168:171], a[232:235], v[40:43], v[168:171]\n"
  "buffer_load_dwordx4 a[156:159], %[voffX], %[srdWS], s93 offen offset:768 sc1\n"
  "v_mfma_f32_16x16x32_bf16 v[172:175], a[232:235], v[44:47], v[172:175]\n"
  "s_add_u32 s92, %[rotU], 3\n"
  "s_and_b32 s92, s92, 3\n"
  "s_lshl_b32 s92, s92, 10\n"
  "v_mfma_f32_16x16x32_bf16 v[176:179], a[232:235], v[48:51], v[176:179]\n"
  "s_add_u32 s92, s92, %[oY]\n"
  "v_mfma_f32_16x16x32_bf16 v[180:183], a[236:239], v[36:39], v[180:183]\n"
  "s_add_u32 s93, s92, 0x4000\n"
  "v_mfma_f32_16x16x32_bf16 v[184:187], a[236:239], v[40:43], v[184:187]\n"
  "buffer_load_dwordx4 a[160:163], %[voffX], %[srdWS], s92 offen offset:0 sc1\n"
  "v_mfma_f32_16x16x32_bf16 v[188:191], a[236:239], v[44:47], v[188:191]\n"
  "v_mfma_f32_16x16x32_bf16 v[192:195], a[236:239], v[48:51], v[192:195]\n"
  "s_waitcnt vmcnt(63) lgkmcnt(0)\n"
  "s_barrier\n"
  "v_mfma_f32_16x16x32_bf16 v[132:135], a[240:243], v[52:55], v[132:135]\n"
  "v_mfma_f32_16x16x32_bf16 v[136:139], a[240:243], v[56:59], v[136:139]\n"
  "ds_read_b64_tr_b16 v[68:69], v30 offset:0\n"
  "ds_read_b64_tr_b16 v[70:71], v30 offset:3072\n"
  "v_mfma_f32_16x16x32_bf16 v[140:143], a[240:243], v[60:63], v[140:143]\n"
  "ds_read_b64_tr_b16 v[72:73], v31 offset:0\n"
  "buffer_load_dwordx4 a[56:59], %[voffX], %[srdWS], s98 offen offset:768 sc1\n"
  "v_mfma_f32_16x16x32_bf16 v[144:147], a[240:243], v[64:67], v[144:147]\n"
  "ds_read_b64_tr_b16 v[74:75], v31 offset:3072\n"
  "v_mfma_f32_16x16x32_bf16 v[148:151], a[244:247], v[52:55], v[148:151]\n"
  "ds_read_b64_tr_b16 v[76:77], v30 offset:1024\n"
  "v_mfma_f32_16x16x32_bf16 v[152:155], a[244:247], v[56:59], v[152:155]\n"
  "ds_read_b64_tr_b16 v[78:79], v30 offset:4096\n"
  "v_mfma_f32_16x16x32_bf16 v[156:159], a[244:247], v[60:63], v[156:159]\n"
  "ds_read_b64_tr_b16 v[80:81], v31 offset:1024\n"
  "v_mfma_f32_16x16x32_bf16 v[160:163], a[244:247], v[64:67], v[160:163]\n"
  "ds_read_b64_tr_b16 v[82:83], v31 offset:4096\n"
  "buffer_load_dwordx4 a[164:167], %[voffX], %[srdWS], s93 offen offset:0 sc1\n"
  "v_mfma_f32_16x16x32_bf16 v[164:167], a[248:251], v[52:55], v[164:167]\n"
  "ds_read_b64_tr_b16 v[84:85], v30 offset:2048\n"
  "v_mfma_f32_16x16x32_bf16 v[168:171], a[248:251], v[56:59], v[168:171]\n"
  "ds_read_b64_tr_b16 v[86:87], v30 offset:5120\n"
  "v_mfma_f32_16x16x32_bf16 v[172:175], a[248:251], v[60:63], v[172:175]\n"
  "ds_read_b64_tr_b16 v[88:89], v31 offset:2048\n"
  "v_mfma_f32_16x16x32_bf16 v[176:179], a[248:251], v[64:67], v[176:179]\n"
  "ds_read_b64_tr_b16 v[90:91], v31 offset:5120\n"
  "buffer_load_dwordx4 a[168:171], %[voffX], %[srdWS], s92 offen offset:256 sc1\n"
  "v_mfma_f32_16x16x32_bf16 v[180:183], a[252:255], v[52:55], v[180:183]\n"
  "v_mfma_f32_16x16x32_bf16 v[184:187], a[252:255], v[56:59], v[184:187]\n"
  "v_mfma_f32_16x16x32_bf16 v[188:191], a[252:255], v[60:63], v[188:191]\n"
  "v_mfma_f32_16x16x32_bf16 v[192:195], a[252:255], v[64:67], v[192:195]\n"
  "s_branch KVEND_%=\n"
  "KVV_%=:\n"
  "s_waitcnt vmcnt(16) lgkmcnt(0)\n"
  "v_mfma_f32_16x16x32_bf16 v[68:71], v[36:39], a[0:3], 0\n"
  "ds_read_b64_tr_b16 a[16:17], v24 offset:4096\n"
  "ds_read_b64_tr_b16 a[18:19], v24 offset:6144\n"
  "v_mfma_f32_16x16x32_bf16 v[72:75], v[36:39], a[4:7], 0\n"
  "ds_read_b64_tr_b16 a[20:21], v25 offset:4096\n"
  "v_mfma_f32_16x16x32_bf16 v[76:79], v[36:39], a[8:11], 0\n"
  "ds_read_b64_tr_b16 a[22:23], v25 offset:6144\n"
  "s_add_u32 s82, %[rot], 1\n"
  "s_and_b32 s82, s82, 3\n"
  "s_lshl_b32 s82, s82, 20\n"
  "s_add_u32 m0, s73, 81920\n"
  "s_add_u32 s64, s82, 0\n"
  "buffer_load_dwordx4 %[voffW], %[srdW], s64 offen lds\n"
  "v_mfma_f32_16x16x32_bf16 v[80:83], v[36:39], a[12:15], 0\n"
  "ds_read_b64_tr_b16 a[24:25], v24 offset:4608\n"
  "s_add_u32 s83, %[rot], 1\n"
  "s_and_b32 s83, s83, 3\n"
  "s_lshl_b32 s83, s83, 8\n"
  "s_add_u32 m0, s72, 16384\n"
  "v_mfma_f32_16x16x32_bf16 v[84:87], v[40:43], a[0:3], 0\n"
  "ds_read_b64_tr_b16 a[26:27], v24 offset:6656\n"
  "s_add_u32 s64, s83, 0\n"
  "v_mfma_f32_16x16x32_bf16 v[88:91], v[40:43], a[4:7], 0\n"
  "ds_read_b64_tr_b16 a[28:29], v25 offset:4608\n"
  "ds_read_b64_tr_b16 a[30:31], v25 offset:6656\n"
  "buffer_load_dwordx4 v16, %[srdC], s64 offen lds\n"
  "v_mfma_f32_16x16x32_bf16 v[92:95], v[40:43], a[8:11], 0\n"
  "ds_read_b128 v[52:55], v21 offset:0\n"
  "s_add_u32 m0, s72, 17408\n"
  "v_mfma_f32_16x16x32_bf16 v[96:99], v[40:43], a[12:15], 0\n"
  "ds_read_b128 v[56:59], v21 offset:4096\n"
  "s_add_u32 s64, s83, 0\n"
  "v_mfma_f32_16x16x32_bf16 v[100:103], v[44:47], a[0:3], 0\n"
  "ds_read_b128 v[60:63], v21 offset:8192\n"
  "buffer_load_dwordx4 v17, %[srdC], s64 offen lds\n"
  "v_mfma_f32_16x16x32_bf16 v[104:107], v[44:47], a[4:7], 0\n"
  "ds_read_b128 v[64:67], v21 offset:12288\n"
  "s_add_u32 m0, s72, 18432\n"
  "v_mfma_f32_16x16x32_bf16 v[108:111], v[44:47], a[8:11], 0\n"
  "s_add_u32 s64, s83, 0\n"
  "v_mfma_f32_16x16x32_bf16 v[112:115], v[44:47], a[12:15], 0\n"
  "buffer_load_dwordx4 v18, %[srdC], s64 offen lds\n"
  "v_mfma_f32_16x16x32_bf16 v[116:119], v[48:51], a[0:3], 0\n"
  "s_add_u32 m0, s72, 19456\n"
  "v_mfma_f32_16x16x32_bf16 v[120:123], v[48:51], a[4:7], 0\n"
  "s_add_u32 s64, s83, 0\n"
  "v_mfma_f32_16x16x32_bf16 v[124:127], v[48:51], a[8:11], 0\n"
  "buffer_load_dwordx4 v19, %[srdC], s64 offen lds\n"
  "v_mfma_f32_16x16x32_bf16 v[128:131], v[48:51], a[12:15], 0\n"
  "s_add_u32 m0, s73, 82944\n"
  "s_waitcnt vmcnt(17) lgkmcnt(0)\n"
  "v_mfma_f32_16x16x32_bf16 v[68:71], v[52:55], a[16:19], v[68:71]\n"
  "ds_read_b64_tr_b16 a[32:33], v24 offset:8192\n"
  "ds_read_b64_tr_b16 a[34:35], v24 offset:10240\n"
  "s_add_u32 s64, s82, 131072\n"
  "v_mfma_f32_16x16x32_bf16 v[72:75], v[52:55], a[20:23], v[72:75]\n"
  "ds_read_b64_tr_b16 a[36:37], v25 offset:8192\n"
  "buffer_load_dwordx4 %[voffW], %[srdW], s64 offen lds\n"
  "v_mfma_f32_16x16x32_bf16 v[76:79], v[52:55], a[24:27], v[76:79]\n"
  "ds_read_b64_tr_b16 a[38:39], v25 offset:10240\n"
  "s_add_u32 m0, s73, 83968\n"
  "v_mfma_f32_16x16x32_bf16 v[80:83], v[52:55], a[28:31], v[80:83]\n"
  "ds_read_b64_tr_b16 a[40:41], v24 offset:8704\n"
  "s_add_u32 s64, s82, 32768\n"
  "v_mfma_f32_16x16x32_bf16 v[84:87], v[56:59], a[16:19], v[84:87]\n"
  "ds_read_b64_tr_b16 a[42:43], v24 offset:10752\n"
  "buffer_load_dwordx4 %[voffW], %[srdW], s64 offen lds\n"
  "v_mfma_f32_16x16x32_bf16 v[88:91], v[56:59], a[20:23], v[88:91]\n"
  "ds_read_b64_tr_b16 a[44:45], v25 offset:8704\n"
  "ds_read_b64_tr_b16 a[46:47], v25 offset:10752\n"
  "s_add_u32 m0, s73, 84992\n"
  "v_mfma_f32_16x16x32_bf16 v[92:95], v[56:59], a[24:27], v[92:95]\n"
  "ds_read_b128 v[36:39], v22 offset:0\n"
  "s_add_u32 s64, s82, 163840\n"
  "v_mfma_f32_16x16x32_bf16 v[96:99], v[56:59], a[28:31], v[96:99]\n"
  "ds_read_b128 v[40:43], v22 offset:4096\n"
  "buffer_load_dwordx4 %[voffW], %[srdW], s64 offen lds\n"
  "v_mfma_f32_16x16x32_bf16 v[100:103], v[60:63], a[16:19], v[100:103]\n"
  "ds_read_b128 v[44:47], v22 offset:8192\n"
  "s_add_u32 m0, s73, 86016\n"
  "v_mfma_f32_16x16x32_bf16 v[104:107], v[60:63], a[20:23], v[104:107]\n"
  "ds_read_b128 v[48:51], v22 offset:12288\n"
  "s_add_u32 s64, s82, 262144\n"
  "v_mfma_f32_16x16x32_bf16 v[108:111], v[60:63], a[24:27], v[108:111]\n"
  "buffer_load_dwordx4 %[voffW], %[srdW], s64 offen lds\n"
  "v_mfma_f32_16x16x32_bf16 v[112:115], v[60:63], a[28:31], v[112:115]\n"
  "s_add_u32 m0, s73, 87040\n"
  "v_mfma_f32_16x16x32_bf16 v[116:119], v[64:67], a[16:19], v[116:119]\n"
  "s_add_u32 s64, s82, 393216\n"
  "v_mfma_f32_16x16x32_bf16 v[120:123], v[64:67], a[20:23], v[120:123]\n"
  "buffer_load_dwordx4 %[voffW], %[srdW], s64 offen lds\n"
  "v_mfma_f32_16x16x32_bf16 v[124:127], v[64:67], a[24:27], v[124:127]\n"
  "s_add_u32 m0, s73, 88064\n"
  "v_mfma_f32_16x16x32_bf16 v[128:131], v[64:67], a[28:31], v[128:131]\n"
  "s_add_u32 s64, s82, 294912\n"
  "s_waitcnt vmcnt(18) lgkmcnt(0)\n"
  "v_mfma_f32_16x16x32_bf16 v[68:71], v[36:39], a[32:35], v[68:71]\n"
  "ds_read_b64_tr_b16 a[48:49], v24 offset:12288\n"
  "ds_read_b64_tr_b16 a[50:51], v24 offset:14336\n"
  "buffer_load_dwordx4 %[voffW], %[srdW], s64 offen lds\n"
  "v_mfma_f32_16x16x32_bf16 v[72:75], v[36:39], a[36:39], v[72:75]\n"
  "ds_read_b64_tr_b16 a[52:53], v25 offset:12288\n"
  "s_add_u32 m0, s73, 89088\n"
  "v_mfma_f32_16x16x32_bf16 v[76:79], v[36:39], a[40:43], v[76:79]\n"
  "ds_read_b64_tr_b16 a[54:55], v25 offset:14336\n"
  "s_add_u32 s64, s82, 425984\n"
  "v_mfma_f32_16x16x32_bf16 v[80:83], v[36:39], a[44:47], v[80:83]\n"
  "ds_read_b64_tr_b16 a[56:57], v24 offset:12800\n"
  "buffer_load_dwordx4 %[voffW], %[srdW], s64 offen lds\n"
  "v_mfma_f32_16x16x32_bf16 v[84:87], v[40:43], a[32:35], v[84:87]\n"
  "ds_read_b64_tr_b16 a[58:59], v24 offset:14848\n"
  "s_add_u32 m0, s73, 65536\n"
  "v_mfma_f32_16x16x32_bf16 v[88:91], v[40:43], a[36:39], v[88:91]\n"
  "ds_read_b64_tr_b16 a[60:61], v25 offset:12800\n"
  "ds_read_b64_tr_b16 a[62:63], v25 offset:14848\n"
  "s_add_u32 s64, s82, 524288\n"
  "v_mfma_f32_16x16x32_bf16 v[92:95], v[40:43], a[40:43], v[92:95]\n"
  "ds_read_b128 v[52:55], v23 offset:0\n"
  "buffer_load_dwordx4 %[voffW], %[srdW], s64 offen lds\n"
  "v_mfma_f32_16x16x32_bf16 v[96:99], v[40:43], a[44:47], v[96:99]\n"
  "ds_read_b128 v[56:59], v23 offset:4096\n"
  "s_add_u32 m0, s73, 66560\n"
  "v_mfma_f32_16x16x32_bf16 v[100:103], v[44:47], a[32:35], v[100:103]\n"
  "ds_read_b128 v[60:63], v23 offset:8192\n"
  "s_add_u32 s64, s82, 655360\n"
  "v_mfma_f32_16x16x32_bf16 v[104:107], v[44:47], a[36:39], v[104:107]\n"
  "ds_read_b128 v[64:67], v23 offset:12288\n"
  "buffer_load_dwordx4 %[voffW], %[srdW], s64 offen lds\n"
  "v_mfma_f32_16x16x32_bf16 v[108:111], v[44:47], a[40:43], v[108:111]\n"
  "s_add_u32 m0, s73, 67584\n"
  "v_mfma_f32_16x16x32_bf16 v[112:115], v[44:47], a[44:47], v[112:115]\n"
  "s_add_u32 s64, s82, 557056\n"
  "v_mfma_f32_16x16x32_bf16 v[116:119], v[48:51], a[32:35], v[116:119]\n"
  "buffer_load_dwordx4 %[voffW], %[srdW], s64 offen lds\n"
  "v_mfma_f32_16x16x32_bf16 v[120:123], v[48:51], a[36:39], v[120:123]\n"
  "s_add_u32 m0, s73, 68608\n"
  "v_mfma_f32_16x16x32_bf16 v[124:127], v[48:51], a[40:43], v[124:127]\n"
  "s_add_u32 s64, s82, 688128\n"
  "v_mfma_f32_16x16x32_bf16 v[128:131], v[48:51], a[44:47], v[128:131]\n"
  "buffer_load_dwordx4 %[voffW], %[srdW], s64 offen lds\n"
  "s_waitcnt vmcnt(8) lgkmcnt(0)\n"
  "s_barrier\n"
  "v_mfma_f32_16x16x32_bf16 v[68:71], v[52:55], a[48:51], v[68:71]\n"
  "ds_read_b64_tr_b16 a[64:65], v24 offset:16384\n"
  "ds_read_b64_tr_b16 a[66:67], v24 offset:18432\n"
  "s_add_u32 m0, s73, 69632\n"
  "v_mfma_f32_16x16x32_bf16 v[72:75], v[52:55], a[52:55], v[72:75]\n"
  "ds_read_b64_tr_b16 a[68:69], v25 offset:16384\n"
  "s_add_u32 s64, s82, 786432\n"
  "v_mfma_f32_16x16x32_bf16 v[76:79], v[52:55], a[56:59], v[76:79]\n"
  "ds_read_b64_tr_b16 a[70:71], v25 offset:18432\n"
  "buffer_load_dwordx4 %[voffW], %[srdW], s64 offen lds\n"
  "v_mfma_f32_16x16x32_bf16 v[80:83], v[52:55], a[60:63], v[80:83]\n"
  "ds_read_b64_tr_b16 a[72:73], v24 offset:16896\n"
  "s_add_u32 m0, s73, 70656\n"
  "v_mfma_f32_16x16x32_bf16 v[84:87], v[56:59], a[48:51], v[84:87]\n"
  "ds_read_b64_tr_b16 a[74:75], v24 offset:18944\n"
  "s_add_u32 s64, s82, 917504\n"
  "v_mfma_f32_16x16x32_bf16 v[88:91], v[56:59], a[52:55], v[88:91]\n"
  "ds_read_b64_tr_b16 a[76:77], v25 offset:16896\n"
  "ds_read_b64_tr_b16 a[78:79], v25 offset:18944\n"
  "buffer_load_dwordx4 %[voffW], %[srdW], s64 offen lds\n"
  "v_mfma_f32_16x16x32_bf16 v[92:95], v[56:59], a[56:59], v[92:95]\n"
  "ds_read_b128 v[36:39], v20 offset:16384\n"
  "s_add_u32 m0, s73, 71680\n"
  "v_mfma_f32_16x16x32_bf16 v[96:99], v[56:59], a[60:63], v[96:99]\n"
  "ds_read_b128 v[40:43], v20 offset:20480\n"
  "s_add_u32 s64, s82, 819200\n"
  "v_mfma_f32_16x16x32_bf16 v[100:103], v[60:63], a[48:51], v[100:103]\n"
  "ds_read_b128 v[44:47], v20 offset:24576\n"
  "buffer_load_dwordx4 %[voffW], %[srdW], s64 offen lds\n"
  "v_mfma_f32_16x16x32_bf16 v[104:107], v[60:63], a[52:55], v[104:107]\n"
  "ds_read_b128 v[48:51], v20 offset:28672\n"
  "s_add_u32 m0, s73, 72704\n"
  "v_mfma_f32_16x16x32_bf16 v[108:111], v[60:63], a[56:59], v[108:111]\n"
  "s_add_u32 s64, s82, 950272\n"
  "v_mfma_f32_16x16x32_bf16 v[112:115], v[60:63], a[60:63], v[112:115]\n"
  "buffer_load_dwordx4 %[voffW], %[srdW], s64 offen lds\n"
  "s_add_u32 s83, %[rot], 2\n"
  "s_and_b32 s83, s83, 3\n"
  "s_lshl_b32 s83, s83, 8\n"
  "v_mfma_f32_16x16x32_bf16 v[116:119], v[64:67], a[48:51], v[116:119]\n"
  "s_add_u32 m0, s72, 32768\n"
  "v_mfma_f32_16x16x32_bf16 v[120:123], v[64:67], a[52:55], v[120:123]\n"
  "s_add_u32 s64, s83, 0\n"
  "v_mfma_f32_16x16x32_bf16 v[124:127], v[64:67], a[56:59], v[124:127]\n"
  "buffer_load_dwordx4 v16, %[srdC], s64 offen lds\n"
  "v_mfma_f32_16x16x32_bf16 v[128:131], v[64:67], a[60:63], v[128:131]\n"
  "s_add_u32 m0, s72, 33792\n"
  "s_waitcnt vmcnt(9) lgkmcnt(0)\n"
  "v_mfma_f32_16x16x32_bf16 v[68:71], v[36:39], a[64:67], v[68:71]\n"
  "ds_read_b64_tr_b16 a[80:81], v24 offset:20480\n"
  "ds_read_b64_tr_b16 a[82:83], v24 offset:22528\n"
  "s_add_u32 s64, s83, 0\n"
  "v_mfma_f32_16x16x32_bf16 v[72:75], v[36:39], a[68:71], v[72:75]\n"
  "ds_read_b64_tr_b16 a[84:85], v25 offset:20480\n"
  "buffer_load_dwordx4 v17, %[srdC], s64 offen lds\n"
  "v_mfma_f32_16x16x32_bf16 v[76:79], v[36:39], a[72:75], v[76:79]\n"
  "ds_read_b64_tr_b16 a[86:87], v25 offset:22528\n"
  "s_add_u32 m0, s72, 34816\n"
  "v_mfma_f32_16x16x32_bf16 v[80:83], v[36:39], a[76:79], v[80:83]\n"
  "ds_read_b64_tr_b16 a[88:89], v24 offset:20992\n"
  "s_add_u32 s64, s83, 0\n"
  "v_mfma_f32_16x16x32_bf16 v[84:87], v[40:43], a[64:67], v[84:87]\n"
  "ds_read_b64_tr_b16 a[90:91], v24 offset:23040\n"
  "buffer_load_dwordx4 v18, %[srdC], s64 offen lds\n"
  "v_mfma_f32_16x16x32_bf16 v[88:91], v[40:43], a[68:71], v[88:91]\n"
  "ds_read_b64_tr_b16 a[92:93], v25 offset:20992\n"
  "ds_read_b64_tr_b16 a[94:95], v25 offset:23040\n"
  "s_add_u32 m0, s72, 35840\n"
  "v_mfma_f32_16x16x32_bf16 v[92:95], v[40:43], a[72:75], v[92:95]\n"
  "ds_read_b128 v[52:55], v21 offset:16384\n"
  "s_add_u32 s64, s83, 0\n"
  "v_mfma_f32_16x16x32_bf16 v[96:99], v[40:43], a[76:79], v[96:99]\n"
  "ds_read_b128 v[56:59], v21 offset:20480\n"
  "buffer_load_dwordx4 v19, %[srdC], s64 offen lds\n"
  "s_add_u32 s82, %[rot], 2\n"
  "s_and_b32 s82, s82, 3\n"
  "s_lshl_b32 s82, s82, 20\n"
  "v_mfma_f32_16x16x32_bf16 v[100:103], v[44:47], a[64:67], v[100:103]\n"
  "ds_read_b128 v[60:63], v21 offset:24576\n"
  "s_add_u32 m0, s73, 73728\n"
  "v_mfma_f32_16x16x32_bf16 v[104:107], v[44:47], a[68:71], v[104:107]\n"
  "ds_read_b128 v[64:67], v21 offset:28672\n"
  "s_add_u32 s64, s82, 0\n"
  "v_mfma_f32_16x16x32_bf16 v[108:111], v[44:47], a[72:75], v[108:111]\n"
  "buffer_load_dwordx4 %[voffW], %[srdW], s64 offen lds\n"
  "v_mfma_f32_16x16x32_bf16 v[112:115], v[44:47], a[76:79], v[112:115]\n"
  "s_add_u32 m0, s73, 74752\n"
  "v_mfma_f32_16x16x32_bf16 v[116:119], v[48:51], a[64:67], v[116:119]\n"
  "s_add_u32 s64, s82, 131072\n"
  "v_mfma_f32_16x16x32_bf16 v[120:123], v[48:51], a[68:71], v[120:123]\n"
  "buffer_load_dwordx4 %[voffW], %[srdW], s64 offen lds\n"
  "v_mfma_f32_16x16x32_bf16 v[124:127], v[48:51], a[72:75], v[124:127]\n"
  "s_add_u32 m0, s73, 75776\n"
  "v_mfma_f32_16x16x32_bf16 v[128:131], v[48:51], a[76:79], v[128:131]\n"
  "s_add_u32 s64, s82, 32768\n"
  "s_waitcnt vmcnt(10) lgkmcnt(0)\n"
  "v_mfma_f32_16x16x32_bf16 v[68:71], v[52:55], a[80:83], v[68:71]\n"
  "ds_read_b64_tr_b16 a[96:97], v24 offset:0\n"
  "ds_read_b64_tr_b16 a[98:99], v24 offset:2048\n"
  "buffer_load_dwordx4 %[voffW], %[srdW], s64 offen lds\n"
  "v_mfma_f32_16x16x32_bf16 v[72:75], v[52:55], a[84:87], v[72:75]\n"
  "ds_read_b64_tr_b16 a[100:101], v25 offset:0\n"
  "s_add_u32 m0, s73, 76800\n"
  "v_mfma_f32_16x16x32_bf16 v[76:79], v[52:55], a[88:91], v[76:79]\n"
  "ds_read_b64_tr_b16 a[102:103], v25 offset:2048\n"
  "s_add_u32 s64, s82, 163840\n"
  "v_mfma_f32_16x16x32_bf16 v[80:83], v[52:55], a[92:95], v[80:83]\n"
  "ds_read_b64_tr_b16 a[104:105], v24 offset:512\n"
  "buffer_load_dwordx4 %[voffW], %[srdW], s64 offen lds\n"
  "v_mfma_f32_16x16x32_bf16 v[84:87], v[56:59], a[80:83], v[84:87]\n"
  "ds_read_b64_tr_b16 a[106:107], v24 offset:2560\n"
  "s_add_u32 m0, s73, 77824\n"
  "v_mfma_f32_16x16x32_bf16 v[88:91], v[56:59], a[84:87], v[88:91]\n"
  "ds_read_b64_tr_b16 a[108:109], v25 offset:512\n"
  "ds_read_b64_tr_b16 a[110:111], v25 offset:2560\n"
  "s_add_u32 s64, s82, 262144\n"
  "v_mfma_f32_16x16x32_bf16 v[92:95], v[56:59], a[88:91], v[92:95]\n"
  "ds_read_b128 v[36:39], v22 offset:16384\n"
  "buffer_load_dwordx4 %[voffW], %[srdW], s64 offen lds\n"
  "v_mfma_f32_16x16x32_bf16 v[96:99], v[56:59], a[92:95], v[96:99]\n"
  "ds_read_b128 v[40:43], v22 offset:20480\n"
  "s_add_u32 m0, s73, 78848\n"
  "v_mfma_f32_16x16x32_bf16 v[100:103], v[60:63], a[80:83], v[100:103]\n"
  "ds_read_b128 v[44:47], v22 offset:24576\n"
  "s_add_u32 s64, s82, 393216\n"
  "v_mfma_f32_16x16x32_bf16 v[104:107], v[60:63], a[84:87], v[104:107]\n"
  "ds_read_b128 v[48:51], v22 offset:28672\n"
  "buffer_load_dwordx4 %[voffW], %[srdW], s64 offen lds\n"
  "v_mfma_f32_16x16x32_bf16 v[108:111], v[60:63], a[88:91], v[108:111]\n"
  "s_add_u32 m0, s73, 79872\n"
  "v_mfma_f32_16x16x32_bf16 v[112:115], v[60:63], a[92:95], v[112:115]\n"
  "s_add_u32 s64, s82, 294912\n"
  "v_mfma_f32_16x16x32_bf16 v[116:119], v[64:67], a[80:83], v[116:119]\n"
  "buffer_load_dwordx4 %[voffW], %[srdW], s64 offen lds\n"
  "v_mfma_f32_16x16x32_bf16 v[120:123], v[64:67], a[84:87], v[120:123]\n"
  "s_add_u32 m0, s73, 80896\n"
  "v_mfma_f32_16x16x32_bf16 v[124:127], v[64:67], a[88:91], v[124:127]\n"
  "s_add_u32 s64, s82, 425984\n"
  "v_mfma_f32_16x16x32_bf16 v[128:131], v[64:67], a[92:95], v[128:131]\n"
  "buffer_load_dwordx4 %[voffW], %[srdW], s64 offen lds\n"
  "s_waitcnt vmcnt(12) lgkmcnt(0)\n"
  "v_mfma_f32_16x16x32_bf16 v[68:71], v[36:39], a[96:99], v[68:71]\n"
  "ds_read_b64_tr_b16 a[112:113], v24 offset:4096\n"
  "ds_read_b64_tr_b16 a[114:115], v24 offset:6144\n"
  "s_add_u32 m0, s73, 81920\n"
  "v_mfma_f32_16x16x32_bf16 v[72:75], v[36:39], a[100:103], v[72:75]\n"
  "ds_read_b64_tr_b16 a[116:117], v25 offset:4096\n"
  "s_add_u32 s64, s82, 524288\n"
  "v_mfma_f32_16x16x32_bf16 v[76:79], v[36:39], a[104:107], v[76:79]\n"
  "ds_read_b64_tr_b16 a[118:119], v25 offset:6144\n"
  "buffer_load_dwordx4 %[voffW], %[srdW], s64 offen lds\n"
  "v_mfma_f32_16x16x32_bf16 v[80:83], v[36:39], a[108:111], v[80:83]\n"
  "ds_read_b64_tr_b16 a[120:121], v24 offset:4608\n"
  "s_add_u32 m0, s73, 82944\n"
  "v_mfma_f32_16x16x32_bf16 v[84:87], v[40:43], a[96:99], v[84:87]\n"
  "ds_read_b64_tr_b16 a[122:123], v24 offset:6656\n"
  "s_add_u32 s64, s82, 655360\n"
  "v_mfma_f32_16x16x32_bf16 v[88:91], v[40:43], a[100:103], v[88:91]\n"
  "ds_read_b64_tr_b16 a[124:125], v25 offset:4608\n"
  "ds_read_b64_tr_b16 a[126:127], v25 offset:6656\n"
  "buffer_load_dwordx4 %[voffW], %[srdW], s64 offen lds\n"
  "v_mfma_f32_16x16x32_bf16 v[92:95], v[40:43], a[104:107], v[92:95]\n"
  "ds_read_b128 v[52:55], v23 offset:16384\n"
  "s_add_u32 m0, s73, 83968\n"
  "v_mfma_f32_16x16x32_bf16 v[96:99], v[40:43], a[108:111], v[96:99]\n"
  "ds_read_b128 v[56:59], v23 offset:20480\n"
  "s_add_u32 s64, s82, 557056\n"
  "v_mfma_f32_16x16x32_bf16 v[100:103], v[44:47], a[96:99], v[100:103]\n"
  "ds_read_b128 v[60:63], v23 offset:24576\n"
  "buffer_load_dwordx4 %[voffW], %[srdW], s64 offen lds\n"
  "v_mfma_f32_16x16x32_bf16 v[104:107], v[44:47], a[100:103], v[104:107]\n"
  "ds_read_b128 v[64:67], v23 offset:28672\n"
  "s_add_u32 m0, s73, 84992\n"
  "v_mfma_f32_16x16x32_bf16 v[108:111], v[44:47], a[104:107], v[108:111]\n"
  "s_add_u32 s64, s82, 688128\n"
  "v_mfma_f32_16x16x32_bf16 v[112:115], v[44:47], a[108:111], v[112:115]\n"
  "buffer_load_dwordx4 %[voffW], %[srdW], s64 offen lds\n"
  "v_mfma_f32_16x16x32_bf16 v[116:119], v[48:51], a[96:99], v[116:119]\n"
  "s_add_u32 m0, s73, 86016\n"
  "v_mfma_f32_16x16x32_bf16 v[120:123], v[48:51], a[100:103], v[120:123]\n"
  "s_add_u32 s64, s82, 786432\n"
  "v_mfma_f32_16x16x32_bf16 v[124:127], v[48:51], a[104:107], v[124:127]\n"
  "buffer_load_dwordx4 %[voffW], %[srdW], s64 offen lds\n"
  "v_mfma_f32_16x16x32_bf16 v[128:131], v[48:51], a[108:111], v[128:131]\n"
  "s_add_u32 m0, s73, 87040\n"
  "s_waitcnt vmcnt(9) lgkmcnt(0)\n"
  "s_barrier\n"
  "v_mfma_f32_16x16x32_bf16 v[68:71], v[52:55], a[112:115], v[68:71]\n"
  "ds_read_b64_tr_b16 a[128:129], v24 offset:8192\n"
  "ds_read_b64_tr_b16 a[130:131], v24 offset:10240\n"
  "s_add_u32 s64, s82, 917504\n"
  "v_mfma_f32_16x16x32_bf16 v[72:75], v[52:55], a[116:119], v[72:75]\n"
  "ds_read_b64_tr_b16 a[132:133], v25 offset:8192\n"
  "buffer_load_dwordx4 %[voffW], %[srdW], s64 offen lds\n"
  "v_mfma_f32_16x16x32_bf16 v[76:79], v[52:55], a[120:123], v[76:79]\n"
  "ds_read_b64_tr_b16 a[134:135], v25 offset:10240\n"
  "s_add_u32 m0, s73, 88064\n"
  "v_mfma_f32_16x16x32_bf16 v[80:83], v[52:55], a[124:127], v[80:83]\n"
  "ds_read_b64_tr_b16 a[136:137], v24 offset:8704\n"
  "s_add_u32 s64, s82, 819200\n"
  "v_mfma_f32_16x16x32_bf16 v[84:87], v[56:59], a[112:115], v[84:87]\n"
  "ds_read_b64_tr_b16 a[138:139], v24 offset:10752\n"
  "buffer_load_dwordx4 %[voffW], %[srdW], s64 offen lds\n"
  "v_mfma_f32_16x16x32_bf16 v[88:91], v[56:59], a[116:119], v[88:91]\n"
  "ds_read_b64_tr_b16 a[140:141], v25 offset:8704\n"
  "ds_read_b64_tr_b16 a[142:143], v25 offset:10752\n"
  "s_add_u32 m0, s73, 89088\n"
  "v_mfma_f32_16x16x32_bf16 v[92:95], v[56:59], a[120:123], v[92:95]\n"
  "ds_read_b128 v[36:39], v20 offset:32768\n"
  "s_add_u32 s64, s82, 950272\n"
  "v_mfma_f32_16x16x32_bf16 v[96:99], v[56:59], a[124:127], v[96:99]\n"
  "ds_read_b128 v[40:43], v20 offset:36864\n"
  "buffer_load_dwordx4 %[voffW], %[srdW], s64 offen lds\n"
  "s_add_u32 s83, %[rot], 3\n"
  "s_and_b32 s83, s83, 3\n"
  "s_lshl_b32 s83, s83, 8\n"
  "v_mfma_f32_16x16x32_bf16 v[100:103], v[60:63], a[112:115], v[100:103]\n"
  "ds_read_b128 v[44:47], v20 offset:40960\n"
  "s_add_u32 m0, s72, 49152\n"
  "v_mfma_f32_16x16x32_bf16 v[104:107], v[60:63], a[116:119], v[104:107]\n"
  "ds_read_b128 v[48:51], v20 offset:45056\n"
  "s_add_u32 s64, s83, 0\n"
  "v_mfma_f32_16x16x32_bf16 v[108:111], v[60:63], a[120:123], v[108:111]\n"
  "buffer_load_dwordx4 v16, %[srdC], s64 offen lds\n"
  "v_mfma_f32_16x16x32_bf16 v[112:115], v[60:63], a[124:127], v[112:115]\n"
  "s_add_u32 m0, s72, 50176\n"
  "v_mfma_f32_16x16x32_bf16 v[116:119], v[64:67], a[112:115], v[116:119]\n"
  "s_add_u32 s64, s83, 0\n"
  "v_mfma_f32_16x16x32_bf16 v[120:123], v[64:67], a[116:119], v[120:123]\n"
  "buffer_load_dwordx4 v17, %[srdC], s64 offen lds\n"
  "v_mfma_f32_16x16x32_bf16 v[124:127], v[64:67], a[120:123], v[124:127]\n"
  "s_add_u32 m0, s72, 51200\n"
  "v_mfma_f32_16x16x32_bf16 v[128:131], v[64:67], a[124:127], v[128:131]\n"
  "s_add_u32 s64, s83, 0\n"
  "s_waitcnt vmcnt(10) lgkmcnt(0)\n"
  "v_mfma_f32_16x16x32_bf16 v[68:71], v[36:39], a[128:131], v[68:71]\n"
  "ds_read_b64_tr_b16 a[144:145], v24 offset:12288\n"
  "ds_read_b64_tr_b16 a[146:147], v24 offset:14336\n"
  "buffer_load_dwordx4 v18, %[srdC], s64 offen lds\n"
  "v_mfma_f32_16x16x32_bf16 v[72:75], v[36:39], a[132:135], v[72:75]\n"
  "ds_read_b64_tr_b16 a[148:149], v25 offset:12288\n"
  "s_add_u32 m0, s72, 52224\n"
  "v_mfma_f32_16x16x32_bf16 v[76:79], v[36:39], a[136:139], v[76:79]\n"
  "ds_read_b64_tr_b16 a[150:151], v25 offset:14336\n"
  "s_add_u32 s64, s83, 0\n"
  "v_mfma_f32_16x16x32_bf16 v[80:83], v[36:39], a[140:143], v[80:83]\n"
  "ds_read_b64_tr_b16 a[152:153], v24 offset:12800\n"
  "buffer_load_dwordx4 v19, %[srdC], s64 offen lds\n"
  "s_add_u32 s82, %[rot], 3\n"
  "s_and_b32 s82, s82, 3\n"
  "s_lshl_b32 s82, s82, 20\n"
  "v_mfma_f32_16x16x32_bf16 v[84:87], v[40:43], a[128:131], v[84:87]\n"
  "ds_read_b64_tr_b16 a[154:155], v24 offset:14848\n"
  "s_add_u32 m0, s73, 65536\n"
  "v_mfma_f32_16x16x32_bf16 v[88:91], v[40:43], a[132:135], v[88:91]\n"
  "ds_read_b64_tr_b16 a[156:157], v25 offset:12800\n"
  "ds_read_b64_tr_b16 a[158:159], v25 offset:14848\n"
  "s_add_u32 s64, s82, 0\n"
  "v_mfma_f32_16x16x32_bf16 v[92:95], v[40:43], a[136:139], v[92:95]\n"
  "ds_read_b128 v[52:55], v21 offset:32768\n"
  "buffer_load_dwordx4 %[voffW], %[srdW], s64 offen lds\n"
  "v_mfma_f32_16x16x32_bf16 v[96:99], v[40:43], a[140:143], v[96:99]\n"
  "ds_read_b128 v[56:59], v21 offset:36864\n"
  "s_add_u32 m0, s73, 66560\n"
  "v_mfma_f32_16x16x32_bf16 v[100:103], v[44:47], a[128:131], v[100:103]\n"
  "ds_read_b128 v[60:63], v21 offset:40960\n"
  "s_add_u32 s64, s82, 131072\n"
  "v_mfma_f32_16x16x32_bf16 v[104:107], v[44:47], a[132:135], v[104:107]\n"
  "ds_read_b128 v[64:67], v21 offset:45056\n"
  "buffer_load_dwordx4 %[voffW], %[srdW], s64 offen lds\n"
  "v_mfma_f32_16x16x32_bf16 v[108:111], v[44:47], a[136:139], v[108:111]\n"
  "s_add_u32 m0, s73, 67584\n"
  "v_mfma_f32_16x16x32_bf16 v[112:115], v[44:47], a[140:143], v[112:115]\n"
  "s_add_u32 s64, s82, 32768\n"
  "v_mfma_f32_16x16x32_bf16 v[116:119], v[48:51], a[128:131], v[116:119]\n"
  "buffer_load_dwordx4 %[voffW], %[srdW], s64 offen lds\n"
  "v_mfma_f32_16x16x32_bf16 v[120:123], v[48:51], a[132:135], v[120:123]\n"
  "s_add_u32 m0, s73, 68608\n"
  "v_mfma_f32_16x16x32_bf16 v[124:127], v[48:51], a[136:139], v[124:127]\n"
  "s_add_u32 s64, s82, 163840\n"
  "v_mfma_f32_16x16x32_bf16 v[128:131], v[48:51], a[140:143], v[128:131]\n"
  "buffer_load_dwordx4 %[voffW], %[srdW], s64 offen lds\n"
  "s_waitcnt vmcnt(12) lgkmcnt(0)\n"
  "v_mfma_f32_16x16x32_bf16 v[68:71], v[52:55], a[144:147], v[68:71]\n"
  "ds_read_b64_tr_b16 a[160:161], v24 offset:16384\n"
  "ds_read_b64_tr_b16 a[162:163], v24 offset:18432\n"
  "s_add_u32 m0, s73, 69632\n"
  "v_mfma_f32_16x16x32_bf16 v[72:75], v[52:55], a[148:151], v[72:75]\n"
  "ds_read_b64_tr_b16 a[164:165], v25 offset:16384\n"
  "s_add_u32 s64, s82, 262144\n"
  "v_mfma_f32_16x16x32_bf16 v[76:79], v[52:55], a[152:155], v[76:79]\n"
  "ds_read_b64_tr_b16 a[166:167], v25 offset:18432\n"
  "buffer_load_dwordx4 %[voffW], %[srdW], s64 offen lds\n"
  "v_mfma_f32_16x16x32_bf16 v[80:83], v[52:55], a[156:159], v[80:83]\n"
  "ds_read_b64_tr_b16 a[168:169], v24 offset:16896\n"
  "s_add_u32 m0, s73, 70656\n"
  "v_mfma_f32_16x16x32_bf16 v[84:87], v[56:59], a[144:147], v[84:87]\n"
  "ds_read_b64_tr_b16 a[170:171], v24 offset:18944\n"
  "s_add_u32 s64, s82, 393216\n"
  "v_mfma_f32_16x16x32_bf16 v[88:91], v[56:59], a[148:151], v[88:91]\n"
  "ds_read_b64_tr_b16 a[172:173], v25 offset:16896\n"
  "ds_read_b64_tr_b16 a[174:175], v25 offset:18944\n"
  "buffer_load_dwordx4 %[voffW], %[srdW], s64 offen lds\n"
  "v_mfma_f32_16x16x32_bf16 v[92:95], v[56:59], a[152:155], v[92:95]\n"
  "ds_read_b128 v[36:39], v22 offset:32768\n"
  "s_add_u32 m0, s73, 71680\n"
  "v_mfma_f32_16x16x32_bf16 v[96:99], v[56:59], a[156:159], v[96:99]\n"
  "ds_read_b128 v[40:43], v22 offset:36864\n"
  "s_add_u32 s64, s82, 294912\n"
  "v_mfma_f32_16x16x32_bf16 v[100:103], v[60:63], a[144:147], v[100:103]\n"
  "ds_read_b128 v[44:47], v22 offset:40960\n"
  "buffer_load_dwordx4 %[voffW], %[srdW], s64 offen lds\n"
  "v_mfma_f32_16x16x32_bf16 v[104:107], v[60:63], a[148:151], v[104:107]\n"
  "ds_read_b128 v[48:51], v22 offset:45056\n"
  "s_add_u32 m0, s73, 72704\n"
  "v_mfma_f32_16x16x32_bf16 v[108:111], v[60:63], a[152:155], v[108:111]\n"
  "s_add_u32 s64, s82, 425984\n"
  "v_mfma_f32_16x16x32_bf16 v[112:115], v[60:63], a[156:159], v[112:115]\n"
  "buffer_load_dwordx4 %[voffW], %[srdW], s64 offen lds\n"
  "v_mfma_f32_16x16x32_bf16 v[116:119], v[64:67], a[144:147], v[116:119]\n"
  "s_add_u32 m0, s73, 73728\n"
  "v_mfma_f32_16x16x32_bf16 v[120:123], v[64:67], a[148:151], v[120:123]\n"
  "s_add_u32 s64, s82, 524288\n"
  "v_mfma_f32_16x16x32_bf16 v[124:127], v[64:67], a[152:155], v[124:127]\n"
  "buffer_load_dwordx4 %[voffW], %[srdW], s64 offen lds\n"
  "v_mfma_f32_16x16x32_bf16 v[128:131], v[64:67], a[156:159], v[128:131]\n"
  "s_add_u32 m0, s73, 74752\n"
  "s_waitcnt vmcnt(13) lgkmcnt(0)\n"
  "v_mfma_f32_16x16x32_bf16 v[68:71], v[36:39], a[160:163], v[68:71]\n"
  "ds_read_b64_tr_b16 a[176:177], v24 offset:20480\n"
  "ds_read_b64_tr_b16 a[178:179], v24 offset:22528\n"
  "s_add_u32 s64, s82, 655360\n"
  "v_mfma_f32_16x16x32_bf16 v[72:75], v[36:39], a[164:167], v[72:75]\n"
  "ds_read_b64_tr_b16 a[180:181], v25 offset:20480\n"
  "buffer_load_dwordx4 %[voffW], %[srdW], s64 offen lds\n"
  "v_mfma_f32_16x16x32_bf16 v[76:79], v[36:39], a[168:171], v[76:79]\n"
  "ds_read_b64_tr_b16 a[182:183], v25 offset:22528\n"
  "s_add_u32 m0, s73, 75776\n"
  "v_mfma_f32_16x16x32_bf16 v[80:83], v[36:39], a[172:175], v[80:83]\n"
  "ds_read_b64_tr_b16 a[184:185], v24 offset:20992\n"
  "s_add_u32 s64, s82, 557056\n"
  "v_mfma_f32_16x16x32_bf16 v[84:87], v[40:43], a[160:163], v[84:87]\n"
  "ds_read_b64_tr_b16 a[186:187], v24 offset:23040\n"
  "buffer_load_dwordx4 %[voffW], %[srdW], s64 offen lds\n"
  "v_mfma_f32_16x16x32_bf16 v[88:91], v[40:43], a[164:167], v[88:91]\n"
  "ds_read_b64_tr_b16 a[188:189], v25 offset:20992\n"
  "ds_read_b64_tr_b16 a[190:191], v25 offset:23040\n"
  "s_add_u32 m0, s73, 76800\n"
  "v_mfma_f32_16x16x32_bf16 v[92:95], v[40:43], a[168:171], v[92:95]\n"
  "ds_read_b128 v[52:55], v23 offset:32768\n"
  "s_add_u32 s64, s82, 688128\n"
  "v_mfma_f32_16x16x32_bf16 v[96:99], v[40:43], a[172:175], v[96:99]\n"
  "ds_read_b128 v[56:59], v23 offset:36864\n"
  "buffer_load_dwordx4 %[voffW], %[srdW], s64 offen lds\n"
  "s_add_u32 s83, %[rot], 0\n"
  "s_and_b32 s83, s83, 3\n"
  "s_lshl_b32 s83, s83, 8\n"
  "v_mfma_f32_16x16x32_bf16 v[100:103], v[44:47], a[160:163], v[100:103]\n"
  "ds_read_b128 v[60:63], v23 offset:40960\n"
  "s_add_u32 m0, s72, 0\n"
  "v_mfma_f32_16x16x32_bf16 v[104:107], v[44:47], a[164:167], v[104:107]\n"
  "ds_read_b128 v[64:67], v23 offset:45056\n"
  "s_add_u32 s64, s83, 65536\n"
  "v_mfma_f32_16x16x32_bf16 v[108:111], v[44:47], a[168:171], v[108:111]\n"
  "buffer_load_dwordx4 v16, %[srdC], s64 offen lds\n"
  "v_mfma_f32_16x16x32_bf16 v[112:115], v[44:47], a[172:175], v[112:115]\n"
  "s_add_u32 m0, s72, 1024\n"
  "v_mfma_f32_16x16x32_bf16 v[116:119], v[48:51], a[160:163], v[116:119]\n"
  "s_add_u32 s64, s83, 65536\n"
  "v_mfma_f32_16x16x32_bf16 v[120:123], v[48:51], a[164:167], v[120:123]\n"
  "buffer_load_dwordx4 v17, %[srdC], s64 offen lds\n"
  "v_mfma_f32_16x16x32_bf16 v[124:127], v[48:51], a[168:171], v[124:127]\n"
  "s_add_u32 m0, s72, 2048\n"
  "v_mfma_f32_16x16x32_bf16 v[128:131], v[48:51], a[172:175], v[128:131]\n"
  "s_add_u32 s64, s83, 65536\n"
  "s_waitcnt vmcnt(10) lgkmcnt(0)\n"
  "s_barrier\n"
  "v_mfma_f32_16x16x32_bf16 v[68:71], v[52:55], a[176:179], v[68:71]\n"
  "ds_read_b64_tr_b16 a[192:193], v24 offset:0\n"
  "ds_read_b64_tr_b16 a[194:195], v24 offset:2048\n"
  "buffer_load_dwordx4 v18, %[srdC], s64 offen lds\n"
  "v_mfma_f32_16x16x32_bf16 v[72:75], v[52:55], a[180:183], v[72:75]\n"
  "ds_read_b64_tr_b16 a[196:197], v25 offset:0\n"
  "s_add_u32 m0, s72, 3072\n"
  "v_mfma_f32_16x16x32_bf16 v[76:79], v[52:55], a[184:187], v[76:79]\n"
  "ds_read_b64_tr_b16 a[198:199], v25 offset:2048\n"
  "s_add_u32 s64, s83, 65536\n"
  "v_mfma_f32_16x16x32_bf16 v[80:83], v[52:55], a[188:191], v[80:83]\n"
  "ds_read_b64_tr_b16 a[200:201], v24 offset:512\n"
  "buffer_load_dwordx4 v19, %[srdC], s64 offen lds\n"
  "v_mfma_f32_16x16x32_bf16 v[84:87], v[56:59], a[176:179], v[84:87]\n"
  "ds_read_b64_tr_b16 a[202:203], v24 offset:2560\n"
  "s_add_u32 m0, s73, 77824\n"
  "v_mfma_f32_16x16x32_bf16 v[88:91], v[56:59], a[180:183], v[88:91]\n"
  "ds_read_b64_tr_b16 a[204:205], v25 offset:512\n"
  "ds_read_b64_tr_b16 a[206:207], v25 offset:2560\n"
  "s_add_u32 s64, s82, 786432\n"
  "v_mfma_f32_16x16x32_bf16 v[92:95], v[56:59], a[184:187], v[92:95]\n"
  "ds_read_b128 v[36:39], v20 offset:49152\n"
  "buffer_load_dwordx4 %[voffW], %[srdW], s64 offen lds\n"
  "v_mfma_f32_16x16x32_bf16 v[96:99], v[56:59], a[188:191], v[96:99]\n"
  "ds_read_b128 v[40:43], v20 offset:53248\n"
  "s_add_u32 m0, s73, 78848\n"
  "v_mfma_f32_16x16x32_bf16 v[100:103], v[60:63], a[176:179], v[100:103]\n"
  "ds_read_b128 v[44:47], v20 offset:57344\n"
  "s_add_u32 s64, s82, 917504\n"
  "v_mfma_f32_16x16x32_bf16 v[104:107], v[60:63], a[180:183], v[104:107]\n"
  "ds_read_b128 v[48:51], v20 offset:61440\n"
  "buffer_load_dwordx4 %[voffW], %[srdW], s64 offen lds\n"
  "v_mfma_f32_16x16x32_bf16 v[108:111], v[60:63], a[184:187], v[108:111]\n"
  "s_add_u32 m0, s73, 79872\n"
  "v_mfma_f32_16x16x32_bf16 v[112:115], v[60:63], a[188:191], v[112:115]\n"
  "s_add_u32 s64, s82, 819200\n"
  "v_mfma_f32_16x16x32_bf16 v[116:119], v[64:67], a[176:179], v[116:119]\n"
  "buffer_load_dwordx4 %[voffW], %[srdW], s64 offen lds\n"
  "v_mfma_f32_16x16x32_bf16 v[120:123], v[64:67], a[180:183], v[120:123]\n"
  "s_add_u32 m0, s73, 80896\n"
  "v_mfma_f32_16x16x32_bf16 v[124:127], v[64:67], a[184:187], v[124:127]\n"
  "s_add_u32 s64, s82, 950272\n"
  "v_mfma_f32_16x16x32_bf16 v[128:131], v[64:67], a[188:191], v[128:131]\n"
  "buffer_load_dwordx4 %[voffW], %[srdW], s64 offen lds\n"
  "s_add_u32 s83, %[rot], 1\n"
  "s_and_b32 s83, s83, 3\n"
  "s_lshl_b32 s83, s83, 8\n"
  "s_waitcnt vmcnt(12) lgkmcnt(0)\n"
  "v_mfma_f32_16x16x32_bf16 v[68:71], v[36:39], a[192:195], v[68:71]\n"
  "ds_read_b64_tr_b16 a[208:209], v24 offset:4096\n"
  "ds_read_b64_tr_b16 a[210:211], v24 offset:6144\n"
  "s_add_u32 m0, s72, 16384\n"
  "v_mfma_f32_16x16x32_bf16 v[72:75], v[36:39], a[196:199], v[72:75]\n"
  "ds_read_b64_tr_b16 a[212:213], v25 offset:4096\n"
  "s_add_u32 s64, s83, 65536\n"
  "v_mfma_f32_16x16x32_bf16 v[76:79], v[36:39], a[200:203], v[76:79]\n"
  "ds_read_b64_tr_b16 a[214:215], v25 offset:6144\n"
  "buffer_load_dwordx4 v16, %[srdC], s64 offen lds\n"
  "v_mfma_f32_16x16x32_bf16 v[80:83], v[36:39], a[204:207], v[80:83]\n"
  "ds_read_b64_tr_b16 a[216:217], v24 offset:4608\n"
  "s_add_u32 m0, s72, 17408\n"
  "v_mfma_f32_16x16x32_bf16 v[84:87], v[40:43], a[192:195], v[84:87]\n"
  "ds_read_b64_tr_b16 a[218:219], v24 offset:6656\n"
  "s_add_u32 s64, s83, 65536\n"
  "v_mfma_f32_16x16x32_bf16 v[88:91], v[40:43], a[196:199], v[88:91]\n"
  "ds_read_b64_tr_b16 a[220:221], v25 offset:4608\n"
  "ds_read_b64_tr_b16 a[222:223], v25 offset:6656\n"
  "buffer_load_dwordx4 v17, %[srdC], s64 offen lds\n"
  "v_mfma_f32_16x16x32_bf16 v[92:95], v[40:43], a[200:203], v[92:95]\n"
  "ds_read_b128 v[52:55], v21 offset:49152\n"
  "s_add_u32 m0, s72, 18432\n"
  "v_mfma_f32_16x16x32_bf16 v[96:99], v[40:43], a[204:207], v[96:99]\n"
  "ds_read_b128 v[56:59], v21 offset:53248\n"
  "s_add_u32 s64, s83, 65536\n"
  "v_mfma_f32_16x16x32_bf16 v[100:103], v[44:47], a[192:195], v[100:103]\n"
  "ds_read_b128 v[60:63], v21 offset:57344\n"
  "buffer_load_dwordx4 v18, %[srdC], s64 offen lds\n"
  "v_mfma_f32_16x16x32_bf16 v[104:107], v[44:47], a[196:199], v[104:107]\n"
  "ds_read_b128 v[64:67], v21 offset:61440\n"
  "s_add_u32 m0, s72, 19456\n"
  "v_mfma_f32_16x16x32_bf16 v[108:111], v[44:47], a[200:203], v[108:111]\n"
  "s_add_u32 s64, s83, 65536\n"
  "v_mfma_f32_16x16x32_bf16 v[112:115], v[44:47], a[204:207], v[112:115]\n"
  "buffer_load_dwordx4 v19, %[srdC], s64 offen lds\n"
  "s_add_u32 s83, %[rot], 2\n"
  "s_and_b32 s83, s83, 3\n"
  "s_lshl_b32 s83, s83, 8\n"
  "v_mfma_f32_16x16x32_bf16 v[116:119], v[48:51], a[192:195], v[116:119]\n"
  "s_add_u32 m0, s72, 32768\n"
  "v_mfma_f32_16x16x32_bf16 v[120:123], v[48:51], a[196:199], v[120:123]\n"
  "s_add_u32 s64, s83, 65536\n"
  "v_mfma_f32_16x16x32_bf16 v[124:127], v[48:51], a[200:203], v[124:127]\n"
  "buffer_load_dwordx4 v16, %[srdC], s64 offen lds\n"
  "v_mfma_f32_16x16x32_bf16 v[128:131], v[48:51], a[204:207], v[128:131]\n"
  "s_add_u32 m0, s72, 33792\n"
  "s_waitcnt vmcnt(13) lgkmcnt(0)\n"
  "v_mfma_f32_16x16x32_bf16 v[68:71], v[52:55], a[208:211], v[68:71]\n"
  "ds_read_b64_tr_b16 a[224:225], v24 offset:8192\n"
  "ds_read_b64_tr_b16 a[226:227], v24 offset:10240\n"
  "s_add_u32 s64, s83, 65536\n"
  "v_mfma_f32_16x16x32_bf16 v[72:75], v[52:55], a[212:215], v[72:75]\n"
  "ds_read_b64_tr_b16 a[228:229], v25 offset:8192\n"
  "buffer_load_dwordx4 v17, %[srdC], s64 offen lds\n"
  "v_mfma_f32_16x16x32_bf16 v[76:79], v[52:55], a[216:219], v[76:79]\n"
  "ds_read_b64_tr_b16 a[230:231], v25 offset:10240\n"
  "s_add_u32 m0, s72, 34816\n"
  "v_mfma_f32_16x16x32_bf16 v[80:83], v[52:55], a[220:223], v[80:83]\n"
  "ds_read_b64_tr_b16 a[232:233], v24 offset:8704\n"
  "s_add_u32 s64, s83, 65536\n"
  "v_mfma_f32_16x16x32_bf16 v[84:87], v[56:59], a[208:211], v[84:87]\n"
  "ds_read_b64_tr_b16 a[234:235], v24 offset:10752\n"
  "buffer_load_dwordx4 v18, %[srdC], s64 offen lds\n"
  "v_mfma_f32_16x16x32_bf16 v[88:91], v[56:59], a[212:215], v[88:91]\n"
  "ds_read_b64_tr_b16 a[236:237], v25 offset:8704\n"
  "ds_read_b64_tr_b16 a[238:239], v25 offset:10752\n"
  "s_add_u32 m0, s72, 35840\n"
  "v_mfma_f32_16x16x32_bf16 v[92:95], v[56:59], a[216:219], v[92:95]\n"
  "ds_read_b128 v[36:39], v22 offset:49152\n"
  "s_add_u32 s64, s83, 65536\n"
  "v_mfma_f32_16x16x32_bf16 v[96:99], v[56:59], a[220:223], v[96:99]\n"
  "ds_read_b128 v[40:43], v22 offset:53248\n"
  "buffer_load_dwordx4 v19, %[srdC], s64 offen lds\n"
  "v_mfma_f32_16x16x32_bf16 v[100:103], v[60:63], a[208:211], v[100:103]\n"
  "ds_read_b128 v[44:47], v22 offset:57344\n"
  "v_mfma_f32_16x16x32_bf16 v[104:107], v[60:63], a[212:215], v[104:107]\n"
  "ds_read_b128 v[48:51], v22 offset:61440\n"
  "v_mfma_f32_16x16x32_bf16 v[108:111], v[60:63], a[216:219], v[108:111]\n"
  "v_mfma_f32_16x16x32_bf16 v[112:115], v[60:63], a[220:223], v[112:115]\n"
  "v_mfma_f32_16x16x32_bf16 v[116:119], v[64:67], a[208:211], v[116:119]\n"
  "v_mfma_f32_16x16x32_bf16 v[120:123], v[64:67], a[212:215], v[120:123]\n"
  "v_mfma_f32_16x16x32_bf16 v[124:127], v[64:67], a[216:219], v[124:127]\n"
  "v_mfma_f32_16x16x32_bf16 v[128:131], v[64:67], a[220:223], v[128:131]\n"
  "s_waitcnt vmcnt(8) lgkmcnt(0)\n"
  "v_mfma_f32_16x16x32_bf16 v[68:71], v[36:39], a[224:227], v[68:71]\n"
  "ds_read_b64_tr_b16 a[240:241], v24 offset:12288\n"
  "ds_read_b64_tr_b16 a[242:243], v24 offset:14336\n"
  "v_mfma_f32_16x16x32_bf16 v[72:75], v[36:39], a[228:231], v[72:75]\n"
  "ds_read_b64_tr_b16 a[244:245], v25 offset:12288\n"
  "v_mfma_f32_16x16x32_bf16 v[76:79], v[36:39], a[232:235], v[76:79]\n"
  "ds_read_b64_tr_b16 a[246:247], v25 offset:14336\n"
  "v_mfma_f32_16x16x32_bf16 v[80:83], v[36:39], a[236:239], v[80:83]\n"
  "ds_read_b64_tr_b16 a[248:249], v24 offset:12800\n"
  "v_mfma_f32_16x16x32_bf16 v[84:87], v[40:43], a[224:227], v[84:87]\n"
  "ds_read_b64_tr_b16 a[250:251], v24 offset:14848\n"
  "v_mfma_f32_16x16x32_bf16 v[88:91], v[40:43], a[228:231], v[88:91]\n"
  "ds_read_b64_tr_b16 a[252:253], v25 offset:12800\n"
  "ds_read_b64_tr_b16 a[254:255], v25 offset:14848\n"
  "v_mfma_f32_16x16x32_bf16 v[92:95], v[40:43], a[232:235], v[92:95]\n"
  "ds_read_b128 v[52:55], v23 offset:49152\n"
  "v_mfma_f32_16x16x32_bf16 v[96:99], v[40:43], a[236:239], v[96:99]\n"
  "ds_read_b128 v[56:59], v23 offset:53248\n"
  "v_mfma_f32_16x16x32_bf16 v[100:103], v[44:47], a[224:227], v[100:103]\n"
  "ds_read_b128 v[60:63], v23 offset:57344\n"
  "v_mfma_f32_16x16x32_bf16 v[104:107], v[44:47], a[228:231], v[104:107]\n"
  "ds_read_b128 v[64:67], v23 offset:61440\n"
  "v_mfma_f32_16x16x32_bf16 v[108:111], v[44:47], a[232:235], v[108:111]\n"
  "v_mfma_f32_16x16x32_bf16 v[112:115], v[44:47], a[236:239], v[112:115]\n"
  "v_mfma_f32_16x16x32_bf16 v[116:119], v[48:51], a[224:227], v[116:119]\n"
  "v_mfma_f32_16x16x32_bf16 v[120:123], v[48:51], a[228:231], v[120:123]\n"
  "v_mfma_f32_16x16x32_bf16 v[124:127], v[48:51], a[232:235], v[124:127]\n"
  "v_mfma_f32_16x16x32_bf16 v[128:131], v[48:51], a[236:239], v[128:131]\n"
  "s_waitcnt vmcnt(63) lgkmcnt(0)\n"
  "s_barrier\n"
  "v_mfma_f32_16x16x32_bf16 v[68:71], v[52:55], a[240:243], v[68:71]\n"
  "ds_read_b128 v[36:39], v20 offset:0\n"
  "v_mfma_f32_16x16x32_bf16 v[72:75], v[52:55], a[244:247], v[72:75]\n"
  "v_mfma_f32_16x16x32_bf16 v[76:79], v[52:55], a[248:251], v[76:79]\n"
  "ds_read_b128 v[40:43], v20 offset:4096\n"
  "v_mfma_f32_16x16x32_bf16 v[80:83], v[52:55], a[252:255], v[80:83]\n"
  "v_mfma_f32_16x16x32_bf16 v[84:87], v[56:59], a[240:243], v[84:87]\n"
  "ds_read_b128 v[44:47], v20 offset:8192\n"
  "v_mfma_f32_16x16x32_bf16 v[88:91], v[56:59], a[244:247], v[88:91]\n"
  "v_mfma_f32_16x16x32_bf16 v[92:95], v[56:59], a[248:251], v[92:95]\n"
  "ds_read_b128 v[48:51], v20 offset:12288\n"
  "v_mfma_f32_16x16x32_bf16 v[96:99], v[56:59], a[252:255], v[96:99]\n"
  "v_mfma_f32_16x16x32_bf16 v[100:103], v[60:63], a[240:243], v[100:103]\n"
  "v_mfma_f32_16x16x32_bf16 v[104:107], v[60:63], a[244:247], v[104:107]\n"
  "v_mfma_f32_16x16x32_bf16 v[108:111], v[60:63], a[248:251], v[108:111]\n"
  "v_mfma_f32_16x16x32_bf16 v[112:115], v[60:63], a[252:255], v[112:115]\n"
  "v_mfma_f32_16x16x32_bf16 v[116:119], v[64:67], a[240:243], v[116:119]\n"
  "v_mfma_f32_16x16x32_bf16 v[120:123], v[64:67], a[244:247], v[120:123]\n"
  "s_add_u32 s83, %[rot], 3\n"
  "s_and_b32 s83, s83, 3\n"
  "s_lshl_b32 s83, s83, 8\n"
  "v_mfma_f32_16x16x32_bf16 v[124:127], v[64:67], a[248:251], v[124:127]\n"
  "s_add_u32 m0, s72, 49152\n"
  "v_mfma_f32_16x16x32_bf16 v[128:131], v[64:67], a[252:255], v[128:131]\n"
  "s_add_u32 s64, s83, 65536\n"
  "s_waitcnt lgkmcnt(0)\n"
  "v_mfma_f32_16x16x32_bf16 v[132:135], v[36:39], a[0:3], 0\n"
  "ds_read_b128 v[52:55], v21 offset:0\n"
  "buffer_load_dwordx4 v16, %[srdC], s64 offen lds\n"
  "v_mfma_f32_16x16x32_bf16 v[136:139], v[36:39], a[4:7], 0\n"
  "v_mfma_f32_16x16x32_bf16 v[140:143], v[36:39], a[8:11], 0\n"
  "ds_read_b128 v[56:59], v21 offset:4096\n"
  "s_add_u32 m0, s72, 50176\n"
  "v_mfma_f32_16x16x32_bf16 v[144:147], v[36:39], a[12:15], 0\n"
  "s_add_u32 s64, s83, 65536\n"
  "v_mfma_f32_16x16x32_bf16 v[148:151], v[40:43], a[0:3], 0\n"
  "ds_read_b128 v[60:63], v21 offset:8192\n"
  "buffer_load_dwordx4 v17, %[srdC], s64 offen lds\n"
  "v_mfma_f32_16x16x32_bf16 v[152:155], v[40:43], a[4:7], 0\n"
  "v_mfma_f32_16x16x32_bf16 v[156:159], v[40:43], a[8:11], 0\n"
  "ds_read_b128 v[64:67], v21 offset:12288\n"
  "v_mfma_f32_16x16x32_bf16 v[160:163], v[40:43], a[12:15], 0\n"
  "s_add_u32 m0, s72, 51200\n"
  "v_mfma_f32_16x16x32_bf16 v[164:167], v[44:47], a[0:3], 0\n"
  "s_add_u32 s64, s83, 65536\n"
  "v_mfma_f32_16x16x32_bf16 v[168:171], v[44:47], a[4:7], 0\n"
  "buffer_load_dwordx4 v18, %[srdC], s64 offen lds\n"
  "v_mfma_f32_16x16x32_bf16 v[172:175], v[44:47], a[8:11], 0\n"
  "v_mfma_f32_16x16x32_bf16 v[176:179], v[44:47], a[12:15], 0\n"
  "s_add_u32 m0, s72, 52224\n"
  "v_mfma_f32_16x16x32_bf16 v[180:183], v[48:51], a[0:3], 0\n"
  "s_add_u32 s64, s83, 65536\n"
  "v_mfma_f32_16x16x32_bf16 v[184:187], v[48:51], a[4:7], 0\n"
  "buffer_load_dwordx4 v19, %[srdC], s64 offen lds\n"
  "v_mfma_f32_16x16x32_bf16 v[188:191], v[48:51], a[8:11], 0\n"
  "v_mfma_f32_16x16x32_bf16 v[192:195], v[48:51], a[12:15], 0\n"
  "s_waitcnt lgkmcnt(0)\n"
  "v_mfma_f32_16x16x32_bf16 v[132:135], v[52:55], a[16:19], v[132:135]\n"
  "ds_read_b128 v[36:39], v22 offset:0\n"
  "v_mfma_f32_16x16x32_bf16 v[136:139], v[52:55], a[20:23], v[136:139]\n"
  "v_mfma_f32_16x16x32_bf16 v[140:143], v[52:55], a[24:27], v[140:143]\n"
  "ds_read_b128 v[40:43], v22 offset:4096\n"
  "v_mfma_f32_16x16x32_bf16 v[144:147], v[52:55], a[28:31], v[144:147]\n"
  "v_mfma_f32_16x16x32_bf16 v[148:151], v[56:59], a[16:19], v[148:151]\n"
  "ds_read_b128 v[44:47], v22 offset:8192\n"
  "v_mfma_f32_16x16x32_bf16 v[152:155], v[56:59], a[20:23], v[152:155]\n"
  "v_mfma_f32_16x16x32_bf16 v[156:159], v[56:59], a[24:27], v[156:159]\n"
  "ds_read_b128 v[48:51], v22 offset:12288\n"
  "v_mfma_f32_16x16x32_bf16 v[160:163], v[56:59], a[28:31], v[160:163]\n"
  "v_mfma_f32_16x16x32_bf16 v[164:167], v[60:63], a[16:19], v[164:167]\n"
  "v_mfma_f32_16x16x32_bf16 v[168:171], v[60:63], a[20:23], v[168:171]\n"
  "v_mfma_f32_16x16x32_bf16 v[172:175], v[60:63], a[24:27], v[172:175]\n"
  "v_cvt_pk_bf16_f32 v68, v68, v69\n"
  "v_cvt_pk_bf16_f32 v69, v70, v71\n"
  "v_mfma_f32_16x16x32_bf16 v[176:179], v[60:63], a[28:31], v[176:179]\n"
  "v_cvt_pk_bf16_f32 v70, v84, v85\n"
  "v_cvt_pk_bf16_f32 v71, v86, v87\n"
  "v_mfma_f32_16x16x32_bf16 v[180:183], v[64:67], a[16:19], v[180:183]\n"
  "buffer_store_dwordx4 v[68:71], %[L16], %[srdWS], s66 offen sc1\n"
  "v_mfma_f32_16x16x32_bf16 v[184:187], v[64:67], a[20:23], v[184:187]\n"
  "v_mfma_f32_16x16x32_bf16 v[188:191], v[64:67], a[24:27], v[188:191]\n"
  "v_mfma_f32_16x16x32_bf16 v[192:195], v[64:67], a[28:31], v[192:195]\n"
  "s_waitcnt lgkmcnt(0)\n"
  "v_mfma_f32_16x16x32_bf16 v[132:135], v[36:39], a[32:35], v[132:135]\n"
  "ds_read_b128 v[52:55], v23 offset:0\n"
  "v_mfma_f32_16x16x32_bf16 v[136:139], v[36:39], a[36:39], v[136:139]\n"
  "v_mfma_f32_16x16x32_bf16 v[140:143], v[36:39], a[40:43], v[140:143]\n"
  "ds_read_b128 v[56:59], v23 offset:4096\n"
  "s_add_u32 s86, %[rotU], 0\n"
  "s_and_b32 s86, s86, 3\n"
  "s_mul_i32 s86, s86, 352256\n"
  "s_add_u32 m0, s74, 65536\n"
  "s_add_u32 s64, s86, 0\n"
  "buffer_load_dwordx4 %[voffW1], %[srdW1], s64 offen lds\n"
  "v_mfma_f32_16x16x32_bf16 v[144:147], v[36:39], a[44:47], v[144:147]\n"
  "v_mfma_f32_16x16x32_bf16 v[148:151], v[40:43], a[32:35], v[148:151]\n"
  "ds_read_b128 v[60:63], v23 offset:8192\n"
  "v_mfma_f32_16x16x32_bf16 v[152:155], v[40:43], a[36:39], v[152:155]\n"
  "v_mfma_f32_16x16x32_bf16 v[156:159], v[40:43], a[40:43], v[156:159]\n"
  "ds_read_b128 v[64:67], v23 offset:12288\n"
  "v_mfma_f32_16x16x32_bf16 v[160:163], v[40:43], a[44:47], v[160:163]\n"
  "v_mfma_f32_16x16x32_bf16 v[164:167], v[44:47], a[32:35], v[164:167]\n"
  "v_mfma_f32_16x16x32_bf16 v[168:171], v[44:47], a[36:39], v[168:171]\n"
  "v_mfma_f32_16x16x32_bf16 v[172:175], v[44:47], a[40:43], v[172:175]\n"
  "v_mfma_f32_16x16x32_bf16 v[176:179], v[44:47], a[44:47], v[176:179]\n"
  "v_mfma_f32_16x16x32_bf16 v[180:183], v[48:51], a[32:35], v[180:183]\n"
  "v_mfma_f32_16x16x32_bf16 v[184:187], v[48:51], a[36:39], v[184:187]\n"
  "v_mfma_f32_16x16x32_bf16 v[188:191], v[48:51], a[40:43], v[188:191]\n"
  "v_mfma_f32_16x16x32_bf16 v[192:195], v[48:51], a[44:47], v[192:195]\n"
  "s_waitcnt vmcnt(10) lgkmcnt(0)\n"
  "s_barrier\n"
  "v_mfma_f32_16x16x32_bf16 v[132:135], v[52:55], a[48:51], v[132:135]\n"
  "ds_read_b128 v[36:39], v20 offset:16384\n"
  "v_mfma_f32_16x16x32_bf16 v[136:139], v[52:55], a[52:55], v[136:139]\n"
  "v_mfma_f32_16x16x32_bf16 v[140:143], v[52:55], a[56:59], v[140:143]\n"
  "ds_read_b128 v[40:43], v20 offset:20480\n"
  "s_add_u32 s83, %[rot], 0\n"
  "s_and_b32 s83, s83, 3\n"
  "s_lshl_b32 s83, s83, 8\n"
  "s_add_u32 m0, s72, 0\n"
  "s_add_u32 s64, s83, 131072\n"
  "buffer_load_dwordx4 v16, %[srdC], s64 offen lds\n"
  "v_mfma_f32_16x16x32_bf16 v[144:147], v[52:55], a[60:63], v[144:147]\n"
  "v_mfma_f32_16x16x32_bf16 v[148:151], v[56:59], a[48:51], v[148:151]\n"
  "ds_read_b128 v[44:47], v20 offset:24576\n"
  "s_add_u32 m0, s72, 1024\n"
  "v_mfma_f32_16x16x32_bf16 v[152:155], v[56:59], a[52:55], v[152:155]\n"
  "s_add_u32 s64, s83, 131072\n"
  "v_mfma_f32_16x16x32_bf16 v[156:159], v[56:59], a[56:59], v[156:159]\n"
  "ds_read_b128 v[48:51], v20 offset:28672\n"
  "buffer_load_dwordx4 v17, %[srdC], s64 offen lds\n"
  "v_mfma_f32_16x16x32_bf16 v[160:163], v[56:59], a[60:63], v[160:163]\n"
  "v_cvt_pk_bf16_f32 v72, v72, v73\n"
  "v_cvt_pk_bf16_f32 v73, v74, v75\n"
  "v_mfma_f32_16x16x32_bf16 v[164:167], v[60:63], a[48:51], v[164:167]\n"
  "v_cvt_pk_bf16_f32 v74, v88, v89\n"
  "v_cvt_pk_bf16_f32 v75, v90, v91\n"
  "v_mfma_f32_16x16x32_bf16 v[168:171], v[60:63], a[52:55], v[168:171]\n"
  "s_add_u32 m0, s72, 2048\n"
  "s_add_u32 s66, s66, s67\n"
  "v_mfma_f32_16x16x32_bf16 v[172:175], v[60:63], a[56:59], v[172:175]\n"
  "s_add_u32 s64, s83, 131072\n"
  "buffer_store_dwordx4 v[72:75], %[L16], %[srdWS], s66 offen sc1\n"
  "v_mfma_f32_16x16x32_bf16 v[176:179], v[60:63], a[60:63], v[176:179]\n"
  "buffer_load_dwordx4 v18, %[srdC], s64 offen lds\n"
  "v_mfma_f32_16x16x32_bf16 v[180:183], v[64:67], a[48:51], v[180:183]\n"
  "v_mfma_f32_16x16x32_bf16 v[184:187], v[64:67], a[52:55], v[184:187]\n"
  "s_add_u32 m0, s72, 3072\n"
  "v_mfma_f32_16x16x32_bf16 v[188:191], v[64:67], a[56:59], v[188:191]\n"
  "s_add_u32 s64, s83, 131072\n"
  "v_mfma_f32_16x16x32_bf16 v[192:195], v[64:67], a[60:63], v[192:195]\n"
  "buffer_load_dwordx4 v19, %[srdC], s64 offen lds\n"
  "s_waitcnt lgkmcnt(0)\n"
  "v_mfma_f32_16x16x32_bf16 v[132:135], v[36:39], a[64:67], v[132:135]\n"
  "ds_read_b128 v[52:55], v21 offset:16384\n"
  "v_mfma_f32_16x16x32_bf16 v[136:139], v[36:39], a[68:71], v[136:139]\n"
  "v_mfma_f32_16x16x32_bf16 v[140:143], v[36:39], a[72:75], v[140:143]\n"
  "ds_read_b128 v[56:59], v21 offset:20480\n"
  "s_add_u32 m0, s74, 66560\n"
  "v_mfma_f32_16x16x32_bf16 v[144:147], v[36:39], a[76:79], v[144:147]\n"
  "s_add_u32 s64, s86, 64\n"
  "v_mfma_f32_16x16x32_bf16 v[148:151], v[40:43], a[64:67], v[148:151]\n"
  "ds_read_b128 v[60:63], v21 offset:24576\n"
  "buffer_load_dwordx4 %[voffW1], %[srdW1], s64 offen lds\n"
  "v_mfma_f32_16x16x32_bf16 v[152:155], v[40:43], a[68:71], v[152:155]\n"
  "v_mfma_f32_16x16x32_bf16 v[156:159], v[40:43], a[72:75], v[156:159]\n"
  "ds_read_b128 v[64:67], v21 offset:28672\n"
  "v_mfma_f32_16x16x32_bf16 v[160:163], v[40:43], a[76:79], v[160:163]\n"
  "v_mfma_f32_16x16x32_bf16 v[164:167], v[44:47], a[64:67], v[164:167]\n"
  "v_mfma_f32_16x16x32_bf16 v[168:171], v[44:47], a[68:71], v[168:171]\n"
  "v_mfma_f32_16x16x32_bf16 v[172:175], v[44:47], a[72:75], v[172:175]\n"
  "v_mfma_f32_16x16x32_bf16 v[176:179], v[44:47], a[76:79], v[176:179]\n"
  "v_mfma_f32_16x16x32_bf16 v[180:183], v[48:51], a[64:67], v[180:183]\n"
  "v_mfma_f32_16x16x32_bf16 v[184:187], v[48:51], a[68:71], v[184:187]\n"
  "v_mfma_f32_16x16x32_bf16 v[188:191], v[48:51], a[72:75], v[188:191]\n"
  "v_mfma_f32_16x16x32_bf16 v[192:195], v[48:51], a[76:79], v[192:195]\n"
  "s_waitcnt lgkmcnt(0)\n"
  "v_mfma_f32_16x16x32_bf16 v[132:135], v[52:55], a[80:83], v[132:135]\n"
  "ds_read_b128 v[36:39], v22 offset:16384\n"
  "v_mfma_f32_16x16x32_bf16 v[136:139], v[52:55], a[84:87], v[136:139]\n"
  "v_mfma_f32_16x16x32_bf16 v[140:143], v[52:55], a[88:91], v[140:143]\n"
  "ds_read_b128 v[40:43], v22 offset:20480\n"
  "v_cvt_pk_bf16_f32 v76, v76, v77\n"
  "v_mfma_f32_16x16x32_bf16 v[144:147], v[52:55], a[92:95], v[144:147]\n"
  "v_cvt_pk_bf16_f32 v77, v78, v79\n"
  "v_cvt_pk_bf16_f32 v78, v92, v93\n"
  "v_mfma_f32_16x16x32_bf16 v[148:151], v[56:59], a[80:83], v[148:151]\n"
  "ds_read_b128 v[44:47], v22 offset:24576\n"
  "s_add_u32 m0, s74, 67584\n"
  "v_mfma_f32_16x16x32_bf16 v[152:155], v[56:59], a[84:87], v[152:155]\n"
  "s_add_u32 s64, s86, 128\n"
  "v_cvt_pk_bf16_f32 v79, v94, v95\n"
  "v_mfma_f32_16x16x32_bf16 v[156:159], v[56:59], a[88:91], v[156:159]\n"
  "ds_read_b128 v[48:51], v22 offset:28672\n"
  "buffer_load_dwordx4 %[voffW1], %[srdW1], s64 offen lds\n"
  "v_mfma_f32_16x16x32_bf16 v[160:163], v[56:59], a[92:95], v[160:163]\n"
  "s_add_u32 s66, s66, s67\n"
  "buffer_store_dwordx4 v[76:79], %[L16], %[srdWS], s66 offen sc1\n"
  "v_mfma_f32_16x16x32_bf16 v[164:167], v[60:63], a[80:83], v[164:167]\n"
  "v_mfma_f32_16x16x32_bf16 v[168:171], v[60:63], a[84:87], v[168:171]\n"
  "v_mfma_f32_16x16x32_bf16 v[172:175], v[60:63], a[88:91], v[172:175]\n"
  "v_mfma_f32_16x16x32_bf16 v[176:179], v[60:63], a[92:95], v[176:179]\n"
  "v_mfma_f32_16x16x32_bf16 v[180:183], v[64:67], a[80:83], v[180:183]\n"
  "v_mfma_f32_16x16x32_bf16 v[184:187], v[64:67], a[84:87], v[184:187]\n"
  "v_mfma_f32_16x16x32_bf16 v[188:191], v[64:67], a[88:91], v[188:191]\n"
  "v_mfma_f32_16x16x32_bf16 v[192:195], v[64:67], a[92:95], v[192:195]\n"
  "s_waitcnt lgkmcnt(0)\n"
  "v_mfma_f32_16x16x32_bf16 v[132:135], v[36:39], a[96:99], v[132:135]\n"
  "ds_read_b128 v[52:55], v23 offset:16384\n"
  "v_mfma_f32_16x16x32_bf16 v[136:139], v[36:39], a[100:103], v[136:139]\n"
  "v_mfma_f32_16x16x32_bf16 v[140:143], v[36:39], a[104:107], v[140:143]\n"
  "ds_read_b128 v[56:59], v23 offset:20480\n"
  "v_mfma_f32_16x16x32_bf16 v[144:147], v[36:39], a[108:111], v[144:147]\n"
  "v_mfma_f32_16x16x32_bf16 v[148:151], v[40:43], a[96:99], v[148:151]\n"
  "ds_read_b128 v[60:63], v23 offset:24576\n"
  "v_mfma_f32_16x16x32_bf16 v[152:155], v[40:43], a[100:103], v[152:155]\n"
  "v_mfma_f32_16x16x32_bf16 v[156:159], v[40:43], a[104:107], v[156:159]\n"
  "ds_read_b128 v[64:67], v23 offset:28672\n"
  "s_add_u32 m0, s74, 68608\n"
  "v_mfma_f32_16x16x32_bf16 v[160:163], v[40:43], a[108:111], v[160:163]\n"
  "s_add_u32 s64, s86, 11008\n"
  "v_mfma_f32_16x16x32_bf16 v[164:167], v[44:47], a[96:99], v[164:167]\n"
  "buffer_load_dwordx4 %[voffW1], %[srdW1], s64 offen lds\n"
  "v_mfma_f32_16x16x32_bf16 v[168:171], v[44:47], a[100:103], v[168:171]\n"
  "v_mfma_f32_16x16x32_bf16 v[172:175], v[44:47], a[104:107], v[172:175]\n"
  "v_mfma_f32_16x16x32_bf16 v[176:179], v[44:47], a[108:111], v[176:179]\n"
  "v_mfma_f32_16x16x32_bf16 v[180:183], v[48:51], a[96:99], v[180:183]\n"
  "v_mfma_f32_16x16x32_bf16 v[184:187], v[48:51], a[100:103], v[184:187]\n"
  "v_mfma_f32_16x16x32_bf16 v[188:191], v[48:51], a[104:107], v[188:191]\n"
  "v_cvt_pk_bf16_f32 v80, v80, v81\n"
  "v_cvt_pk_bf16_f32 v81, v82, v83\n"
  "v_mfma_f32_16x16x32_bf16 v[192:195], v[48:51], a[108:111], v[192:195]\n"
  "v_cvt_pk_bf16_f32 v82, v96, v97\n"
  "v_cvt_pk_bf16_f32 v83, v98, v99\n"
  "s_waitcnt vmcnt(15) lgkmcnt(0)\n"
  "s_barrier\n"
  "v_mfma_f32_16x16x32_bf16 v[132:135], v[52:55], a[112:115], v[132:135]\n"
  "ds_read_b128 v[36:39], v20 offset:32768\n"
  "s_add_u32 s66, s66, s67\n"
  "v_mfma_f32_16x16x32_bf16 v[136:139], v[52:55], a[116:119], v[136:139]\n"
  "buffer_store_dwordx4 v[80:83], %[L16], %[srdWS], s66 offen sc1\n"
  "v_mfma_f32_16x16x32_bf16 v[140:143], v[52:55], a[120:123], v[140:143]\n"
  "ds_read_b128 v[40:43], v20 offset:36864\n"
  "s_add_u32 s83, %[rot], 1\n"
  "s_and_b32 s83, s83, 3\n"
  "s_lshl_b32 s83, s83, 8\n"
  "s_add_u32 m0, s72, 16384\n"
  "s_add_u32 s64, s83, 131072\n"
  "buffer_load_dwordx4 v16, %[srdC], s64 offen lds\n"
  "v_mfma_f32_16x16x32_bf16 v[144:147], v[52:55], a[124:127], v[144:147]\n"
  "v_mfma_f32_16x16x32_bf16 v[148:151], v[56:59], a[112:115], v[148:151]\n"
  "ds_read_b128 v[44:47], v20 offset:40960\n"
  "s_add_u32 m0, s72, 17408\n"
  "v_mfma_f32_16x16x32_bf16 v[152:155], v[56:59], a[116:119], v[152:155]\n"
  "s_add_u32 s64, s83, 131072\n"
  "v_mfma_f32_16x16x32_bf16 v[156:159], v[56:59], a[120:123], v[156:159]\n"
  "ds_read_b128 v[48:51], v20 offset:45056\n"
  "buffer_load_dwordx4 v17, %[srdC], s64 offen lds\n"
  "v_mfma_f32_16x16x32_bf16 v[160:163], v[56:59], a[124:127], v[160:163]\n"
  "v_mfma_f32_16x16x32_bf16 v[164:167], v[60:63], a[112:115], v[164:167]\n"
  "v_mfma_f32_16x16x32_bf16 v[168:171], v[60:63], a[116:119], v[168:171]\n"
  "s_add_u32 m0, s74, 69632\n"
  "v_mfma_f32_16x16x32_bf16 v[172:175], v[60:63], a[120:123], v[172:175]\n"
  "s_add_u32 s64, s86, 11072\n"
  "v_mfma_f32_16x16x32_bf16 v[176:179], v[60:63], a[124:127], v[176:179]\n"
  "buffer_load_dwordx4 %[voffW1], %[srdW1], s64 offen lds\n"
  "v_mfma_f32_16x16x32_bf16 v[180:183], v[64:67], a[112:115], v[180:183]\n"
  "v_mfma_f32_16x16x32_bf16 v[184:187], v[64:67], a[116:119], v[184:187]\n"
  "v_mfma_f32_16x16x32_bf16 v[188:191], v[64:67], a[120:123], v[188:191]\n"
  "s_add_u32 m0, s72, 18432\n"
  "v_mfma_f32_16x16x32_bf16 v[192:195], v[64:67], a[124:127], v[192:195]\n"
  "s_add_u32 s64, s83, 131072\n"
  "s_waitcnt lgkmcnt(0)\n"
  "v_mfma_f32_16x16x32_bf16 v[132:135], v[36:39], a[128:131], v[132:135]\n"
  "ds_read_b128 v[52:55], v21 offset:32768\n"
  "buffer_load_dwordx4 v18, %[srdC], s64 offen lds\n"
  "v_mfma_f32_16x16x32_bf16 v[136:139], v[36:39], a[132:135], v[136:139]\n"
  "v_mfma_f32_16x16x32_bf16 v[140:143], v[36:39], a[136:139], v[140:143]\n"
  "ds_read_b128 v[56:59], v21 offset:36864\n"
  "s_add_u32 m0, s72, 19456\n"
  "v_mfma_f32_16x16x32_bf16 v[144:147], v[36:39], a[140:143], v[144:147]\n"
  "s_add_u32 s64, s83, 131072\n"
  "v_mfma_f32_16x16x32_bf16 v[148:151], v[40:43], a[128:131], v[148:151]\n"
  "ds_read_b128 v[60:63], v21 offset:40960\n"
  "buffer_load_dwordx4 v19, %[srdC], s64 offen lds\n"
  "v_mfma_f32_16x16x32_bf16 v[152:155], v[40:43], a[132:135], v[152:155]\n"
  "v_mfma_f32_16x16x32_bf16 v[156:159], v[40:43], a[136:139], v[156:159]\n"
  "ds_read_b128 v[64:67], v21 offset:45056\n"
  "v_mfma_f32_16x16x32_bf16 v[160:163], v[40:43], a[140:143], v[160:163]\n"
  "v_mfma_f32_16x16x32_bf16 v[164:167], v[44:47], a[128:131], v[164:167]\n"
  "v_mfma_f32_16x16x32_bf16 v[168:171], v[44:47], a[132:135], v[168:171]\n"
  "v_mfma_f32_16x16x32_bf16 v[172:175], v[44:47], a[136:139], v[172:175]\n"
  "v_cvt_pk_bf16_f32 v100, v100, v101\n"
  "v_cvt_pk_bf16_f32 v101, v102, v103\n"
  "v_mfma_f32_16x16x32_bf16 v[176:179], v[44:47], a[140:143], v[176:179]\n"
  "s_add_u32 m0, s74, 70656\n"
  "v_cvt_pk_bf16_f32 v102, v116, v117\n"
  "v_mfma_f32_16x16x32_bf16 v[180:183], v[48:51], a[128:131], v[180:183]\n"
  "s_add_u32 s64, s86, 11136\n"
  "v_cvt_pk_bf16_f32 v103, v118, v119\n"
  "v_mfma_f32_16x16x32_bf16 v[184:187], v[48:51], a[132:135], v[184:187]\n"
  "buffer_load_dwordx4 %[voffW1], %[srdW1], s64 offen lds\n"
  "s_add_u32 s66, s66, s68\n"
  "v_mfma_f32_16x16x32_bf16 v[188:191], v[48:51], a[136:139], v[188:191]\n"
  "buffer_store_dwordx4 v[100:103], %[L16], %[srdWS], s66 offen sc1\n"
  "v_mfma_f32_16x16x32_bf16 v[192:195], v[48:51], a[140:143], v[192:195]\n"
  "s_waitcnt lgkmcnt(0)\n"
  "v_mfma_f32_16x16x32_bf16 v[132:135], v[52:55], a[144:147], v[132:135]\n"
  "ds_read_b128 v[36:39], v22 offset:32768\n"
  "v_mfma_f32_16x16x32_bf16 v[136:139], v[52:55], a[148:151], v[136:139]\n"
  "v_mfma_f32_16x16x32_bf16 v[140:143], v[52:55], a[152:155], v[140:143]\n"
  "ds_read_b128 v[40:43], v22 offset:36864\n"
  "v_mfma_f32_16x16x32_bf16 v[144:147], v[52:55], a[156:159], v[144:147]\n"
  "v_mfma_f32_16x16x32_bf16 v[148:151], v[56:59], a[144:147], v[148:151]\n"
  "ds_read_b128 v[44:47], v22 offset:40960\n"
  "v_mfma_f32_16x16x32_bf16 v[152:155], v[56:59], a[148:151], v[152:155]\n"
  "v_mfma_f32_16x16x32_bf16 v[156:159], v[56:59], a[152:155], v[156:159]\n"
  "ds_read_b128 v[48:51], v22 offset:45056\n"
  "v_mfma_f32_16x16x32_bf16 v[160:163], v[56:59], a[156:159], v[160:163]\n"
  "v_mfma_f32_16x16x32_bf16 v[164:167], v[60:63], a[144:147], v[164:167]\n"
  "v_mfma_f32_16x16x32_bf16 v[168:171], v[60:63], a[148:151], v[168:171]\n"
  "v_mfma_f32_16x16x32_bf16 v[172:175], v[60:63], a[152:155], v[172:175]\n"
  "v_mfma_f32_16x16x32_bf16 v[176:179], v[60:63], a[156:159], v[176:179]\n"
  "v_mfma_f32_16x16x32_bf16 v[180:183], v[64:67], a[144:147], v[180:183]\n"
  "s_add_u32 s86, %[rotU], 1\n"
  "s_and_b32 s86, s86, 3\n"
  "s_mul_i32 s86, s86, 352256\n"
  "v_mfma_f32_16x16x32_bf16 v[184:187], v[64:67], a[148:151], v[184:187]\n"
  "s_add_u32 m0, s74, 90112\n"
  "v_mfma_f32_16x16x32_bf16 v[188:191], v[64:67], a[152:155], v[188:191]\n"
  "s_add_u32 s64, s86, 0\n"
  "v_mfma_f32_16x16x32_bf16 v[192:195], v[64:67], a[156:159], v[192:195]\n"
  "buffer_load_dwordx4 %[voffW1], %[srdW1], s64 offen lds\n"
  "s_waitcnt lgkmcnt(0)\n"
  "v_mfma_f32_16x16x32_bf16 v[132:135], v[36:39], a[160:163], v[132:135]\n"
  "ds_read_b128 v[52:55], v23 offset:32768\n"
  "v_mfma_f32_16x16x32_bf16 v[136:139], v[36:39], a[164:167], v[136:139]\n"
  "v_mfma_f32_16x16x32_bf16 v[140:143], v[36:39], a[168:171], v[140:143]\n"
  "ds_read_b128 v[56:59], v23 offset:36864\n"
  "v_mfma_f32_16x16x32_bf16 v[144:147], v[36:39], a[172:175], v[144:147]\n"
  "v_mfma_f32_16x16x32_bf16 v[148:151], v[40:43], a[160:163], v[148:151]\n"
  "ds_read_b128 v[60:63], v23 offset:40960\n"
  "v_mfma_f32_16x16x32_bf16 v[152:155], v[40:43], a[164:167], v[152:155]\n"
  "v_mfma_f32_16x16x32_bf16 v[156:159], v[40:43], a[168:171], v[156:159]\n"
  "ds_read_b128 v[64:67], v23 offset:45056\n"
  "v_cvt_pk_bf16_f32 v104, v104, v105\n"
  "v_mfma_f32_16x16x32_bf16 v[160:163], v[40:43], a[172:175], v[160:163]\n"
  "v_cvt_pk_bf16_f32 v105, v106, v107\n"
  "v_cvt_pk_bf16_f32 v106, v120, v121\n"
  "v_mfma_f32_16x16x32_bf16 v[164:167], v[44:47], a[160:163], v[164:167]\n"
  "v_cvt_pk_bf16_f32 v107, v122, v123\n"
  "s_add_u32 s66, s66, s67\n"
  "v_mfma_f32_16x16x32_bf16 v[168:171], v[44:47], a[164:167], v[168:171]\n"
  "buffer_store_dwordx4 v[104:107], %[L16], %[srdWS], s66 offen sc1\n"
  "v_mfma_f32_16x16x32_bf16 v[172:175], v[44:47], a[168:171], v[172:175]\n"
  "v_mfma_f32_16x16x32_bf16 v[176:179], v[44:47], a[172:175], v[176:179]\n"
  "v_mfma_f32_16x16x32_bf16 v[180:183], v[48:51], a[160:163], v[180:183]\n"
  "v_mfma_f32_16x16x32_bf16 v[184:187], v[48:51], a[164:167], v[184:187]\n"
  "v_mfma_f32_16x16x32_bf16 v[188:191], v[48:51], a[168:171], v[188:191]\n"
  "v_mfma_f32_16x16x32_bf16 v[192:195], v[48:51], a[172:175], v[192:195]\n"
  "s_add_u32 m0, s74, 91136\n"
  "s_waitcnt vmcnt(21) lgkmcnt(0)\n"
  "s_barrier\n"
  "v_mfma_f32_16x16x32_bf16 v[132:135], v[52:55], a[176:179], v[132:135]\n"
  "ds_read_b128 v[36:39], v20 offset:49152\n"
  "s_add_u32 s64, s86, 64\n"
  "v_mfma_f32_16x16x32_bf16 v[136:139], v[52:55], a[180:183], v[136:139]\n"
  "buffer_load_dwordx4 %[voffW1], %[srdW1], s64 offen lds\n"
  "v_mfma_f32_16x16x32_bf16 v[140:143], v[52:55], a[184:187], v[140:143]\n"
  "ds_read_b128 v[40:43], v20 offset:53248\n"
  "v_mfma_f32_16x16x32_bf16 v[144:147], v[52:55], a[188:191], v[144:147]\n"
  "s_add_u32 s83, %[rot], 2\n"
  "s_and_b32 s83, s83, 3\n"
  "s_lshl_b32 s83, s83, 8\n"
  "v_mfma_f32_16x16x32_bf16 v[148:151], v[56:59], a[176:179], v[148:151]\n"
  "ds_read_b128 v[44:47], v20 offset:57344\n"
  "s_add_u32 m0, s72, 32768\n"
  "v_mfma_f32_16x16x32_bf16 v[152:155], v[56:59], a[180:183], v[152:155]\n"
  "s_add_u32 s64, s83, 131072\n"
  "v_mfma_f32_16x16x32_bf16 v[156:159], v[56:59], a[184:187], v[156:159]\n"
  "ds_read_b128 v[48:51], v20 offset:61440\n"
  "buffer_load_dwordx4 v16, %[srdC], s64 offen lds\n"
  "v_mfma_f32_16x16x32_bf16 v[160:163], v[56:59], a[188:191], v[160:163]\n"
  "v_mfma_f32_16x16x32_bf16 v[164:167], v[60:63], a[176:179], v[164:167]\n"
  "s_add_u32 m0, s72, 33792\n"
  "v_mfma_f32_16x16x32_bf16 v[168:171], v[60:63], a[180:183], v[168:171]\n"
  "s_add_u32 s64, s83, 131072\n"
  "v_mfma_f32_16x16x32_bf16 v[172:175], v[60:63], a[184:187], v[172:175]\n"
  "buffer_load_dwordx4 v17, %[srdC], s64 offen lds\n"
  "v_mfma_f32_16x16x32_bf16 v[176:179], v[60:63], a[188:191], v[176:179]\n"
  "v_mfma_f32_16x16x32_bf16 v[180:183], v[64:67], a[176:179], v[180:183]\n"
  "v_mfma_f32_16x16x32_bf16 v[184:187], v[64:67], a[180:183], v[184:187]\n"
  "s_add_u32 m0, s72, 34816\n"
  "v_mfma_f32_16x16x32_bf16 v[188:191], v[64:67], a[184:187], v[188:191]\n"
  "s_add_u32 s64, s83, 131072\n"
  "v_mfma_f32_16x16x32_bf16 v[192:195], v[64:67], a[188:191], v[192:195]\n"
  "buffer_load_dwordx4 v18, %[srdC], s64 offen lds\n"
  "s_waitcnt lgkmcnt(0)\n"
  "v_mfma_f32_16x16x32_bf16 v[132:135], v[36:39], a[192:195], v[132:135]\n"
  "ds_read_b128 v[52:55], v21 offset:49152\n"
  "v_mfma_f32_16x16x32_bf16 v[136:139], v[36:39], a[196:199], v[136:139]\n"
  "s_add_u32 m0, s72, 35840\n"
  "v_mfma_f32_16x16x32_bf16 v[140:143], v[36:39], a[200:203], v[140:143]\n"
  "ds_read_b128 v[56:59], v21 offset:53248\n"
  "s_add_u32 s64, s83, 131072\n"
  "v_mfma_f32_16x16x32_bf16 v[144:147], v[36:39], a[204:207], v[144:147]\n"
  "buffer_load_dwordx4 v19, %[srdC], s64 offen lds\n"
  "v_cvt_pk_bf16_f32 v108, v108, v109\n"
  "v_mfma_f32_16x16x32_bf16 v[148:151], v[40:43], a[192:195], v[148:151]\n"
  "ds_read_b128 v[60:63], v21 offset:57344\n"
  "v_cvt_pk_bf16_f32 v109, v110, v111\n"
  "v_mfma_f32_16x16x32_bf16 v[152:155], v[40:43], a[196:199], v[152:155]\n"
  "v_cvt_pk_bf16_f32 v110, v124, v125\n"
  "v_cvt_pk_bf16_f32 v111, v126, v127\n"
  "v_mfma_f32_16x16x32_bf16 v[156:159], v[40:43], a[200:203], v[156:159]\n"
  "ds_read_b128 v[64:67], v21 offset:61440\n"
  "s_add_u32 m0, s74, 92160\n"
  "v_mfma_f32_16x16x32_bf16 v[160:163], v[40:43], a[204:207], v[160:163]\n"
  "s_add_u32 s64, s86, 128\n"
  "s_add_u32 s66, s66, s67\n"
  "v_mfma_f32_16x16x32_bf16 v[164:167], v[44:47], a[192:195], v[164:167]\n"
  "buffer_load_dwordx4 %[voffW1], %[srdW1], s64 offen lds\n"
  "buffer_store_dwordx4 v[108:111], %[L16], %[srdWS], s66 offen sc1\n"
  "v_mfma_f32_16x16x32_bf16 v[168:171], v[44:47], a[196:199], v[168:171]\n"
  "v_mfma_f32_16x16x32_bf16 v[172:175], v[44:47], a[200:203], v[172:175]\n"
  "v_mfma_f32_16x16x32_bf16 v[176:179], v[44:47], a[204:207], v[176:179]\n"
  "v_mfma_f32_16x16x32_bf16 v[180:183], v[48:51], a[192:195], v[180:183]\n"
  "v_mfma_f32_16x16x32_bf16 v[184:187], v[48:51], a[196:199], v[184:187]\n"
  "v_mfma_f32_16x16x32_bf16 v[188:191], v[48:51], a[200:203], v[188:191]\n"
  "v_mfma_f32_16x16x32_bf16 v[192:195], v[48:51], a[204:207], v[192:195]\n"
  "s_waitcnt lgkmcnt(0)\n"
  "v_mfma_f32_16x16x32_bf16 v[132:135], v[52:55], a[208:211], v[132:135]\n"
  "ds_read_b128 v[36:39], v22 offset:49152\n"
  "v_mfma_f32_16x16x32_bf16 v[136:139], v[52:55], a[212:215], v[136:139]\n"
  "v_mfma_f32_16x16x32_bf16 v[140:143], v[52:55], a[216:219], v[140:143]\n"
  "ds_read_b128 v[40:43], v22 offset:53248\n"
  "v_mfma_f32_16x16x32_bf16 v[144:147], v[52:55], a[220:223], v[144:147]\n"
  "v_mfma_f32_16x16x32_bf16 v[148:151], v[56:59], a[208:211], v[148:151]\n"
  "ds_read_b128 v[44:47], v22 offset:57344\n"
  "v_mfma_f32_16x16x32_bf16 v[152:155], v[56:59], a[212:215], v[152:155]\n"
  "v_mfma_f32_16x16x32_bf16 v[156:159], v[56:59], a[216:219], v[156:159]\n"
  "ds_read_b128 v[48:51], v22 offset:61440\n"
  "v_mfma_f32_16x16x32_bf16 v[160:163], v[56:59], a[220:223], v[160:163]\n"
  "v_mfma_f32_16x16x32_bf16 v[164:167], v[60:63], a[208:211], v[164:167]\n"
  "s_add_u32 m0, s74, 93184\n"
  "v_mfma_f32_16x16x32_bf16 v[168:171], v[60:63], a[212:215], v[168:171]\n"
  "s_add_u32 s64, s86, 11008\n"
  "v_mfma_f32_16x16x32_bf16 v[172:175], v[60:63], a[216:219], v[172:175]\n"
  "buffer_load_dwordx4 %[voffW1], %[srdW1], s64 offen lds\n"
  "v_mfma_f32_16x16x32_bf16 v[176:179], v[60:63], a[220:223], v[176:179]\n"
  "v_mfma_f32_16x16x32_bf16 v[180:183], v[64:67], a[208:211], v[180:183]\n"
  "v_mfma_f32_16x16x32_bf16 v[184:187], v[64:67], a[212:215], v[184:187]\n"
  "v_mfma_f32_16x16x32_bf16 v[188:191], v[64:67], a[216:219], v[188:191]\n"
  "v_cvt_pk_bf16_f32 v112, v112, v113\n"
  "v_cvt_pk_bf16_f32 v113, v114, v115\n"
  "v_mfma_f32_16x16x32_bf16 v[192:195], v[64:67], a[220:223], v[192:195]\n"
  "v_cvt_pk_bf16_f32 v114, v128, v129\n"
  "v_cvt_pk_bf16_f32 v115, v130, v131\n"
  "s_waitcnt lgkmcnt(0)\n"
  "v_mfma_f32_16x16x32_bf16 v[132:135], v[36:39], a[224:227], v[132:135]\n"
  "ds_read_b128 v[52:55], v23 offset:49152\n"
  "s_add_u32 s66, s66, s67\n"
  "v_mfma_f32_16x16x32_bf16 v[136:139], v[36:39], a[228:231], v[136:139]\n"
  "buffer_store_dwordx4 v[112:115], %[L16], %[srdWS], s66 offen sc1\n"
  "v_mfma_f32_16x16x32_bf16 v[140:143], v[36:39], a[232:235], v[140:143]\n"
  "ds_read_b128 v[56:59], v23 offset:53248\n"
  "v_mfma_f32_16x16x32_bf16 v[144:147], v[36:39], a[236:239], v[144:147]\n"
  "v_mfma_f32_16x16x32_bf16 v[148:151], v[40:43], a[224:227], v[148:151]\n"
  "ds_read_b128 v[60:63], v23 offset:57344\n"
  "v_mfma_f32_16x16x32_bf16 v[152:155], v[40:43], a[228:231], v[152:155]\n"
  "v_mfma_f32_16x16x32_bf16 v[156:159], v[40:43], a[232:235], v[156:159]\n"
  "ds_read_b128 v[64:67], v23 offset:61440\n"
  "v_mfma_f32_16x16x32_bf16 v[160:163], v[40:43], a[236:239], v[160:163]\n"
  "v_mfma_f32_16x16x32_bf16 v[164:167], v[44:47], a[224:227], v[164:167]\n"
  "v_mfma_f32_16x16x32_bf16 v[168:171], v[44:47], a[228:231], v[168:171]\n"
  "v_mfma_f32_16x16x32_bf16 v[172:175], v[44:47], a[232:235], v[172:175]\n"
  "s_add_u32 m0, s74, 94208\n"
  "v_mfma_f32_16x16x32_bf16 v[176:179], v[44:47], a[236:239], v[176:179]\n"
  "s_add_u32 s64, s86, 11072\n"
  "v_mfma_f32_16x16x32_bf16 v[180:183], v[48:51], a[224:227], v[180:183]\n"
  "buffer_load_dwordx4 %[voffW1], %[srdW1], s64 offen lds\n"
  "v_mfma_f32_16x16x32_bf16 v[184:187], v[48:51], a[228:231], v[184:187]\n"
  "v_mfma_f32_16x16x32_bf16 v[188:191], v[48:51], a[232:235], v[188:191]\n"
  "v_mfma_f32_16x16x32_bf16 v[192:195], v[48:51], a[236:239], v[192:195]\n"
  "s_waitcnt vmcnt(24) lgkmcnt(0)\n"
  "s_barrier\n"
  "v_mfma_f32_16x16x32_bf16 v[132:135], v[52:55], a[240:243], v[132:135]\n"
  "ds_read_b128 v[36:39], v20 offset:0\n"
  "v_mfma_f32_16x16x32_bf16 v[136:139], v[52:55], a[244:247], v[136:139]\n"
  "v_mfma_f32_16x16x32_bf16 v[140:143], v[52:55], a[248:251], v[140:143]\n"
  "ds_read_b128 v[40:43], v20 offset:4096\n"
  "s_add_u32 s83, %[rot], 3\n"
  "s_and_b32 s83, s83, 3\n"
  "s_lshl_b32 s83, s83, 8\n"
  "s_add_u32 m0, s72, 49152\n"
  "s_add_u32 s64, s83, 131072\n"
  "buffer_load_dwordx4 v16, %[srdC], s64 offen lds\n"
  "v_mfma_f32_16x16x32_bf16 v[144:147], v[52:55], a[252:255], v[144:147]\n"
  "v_mfma_f32_16x16x32_bf16 v[148:151], v[56:59], a[240:243], v[148:151]\n"
  "ds_read_b128 v[44:47], v20 offset:8192\n"
  "s_add_u32 m0, s72, 50176\n"
  "v_mfma_f32_16x16x32_bf16 v[152:155], v[56:59], a[244:247], v[152:155]\n"
  "s_add_u32 s64, s83, 131072\n"
  "v_mfma_f32_16x16x32_bf16 v[156:159], v[56:59], a[248:251], v[156:159]\n"
  "ds_read_b128 v[48:51], v20 offset:12288\n"
  "buffer_load_dwordx4 v17, %[srdC], s64 offen lds\n"
  "v_mfma_f32_16x16x32_bf16 v[160:163], v[56:59], a[252:255], v[160:163]\n"
  "v_mfma_f32_16x16x32_bf16 v[164:167], v[60:63], a[240:243], v[164:167]\n"
  "v_mfma_f32_16x16x32_bf16 v[168:171], v[60:63], a[244:247], v[168:171]\n"
  "s_add_u32 m0, s72, 51200\n"
  "v_mfma_f32_16x16x32_bf16 v[172:175], v[60:63], a[248:251], v[172:175]\n"
  "s_add_u32 s64, s83, 131072\n"
  "v_mfma_f32_16x16x32_bf16 v[176:179], v[60:63], a[252:255], v[176:179]\n"
  "buffer_load_dwordx4 v18, %[srdC], s64 offen lds\n"
  "v_mfma_f32_16x16x32_bf16 v[180:183], v[64:67], a[240:243], v[180:183]\n"
  "v_mfma_f32_16x16x32_bf16 v[184:187], v[64:67], a[244:247], v[184:187]\n"
  "v_mfma_f32_16x16x32_bf16 v[188:191], v[64:67], a[248:251], v[188:191]\n"
  "s_add_u32 m0, s74, 95232\n"
  "v_mfma_f32_16x16x32_bf16 v[192:195], v[64:67], a[252:255], v[192:195]\n"
  "s_add_u32 s64, s86, 11136\n"
  "s_waitcnt lgkmcnt(0)\n"
  "v_mfma_f32_16x16x32_bf16 v[68:71], v[36:39], a[0:3], 0\n"
  "ds_read_b128 v[52:55], v21 offset:0\n"
  "buffer_load_dwordx4 %[voffW1], %[srdW1], s64 offen lds\n"
  "v_mfma_f32_16x16x32_bf16 v[72:75], v[36:39], a[4:7], 0\n"
  "v_mfma_f32_16x16x32_bf16 v[76:79], v[36:39], a[8:11], 0\n"
  "ds_read_b128 v[56:59], v21 offset:4096\n"
  "s_add_u32 m0, s72, 52224\n"
  "v_mfma_f32_16x16x32_bf16 v[80:83], v[36:39], a[12:15], 0\n"
  "s_add_u32 s64, s83, 131072\n"
  "v_mfma_f32_16x16x32_bf16 v[84:87], v[40:43], a[0:3], 0\n"
  "ds_read_b128 v[60:63], v21 offset:8192\n"
  "buffer_load_dwordx4 v19, %[srdC], s64 offen lds\n"
  "v_mfma_f32_16x16x32_bf16 v[88:91], v[40:43], a[4:7], 0\n"
  "v_mfma_f32_16x16x32_bf16 v[92:95], v[40:43], a[8:11], 0\n"
  "ds_read_b128 v[64:67], v21 offset:12288\n"
  "v_mfma_f32_16x16x32_bf16 v[96:99], v[40:43], a[12:15], 0\n"
  "v_mfma_f32_16x16x32_bf16 v[100:103], v[44:47], a[0:3], 0\n"
  "v_mfma_f32_16x16x32_bf16 v[104:107], v[44:47], a[4:7], 0\n"
  "v_mfma_f32_16x16x32_bf16 v[108:111], v[44:47], a[8:11], 0\n"
  "v_mfma_f32_16x16x32_bf16 v[112:115], v[44:47], a[12:15], 0\n"
  "v_mfma_f32_16x16x32_bf16 v[116:119], v[48:51], a[0:3], 0\n"
  "v_mfma_f32_16x16x32_bf16 v[120:123], v[48:51], a[4:7], 0\n"
  "v_mfma_f32_16x16x32_bf16 v[124:127], v[48:51], a[8:11], 0\n"
  "v_mfma_f32_16x16x32_bf16 v[128:131], v[48:51], a[12:15], 0\n"
  "s_add_u32 s86, %[rotU], 2\n"
  "s_and_b32 s86, s86, 3\n"
  "s_mul_i32 s86, s86, 352256\n"
  "s_waitcnt lgkmcnt(0)\n"
  "v_mfma_f32_16x16x32_bf16 v[68:71], v[52:55], a[16:19], v[68:71]\n"
  "ds_read_b128 v[36:39], v22 offset:0\n"
  "s_add_u32 m0, s74, 114688\n"
  "v_mfma_f32_16x16x32_bf16 v[72:75], v[52:55], a[20:23], v[72:75]\n"
  "s_add_u32 s64, s86, 0\n"
  "v_mfma_f32_16x16x32_bf16 v[76:79], v[52:55], a[24:27], v[76:79]\n"
  "ds_read_b128 v[40:43], v22 offset:4096\n"
  "buffer_load_dwordx4 %[voffW1], %[srdW1], s64 offen lds\n"
  "v_mfma_f32_16x16x32_bf16 v[80:83], v[52:55], a[28:31], v[80:83]\n"
  "v_mfma_f32_16x16x32_bf16 v[84:87], v[56:59], a[16:19], v[84:87]\n"
  "ds_read_b128 v[44:47], v22 offset:8192\n"
  "v_mfma_f32_16x16x32_bf16 v[88:91], v[56:59], a[20:23], v[88:91]\n"
  "v_mfma_f32_16x16x32_bf16 v[92:95], v[56:59], a[24:27], v[92:95]\n"
  "ds_read_b128 v[48:51], v22 offset:12288\n"
  "v_mfma_f32_16x16x32_bf16 v[96:99], v[56:59], a[28:31], v[96:99]\n"
  "v_mfma_f32_16x16x32_bf16 v[100:103], v[60:63], a[16:19], v[100:103]\n"
  "v_mfma_f32_16x16x32_bf16 v[104:107], v[60:63], a[20:23], v[104:107]\n"
  "v_mfma_f32_16x16x32_bf16 v[108:111], v[60:63], a[24:27], v[108:111]\n"
  "v_cvt_pk_bf16_f32 v132, v132, v133\n"
  "v_cvt_pk_bf16_f32 v133, v134, v135\n"
  "v_mfma_f32_16x16x32_bf16 v[112:115], v[60:63], a[28:31], v[112:115]\n"
  "v_cvt_pk_bf16_f32 v134, v148, v149\n"
  "v_cvt_pk_bf16_f32 v135, v150, v151\n"
  "v_mfma_f32_16x16x32_bf16 v[116:119], v[64:67], a[16:19], v[116:119]\n"
  "s_add_u32 s66, s66, s69\n"
  "buffer_store_dwordx4 v[132:135], %[L16], %[srdWS], s66 offen sc1\n"
  "v_mfma_f32_16x16x32_bf16 v[120:123], v[64:67], a[20:23], v[120:123]\n"
  "v_mfma_f32_16x16x32_bf16 v[124:127], v[64:67], a[24:27], v[124:127]\n"
  "v_mfma_f32_16x16x32_bf16 v[128:131], v[64:67], a[28:31], v[128:131]\n"
  "s_waitcnt lgkmcnt(0)\n"
  "v_mfma_f32_16x16x32_bf16 v[68:71], v[36:39], a[32:35], v[68:71]\n"
  "ds_read_b128 v[52:55], v23 offset:0\n"
  "v_mfma_f32_16x16x32_bf16 v[72:75], v[36:39], a[36:39], v[72:75]\n"
  "v_mfma_f32_16x16x32_bf16 v[76:79], v[36:39], a[40:43], v[76:79]\n"
  "ds_read_b128 v[56:59], v23 offset:4096\n"
  "s_add_u32 m0, s74, 115712\n"
  "v_mfma_f32_16x16x32_bf16 v[80:83], v[36:39], a[44:47], v[80:83]\n"
  "s_add_u32 s64, s86, 64\n"
  "v_mfma_f32_16x16x32_bf16 v[84:87], v[40:43], a[32:35], v[84:87]\n"
  "ds_read_b128 v[60:63], v23 offset:8192\n"
  "buffer_load_dwordx4 %[voffW1], %[srdW1], s64 offen lds\n"
  "v_mfma_f32_16x16x32_bf16 v[88:91], v[40:43], a[36:39], v[88:91]\n"
  "v_mfma_f32_16x16x32_bf16 v[92:95], v[40:43], a[40:43], v[92:95]\n"
  "ds_read_b128 v[64:67], v23 offset:12288\n"
  "v_mfma_f32_16x16x32_bf16 v[96:99], v[40:43], a[44:47], v[96:99]\n"
  "v_mfma_f32_16x16x32_bf16 v[100:103], v[44:47], a[32:35], v[100:103]\n"
  "v_mfma_f32_16x16x32_bf16 v[104:107], v[44:47], a[36:39], v[104:107]\n"
  "v_mfma_f32_16x16x32_bf16 v[108:111], v[44:47], a[40:43], v[108:111]\n"
  "v_mfma_f32_16x16x32_bf16 v[112:115], v[44:47], a[44:47], v[112:115]\n"
  "v_mfma_f32_16x16x32_bf16 v[116:119], v[48:51], a[32:35], v[116:119]\n"
  "v_mfma_f32_16x16x32_bf16 v[120:123], v[48:51], a[36:39], v[120:123]\n"
  "v_mfma_f32_16x16x32_bf16 v[124:127], v[48:51], a[40:43], v[124:127]\n"
  "v_mfma_f32_16x16x32_bf16 v[128:131], v[48:51], a[44:47], v[128:131]\n"
  "s_waitcnt vmcnt(22) lgkmcnt(0)\n"
  "s_barrier\n"
  "v_mfma_f32_16x16x32_bf16 v[68:71], v[52:55], a[48:51], v[68:71]\n"
  "ds_read_b128 v[36:39], v20 offset:16384\n"
  "v_mfma_f32_16x16x32_bf16 v[72:75], v[52:55], a[52:55], v[72:75]\n"
  "v_mfma_f32_16x16x32_bf16 v[76:79], v[52:55], a[56:59], v[76:79]\n"
  "ds_read_b128 v[40:43], v20 offset:20480\n"
  "s_add_u32 s83, %[rot], 0\n"
  "s_and_b32 s83, s83, 3\n"
  "s_lshl_b32 s83, s83, 8\n"
  "s_add_u32 m0, s72, 0\n"
  "s_add_u32 s64, s83, 196608\n"
  "buffer_load_dwordx4 v16, %[srdC], s64 offen lds\n"
  "v_mfma_f32_16x16x32_bf16 v[80:83], v[52:55], a[60:63], v[80:83]\n"
  "v_mfma_f32_16x16x32_bf16 v[84:87], v[56:59], a[48:51], v[84:87]\n"
  "ds_read_b128 v[44:47], v20 offset:24576\n"
  "v_mfma_f32_16x16x32_bf16 v[88:91], v[56:59], a[52:55], v[88:91]\n"
  "s_add_u32 m0, s74, 116736\n"
  "v_mfma_f32_16x16x32_bf16 v[92:95], v[56:59], a[56:59], v[92:95]\n"
  "ds_read_b128 v[48:51], v20 offset:28672\n"
  "s_add_u32 s64, s86, 128\n"
  "v_mfma_f32_16x16x32_bf16 v[96:99], v[56:59], a[60:63], v[96:99]\n"
  "buffer_load_dwordx4 %[voffW1], %[srdW1], s64 offen lds\n"
  "v_cvt_pk_bf16_f32 v136, v136, v137\n"
  "v_mfma_f32_16x16x32_bf16 v[100:103], v[60:63], a[48:51], v[100:103]\n"
  "v_cvt_pk_bf16_f32 v137, v138, v139\n"
  "v_cvt_pk_bf16_f32 v138, v152, v153\n"
  "v_mfma_f32_16x16x32_bf16 v[104:107], v[60:63], a[52:55], v[104:107]\n"
  "s_add_u32 m0, s72, 1024\n"
  "v_cvt_pk_bf16_f32 v139, v154, v155\n"
  "v_mfma_f32_16x16x32_bf16 v[108:111], v[60:63], a[56:59], v[108:111]\n"
  "s_add_u32 s64, s83, 196608\n"
  "s_add_u32 s66, s66, s67\n"
  "v_mfma_f32_16x16x32_bf16 v[112:115], v[60:63], a[60:63], v[112:115]\n"
  "buffer_load_dwordx4 v17, %[srdC], s64 offen lds\n"
  "buffer_store_dwordx4 v[136:139], %[L16], %[srdWS], s66 offen sc1\n"
  "v_mfma_f32_16x16x32_bf16 v[116:119], v[64:67], a[48:51], v[116:119]\n"
  "v_mfma_f32_16x16x32_bf16 v[120:123], v[64:67], a[52:55], v[120:123]\n"
  "v_mfma_f32_16x16x32_bf16 v[124:127], v[64:67], a[56:59], v[124:127]\n"
  "s_add_u32 m0, s72, 2048\n"
  "v_mfma_f32_16x16x32_bf16 v[128:131], v[64:67], a[60:63], v[128:131]\n"
  "s_add_u32 s64, s83, 196608\n"
  "s_waitcnt lgkmcnt(0)\n"
  "v_mfma_f32_16x16x32_bf16 v[68:71], v[36:39], a[64:67], v[68:71]\n"
  "ds_read_b128 v[52:55], v21 offset:16384\n"
  "buffer_load_dwordx4 v18, %[srdC], s64 offen lds\n"
  "v_mfma_f32_16x16x32_bf16 v[72:75], v[36:39], a[68:71], v[72:75]\n"
  "v_mfma_f32_16x16x32_bf16 v[76:79], v[36:39], a[72:75], v[76:79]\n"
  "ds_read_b128 v[56:59], v21 offset:20480\n"
  "s_add_u32 m0, s72, 3072\n"
  "v_mfma_f32_16x16x32_bf16 v[80:83], v[36:39], a[76:79], v[80:83]\n"
  "s_add_u32 s64, s83, 196608\n"
  "v_mfma_f32_16x16x32_bf16 v[84:87], v[40:43], a[64:67], v[84:87]\n"
  "ds_read_b128 v[60:63], v21 offset:24576\n"
  "buffer_load_dwordx4 v19, %[srdC], s64 offen lds\n"
  "v_mfma_f32_16x16x32_bf16 v[88:91], v[40:43], a[68:71], v[88:91]\n"
  "v_mfma_f32_16x16x32_bf16 v[92:95], v[40:43], a[72:75], v[92:95]\n"
  "ds_read_b128 v[64:67], v21 offset:28672\n"
  "v_mfma_f32_16x16x32_bf16 v[96:99], v[40:43], a[76:79], v[96:99]\n"
  "s_add_u32 m0, s74, 117760\n"
  "v_mfma_f32_16x16x32_bf16 v[100:103], v[44:47], a[64:67], v[100:103]\n"
  "s_add_u32 s64, s86, 11008\n"
  "v_mfma_f32_16x16x32_bf16 v[104:107], v[44:47], a[68:71], v[104:107]\n"
  "buffer_load_dwordx4 %[voffW1], %[srdW1], s64 offen lds\n"
  "v_mfma_f32_16x16x32_bf16 v[108:111], v[44:47], a[72:75], v[108:111]\n"
  "v_mfma_f32_16x16x32_bf16 v[112:115], v[44:47], a[76:79], v[112:115]\n"
  "v_mfma_f32_16x16x32_bf16 v[116:119], v[48:51], a[64:67], v[116:119]\n"
  "v_mfma_f32_16x16x32_bf16 v[120:123], v[48:51], a[68:71], v[120:123]\n"
  "v_mfma_f32_16x16x32_bf16 v[124:127], v[48:51], a[72:75], v[124:127]\n"
  "v_mfma_f32_16x16x32_bf16 v[128:131], v[48:51], a[76:79], v[128:131]\n"
  "s_waitcnt lgkmcnt(0)\n"
  "v_mfma_f32_16x16x32_bf16 v[68:71], v[52:55], a[80:83], v[68:71]\n"
  "ds_read_b128 v[36:39], v22 offset:16384\n"
  "v_mfma_f32_16x16x32_bf16 v[72:75], v[52:55], a[84:87], v[72:75]\n"
  "v_mfma_f32_16x16x32_bf16 v[76:79], v[52:55], a[88:91], v[76:79]\n"
  "ds_read_b128 v[40:43], v22 offset:20480\n"
  "v_cvt_pk_bf16_f32 v140, v140, v141\n"
  "v_mfma_f32_16x16x32_bf16 v[80:83], v[52:55], a[92:95], v[80:83]\n"
  "v_cvt_pk_bf16_f32 v141, v142, v143\n"
  "v_cvt_pk_bf16_f32 v142, v156, v157\n"
  "v_mfma_f32_16x16x32_bf16 v[84:87], v[56:59], a[80:83], v[84:87]\n"
  "ds_read_b128 v[44:47], v22 offset:24576\n"
  "v_cvt_pk_bf16_f32 v143, v158, v159\n"
  "v_mfma_f32_16x16x32_bf16 v[88:91], v[56:59], a[84:87], v[88:91]\n"
  "s_add_u32 s66, s66, s67\n"
  "buffer_store_dwordx4 v[140:143], %[L16], %[srdWS], s66 offen sc1\n"
  "v_mfma_f32_16x16x32_bf16 v[92:95], v[56:59], a[88:91], v[92:95]\n"
  "ds_read_b128 v[48:51], v22 offset:28672\n"
  "v_mfma_f32_16x16x32_bf16 v[96:99], v[56:59], a[92:95], v[96:99]\n"
  "v_mfma_f32_16x16x32_bf16 v[100:103], v[60:63], a[80:83], v[100:103]\n"
  "buffer_load_dwordx2 v[28:29], %[pollLN], %[srdWS], 0 offen sc1\n"
  "v_mfma_f32_16x16x32_bf16 v[104:107], v[60:63], a[84:87], v[104:107]\n"
  "s_add_u32 m0, s74, 118784\n"
  "v_mfma_f32_16x16x32_bf16 v[108:111], v[60:63], a[88:91], v[108:111]\n"
  "s_add_u32 s64, s86, 11072\n"
  "v_mfma_f32_16x16x32_bf16 v[112:115], v[60:63], a[92:95], v[112:115]\n"
  "buffer_load_dwordx4 %[voffW1], %[srdW1], s64 offen lds\n"
  "v_mfma_f32_16x16x32_bf16 v[116:119], v[64:67], a[80:83], v[116:119]\n"
  "v_mfma_f32_16x16x32_bf16 v[120:123], v[64:67], a[84:87], v[120:123]\n"
  "v_mfma_f32_16x16x32_bf16 v[124:127], v[64:67], a[88:91], v[124:127]\n"
  "v_mfma_f32_16x16x32_bf16 v[128:131], v[64:67], a[92:95], v[128:131]\n"
  "s_waitcnt lgkmcnt(0)\n"
  "v_mfma_f32_16x16x32_bf16 v[68:71], v[36:39], a[96:99], v[68:71]\n"
  "ds_read_b128 v[52:55], v23 offset:16384\n"
  "v_mfma_f32_16x16x32_bf16 v[72:75], v[36:39], a[100:103], v[72:75]\n"
  "v_mfma_f32_16x16x32_bf16 v[76:79], v[36:39], a[104:107], v[76:79]\n"
  "ds_read_b128 v[56:59], v23 offset:20480\n"
  "v_mfma_f32_16x16x32_bf16 v[80:83], v[36:39], a[108:111], v[80:83]\n"
  "v_mfma_f32_16x16x32_bf16 v[84:87], v[40:43], a[96:99], v[84:87]\n"
  "ds_read_b128 v[60:63], v23 offset:24576\n"
  "v_mfma_f32_16x16x32_bf16 v[88:91], v[40:43], a[100:103], v[88:91]\n"
  "v_mfma_f32_16x16x32_bf16 v[92:95], v[40:43], a[104:107], v[92:95]\n"
  "ds_read_b128 v[64:67], v23 offset:28672\n"
  "v_mfma_f32_16x16x32_bf16 v[96:99], v[40:43], a[108:111], v[96:99]\n"
  "v_mfma_f32_16x16x32_bf16 v[100:103], v[44:47], a[96:99], v[100:103]\n"
  "v_mfma_f32_16x16x32_bf16 v[104:107], v[44:47], a[100:103], v[104:107]\n"
  "v_mfma_f32_16x16x32_bf16 v[108:111], v[44:47], a[104:107], v[108:111]\n"
  "v_mfma_f32_16x16x32_bf16 v[112:115], v[44:47], a[108:111], v[112:115]\n"
  "s_add_u32 m0, s74, 119808\n"
  "v_mfma_f32_16x16x32_bf16 v[116:119], v[48:51], a[96:99], v[116:119]\n"
  "s_add_u32 s64, s86, 11136\n"
  "v_mfma_f32_16x16x32_bf16 v[120:123], v[48:51], a[100:103], v[120:123]\n"
  "buffer_load_dwordx4 %[voffW1], %[srdW1], s64 offen lds\n"
  "v_mfma_f32_16x16x32_bf16 v[124:127], v[48:51], a[104:107], v[124:127]\n"
  "v_cvt_pk_bf16_f32 v144, v144, v145\n"
  "v_cvt_pk_bf16_f32 v145, v146, v147\n"
  "v_mfma_f32_16x16x32_bf16 v[128:131], v[48:51], a[108:111], v[128:131]\n"
  "v_cvt_pk_bf16_f32 v146, v160, v161\n"
  "v_cvt_pk_bf16_f32 v147, v162, v163\n"
  "s_waitcnt vmcnt(24) lgkmcnt(0)\n"
  "s_barrier\n"
  "v_mfma_f32_16x16x32_bf16 v[68:71], v[52:55], a[112:115], v[68:71]\n"
  "ds_read_b128 v[36:39], v20 offset:32768\n"
  "s_add_u32 s66, s66, s67\n"
  "v_mfma_f32_16x16x32_bf16 v[72:75], v[52:55], a[116:119], v[72:75]\n"
  "buffer_store_dwordx4 v[144:147], %[L16], %[srdWS], s66 offen sc1\n"
  "v_mfma_f32_16x16x32_bf16 v[76:79], v[52:55], a[120:123], v[76:79]\n"
  "ds_read_b128 v[40:43], v20 offset:36864\n"
  "s_add_u32 s83, %[rot], 1\n"
  "s_and_b32 s83, s83, 3\n"
  "s_lshl_b32 s83, s83, 8\n"
  "s_add_u32 m0, s72, 16384\n"
  "s_add_u32 s64, s83, 196608\n"
  "buffer_load_dwordx4 v16, %[srdC], s64 offen lds\n"
  "v_mfma_f32_16x16x32_bf16 v[80:83], v[52:55], a[124:127], v[80:83]\n"
  "v_mfma_f32_16x16x32_bf16 v[84:87], v[56:59], a[112:115], v[84:87]\n"
  "ds_read_b128 v[44:47], v20 offset:40960\n"
  "s_add_u32 m0, s72, 17408\n"
  "v_mfma_f32_16x16x32_bf16 v[88:91], v[56:59], a[116:119], v[88:91]\n"
  "s_add_u32 s64, s83, 196608\n"
  "v_mfma_f32_16x16x32_bf16 v[92:95], v[56:59], a[120:123], v[92:95]\n"
  "ds_read_b128 v[48:51], v20 offset:45056\n"
  "buffer_load_dwordx4 v17, %[srdC], s64 offen lds\n"
  "v_mfma_f32_16x16x32_bf16 v[96:99], v[56:59], a[124:127], v[96:99]\n"
  "v_mfma_f32_16x16x32_bf16 v[100:103], v[60:63], a[112:115], v[100:103]\n"
  "v_mfma_f32_16x16x32_bf16 v[104:107], v[60:63], a[116:119], v[104:107]\n"
  "s_add_u32 m0, s72, 18432\n"
  "v_mfma_f32_16x16x32_bf16 v[108:111], v[60:63], a[120:123], v[108:111]\n"
  "s_add_u32 s64, s83, 196608\n"
  "v_mfma_f32_16x16x32_bf16 v[112:115], v[60:63], a[124:127], v[112:115]\n"
  "buffer_load_dwordx4 v18, %[srdC], s64 offen lds\n"
  "v_mfma_f32_16x16x32_bf16 v[116:119], v[64:67], a[112:115], v[116:119]\n"
  "v_mfma_f32_16x16x32_bf16 v[120:123], v[64:67], a[116:119], v[120:123]\n"
  "v_mfma_f32_16x16x32_bf16 v[124:127], v[64:67], a[120:123], v[124:127]\n"
  "s_add_u32 m0, s72, 19456\n"
  "v_mfma_f32_16x16x32_bf16 v[128:131], v[64:67], a[124:127], v[128:131]\n"
  "s_add_u32 s64, s83, 196608\n"
  "s_waitcnt lgkmcnt(0)\n"
  "v_mfma_f32_16x16x32_bf16 v[68:71], v[36:39], a[128:131], v[68:71]\n"
  "ds_read_b128 v[52:55], v21 offset:32768\n"
  "buffer_load_dwordx4 v19, %[srdC], s64 offen lds\n"
  "v_mfma_f32_16x16x32_bf16 v[72:75], v[36:39], a[132:135], v[72:75]\n"
  "s_add_u32 s86, %[rotU], 3\n"
  "s_and_b32 s86, s86, 3\n"
  "s_mul_i32 s86, s86, 352256\n"
  "v_mfma_f32_16x16x32_bf16 v[76:79], v[36:39], a[136:139], v[76:79]\n"
  "ds_read_b128 v[56:59], v21 offset:36864\n"
  "s_add_u32 m0, s74, 139264\n"
  "v_mfma_f32_16x16x32_bf16 v[80:83], v[36:39], a[140:143], v[80:83]\n"
  "s_add_u32 s64, s86, 0\n"
  "v_mfma_f32_16x16x32_bf16 v[84:87], v[40:43], a[128:131], v[84:87]\n"
  "ds_read_b128 v[60:63], v21 offset:40960\n"
  "buffer_load_dwordx4 %[voffW1], %[srdW1], s64 offen lds\n"
  "v_mfma_f32_16x16x32_bf16 v[88:91], v[40:43], a[132:135], v[88:91]\n"
  "v_mfma_f32_16x16x32_bf16 v[92:95], v[40:43], a[136:139], v[92:95]\n"
  "ds_read_b128 v[64:67], v21 offset:45056\n"
  "v_mfma_f32_16x16x32_bf16 v[96:99], v[40:43], a[140:143], v[96:99]\n"
  "v_mfma_f32_16x16x32_bf16 v[100:103], v[44:47], a[128:131], v[100:103]\n"
  "v_mfma_f32_16x16x32_bf16 v[104:107], v[44:47], a[132:135], v[104:107]\n"
  "v_mfma_f32_16x16x32_bf16 v[108:111], v[44:47], a[136:139], v[108:111]\n"
  "v_cvt_pk_bf16_f32 v164, v164, v165\n"
  "v_cvt_pk_bf16_f32 v165, v166, v167\n"
  "v_mfma_f32_16x16x32_bf16 v[112:115], v[44:47], a[140:143], v[112:115]\n"
  "v_cvt_pk_bf16_f32 v166, v180, v181\n"
  "v_cvt_pk_bf16_f32 v167, v182, v183\n"
  "v_mfma_f32_16x16x32_bf16 v[116:119], v[48:51], a[128:131], v[116:119]\n"
  "s_add_u32 s66, s66, s68\n"
  "buffer_store_dwordx4 v[164:167], %[L16], %[srdWS], s66 offen sc1\n"
  "v_mfma_f32_16x16x32_bf16 v[120:123], v[48:51], a[132:135], v[120:123]\n"
  "v_mfma_f32_16x16x32_bf16 v[124:127], v[48:51], a[136:139], v[124:127]\n"
  "v_mfma_f32_16x16x32_bf16 v[128:131], v[48:51], a[140:143], v[128:131]\n"
  "s_waitcnt lgkmcnt(0)\n"
  "v_mfma_f32_16x16x32_bf16 v[68:71], v[52:55], a[144:147], v[68:71]\n"
  "ds_read_b128 v[36:39], v22 offset:32768\n"
  "v_mfma_f32_16x16x32_bf16 v[72:75], v[52:55], a[148:151], v[72:75]\n"
  "v_mfma_f32_16x16x32_bf16 v[76:79], v[52:55], a[152:155], v[76:79]\n"
  "ds_read_b128 v[40:43], v22 offset:36864\n"
  "v_mfma_f32_16x16x32_bf16 v[80:83], v[52:55], a[156:159], v[80:83]\n"
  "s_waitcnt vmcnt(9)\n"
  "v_cmp_eq_u64 vcc, %[tok], v[28:29]\n"
  "s_nop 0\n"
  "s_cmp_eq_u64 vcc, -1\n"
  "s_cbranch_scc0 SPINLNV_%=\n"
  "BACKLNV_%=:\n"
  "v_mfma_f32_16x16x32_bf16 v[84:87], v[56:59], a[144:147], v[84:87]\n"
  "ds_read_b128 v[44:47], v22 offset:40960\n"
  "s_add_u32 m0, s74, 140288\n"
  "v_mfma_f32_16x16x32_bf16 v[88:91], v[56:59], a[148:151], v[88:91]\n"
  "s_add_u32 s64, s86, 64\n"
  "v_mfma_f32_16x16x32_bf16 v[92:95], v[56:59], a[152:155], v[92:95]\n"
  "ds_read_b128 v[48:51], v22 offset:45056\n"
  "buffer_load_dwordx4 %[voffW1], %[srdW1], s64 offen lds\n"
  "v_mfma_f32_16x16x32_bf16 v[96:99], v[56:59], a[156:159], v[96:99]\n"
  "v_mfma_f32_16x16x32_bf16 v[100:103], v[60:63], a[144:147], v[100:103]\n"
  "v_mfma_f32_16x16x32_bf16 v[104:107], v[60:63], a[148:151], v[104:107]\n"
  "v_mfma_f32_16x16x32_bf16 v[108:111], v[60:63], a[152:155], v[108:111]\n"
  "v_mfma_f32_16x16x32_bf16 v[112:115], v[60:63], a[156:159], v[112:115]\n"
  "v_mfma_f32_16x16x32_bf16 v[116:119], v[64:67], a[144:147], v[116:119]\n"
  "v_mfma_f32_16x16x32_bf16 v[120:123], v[64:67], a[148:151], v[120:123]\n"
  "v_mfma_f32_16x16x32_bf16 v[124:127], v[64:67], a[152:155], v[124:127]\n"
  "v_mfma_f32_16x16x32_bf16 v[128:131], v[64:67], a[156:159], v[128:131]\n"
  "s_waitcnt lgkmcnt(0)\n"
  "v_mfma_f32_16x16x32_bf16 v[68:71], v[36:39], a[160:163], v[68:71]\n"
  "ds_read_b128 v[52:55], v23 offset:32768\n"
  "v_mfma_f32_16x16x32_bf16 v[72:75], v[36:39], a[164:167], v[72:75]\n"
  "v_mfma_f32_16x16x32_bf16 v[76:79], v[36:39], a[168:171], v[76:79]\n"
  "ds_read_b128 v[56:59], v23 offset:36864\n"
  "v_mfma_f32_16x16x32_bf16 v[80:83], v[36:39], a[172:175], v[80:83]\n"
  "v_mfma_f32_16x16x32_bf16 v[84:87], v[40:43], a[160:163], v[84:87]\n"
  "ds_read_b128 v[60:63], v23 offset:40960\n"
  "v_mfma_f32_16x16x32_bf16 v[88:91], v[40:43], a[164:167], v[88:91]\n"
  "v_mfma_f32_16x16x32_bf16 v[92:95], v[40:43], a[168:171], v[92:95]\n"
  "ds_read_b128 v[64:67], v23 offset:45056\n"
  "s_add_u32 m0, s74, 141312\n"
  "v_mfma_f32_16x16x32_bf16 v[96:99], v[40:43], a[172:175], v[96:99]\n"
  "s_add_u32 s64, s86, 128\n"
  "v_cvt_pk_bf16_f32 v168, v168, v169\n"
  "v_mfma_f32_16x16x32_bf16 v[100:103], v[44:47], a[160:163], v[100:103]\n"
  "buffer_load_dwordx4 %[voffW1], %[srdW1], s64 offen lds\n"
  "v_cvt_pk_bf16_f32 v169, v170, v171\n"
  "v_mfma_f32_16x16x32_bf16 v[104:107], v[44:47], a[164:167], v[104:107]\n"
  "v_cvt_pk_bf16_f32 v170, v184, v185\n"
  "v_cvt_pk_bf16_f32 v171, v186, v187\n"
  "v_mfma_f32_16x16x32_bf16 v[108:111], v[44:47], a[168:171], v[108:111]\n"
  "s_add_u32 s66, s66, s67\n"
  "buffer_store_dwordx4 v[168:171], %[L16], %[srdWS], s66 offen sc1\n"
  "v_mfma_f32_16x16x32_bf16 v[112:115], v[44:47], a[172:175], v[112:115]\n"
  "v_mfma_f32_16x16x32_bf16 v[116:119], v[48:51], a[160:163], v[116:119]\n"
  "v_mfma_f32_16x16x32_bf16 v[120:123], v[48:51], a[164:167], v[120:123]\n"
  "v_mfma_f32_16x16x32_bf16 v[124:127], v[48:51], a[168:171], v[124:127]\n"
  "v_mfma_f32_16x16x32_bf16 v[128:131], v[48:51], a[172:175], v[128:131]\n"
  "s_waitcnt vmcnt(63) lgkmcnt(0)\n"
  "s_barrier\n"
  "v_mfma_f32_16x16x32_bf16 v[68:71], v[52:55], a[176:179], v[68:71]\n"
  "ds_read_b128 v[36:39], v20 offset:49152\n"
  "v_mfma_f32_16x16x32_bf16 v[72:75], v[52:55], a[180:183], v[72:75]\n"
  "v_mfma_f32_16x16x32_bf16 v[76:79], v[52:55], a[184:187], v[76:79]\n"
  "ds_read_b128 v[40:43], v20 offset:53248\n"
  "s_add_u32 s83, %[rot], 2\n"
  "s_and_b32 s83, s83, 3\n"
  "s_lshl_b32 s83, s83, 8\n"
  "s_add_u32 m0, s72, 32768\n"
  "s_add_u32 s64, s83, 196608\n"
  "buffer_load_dwordx4 v16, %[srdC], s64 offen lds\n"
  "v_mfma_f32_16x16x32_bf16 v[80:83], v[52:55], a[188:191], v[80:83]\n"
  "v_mfma_f32_16x16x32_bf16 v[84:87], v[56:59], a[176:179], v[84:87]\n"
  "ds_read_b128 v[44:47], v20 offset:57344\n"
  "v_mfma_f32_16x16x32_bf16 v[88:91], v[56:59], a[180:183], v[88:91]\n"
  "s_add_u32 m0, s72, 33792\n"
  "v_mfma_f32_16x16x32_bf16 v[92:95], v[56:59], a[184:187], v[92:95]\n"
  "ds_read_b128 v[48:51], v20 offset:61440\n"
  "s_add_u32 s64, s83, 196608\n"
  "v_mfma_f32_16x16x32_bf16 v[96:99], v[56:59], a[188:191], v[96:99]\n"
  "buffer_load_dwordx4 v17, %[srdC], s64 offen lds\n"
  "v_mfma_f32_16x16x32_bf16 v[100:103], v[60:63], a[176:179], v[100:103]\n"
  "v_mfma_f32_16x16x32_bf16 v[104:107], v[60:63], a[180:183], v[104:107]\n"
  "s_add_u32 m0, s74, 142336\n"
  "v_mfma_f32_16x16x32_bf16 v[108:111], v[60:63], a[184:187], v[108:111]\n"
  "s_add_u32 s64, s86, 11008\n"
  "v_mfma_f32_16x16x32_bf16 v[112:115], v[60:63], a[188:191], v[112:115]\n"
  "buffer_load_dwordx4 %[voffW1], %[srdW1], s64 offen lds\n"
  "v_mfma_f32_16x16x32_bf16 v[116:119], v[64:67], a[176:179], v[116:119]\n"
  "v_mfma_f32_16x16x32_bf16 v[120:123], v[64:67], a[180:183], v[120:123]\n"
  "v_mfma_f32_16x16x32_bf16 v[124:127], v[64:67], a[184:187], v[124:127]\n"
  "s_add_u32 m0, s72, 34816\n"
  "v_mfma_f32_16x16x32_bf16 v[128:131], v[64:67], a[188:191], v[128:131]\n"
  "s_add_u32 s64, s83, 196608\n"
  "s_waitcnt lgkmcnt(0)\n"
  "v_mfma_f32_16x16x32_bf16 v[68:71], v[36:39], a[192:195], v[68:71]\n"
  "ds_read_b128 v[52:55], v21 offset:49152\n"
  "buffer_load_dwordx4 v18, %[srdC], s64 offen lds\n"
  "v_mfma_f32_16x16x32_bf16 v[72:75], v[36:39], a[196:199], v[72:75]\n"
  "v_mfma_f32_16x16x32_bf16 v[76:79], v[36:39], a[200:203], v[76:79]\n"
  "ds_read_b128 v[56:59], v21 offset:53248\n"
  "s_add_u32 m0, s72, 35840\n"
  "v_mfma_f32_16x16x32_bf16 v[80:83], v[36:39], a[204:207], v[80:83]\n"
  "s_add_u32 s64, s83, 196608\n"
  "v_cvt_pk_bf16_f32 v172, v172, v173\n"
  "v_mfma_f32_16x16x32_bf16 v[84:87], v[40:43], a[192:195], v[84:87]\n"
  "ds_read_b128 v[60:63], v21 offset:57344\n"
  "buffer_load_dwordx4 v19, %[srdC], s64 offen lds\n"
  "v_mfma_f32_16x16x32_bf16 v[88:91], v[40:43], a[196:199], v[88:91]\n"
  "v_cvt_pk_bf16_f32 v173, v174, v175\n"
  "v_cvt_pk_bf16_f32 v174, v188, v189\n"
  "v_mfma_f32_16x16x32_bf16 v[92:95], v[40:43], a[200:203], v[92:95]\n"
  "ds_read_b128 v[64:67], v21 offset:61440\n"
  "v_cvt_pk_bf16_f32 v175, v190, v191\n"
  "v_mfma_f32_16x16x32_bf16 v[96:99], v[40:43], a[204:207], v[96:99]\n"
  "s_add_u32 s66, s66, s67\n"
  "buffer_store_dwordx4 v[172:175], %[L16], %[srdWS], s66 offen sc1\n"
  "v_mfma_f32_16x16x32_bf16 v[100:103], v[44:47], a[192:195], v[100:103]\n"
  "v_mfma_f32_16x16x32_bf16 v[104:107], v[44:47], a[196:199], v[104:107]\n"
  "v_mfma_f32_16x16x32_bf16 v[108:111], v[44:47], a[200:203], v[108:111]\n"
  "v_mfma_f32_16x16x32_bf16 v[112:115], v[44:47], a[204:207], v[112:115]\n"
  "s_add_u32 m0, s74, 143360\n"
  "v_mfma_f32_16x16x32_bf16 v[116:119], v[48:51], a[192:195], v[116:119]\n"
  "s_add_u32 s64, s86, 11072\n"
  "v_mfma_f32_16x16x32_bf16 v[120:123], v[48:51], a[196:199], v[120:123]\n"
  "buffer_load_dwordx4 %[voffW1], %[srdW1], s64 offen lds\n"
  "v_mfma_f32_16x16x32_bf16 v[124:127], v[48:51], a[200:203], v[124:127]\n"
  "v_mfma_f32_16x16x32_bf16 v[128:131], v[48:51], a[204:207], v[128:131]\n"
  "s_waitcnt lgkmcnt(0)\n"
  "v_mfma_f32_16x16x32_bf16 v[68:71], v[52:55], a[208:211], v[68:71]\n"
  "ds_read_b128 v[36:39], v22 offset:49152\n"
  "v_mfma_f32_16x16x32_bf16 v[72:75], v[52:55], a[212:215], v[72:75]\n"
  "v_mfma_f32_16x16x32_bf16 v[76:79], v[52:55], a[216:219], v[76:79]\n"
  "ds_read_b128 v[40:43], v22 offset:53248\n"
  "v_mfma_f32_16x16x32_bf16 v[80:83], v[52:55], a[220:223], v[80:83]\n"
  "v_mfma_f32_16x16x32_bf16 v[84:87], v[56:59], a[208:211], v[84:87]\n"
  "ds_read_b128 v[44:47], v22 offset:57344\n"
  "v_mfma_f32_16x16x32_bf16 v[88:91], v[56:59], a[212:215], v[88:91]\n"
  "v_mfma_f32_16x16x32_bf16 v[92:95], v[56:59], a[216:219], v[92:95]\n"
  "ds_read_b128 v[48:51], v22 offset:61440\n"
  "v_mfma_f32_16x16x32_bf16 v[96:99], v[56:59], a[220:223], v[96:99]\n"
  "v_mfma_f32_16x16x32_bf16 v[100:103], v[60:63], a[208:211], v[100:103]\n"
  "v_mfma_f32_16x16x32_bf16 v[104:107], v[60:63], a[212:215], v[104:107]\n"
  "v_mfma_f32_16x16x32_bf16 v[108:111], v[60:63], a[216:219], v[108:111]\n"
  "v_mfma_f32_16x16x32_bf16 v[112:115], v[60:63], a[220:223], v[112:115]\n"
  "v_mfma_f32_16x16x32_bf16 v[116:119], v[64:67], a[208:211], v[116:119]\n"
  "v_mfma_f32_16x16x32_bf16 v[120:123], v[64:67], a[212:215], v[120:123]\n"
  "s_add_u32 m0, s74, 144384\n"
  "v_mfma_f32_16x16x32_bf16 v[124:127], v[64:67], a[216:219], v[124:127]\n"
  "s_add_u32 s64, s86, 11136\n"
  "v_cvt_pk_bf16_f32 v176, v176, v177\n"
  "v_mfma_f32_16x16x32_bf16 v[128:131], v[64:67], a[220:223], v[128:131]\n"
  "buffer_load_dwordx4 %[voffW1], %[srdW1], s64 offen lds\n"
  "v_cvt_pk_bf16_f32 v177, v178, v179\n"
  "s_waitcnt lgkmcnt(0)\n"
  "v_mfma_f32_16x16x32_bf16 v[68:71], v[36:39], a[224:227], v[68:71]\n"
  "ds_read_b128 v[52:55], v23 offset:49152\n"
  "v_cvt_pk_bf16_f32 v178, v192, v193\n"
  "v_mfma_f32_16x16x32_bf16 v[72:75], v[36:39], a[228:231], v[72:75]\n"
  "v_cvt_pk_bf16_f32 v179, v194, v195\n"
  "s_add_u32 s66, s66, s67\n"
  "v_mfma_f32_16x16x32_bf16 v[76:79], v[36:39], a[232:235], v[76:79]\n"
  "ds_read_b128 v[56:59], v23 offset:53248\n"
  "buffer_store_dwordx4 v[176:179], %[L16], %[srdWS], s66 offen sc1\n"
  "v_mfma_f32_16x16x32_bf16 v[80:83], v[36:39], a[236:239], v[80:83]\n"
  "v_mfma_f32_16x16x32_bf16 v[84:87], v[40:43], a[224:227], v[84:87]\n"
  "ds_read_b128 v[60:63], v23 offset:57344\n"
  "v_mfma_f32_16x16x32_bf16 v[88:91], v[40:43], a[228:231], v[88:91]\n"
  "v_mfma_f32_16x16x32_bf16 v[92:95], v[40:43], a[232:235], v[92:95]\n"
  "ds_read_b128 v[64:67], v23 offset:61440\n"
  "v_mfma_f32_16x16x32_bf16 v[96:99], v[40:43], a[236:239], v[96:99]\n"
  "v_mfma_f32_16x16x32_bf16 v[100:103], v[44:47], a[224:227], v[100:103]\n"
  "v_mfma_f32_16x16x32_bf16 v[104:107], v[44:47], a[228:231], v[104:107]\n"
  "v_mfma_f32_16x16x32_bf16 v[108:111], v[44:47], a[232:235], v[108:111]\n"
  "v_mfma_f32_16x16x32_bf16 v[112:115], v[44:47], a[236:239], v[112:115]\n"
  "v_mfma_f32_16x16x32_bf16 v[116:119], v[48:51], a[224:227], v[116:119]\n"
  "v_mfma_f32_16x16x32_bf16 v[120:123], v[48:51], a[228:231], v[120:123]\n"
  "v_mfma_f32_16x16x32_bf16 v[124:127], v[48:51], a[232:235], v[124:127]\n"
  "v_mfma_f32_16x16x32_bf16 v[128:131], v[48:51], a[236:239], v[128:131]\n"
  "s_waitcnt vmcnt(63) lgkmcnt(0)\n"
  "s_barrier\n"
  "v_mfma_f32_16x16x32_bf16 v[68:71], v[52:55], a[240:243], v[68:71]\n"
  "ds_read_b128 v[36:39], v20 offset:0\n"
  "v_mfma_f32_16x16x32_bf16 v[72:75], v[52:55], a[244:247], v[72:75]\n"
  "v_mfma_f32_16x16x32_bf16 v[76:79], v[52:55], a[248:251], v[76:79]\n"
  "ds_read_b128 v[40:43], v20 offset:4096\n"
  "s_add_u32 s83, %[rot], 3\n"
  "s_and_b32 s83, s83, 3\n"
  "s_lshl_b32 s83, s83, 8\n"
  "s_add_u32 m0, s72, 49152\n"
  "s_add_u32 s64, s83, 196608\n"
  "buffer_load_dwordx4 v16, %[srdC], s64 offen lds\n"
  "v_mfma_f32_16x16x32_bf16 v[80:83], v[52:55], a[252:255], v[80:83]\n"
  "v_mfma_f32_16x16x32_bf16 v[84:87], v[56:59], a[240:243], v[84:87]\n"
  "ds_read_b128 v[44:47], v20 offset:8192\n"
  "v_mfma_f32_16x16x32_bf16 v[88:91], v[56:59], a[244:247], v[88:91]\n"
  "s_add_u32 m0, s72, 50176\n"
  "v_mfma_f32_16x16x32_bf16 v[92:95], v[56:59], a[248:251], v[92:95]\n"
  "ds_read_b128 v[48:51], v20 offset:12288\n"
  "s_add_u32 s64, s83, 196608\n"
  "v_mfma_f32_16x16x32_bf16 v[96:99], v[56:59], a[252:255], v[96:99]\n"
  "buffer_load_dwordx4 v17, %[srdC], s64 offen lds\n"
  "v_mfma_f32_16x16x32_bf16 v[100:103], v[60:63], a[240:243], v[100:103]\n"
  "v_mfma_f32_16x16x32_bf16 v[104:107], v[60:63], a[244:247], v[104:107]\n"
  "s_add_u32 m0, s72, 51200\n"
  "v_mfma_f32_16x16x32_bf16 v[108:111], v[60:63], a[248:251], v[108:111]\n"
  "s_add_u32 s64, s83, 196608\n"
  "v_mfma_f32_16x16x32_bf16 v[112:115], v[60:63], a[252:255], v[112:115]\n"
  "buffer_load_dwordx4 v18, %[srdC], s64 offen lds\n"
  "v_mfma_f32_16x16x32_bf16 v[116:119], v[64:67], a[240:243], v[116:119]\n"
  "v_mfma_f32_16x16x32_bf16 v[120:123], v[64:67], a[244:247], v[120:123]\n"
  "v_mfma_f32_16x16x32_bf16 v[124:127], v[64:67], a[248:251], v[124:127]\n"
  "s_add_u32 m0, s72, 52224\n"
  "v_mfma_f32_16x16x32_bf16 v[128:131], v[64:67], a[252:255], v[128:131]\n"
  "s_add_u32 s64, s83, 196608\n"
  "s_waitcnt lgkmcnt(0)\n"
  "v_mfma_f32_16x16x32_bf16 v[132:135], v[36:39], a[0:3], 0\n"
  "ds_read_b128 v[52:55], v21 offset:0\n"
  "buffer_load_dwordx4 v19, %[srdC], s64 offen lds\n"
  "v_mfma_f32_16x16x32_bf16 v[136:139], v[36:39], a[4:7], 0\n"
  "v_mfma_f32_16x16x32_bf16 v[140:143], v[36:39], a[8:11], 0\n"
  "ds_read_b128 v[56:59], v21 offset:4096\n"
  "v_mfma_f32_16x16x32_bf16 v[144:147], v[36:39], a[12:15], 0\n"
  "v_mfma_f32_16x16x32_bf16 v[148:151], v[40:43], a[0:3], 0\n"
  "ds_read_b128 v[60:63], v21 offset:8192\n"
  "v_mfma_f32_16x16x32_bf16 v[152:155], v[40:43], a[4:7], 0\n"
  "v_mfma_f32_16x16x32_bf16 v[156:159], v[40:43], a[8:11], 0\n"
  "ds_read_b128 v[64:67], v21 offset:12288\n"
  "v_mfma_f32_16x16x32_bf16 v[160:163], v[40:43], a[12:15], 0\n"
  "v_mfma_f32_16x16x32_bf16 v[164:167], v[44:47], a[0:3], 0\n"
  "v_mfma_f32_16x16x32_bf16 v[168:171], v[44:47], a[4:7], 0\n"
  "v_mfma_f32_16x16x32_bf16 v[172:175], v[44:47], a[8:11], 0\n"
  "v_mfma_f32_16x16x32_bf16 v[176:179], v[44:47], a[12:15], 0\n"
  "v_mfma_f32_16x16x32_bf16 v[180:183], v[48:51], a[0:3], 0\n"
  "v_mfma_f32_16x16x32_bf16 v[184:187], v[48:51], a[4:7], 0\n"
  "v_mfma_f32_16x16x32_bf16 v[188:191], v[48:51], a[8:11], 0\n"
  "v_mfma_f32_16x16x32_bf16 v[192:195], v[48:51], a[12:15], 0\n"
  "s_waitcnt lgkmcnt(0)\n"
  "v_mfma_f32_16x16x32_bf16 v[132:135], v[52:55], a[16:19], v[132:135]\n"
  "ds_read_b128 v[36:39], v22 offset:0\n"
  "v_mfma_f32_16x16x32_bf16 v[136:139], v[52:55], a[20:23], v[136:139]\n"
  "s_add_u32 s96, %[rot], 0\n"
  "s_and_b32 s96, s96, 3\n"
  "s_lshl_b32 s96, s96, 10\n"
  "s_add_u32 s96, s96, %[oX]\n"
  "s_add_u32 s97, s96, 0x4000\n"
  "s_add_u32 s98, s96, 0x8000\n"
  "s_add_u32 s99, s96, 0xc000\n"
  "buffer_load_dwordx4 a[0:3], %[voffX], %[srdWS], s96 offen offset:0 sc1\n"
  "v_mfma_f32_16x16x32_bf16 v[140:143], v[52:55], a[24:27], v[140:143]\n"
  "ds_read_b128 v[40:43], v22 offset:4096\n"
  "v_mfma_f32_16x16x32_bf16 v[144:147], v[52:55], a[28:31], v[144:147]\n"
  "v_mfma_f32_16x16x32_bf16 v[148:151], v[56:59], a[16:19], v[148:151]\n"
  "ds_read_b128 v[44:47], v22 offset:8192\n"
  "v_mfma_f32_16x16x32_bf16 v[152:155], v[56:59], a[20:23], v[152:155]\n"
  "v_mfma_f32_16x16x32_bf16 v[156:159], v[56:59], a[24:27], v[156:159]\n"
  "ds_read_b128 v[48:51], v22 offset:12288\n"
  "v_mfma_f32_16x16x32_bf16 v[160:163], v[56:59], a[28:31], v[160:163]\n"
  "v_mfma_f32_16x16x32_bf16 v[164:167], v[60:63], a[16:19], v[164:167]\n"
  "v_mfma_f32_16x16x32_bf16 v[168:171], v[60:63], a[20:23], v[168:171]\n"
  "v_mfma_f32_16x16x32_bf16 v[172:175], v[60:63], a[24:27], v[172:175]\n"
  "v_cvt_pk_bf16_f32 v68, v68, v69\n"
  "v_cvt_pk_bf16_f32 v69, v70, v71\n"
  "v_mfma_f32_16x16x32_bf16 v[176:179], v[60:63], a[28:31], v[176:179]\n"
  "v_cvt_pk_bf16_f32 v70, v84, v85\n"
  "v_cvt_pk_bf16_f32 v71, v86, v87\n"
  "v_mfma_f32_16x16x32_bf16 v[180:183], v[64:67], a[16:19], v[180:183]\n"
  "s_add_u32 s66, s66, s69\n"
  "buffer_store_dwordx4 v[68:71], %[L16], %[srdWS], s66 offen sc1\n"
  "v_mfma_f32_16x16x32_bf16 v[184:187], v[64:67], a[20:23], v[184:187]\n"
  "v_mfma_f32_16x16x32_bf16 v[188:191], v[64:67], a[24:27], v[188:191]\n"
  "v_mfma_f32_16x16x32_bf16 v[192:195], v[64:67], a[28:31], v[192:195]\n"
  "s_waitcnt lgkmcnt(0)\n"
  "v_mfma_f32_16x16x32_bf16 v[132:135], v[36:39], a[32:35], v[132:135]\n"
  "ds_read_b128 v[52:55], v23 offset:0\n"
  "v_mfma_f32_16x16x32_bf16 v[136:139], v[36:39], a[36:39], v[136:139]\n"
  "buffer_load_dwordx4 a[4:7], %[voffX], %[srdWS], s97 offen offset:0 sc1\n"
  "v_mfma_f32_16x16x32_bf16 v[140:143], v[36:39], a[40:43], v[140:143]\n"
  "ds_read_b128 v[56:59], v23 offset:4096\n"
  "v_mfma_f32_16x16x32_bf16 v[144:147], v[36:39], a[44:47], v[144:147]\n"
  "v_mfma_f32_16x16x32_bf16 v[148:151], v[40:43], a[32:35], v[148:151]\n"
  "ds_read_b128 v[60:63], v23 offset:8192\n"
  "v_mfma_f32_16x16x32_bf16 v[152:155], v[40:43], a[36:39], v[152:155]\n"
  "v_mfma_f32_16x16x32_bf16 v[156:159], v[40:43], a[40:43], v[156:159]\n"
  "ds_read_b128 v[64:67], v23 offset:12288\n"
  "v_mfma_f32_16x16x32_bf16 v[160:163], v[40:43], a[44:47], v[160:163]\n"
  "v_mfma_f32_16x16x32_bf16 v[164:167], v[44:47], a[32:35], v[164:167]\n"
  "v_mfma_f32_16x16x32_bf16 v[168:171], v[44:47], a[36:39], v[168:171]\n"
  "v_mfma_f32_16x16x32_bf16 v[172:175], v[44:47], a[40:43], v[172:175]\n"
  "v_mfma_f32_16x16x32_bf16 v[176:179], v[44:47], a[44:47], v[176:179]\n"
  "v_mfma_f32_16x16x32_bf16 v[180:183], v[48:51], a[32:35], v[180:183]\n"
  "v_mfma_f32_16x16x32_bf16 v[184:187], v[48:51], a[36:39], v[184:187]\n"
  "v_mfma_f32_16x16x32_bf16 v[188:191], v[48:51], a[40:43], v[188:191]\n"
  "v_mfma_f32_16x16x32_bf16 v[192:195], v[48:51], a[44:47], v[192:195]\n"
  "s_waitcnt vmcnt(21) lgkmcnt(0)\n"
  "s_barrier\n"
  "v_mfma_f32_16x16x32_bf16 v[132:135], v[52:55], a[48:51], v[132:135]\n"
  "ds_read_b128 v[36:39], v20 offset:16384\n"
  "v_mfma_f32_16x16x32_bf16 v[136:139], v[52:55], a[52:55], v[136:139]\n"
  "buffer_load_dwordx4 a[8:11], %[voffX], %[srdWS], s98 offen offset:0 sc1\n"
  "v_mfma_f32_16x16x32_bf16 v[140:143], v[52:55], a[56:59], v[140:143]\n"
  "ds_read_b128 v[40:43], v20 offset:20480\n"
  "v_mfma_f32_16x16x32_bf16 v[144:147], v[52:55], a[60:63], v[144:147]\n"
  "v_mfma_f32_16x16x32_bf16 v[148:151], v[56:59], a[48:51], v[148:151]\n"
  "ds_read_b128 v[44:47], v20 offset:24576\n"
  "v_mfma_f32_16x16x32_bf16 v[152:155], v[56:59], a[52:55], v[152:155]\n"
  "v_mfma_f32_16x16x32_bf16 v[156:159], v[56:59], a[56:59], v[156:159]\n"
  "ds_read_b128 v[48:51], v20 offset:28672\n"
  "v_cvt_pk_bf16_f32 v72, v72, v73\n"
  "v_mfma_f32_16x16x32_bf16 v[160:163], v[56:59], a[60:63], v[160:163]\n"
  "v_cvt_pk_bf16_f32 v73, v74, v75\n"
  "v_cvt_pk_bf16_f32 v74, v88, v89\n"
  "v_mfma_f32_16x16x32_bf16 v[164:167], v[60:63], a[48:51], v[164:167]\n"
  "v_cvt_pk_bf16_f32 v75, v90, v91\n"
  "s_add_u32 s66, s66, s67\n"
  "v_mfma_f32_16x16x32_bf16 v[168:171], v[60:63], a[52:55], v[168:171]\n"
  "buffer_store_dwordx4 v[72:75], %[L16], %[srdWS], s66 offen sc1\n"
  "v_mfma_f32_16x16x32_bf16 v[172:175], v[60:63], a[56:59], v[172:175]\n"
  "v_mfma_f32_16x16x32_bf16 v[176:179], v[60:63], a[60:63], v[176:179]\n"
  "v_mfma_f32_16x16x32_bf16 v[180:183], v[64:67], a[48:51], v[180:183]\n"
  "v_mfma_f32_16x16x32_bf16 v[184:187], v[64:67], a[52:55], v[184:187]\n"
  "v_mfma_f32_16x16x32_bf16 v[188:191], v[64:67], a[56:59], v[188:191]\n"
  "v_mfma_f32_16x16x32_bf16 v[192:195], v[64:67], a[60:63], v[192:195]\n"
  "s_waitcnt lgkmcnt(0)\n"
  "v_mfma_f32_16x16x32_bf16 v[132:135], v[36:39], a[64:67], v[132:135]\n"
  "ds_read_b128 v[52:55], v21 offset:16384\n"
  "v_mfma_f32_16x16x32_bf16 v[136:139], v[36:39], a[68:71], v[136:139]\n"
  "buffer_load_dwordx4 a[12:15], %[voffX], %[srdWS], s99 offen offset:0 sc1\n"
  "v_mfma_f32_16x16x32_bf16 v[140:143], v[36:39], a[72:75], v[140:143]\n"
  "ds_read_b128 v[56:59], v21 offset:20480\n"
  "v_mfma_f32_16x16x32_bf16 v[144:147], v[36:39], a[76:79], v[144:147]\n"
  "v_mfma_f32_16x16x32_bf16 v[148:151], v[40:43], a[64:67], v[148:151]\n"
  "ds_read_b128 v[60:63], v21 offset:24576\n"
  "v_mfma_f32_16x16x32_bf16 v[152:155], v[40:43], a[68:71], v[152:155]\n"
  "v_mfma_f32_16x16x32_bf16 v[156:159], v[40:43], a[72:75], v[156:159]\n"
  "ds_read_b128 v[64:67], v21 offset:28672\n"
  "v_mfma_f32_16x16x32_bf16 v[160:163], v[40:43], a[76:79], v[160:163]\n"
  "v_mfma_f32_16x16x32_bf16 v[164:167], v[44:47], a[64:67], v[164:167]\n"
  "v_mfma_f32_16x16x32_bf16 v[168:171], v[44:47], a[68:71], v[168:171]\n"
  "v_mfma_f32_16x16x32_bf16 v[172:175], v[44:47], a[72:75], v[172:175]\n"
  "v_mfma_f32_16x16x32_bf16 v[176:179], v[44:47], a[76:79], v[176:179]\n"
  "v_mfma_f32_16x16x32_bf16 v[180:183], v[48:51], a[64:67], v[180:183]\n"
  "v_mfma_f32_16x16x32_bf16 v[184:187], v[48:51], a[68:71], v[184:187]\n"
  "v_mfma_f32_16x16x32_bf16 v[188:191], v[48:51], a[72:75], v[188:191]\n"
  "v_mfma_f32_16x16x32_bf16 v[192:195], v[48:51], a[76:79], v[192:195]\n"
  "s_waitcnt lgkmcnt(0)\n"
  "v_mfma_f32_16x16x32_bf16 v[132:135], v[52:55], a[80:83], v[132:135]\n"
  "ds_read_b128 v[36:39], v22 offset:16384\n"
  "v_mfma_f32_16x16x32_bf16 v[136:139], v[52:55], a[84:87], v[136:139]\n"
  "buffer_load_dwordx4 a[16:19], %[voffX], %[srdWS], s96 offen offset:256 sc1\n"
  "v_mfma_f32_16x16x32_bf16 v[140:143], v[52:55], a[88:91], v[140:143]\n"
  "ds_read_b128 v[40:43], v22 offset:20480\n"
  "v_cvt_pk_bf16_f32 v76, v76, v77\n"
  "v_mfma_f32_16x16x32_bf16 v[144:147], v[52:55], a[92:95], v[144:147]\n"
  "s_add_u32 s92, %[rotU], 0\n"
  "s_and_b32 s92, s92, 3\n"
  "s_lshl_b32 s92, s92, 10\n"
  "v_mfma_f32_16x16x32_bf16 v[148:151], v[56:59], a[80:83], v[148:151]\n"
  "ds_read_b128 v[44:47], v22 offset:24576\n"
  "s_add_u32 s92, s92, %[oY]\n"
  "v_mfma_f32_16x16x32_bf16 v[152:155], v[56:59], a[84:87], v[152:155]\n"
  "s_add_u32 s93, s92, 0x4000\n"
  "v_cvt_pk_bf16_f32 v77, v78, v79\n"
  "v_mfma_f32_16x16x32_bf16 v[156:159], v[56:59], a[88:91], v[156:159]\n"
  "ds_read_b128 v[48:51], v22 offset:28672\n"
  "buffer_load_dwordx4 a[64:67], %[voffX], %[srdWS], s92 offen offset:0 sc1\n"
  "v_mfma_f32_16x16x32_bf16 v[160:163], v[56:59], a[92:95], v[160:163]\n"
  "v_cvt_pk_bf16_f32 v78, v92, v93\n"
  "v_cvt_pk_bf16_f32 v79, v94, v95\n"
  "v_mfma_f32_16x16x32_bf16 v[164:167], v[60:63], a[80:83], v[164:167]\n"
  "s_add_u32 s66, s66, s67\n"
  "buffer_store_dwordx4 v[76:79], %[L16], %[srdWS], s66 offen sc1\n"
  "v_mfma_f32_16x16x32_bf16 v[168:171], v[60:63], a[84:87], v[168:171]\n"
  "v_mfma_f32_16x16x32_bf16 v[172:175], v[60:63], a[88:91], v[172:175]\n"
  "buffer_load_dwordx4 a[68:71], %[voffX], %[srdWS], s93 offen offset:0 sc1\n"
  "v_mfma_f32_16x16x32_bf16 v[176:179], v[60:63], a[92:95], v[176:179]\n"
  "v_mfma_f32_16x16x32_bf16 v[180:183], v[64:67], a[80:83], v[180:183]\n"
  "v_mfma_f32_16x16x32_bf16 v[184:187], v[64:67], a[84:87], v[184:187]\n"
  "v_mfma_f32_16x16x32_bf16 v[188:191], v[64:67], a[88:91], v[188:191]\n"
  "v_mfma_f32_16x16x32_bf16 v[192:195], v[64:67], a[92:95], v[192:195]\n"
  "buffer_load_dwordx4 a[72:75], %[voffX], %[srdWS], s92 offen offset:256 sc1\n"
  "s_waitcnt lgkmcnt(0)\n"
  "v_mfma_f32_16x16x32_bf16 v[132:135], v[36:39], a[96:99], v[132:135]\n"
  "ds_read_b128 v[52:55], v23 offset:16384\n"
  "v_mfma_f32_16x16x32_bf16 v[136:139], v[36:39], a[100:103], v[136:139]\n"
  "v_mfma_f32_16x16x32_bf16 v[140:143], v[36:39], a[104:107], v[140:143]\n"
  "ds_read_b128 v[56:59], v23 offset:20480\n"
  "v_mfma_f32_16x16x32_bf16 v[144:147], v[36:39], a[108:111], v[144:147]\n"
  "buffer_load_dwordx4 a[20:23], %[voffX], %[srdWS], s97 offen offset:256 sc1\n"
  "v_mfma_f32_16x16x32_bf16 v[148:151], v[40:43], a[96:99], v[148:151]\n"
  "ds_read_b128 v[60:63], v23 offset:24576\n"
  "v_mfma_f32_16x16x32_bf16 v[152:155], v[40:43], a[100:103], v[152:155]\n"
  "v_mfma_f32_16x16x32_bf16 v[156:159], v[40:43], a[104:107], v[156:159]\n"
  "ds_read_b128 v[64:67], v23 offset:28672\n"
  "v_mfma_f32_16x16x32_bf16 v[160:163], v[40:43], a[108:111], v[160:163]\n"
  "v_mfma_f32_16x16x32_bf16 v[164:167], v[44:47], a[96:99], v[164:167]\n"
  "buffer_load_dwordx4 a[76:79], %[voffX], %[srdWS], s93 offen offset:256 sc1\n"
  "v_mfma_f32_16x16x32_bf16 v[168:171], v[44:47], a[100:103], v[168:171]\n"
  "v_mfma_f32_16x16x32_bf16 v[172:175], v[44:47], a[104:107], v[172:175]\n"
  "v_mfma_f32_16x16x32_bf16 v[176:179], v[44:47], a[108:111], v[176:179]\n"
  "v_mfma_f32_16x16x32_bf16 v[180:183], v[48:51], a[96:99], v[180:183]\n"
  "v_mfma_f32_16x16x32_bf16 v[184:187], v[48:51], a[100:103], v[184:187]\n"
  "buffer_load_dwordx4 a[80:83], %[voffX], %[srdWS], s92 offen offset:512 sc1\n"
  "v_mfma_f32_16x16x32_bf16 v[188:191], v[48:51], a[104:107], v[188:191]\n"
  "v_cvt_pk_bf16_f32 v80, v80, v81\n"
  "v_cvt_pk_bf16_f32 v81, v82, v83\n"
  "v_mfma_f32_16x16x32_bf16 v[192:195], v[48:51], a[108:111], v[192:195]\n"
  "v_cvt_pk_bf16_f32 v82, v96, v97\n"
  "v_cvt_pk_bf16_f32 v83, v98, v99\n"
  "s_waitcnt vmcnt(22) lgkmcnt(0)\n"
  "s_barrier\n"
  "v_mfma_f32_16x16x32_bf16 v[132:135], v[52:55], a[112:115], v[132:135]\n"
  "ds_read_b128 v[36:39], v20 offset:32768\n"
  "s_add_u32 s66, s66, s67\n"
  "v_mfma_f32_16x16x32_bf16 v[136:139], v[52:55], a[116:119], v[136:139]\n"
  "buffer_load_dwordx4 a[24:27], %[voffX], %[srdWS], s98 offen offset:256 sc1\n"
  "buffer_store_dwordx4 v[80:83], %[L16], %[srdWS], s66 offen sc1\n"
  "v_mfma_f32_16x16x32_bf16 v[140:143], v[52:55], a[120:123], v[140:143]\n"
  "ds_read_b128 v[40:43], v20 offset:36864\n"
  "v_mfma_f32_16x16x32_bf16 v[144:147], v[52:55], a[124:127], v[144:147]\n"
  "v_mfma_f32_16x16x32_bf16 v[148:151], v[56:59], a[112:115], v[148:151]\n"
  "ds_read_b128 v[44:47], v20 offset:40960\n"
  "v_mfma_f32_16x16x32_bf16 v[152:155], v[56:59], a[116:119], v[152:155]\n"
  "v_mfma_f32_16x16x32_bf16 v[156:159], v[56:59], a[120:123], v[156:159]\n"
  "ds_read_b128 v[48:51], v20 offset:45056\n"
  "buffer_load_dwordx4 a[84:87], %[voffX], %[srdWS], s93 offen offset:512 sc1\n"
  "v_mfma_f32_16x16x32_bf16 v[160:163], v[56:59], a[124:127], v[160:163]\n"
  "v_mfma_f32_16x16x32_bf16 v[164:167], v[60:63], a[112:115], v[164:167]\n"
  "v_mfma_f32_16x16x32_bf16 v[168:171], v[60:63], a[116:119], v[168:171]\n"
  "v_mfma_f32_16x16x32_bf16 v[172:175], v[60:63], a[120:123], v[172:175]\n"
  "buffer_load_dwordx4 a[88:91], %[voffX], %[srdWS], s92 offen offset:768 sc1\n"
  "v_mfma_f32_16x16x32_bf16 v[176:179], v[60:63], a[124:127], v[176:179]\n"
  "v_mfma_f32_16x16x32_bf16 v[180:183], v[64:67], a[112:115], v[180:183]\n"
  "v_mfma_f32_16x16x32_bf16 v[184:187], v[64:67], a[116:119], v[184:187]\n"
  "v_mfma_f32_16x16x32_bf16 v[188:191], v[64:67], a[120:123], v[188:191]\n"
  "v_mfma_f32_16x16x32_bf16 v[192:195], v[64:67], a[124:127], v[192:195]\n"
  "buffer_load_dwordx4 a[92:95], %[voffX], %[srdWS], s93 offen offset:768 sc1\n"
  "s_waitcnt lgkmcnt(0)\n"
  "v_mfma_f32_16x16x32_bf16 v[132:135], v[36:39], a[128:131], v[132:135]\n"
  "ds_read_b128 v[52:55], v21 offset:32768\n"
  "v_mfma_f32_16x16x32_bf16 v[136:139], v[36:39], a[132:135], v[136:139]\n"
  "v_mfma_f32_16x16x32_bf16 v[140:143], v[36:39], a[136:139], v[140:143]\n"
  "ds_read_b128 v[56:59], v21 offset:36864\n"
  "v_mfma_f32_16x16x32_bf16 v[144:147], v[36:39], a[140:143], v[144:147]\n"
  "v_mfma_f32_16x16x32_bf16 v[148:151], v[40:43], a[128:131], v[148:151]\n"
  "ds_read_b128 v[60:63], v21 offset:40960\n"
  "v_lshrrev_b32 v31, 1, %[TRB]\n"
  "buffer_load_dwordx4 a[28:31], %[voffX], %[srdWS], s99 offen offset:256 sc1\n"
  "v_mfma_f32_16x16x32_bf16 v[152:155], v[40:43], a[132:135], v[152:155]\n"
  "s_add_u32 s92, %[rotU], 1\n"
  "s_and_b32 s92, s92, 3\n"
  "s_lshl_b32 s92, s92, 10\n"
  "v_mfma_f32_16x16x32_bf16 v[156:159], v[40:43], a[136:139], v[156:159]\n"
  "ds_read_b128 v[64:67], v21 offset:45056\n"
  "v_and_b32 v31, 0x200, v31\n"
  "s_add_u32 s92, s92, %[oY]\n"
  "v_mfma_f32_16x16x32_bf16 v[160:163], v[40:43], a[140:143], v[160:163]\n"
  "s_add_u32 s93, s92, 0x4000\n"
  "v_mfma_f32_16x16x32_bf16 v[164:167], v[44:47], a[128:131], v[164:167]\n"
  "v_sub_u32 v30, %[TRB], v31\n"
  "buffer_load_dwordx4 a[96:99], %[voffX], %[srdWS], s92 offen offset:0 sc1\n"
  "v_mfma_f32_16x16x32_bf16 v[168:171], v[44:47], a[132:135], v[168:171]\n"
  "v_mfma_f32_16x16x32_bf16 v[172:175], v[44:47], a[136:139], v[172:175]\n"
  "v_add_u32 v30, 0x10000, v30\n"
  "v_cvt_pk_bf16_f32 v100, v100, v101\n"
  "v_mfma_f32_16x16x32_bf16 v[176:179], v[44:47], a[140:143], v[176:179]\n"
  "v_cvt_pk_bf16_f32 v101, v102, v103\n"
  "v_cvt_pk_bf16_f32 v102, v116, v117\n"
  "v_mfma_f32_16x16x32_bf16 v[180:183], v[48:51], a[128:131], v[180:183]\n"
  "v_xor_b32 v31, 32, v30\n"
  "v_cvt_pk_bf16_f32 v103, v118, v119\n"
  "v_mfma_f32_16x16x32_bf16 v[184:187], v[48:51], a[132:135], v[184:187]\n"
  "buffer_load_dwordx4 a[100:103], %[voffX], %[srdWS], s93 offen offset:0 sc1\n"
  "s_add_u32 s66, s66, s68\n"
  "v_mfma_f32_16x16x32_bf16 v[188:191], v[48:51], a[136:139], v[188:191]\n"
  "v_add_u32 v32, 0xc000, v30\n"
  "buffer_store_dwordx4 v[100:103], %[L16], %[srdWS], s66 offen sc1\n"
  "v_mfma_f32_16x16x32_bf16 v[192:195], v[48:51], a[140:143], v[192:195]\n"
  "s_waitcnt lgkmcnt(0)\n"
  "v_mfma_f32_16x16x32_bf16 v[132:135], v[52:55], a[144:147], v[132:135]\n"
  "ds_read_b128 v[36:39], v22 offset:32768\n"
  "v_xor_b32 v33, 32, v32\n"
  "v_mfma_f32_16x16x32_bf16 v[136:139], v[52:55], a[148:151], v[136:139]\n"
  "buffer_load_dwordx4 a[32:35], %[voffX], %[srdWS], s96 offen offset:512 sc1\n"
  "v_mfma_f32_16x16x32_bf16 v[140:143], v[52:55], a[152:155], v[140:143]\n"
  "ds_read_b128 v[40:43], v22 offset:36864\n"
  "v_mfma_f32_16x16x32_bf16 v[144:147], v[52:55], a[156:159], v[144:147]\n"
  "v_mfma_f32_16x16x32_bf16 v[148:151], v[56:59], a[144:147], v[148:151]\n"
  "ds_read_b128 v[44:47], v22 offset:40960\n"
  "v_mfma_f32_16x16x32_bf16 v[152:155], v[56:59], a[148:151], v[152:155]\n"
  "v_mfma_f32_16x16x32_bf16 v[156:159], v[56:59], a[152:155], v[156:159]\n"
  "ds_read_b128 v[48:51], v22 offset:45056\n"
  "buffer_load_dwordx4 a[104:107], %[voffX], %[srdWS], s92 offen offset:256 sc1\n"
  "v_mfma_f32_16x16x32_bf16 v[160:163], v[56:59], a[156:159], v[160:163]\n"
  "v_mfma_f32_16x16x32_bf16 v[164:167], v[60:63], a[144:147], v[164:167]\n"
  "v_mfma_f32_16x16x32_bf16 v[168:171], v[60:63], a[148:151], v[168:171]\n"
  "v_mfma_f32_16x16x32_bf16 v[172:175], v[60:63], a[152:155], v[172:175]\n"
  "v_mfma_f32_16x16x32_bf16 v[176:179], v[60:63], a[156:159], v[176:179]\n"
  "buffer_load_dwordx4 a[108:111], %[voffX], %[srdWS], s93 offen offset:256 sc1\n"
  "v_mfma_f32_16x16x32_bf16 v[180:183], v[64:67], a[144:147], v[180:183]\n"
  "v_mfma_f32_16x16x32_bf16 v[184:187], v[64:67], a[148:151], v[184:187]\n"
  "v_mfma_f32_16x16x32_bf16 v[188:191], v[64:67], a[152:155], v[188:191]\n"
  "v_mfma_f32_16x16x32_bf16 v[192:195], v[64:67], a[156:159], v[192:195]\n"
  "buffer_load_dwordx4 a[112:115], %[voffX], %[srdWS], s92 offen offset:512 sc1\n"
  "s_waitcnt lgkmcnt(0)\n"
  "v_mfma_f32_16x16x32_bf16 v[132:135], v[36:39], a[160:163], v[132:135]\n"
  "ds_read_b128 v[52:55], v23 offset:32768\n"
  "v_mfma_f32_16x16x32_bf16 v[136:139], v[36:39], a[164:167], v[136:139]\n"
  "v_mfma_f32_16x16x32_bf16 v[140:143], v[36:39], a[168:171], v[140:143]\n"
  "ds_read_b128 v[56:59], v23 offset:36864\n"
  "v_mfma_f32_16x16x32_bf16 v[144:147], v[36:39], a[172:175], v[144:147]\n"
  "v_mfma_f32_16x16x32_bf16 v[148:151], v[40:43], a[160:163], v[148:151]\n"
  "ds_read_b128 v[60:63], v23 offset:40960\n"
  "buffer_load_dwordx4 a[36:39], %[voffX], %[srdWS], s97 offen offset:512 sc1\n"
  "v_mfma_f32_16x16x32_bf16 v[152:155], v[40:43], a[164:167], v[152:155]\n"
  "v_mfma_f32_16x16x32_bf16 v[156:159], v[40:43], a[168:171], v[156:159]\n"
  "ds_read_b128 v[64:67], v23 offset:45056\n"
  "v_cvt_pk_bf16_f32 v104, v104, v105\n"
  "v_mfma_f32_16x16x32_bf16 v[160:163], v[40:43], a[172:175], v[160:163]\n"
  "v_cvt_pk_bf16_f32 v105, v106, v107\n"
  "v_cvt_pk_bf16_f32 v106, v120, v121\n"
  "v_mfma_f32_16x16x32_bf16 v[164:167], v[44:47], a[160:163], v[164:167]\n"
  "buffer_load_dwordx4 a[116:119], %[voffX], %[srdWS], s93 offen offset:512 sc1\n"
  "v_cvt_pk_bf16_f32 v107, v122, v123\n"
  "v_mfma_f32_16x16x32_bf16 v[168:171], v[44:47], a[164:167], v[168:171]\n"
  "s_add_u32 s66, s66, s67\n"
  "buffer_store_dwordx4 v[104:107], %[L16], %[srdWS], s66 offen sc1\n"
  "v_mfma_f32_16x16x32_bf16 v[172:175], v[44:47], a[168:171], v[172:175]\n"
  "v_mfma_f32_16x16x32_bf16 v[176:179], v[44:47], a[172:175], v[176:179]\n"
  "v_mfma_f32_16x16x32_bf16 v[180:183], v[48:51], a[160:163], v[180:183]\n"
  "v_mfma_f32_16x16x32_bf16 v[184:187], v[48:51], a[164:167], v[184:187]\n"
  "buffer_load_dwordx4 a[120:123], %[voffX], %[srdWS], s92 offen offset:768 sc1\n"
  "v_mfma_f32_16x16x32_bf16 v[188:191], v[48:51], a[168:171], v[188:191]\n"
  "v_mfma_f32_16x16x32_bf16 v[192:195], v[48:51], a[172:175], v[192:195]\n"
  "s_waitcnt vmcnt(31) lgkmcnt(0)\n"
  "s_barrier\n"
  "v_mfma_f32_16x16x32_bf16 v[132:135], v[52:55], a[176:179], v[132:135]\n"
  "ds_read_b128 v[36:39], v20 offset:49152\n"
  "v_mfma_f32_16x16x32_bf16 v[136:139], v[52:55], a[180:183], v[136:139]\n"
  "v_mfma_f32_16x16x32_bf16 v[140:143], v[52:55], a[184:187], v[140:143]\n"
  "ds_read_b128 v[40:43], v20 offset:53248\n"
  "buffer_load_dwordx4 a[40:43], %[voffX], %[srdWS], s98 offen offset:512 sc1\n"
  "v_mfma_f32_16x16x32_bf16 v[144:147], v[52:55], a[188:191], v[144:147]\n"
  "v_mfma_f32_16x16x32_bf16 v[148:151], v[56:59], a[176:179], v[148:151]\n"
  "ds_read_b128 v[44:47], v20 offset:57344\n"
  "v_mfma_f32_16x16x32_bf16 v[152:155], v[56:59], a[180:183], v[152:155]\n"
  "v_mfma_f32_16x16x32_bf16 v[156:159], v[56:59], a[184:187], v[156:159]\n"
  "ds_read_b128 v[48:51], v20 offset:61440\n"
  "buffer_load_dwordx4 a[124:127], %[voffX], %[srdWS], s93 offen offset:768 sc1\n"
  "v_mfma_f32_16x16x32_bf16 v[160:163], v[56:59], a[188:191], v[160:163]\n"
  "v_mfma_f32_16x16x32_bf16 v[164:167], v[60:63], a[176:179], v[164:167]\n"
  "s_add_u32 s92, %[rotU], 2\n"
  "s_and_b32 s92, s92, 3\n"
  "s_lshl_b32 s92, s92, 10\n"
  "v_mfma_f32_16x16x32_bf16 v[168:171], v[60:63], a[180:183], v[168:171]\n"
  "s_add_u32 s92, s92, %[oY]\n"
  "v_mfma_f32_16x16x32_bf16 v[172:175], v[60:63], a[184:187], v[172:175]\n"
  "s_add_u32 s93, s92, 0x4000\n"
  "v_mfma_f32_16x16x32_bf16 v[176:179], v[60:63], a[188:191], v[176:179]\n"
  "buffer_load_dwordx4 a[128:131], %[voffX], %[srdWS], s92 offen offset:0 sc1\n"
  "v_mfma_f32_16x16x32_bf16 v[180:183], v[64:67], a[176:179], v[180:183]\n"
  "v_mfma_f32_16x16x32_bf16 v[184:187], v[64:67], a[180:183], v[184:187]\n"
  "v_mfma_f32_16x16x32_bf16 v[188:191], v[64:67], a[184:187], v[188:191]\n"
  "v_mfma_f32_16x16x32_bf16 v[192:195], v[64:67], a[188:191], v[192:195]\n"
  "buffer_load_dwordx4 a[132:135], %[voffX], %[srdWS], s93 offen offset:0 sc1\n"
  "s_waitcnt lgkmcnt(0)\n"
  "v_mfma_f32_16x16x32_bf16 v[132:135], v[36:39], a[192:195], v[132:135]\n"
  "ds_read_b128 v[52:55], v21 offset:49152\n"
  "v_mfma_f32_16x16x32_bf16 v[136:139], v[36:39], a[196:199], v[136:139]\n"
  "v_mfma_f32_16x16x32_bf16 v[140:143], v[36:39], a[200:203], v[140:143]\n"
  "ds_read_b128 v[56:59], v21 offset:53248\n"
  "v_cvt_pk_bf16_f32 v108, v108, v109\n"
  "v_mfma_f32_16x16x32_bf16 v[144:147], v[36:39], a[204:207], v[144:147]\n"
  "v_cvt_pk_bf16_f32 v109, v110, v111\n"
  "v_cvt_pk_bf16_f32 v110, v124, v125\n"
  "v_mfma_f32_16x16x32_bf16 v[148:151], v[40:43], a[192:195], v[148:151]\n"
  "ds_read_b128 v[60:63], v21 offset:57344\n"
  "buffer_load_dwordx4 a[44:47], %[voffX], %[srdWS], s99 offen offset:512 sc1\n"
  "v_mfma_f32_16x16x32_bf16 v[152:155], v[40:43], a[196:199], v[152:155]\n"
  "v_cvt_pk_bf16_f32 v111, v126, v127\n"
  "s_add_u32 s66, s66, s67\n"
  "v_mfma_f32_16x16x32_bf16 v[156:159], v[40:43], a[200:203], v[156:159]\n"
  "ds_read_b128 v[64:67], v21 offset:61440\n"
  "buffer_store_dwordx4 v[108:111], %[L16], %[srdWS], s66 offen sc1\n"
  "v_mfma_f32_16x16x32_bf16 v[160:163], v[40:43], a[204:207], v[160:163]\n"
  "v_mfma_f32_16x16x32_bf16 v[164:167], v[44:47], a[192:195], v[164:167]\n"
  "v_mfma_f32_16x16x32_bf16 v[168:171], v[44:47], a[196:199], v[168:171]\n"
  "buffer_load_dwordx4 a[136:139], %[voffX], %[srdWS], s92 offen offset:256 sc1\n"
  "v_mfma_f32_16x16x32_bf16 v[172:175], v[44:47], a[200:203], v[172:175]\n"
  "v_mfma_f32_16x16x32_bf16 v[176:179], v[44:47], a[204:207], v[176:179]\n"
  "v_mfma_f32_16x16x32_bf16 v[180:183], v[48:51], a[192:195], v[180:183]\n"
  "v_mfma_f32_16x16x32_bf16 v[184:187], v[48:51], a[196:199], v[184:187]\n"
  "buffer_load_dwordx4 a[140:143], %[voffX], %[srdWS], s93 offen offset:256 sc1\n"
  "v_mfma_f32_16x16x32_bf16 v[188:191], v[48:51], a[200:203], v[188:191]\n"
  "v_mfma_f32_16x16x32_bf16 v[192:195], v[48:51], a[204:207], v[192:195]\n"
  "s_waitcnt lgkmcnt(0)\n"
  "v_mfma_f32_16x16x32_bf16 v[132:135], v[52:55], a[208:211], v[132:135]\n"
  "ds_read_b128 v[36:39], v22 offset:49152\n"
  "v_mfma_f32_16x16x32_bf16 v[136:139], v[52:55], a[212:215], v[136:139]\n"
  "v_mfma_f32_16x16x32_bf16 v[140:143], v[52:55], a[216:219], v[140:143]\n"
  "ds_read_b128 v[40:43], v22 offset:53248\n"
  "buffer_load_dwordx4 a[48:51], %[voffX], %[srdWS], s96 offen offset:768 sc1\n"
  "v_mfma_f32_16x16x32_bf16 v[144:147], v[52:55], a[220:223], v[144:147]\n"
  "v_mfma_f32_16x16x32_bf16 v[148:151], v[56:59], a[208:211], v[148:151]\n"
  "ds_read_b128 v[44:47], v22 offset:57344\n"
  "v_mfma_f32_16x16x32_bf16 v[152:155], v[56:59], a[212:215], v[152:155]\n"
  "v_mfma_f32_16x16x32_bf16 v[156:159], v[56:59], a[216:219], v[156:159]\n"
  "ds_read_b128 v[48:51], v22 offset:61440\n"
  "buffer_load_dwordx4 a[144:147], %[voffX], %[srdWS], s92 offen offset:512 sc1\n"
  "v_mfma_f32_16x16x32_bf16 v[160:163], v[56:59], a[220:223], v[160:163]\n"
  "v_mfma_f32_16x16x32_bf16 v[164:167], v[60:63], a[208:211], v[164:167]\n"
  "v_mfma_f32_16x16x32_bf16 v[168:171], v[60:63], a[212:215], v[168:171]\n"
  "v_mfma_f32_16x16x32_bf16 v[172:175], v[60:63], a[216:219], v[172:175]\n"
  "v_mfma_f32_16x16x32_bf16 v[176:179], v[60:63], a[220:223], v[176:179]\n"
  "buffer_load_dwordx4 a[148:151], %[voffX], %[srdWS], s93 offen offset:512 sc1\n"
  "v_mfma_f32_16x16x32_bf16 v[180:183], v[64:67], a[208:211], v[180:183]\n"
  "v_mfma_f32_16x16x32_bf16 v[184:187], v[64:67], a[212:215], v[184:187]\n"
  "v_mfma_f32_16x16x32_bf16 v[188:191], v[64:67], a[216:219], v[188:191]\n"
  "v_cvt_pk_bf16_f32 v112, v112, v113\n"
  "v_cvt_pk_bf16_f32 v113, v114, v115\n"
  "v_mfma_f32_16x16x32_bf16 v[192:195], v[64:67], a[220:223], v[192:195]\n"
  "v_cvt_pk_bf16_f32 v114, v128, v129\n"
  "v_cvt_pk_bf16_f32 v115, v130, v131\n"
  "s_waitcnt lgkmcnt(0)\n"
  "v_mfma_f32_16x16x32_bf16 v[132:135], v[36:39], a[224:227], v[132:135]\n"
  "ds_read_b128 v[52:55], v23 offset:49152\n"
  "buffer_load_dwordx4 a[152:155], %[voffX], %[srdWS], s92 offen offset:768 sc1\n"
  "v_mfma_f32_16x16x32_bf16 v[136:139], v[36:39], a[228:231], v[136:139]\n"
  "s_add_u32 s66, s66, s67\n"
  "buffer_store_dwordx4 v[112:115], %[L16], %[srdWS], s66 offen sc1\n"
  "v_mfma_f32_16x16x32_bf16 v[140:143], v[36:39], a[232:235], v[140:143]\n"
  "ds_read_b128 v[56:59], v23 offset:53248\n"
  "v_mfma_f32_16x16x32_bf16 v[144:147], v[36:39], a[236:239], v[144:147]\n"
  "v_mfma_f32_16x16x32_bf16 v[148:151], v[40:43], a[224:227], v[148:151]\n"
  "ds_read_b128 v[60:63], v23 offset:57344\n"
  "buffer_load_dwordx4 a[52:55], %[voffX], %[srdWS], s97 offen offset:768 sc1\n"
  "v_mfma_f32_16x16x32_bf16 v[152:155], v[40:43], a[228:231], v[152:155]\n"
  "v_mfma_f32_16x16x32_bf16 v[156:159], v[40:43], a[232:235], v[156:159]\n"
  "ds_read_b128 v[64:67], v23 offset:61440\n"
  "v_mfma_f32_16x16x32_bf16 v[160:163], v[40:43], a[236:239], v[160:163]\n"
  "v_mfma_f32_16x16x32_bf16 v[164:167], v[44:47], a[224:227], v[164:167]\n"
  "v_mfma_f32_16x16x32_bf16 v[168:171], v[44:47], a[228:231], v[168:171]\n"
  "buffer_load_dwordx4 a[156:159], %[voffX], %[srdWS], s93 offen offset:768 sc1\n"
  "v_mfma_f32_16x16x32_bf16 v[172:175], v[44:47], a[232:235], v[172:175]\n"
  "s_add_u32 s92, %[rotU], 3\n"
  "s_and_b32 s92, s92, 3\n"
  "s_lshl_b32 s92, s92, 10\n"
  "v_mfma_f32_16x16x32_bf16 v[176:179], v[44:47], a[236:239], v[176:179]\n"
  "s_add_u32 s92, s92, %[oY]\n"
  "v_mfma_f32_16x16x32_bf16 v[180:183], v[48:51], a[224:227], v[180:183]\n"
  "s_add_u32 s93, s92, 0x4000\n"
  "v_mfma_f32_16x16x32_bf16 v[184:187], v[48:51], a[228:231], v[184:187]\n"
  "buffer_load_dwordx4 a[160:163], %[voffX], %[srdWS], s92 offen offset:0 sc1\n"
  "v_mfma_f32_16x16x32_bf16 v[188:191], v[48:51], a[232:235], v[188:191]\n"
  "v_mfma_f32_16x16x32_bf16 v[192:195], v[48:51], a[236:239], v[192:195]\n"
  "s_waitcnt vmcnt(63) lgkmcnt(0)\n"
  "s_barrier\n"
  "v_mfma_f32_16x16x32_bf16 v[132:135], v[52:55], a[240:243], v[132:135]\n"
  "v_mfma_f32_16x16x32_bf16 v[136:139], v[52:55], a[244:247], v[136:139]\n"
  "ds_read_b64_tr_b16 v[68:69], v30 offset:0\n"
  "ds_read_b64_tr_b16 v[70:71], v30 offset:3072\n"
  "v_mfma_f32_16x16x32_bf16 v[140:143], v[52:55], a[248:251], v[140:143]\n"
  "ds_read_b64_tr_b16 v[72:73], v31 offset:0\n"
  "buffer_load_dwordx4 a[56:59], %[voffX], %[srdWS], s98 offen offset:768 sc1\n"
  "v_mfma_f32_16x16x32_bf16 v[144:147], v[52:55], a[252:255], v[144:147]\n"
  "ds_read_b64_tr_b16 v[74:75], v31 offset:3072\n"
  "v_mfma_f32_16x16x32_bf16 v[148:151], v[56:59], a[240:243], v[148:151]\n"
  "ds_read_b64_tr_b16 v[76:77], v30 offset:1024\n"
  "v_mfma_f32_16x16x32_bf16 v[152:155], v[56:59], a[244:247], v[152:155]\n"
  "ds_read_b64_tr_b16 v[78:79], v30 offset:4096\n"
  "v_mfma_f32_16x16x32_bf16 v[156:159], v[56:59], a[248:251], v[156:159]\n"
  "ds_read_b64_tr_b16 v[80:81], v31 offset:1024\n"
  "v_mfma_f32_16x16x32_bf16 v[160:163], v[56:59], a[252:255], v[160:163]\n"
  "ds_read_b64_tr_b16 v[82:83], v31 offset:4096\n"
  "buffer_load_dwordx4 a[164:167], %[voffX], %[srdWS], s93 offen offset:0 sc1\n"
  "v_mfma_f32_16x16x32_bf16 v[164:167], v[60:63], a[240:243], v[164:167]\n"
  "ds_read_b64_tr_b16 v[84:85], v30 offset:2048\n"
  "v_mfma_f32_16x16x32_bf16 v[168:171], v[60:63], a[244:247], v[168:171]\n"
  "ds_read_b64_tr_b16 v[86:87], v30 offset:5120\n"
  "v_mfma_f32_16x16x32_bf16 v[172:175], v[60:63], a[248:251], v[172:175]\n"
  "ds_read_b64_tr_b16 v[88:89], v31 offset:2048\n"
  "v_mfma_f32_16x16x32_bf16 v[176:179], v[60:63], a[252:255], v[176:179]\n"
  "ds_read_b64_tr_b16 v[90:91], v31 offset:5120\n"
  "buffer_load_dwordx4 a[168:171], %[voffX], %[srdWS], s92 offen offset:256 sc1\n"
  "v_mfma_f32_16x16x32_bf16 v[180:183], v[64:67], a[240:243], v[180:183]\n"
  "v_mfma_f32_16x16x32_bf16 v[184:187], v[64:67], a[244:247], v[184:187]\n"
  "v_mfma_f32_16x16x32_bf16 v[188:191], v[64:67], a[248:251], v[188:191]\n"
  "v_mfma_f32_16x16x32_bf16 v[192:195], v[64:67], a[252:255], v[192:195]\n"
  "KVEND_%=:\n"
  "s_waitcnt lgkmcnt(0)\n"
  "s_waitcnt vmcnt(41)\n"
  "v_mfma_f32_16x16x32_bf16 v[196:199], v[68:71], a[64:67], 0\n"
  "ds_read_b64_tr_b16 v[92:93], v30 offset:6144\n"
  "ds_read_b64_tr_b16 v[94:95], v30 offset:9216\n"
  "buffer_load_dwordx4 a[172:175], %[voffX], %[srdWS], s93 offen offset:256 sc1\n"
  "s_waitcnt vmcnt(41)\n"
  "v_mfma_f32_16x16x32_bf16 v[200:203], v[68:71], a[68:71], 0\n"
  "ds_read_b64_tr_b16 v[96:97], v31 offset:6144\n"
  "v_mfma_f32_16x16x32_bf16 v[204:207], v[72:75], a[64:67], 0\n"
  "ds_read_b64_tr_b16 v[98:99], v31 offset:9216\n"
  "ds_read_b64_tr_b16 v[100:101], v30 offset:7168\n"
  "v_mfma_f32_16x16x32_bf16 v[208:211], v[72:75], a[68:71], 0\n"
  "ds_read_b64_tr_b16 v[102:103], v30 offset:10240\n"
  "v_mfma_f32_16x16x32_bf16 v[212:215], v[76:79], a[64:67], 0\n"
  "ds_read_b64_tr_b16 v[104:105], v31 offset:7168\n"
  "ds_read_b64_tr_b16 v[106:107], v31 offset:10240\n"
  "buffer_load_dwordx4 a[60:63], %[voffX], %[srdWS], s99 offen offset:768 sc1\n"
  "v_mfma_f32_16x16x32_bf16 v[216:219], v[76:79], a[68:71], 0\n"
  "ds_read_b64_tr_b16 v[108:109], v30 offset:8192\n"
  "v_mfma_f32_16x16x32_bf16 v[220:223], v[80:83], a[64:67], 0\n"
  "ds_read_b64_tr_b16 v[110:111], v30 offset:11264\n"
  "ds_read_b64_tr_b16 v[112:113], v31 offset:8192\n"
  "v_mfma_f32_16x16x32_bf16 v[224:227], v[80:83], a[68:71], 0\n"
  "ds_read_b64_tr_b16 v[114:115], v31 offset:11264\n"
  "v_mfma_f32_16x16x32_bf16 v[228:231], v[84:87], a[64:67], 0\n"
  "v_mov_b32 v20, %[TRB]\n"
  "v_mfma_f32_16x16x32_bf16 v[232:235], v[84:87], a[68:71], 0\n"
  "v_xor_b32 v21, 32, %[TRB]\n"
  "buffer_load_dwordx4 a[176:179], %[voffX], %[srdWS], s92 offen offset:512 sc1\n"
  "v_mfma_f32_16x16x32_bf16 v[236:239], v[88:91], a[64:67], 0\n"
  "v_add_u32 v22, 0x10000, %[TRB]\n"
  "v_mfma_f32_16x16x32_bf16 v[240:243], v[88:91], a[68:71], 0\n"
  "v_xor_b32 v23, 32, v22\n"
  "s_waitcnt lgkmcnt(0)\n"
  "s_waitcnt vmcnt(42)\n"
  "v_mfma_f32_16x16x32_bf16 v[196:199], v[92:95], a[72:75], v[196:199]\n"
  "ds_read_b64_tr_b16 v[68:69], v30 offset:12288\n"
  "ds_read_b64_tr_b16 v[70:71], v30 offset:15360\n"
  "v_cvt_pk_bf16_f32 v132, v132, v133\n"
  "v_cvt_pk_bf16_f32 v133, v134, v135\n"
  "v_cvt_pk_bf16_f32 v134, v148, v149\n"
  "v_cvt_pk_bf16_f32 v135, v150, v151\n"
  "s_waitcnt vmcnt(39)\n"
  "v_mfma_f32_16x16x32_bf16 v[200:203], v[92:95], a[76:79], v[200:203]\n"
  "ds_read_b64_tr_b16 v[72:73], v31 offset:12288\n"
  "s_add_u32 s66, s66, s69\n"
  "v_mfma_f32_16x16x32_bf16 v[204:207], v[96:99], a[72:75], v[204:207]\n"
  "ds_read_b64_tr_b16 v[74:75], v31 offset:15360\n"
  "ds_read_b64_tr_b16 v[76:77], v30 offset:13312\n"
  "buffer_store_dwordx4 v[132:135], %[L16], %[srdWS], s66 offen sc1\n"
  "v_mfma_f32_16x16x32_bf16 v[208:211], v[96:99], a[76:79], v[208:211]\n"
  "ds_read_b64_tr_b16 v[78:79], v30 offset:16384\n"
  "v_mfma_f32_16x16x32_bf16 v[212:215], v[100:103], a[72:75], v[212:215]\n"
  "ds_read_b64_tr_b16 v[80:81], v31 offset:13312\n"
  "ds_read_b64_tr_b16 v[82:83], v31 offset:16384\n"
  "v_mfma_f32_16x16x32_bf16 v[216:219], v[100:103], a[76:79], v[216:219]\n"
  "ds_read_b64_tr_b16 v[84:85], v30 offset:14336\n"
  "v_mfma_f32_16x16x32_bf16 v[220:223], v[104:107], a[72:75], v[220:223]\n"
  "ds_read_b64_tr_b16 v[86:87], v30 offset:17408\n"
  "ds_read_b64_tr_b16 v[88:89], v31 offset:14336\n"
  "buffer_load_dwordx4 a[180:183], %[voffX], %[srdWS], s93 offen offset:512 sc1\n"
  "v_mfma_f32_16x16x32_bf16 v[224:227], v[104:107], a[76:79], v[224:227]\n"
  "ds_read_b64_tr_b16 v[90:91], v31 offset:17408\n"
  "v_mfma_f32_16x16x32_bf16 v[228:231], v[108:111], a[72:75], v[228:231]\n"
  "v_lshrrev_b32 v35, 5, %[L16]\n"
  "v_mfma_f32_16x16x32_bf16 v[232:235], v[108:111], a[76:79], v[232:235]\n"
  "v_and_b32 v35, 0x18, v35\n"
  "v_mfma_f32_16x16x32_bf16 v[236:239], v[112:115], a[72:75], v[236:239]\n"
  "v_mfma_f32_16x16x32_bf16 v[240:243], v[112:115], a[76:79], v[240:243]\n"
  "buffer_load_dwordx4 a[184:187], %[voffX], %[srdWS], s92 offen offset:768 sc1\n"
  "s_waitcnt lgkmcnt(0)\n"
  "s_waitcnt vmcnt(41)\n"
  "v_mfma_f32_16x16x32_bf16 v[196:199], v[68:71], a[80:83], v[196:199]\n"
  "ds_read_b64_tr_b16 v[92:93], v30 offset:18432\n"
  "ds_read_b64_tr_b16 v[94:95], v30 offset:21504\n"
  "s_waitcnt vmcnt(39)\n"
  "v_mfma_f32_16x16x32_bf16 v[200:203], v[68:71], a[84:87], v[200:203]\n"
  "ds_read_b64_tr_b16 v[96:97], v31 offset:18432\n"
  "v_mfma_f32_16x16x32_bf16 v[204:207], v[72:75], a[80:83], v[204:207]\n"
  "ds_read_b64_tr_b16 v[98:99], v31 offset:21504\n"
  "ds_read_b64_tr_b16 v[100:101], v30 offset:19456\n"
  "v_mfma_f32_16x16x32_bf16 v[208:211], v[72:75], a[84:87], v[208:211]\n"
  "ds_read_b64_tr_b16 v[102:103], v30 offset:22528\n"
  "buffer_load_dwordx4 a[188:191], %[voffX], %[srdWS], s93 offen offset:768 sc1\n"
  "v_mfma_f32_16x16x32_bf16 v[212:215], v[76:79], a[80:83], v[212:215]\n"
  "ds_read_b64_tr_b16 v[104:105], v31 offset:19456\n"
  "ds_read_b64_tr_b16 v[106:107], v31 offset:22528\n"
  "v_mfma_f32_16x16x32_bf16 v[216:219], v[76:79], a[84:87], v[216:219]\n"
  "ds_read_b64_tr_b16 v[108:109], v30 offset:20480\n"
  "s_add_u32 s87, %[rot], 0\n"
  "s_and_b32 s87, s87, 3\n"
  "s_lshl_b32 s87, s87, 20\n"
  "v_mfma_f32_16x16x32_bf16 v[220:223], v[80:83], a[80:83], v[220:223]\n"
  "ds_read_b64_tr_b16 v[110:111], v30 offset:23552\n"
  "ds_read_b64_tr_b16 v[112:113], v31 offset:20480\n"
  "s_add_u32 m0, s75, 0\n"
  "v_mfma_f32_16x16x32_bf16 v[224:227], v[80:83], a[84:87], v[224:227]\n"
  "ds_read_b64_tr_b16 v[114:115], v31 offset:23552\n"
  "s_add_u32 s64, s87, 0\n"
  "v_mfma_f32_16x16x32_bf16 v[228:231], v[84:87], a[80:83], v[228:231]\n"
  "buffer_load_dwordx4 %[voffWq], %[srdWq], s64 offen lds\n"
  "v_mfma_f32_16x16x32_bf16 v[232:235], v[84:87], a[84:87], v[232:235]\n"
  "v_cvt_pk_bf16_f32 v136, v136, v137\n"
  "v_cvt_pk_bf16_f32 v137, v138, v139\n"
  "v_mfma_f32_16x16x32_bf16 v[236:239], v[88:91], a[80:83], v[236:239]\n"
  "v_cvt_pk_bf16_f32 v138, v152, v153\n"
  "v_mfma_f32_16x16x32_bf16 v[240:243], v[88:91], a[84:87], v[240:243]\n"
  "v_cvt_pk_bf16_f32 v139, v154, v155\n"
  "s_waitcnt lgkmcnt(0)\n"
  "s_waitcnt vmcnt(40)\n"
  "v_mfma_f32_16x16x32_bf16 v[196:199], v[92:95], a[88:91], v[196:199]\n"
  "ds_read_b64_tr_b16 v[68:69], v30 offset:24576\n"
  "ds_read_b64_tr_b16 v[70:71], v30 offset:27648\n"
  "s_add_u32 s66, s66, s67\n"
  "s_waitcnt vmcnt(38)\n"
  "v_mfma_f32_16x16x32_bf16 v[200:203], v[92:95], a[92:95], v[200:203]\n"
  "ds_read_b64_tr_b16 v[72:73], v31 offset:24576\n"
  "buffer_store_dwordx4 v[136:139], %[L16], %[srdWS], s66 offen sc1\n"
  "v_mfma_f32_16x16x32_bf16 v[204:207], v[96:99], a[88:91], v[204:207]\n"
  "ds_read_b64_tr_b16 v[74:75], v31 offset:27648\n"
  "ds_read_b64_tr_b16 v[76:77], v30 offset:25600\n"
  "v_mfma_f32_16x16x32_bf16 v[208:211], v[96:99], a[92:95], v[208:211]\n"
  "ds_read_b64_tr_b16 v[78:79], v30 offset:28672\n"
  "s_add_u32 m0, s75, 1024\n"
  "v_mfma_f32_16x16x32_bf16 v[212:215], v[100:103], a[88:91], v[212:215]\n"
  "ds_read_b64_tr_b16 v[80:81], v31 offset:25600\n"
  "ds_read_b64_tr_b16 v[82:83], v31 offset:28672\n"
  "s_add_u32 s64, s87, 131072\n"
  "v_mfma_f32_16x16x32_bf16 v[216:219], v[100:103], a[92:95], v[216:219]\n"
  "ds_read_b64_tr_b16 v[84:85], v30 offset:26624\n"
  "buffer_load_dwordx4 %[voffWq], %[srdWq], s64 offen lds\n"
  "v_mfma_f32_16x16x32_bf16 v[220:223], v[104:107], a[88:91], v[220:223]\n"
  "ds_read_b64_tr_b16 v[86:87], v30 offset:29696\n"
  "ds_read_b64_tr_b16 v[88:89], v31 offset:26624\n"
  "v_mfma_f32_16x16x32_bf16 v[224:227], v[104:107], a[92:95], v[224:227]\n"
  "ds_read_b64_tr_b16 v[90:91], v31 offset:29696\n"
  "v_mfma_f32_16x16x32_bf16 v[228:231], v[108:111], a[88:91], v[228:231]\n"
  "s_add_u32 m0, s75, 8192\n"
  "v_mfma_f32_16x16x32_bf16 v[232:235], v[108:111], a[92:95], v[232:235]\n"
  "s_add_u32 s64, s87, 262144\n"
  "v_mfma_f32_16x16x32_bf16 v[236:239], v[112:115], a[88:91], v[236:239]\n"
  "buffer_load_dwordx4 %[voffWq], %[srdWq], s64 offen lds\n"
  "v_mfma_f32_16x16x32_bf16 v[240:243], v[112:115], a[92:95], v[240:243]\n"
  "s_waitcnt lgkmcnt(0)\n"
  "s_waitcnt vmcnt(39)\n"
  "v_mfma_f32_16x16x32_bf16 v[196:199], v[68:71], a[96:99], v[196:199]\n"
  "ds_read_b64_tr_b16 v[92:93], v30 offset:30720\n"
  "ds_read_b64_tr_b16 v[94:95], v30 offset:33792\n"
  "s_add_u32 m0, s75, 9216\n"
  "s_waitcnt vmcnt(38)\n"
  "v_mfma_f32_16x16x32_bf16 v[200:203], v[68:71], a[100:103], v[200:203]\n"
  "ds_read_b64_tr_b16 v[96:97], v31 offset:30720\n"
  "s_add_u32 s64, s87, 393216\n"
  "v_mfma_f32_16x16x32_bf16 v[204:207], v[72:75], a[96:99], v[204:207]\n"
  "ds_read_b64_tr_b16 v[98:99], v31 offset:33792\n"
  "ds_read_b64_tr_b16 v[100:101], v30 offset:31744\n"
  "buffer_load_dwordx4 %[voffWq], %[srdWq], s64 offen lds\n"
  "v_mfma_f32_16x16x32_bf16 v[208:211], v[72:75], a[100:103], v[208:211]\n"
  "ds_read_b64_tr_b16 v[102:103], v30 offset:34816\n"
  "v_mfma_f32_16x16x32_bf16 v[212:215], v[76:79], a[96:99], v[212:215]\n"
  "ds_read_b64_tr_b16 v[104:105], v31 offset:31744\n"
  "ds_read_b64_tr_b16 v[106:107], v31 offset:34816\n"
  "v_mfma_f32_16x16x32_bf16 v[216:219], v[76:79], a[100:103], v[216:219]\n"
  "ds_read_b64_tr_b16 v[108:109], v30 offset:32768\n"
  "v_mfma_f32_16x16x32_bf16 v[220:223], v[80:83], a[96:99], v[220:223]\n"
  "ds_read_b64_tr_b16 v[110:111], v30 offset:35840\n"
  "ds_read_b64_tr_b16 v[112:113], v31 offset:32768\n"
  "v_cvt_pk_bf16_f32 v140, v140, v141\n"
  "v_cvt_pk_bf16_f32 v141, v142, v143\n"
  "v_cvt_pk_bf16_f32 v142, v156, v157\n"
  "v_cvt_pk_bf16_f32 v143, v158, v159\n"
  "s_add_u32 s66, s66, s67\n"
  "v_mfma_f32_16x16x32_bf16 v[224:227], v[80:83], a[100:103], v[224:227]\n"
  "ds_read_b64_tr_b16 v[114:115], v31 offset:35840\n"
  "buffer_store_dwordx4 v[140:143], %[L16], %[srdWS], s66 offen sc1\n"
  "v_mfma_f32_16x16x32_bf16 v[228:231], v[84:87], a[96:99], v[228:231]\n"
  "v_mfma_f32_16x16x32_bf16 v[232:235], v[84:87], a[100:103], v[232:235]\n"
  "v_mfma_f32_16x16x32_bf16 v[236:239], v[88:91], a[96:99], v[236:239]\n"
  "s_add_u32 m0, s75, 16384\n"
  "v_mfma_f32_16x16x32_bf16 v[240:243], v[88:91], a[100:103], v[240:243]\n"
  "s_add_u32 s64, s87, 524288\n"
  "s_waitcnt lgkmcnt(0)\n"
  "s_waitcnt vmcnt(38)\n"
  "v_mfma_f32_16x16x32_bf16 v[196:199], v[92:95], a[104:107], v[196:199]\n"
  "ds_read_b64_tr_b16 v[68:69], v30 offset:36864\n"
  "ds_read_b64_tr_b16 v[70:71], v30 offset:39936\n"
  "buffer_load_dwordx4 %[voffWq], %[srdWq], s64 offen lds\n"
  "s_waitcnt vmcnt(37)\n"
  "v_mfma_f32_16x16x32_bf16 v[200:203], v[92:95], a[108:111], v[200:203]\n"
  "ds_read_b64_tr_b16 v[72:73], v31 offset:36864\n"
  "v_mfma_f32_16x16x32_bf16 v[204:207], v[96:99], a[104:107], v[204:207]\n"
  "ds_read_b64_tr_b16 v[74:75], v31 offset:39936\n"
  "ds_read_b64_tr_b16 v[76:77], v30 offset:37888\n"
  "s_add_u32 m0, s75, 17408\n"
  "v_mfma_f32_16x16x32_bf16 v[208:211], v[96:99], a[108:111], v[208:211]\n"
  "ds_read_b64_tr_b16 v[78:79], v30 offset:40960\n"
  "s_add_u32 s64, s87, 655360\n"
  "v_mfma_f32_16x16x32_bf16 v[212:215], v[100:103], a[104:107], v[212:215]\n"
  "ds_read_b64_tr_b16 v[80:81], v31 offset:37888\n"
  "ds_read_b64_tr_b16 v[82:83], v31 offset:40960\n"
  "buffer_load_dwordx4 %[voffWq], %[srdWq], s64 offen lds\n"
  "v_mfma_f32_16x16x32_bf16 v[216:219], v[100:103], a[108:111], v[216:219]\n"
  "ds_read_b64_tr_b16 v[84:85], v30 offset:38912\n"
  "v_mfma_f32_16x16x32_bf16 v[220:223], v[104:107], a[104:107], v[220:223]\n"
  "ds_read_b64_tr_b16 v[86:87], v30 offset:41984\n"
  "ds_read_b64_tr_b16 v[88:89], v31 offset:38912\n"
  "v_mfma_f32_16x16x32_bf16 v[224:227], v[104:107], a[108:111], v[224:227]\n"
  "ds_read_b64_tr_b16 v[90:91], v31 offset:41984\n"
  "s_add_u32 m0, s75, 24576\n"
  "v_mfma_f32_16x16x32_bf16 v[228:231], v[108:111], a[104:107], v[228:231]\n"
  "s_add_u32 s64, s87, 786432\n"
  "v_mfma_f32_16x16x32_bf16 v[232:235], v[108:111], a[108:111], v[232:235]\n"
  "buffer_load_dwordx4 %[voffWq], %[srdWq], s64 offen lds\n"
  "v_mfma_f32_16x16x32_bf16 v[236:239], v[112:115], a[104:107], v[236:239]\n"
  "v_mfma_f32_16x16x32_bf16 v[240:243], v[112:115], a[108:111], v[240:243]\n"
  "s_add_u32 m0, s75, 25600\n"
  "s_waitcnt lgkmcnt(0)\n"
  "s_waitcnt vmcnt(38)\n"
  "v_mfma_f32_16x16x32_bf16 v[196:199], v[68:71], a[112:115], v[196:199]\n"
  "ds_read_b64_tr_b16 v[92:93], v30 offset:43008\n"
  "ds_read_b64_tr_b16 v[94:95], v30 offset:46080\n"
  "s_add_u32 s64, s87, 917504\n"
  "s_waitcnt vmcnt(36)\n"
  "v_mfma_f32_16x16x32_bf16 v[200:203], v[68:71], a[116:119], v[200:203]\n"
  "ds_read_b64_tr_b16 v[96:97], v31 offset:43008\n"
  "buffer_load_dwordx4 %[voffWq], %[srdWq], s64 offen lds\n"
  "v_mfma_f32_16x16x32_bf16 v[204:207], v[72:75], a[112:115], v[204:207]\n"
  "ds_read_b64_tr_b16 v[98:99], v31 offset:46080\n"
  "ds_read_b64_tr_b16 v[100:101], v30 offset:44032\n"
  "v_mfma_f32_16x16x32_bf16 v[208:211], v[72:75], a[116:119], v[208:211]\n"
  "ds_read_b64_tr_b16 v[102:103], v30 offset:47104\n"
  "v_cvt_pk_bf16_f32 v144, v144, v145\n"
  "v_cvt_pk_bf16_f32 v145, v146, v147\n"
  "v_cvt_pk_bf16_f32 v146, v160, v161\n"
  "v_mfma_f32_16x16x32_bf16 v[212:215], v[76:79], a[112:115], v[212:215]\n"
  "ds_read_b64_tr_b16 v[104:105], v31 offset:44032\n"
  "ds_read_b64_tr_b16 v[106:107], v31 offset:47104\n"
  "v_cvt_pk_bf16_f32 v147, v162, v163\n"
  "v_mfma_f32_16x16x32_bf16 v[216:219], v[76:79], a[116:119], v[216:219]\n"
  "ds_read_b64_tr_b16 v[108:109], v30 offset:45056\n"
  "s_add_u32 s66, s66, s67\n"
  "v_mfma_f32_16x16x32_bf16 v[220:223], v[80:83], a[112:115], v[220:223]\n"
  "ds_read_b64_tr_b16 v[110:111], v30 offset:48128\n"
  "ds_read_b64_tr_b16 v[112:113], v31 offset:45056\n"
  "buffer_store_dwordx4 v[144:147], %[L16], %[srdWS], s66 offen sc1\n"
  "v_mfma_f32_16x16x32_bf16 v[224:227], v[80:83], a[116:119], v[224:227]\n"
  "ds_read_b64_tr_b16 v[114:115], v31 offset:48128\n"
  "v_mfma_f32_16x16x32_bf16 v[228:231], v[84:87], a[112:115], v[228:231]\n"
  "v_mfma_f32_16x16x32_bf16 v[232:235], v[84:87], a[116:119], v[232:235]\n"
  "v_mfma_f32_16x16x32_bf16 v[236:239], v[88:91], a[112:115], v[236:239]\n"
  "v_mfma_f32_16x16x32_bf16 v[240:243], v[88:91], a[116:119], v[240:243]\n"
  "buffer_load_dwordx2 v[244:245], v35, %[srdB1], 0 offen offset:0\n"
  "s_waitcnt lgkmcnt(0)\n"
  "s_waitcnt vmcnt(38)\n"
  "v_mfma_f32_16x16x32_bf16 v[196:199], v[92:95], a[120:123], v[196:199]\n"
  "ds_read_b64_tr_b16 v[68:69], v32 offset:0\n"
  "ds_read_b64_tr_b16 v[70:71], v32 offset:3072\n"
  "s_waitcnt vmcnt(36)\n"
  "v_mfma_f32_16x16x32_bf16 v[200:203], v[92:95], a[124:127], v[200:203]\n"
  "ds_read_b64_tr_b16 v[72:73], v33 offset:0\n"
  "v_mfma_f32_16x16x32_bf16 v[204:207], v[96:99], a[120:123], v[204:207]\n"
  "ds_read_b64_tr_b16 v[74:75], v33 offset:3072\n"
  "ds_read_b64_tr_b16 v[76:77], v32 offset:1024\n"
  "v_mfma_f32_16x16x32_bf16 v[208:211], v[96:99], a[124:127], v[208:211]\n"
  "ds_read_b64_tr_b16 v[78:79], v32 offset:4096\n"
  "buffer_load_dwordx2 v[246:247], v35, %[srdB1], 0 offen offset:32\n"
  "v_mfma_f32_16x16x32_bf16 v[212:215], v[100:103], a[120:123], v[212:215]\n"
  "ds_read_b64_tr_b16 v[80:81], v33 offset:1024\n"
  "ds_read_b64_tr_b16 v[82:83], v33 offset:4096\n"
  "v_mfma_f32_16x16x32_bf16 v[216:219], v[100:103], a[124:127], v[216:219]\n"
  "ds_read_b64_tr_b16 v[84:85], v32 offset:2048\n"
  "v_mfma_f32_16x16x32_bf16 v[220:223], v[104:107], a[120:123], v[220:223]\n"
  "ds_read_b64_tr_b16 v[86:87], v32 offset:5120\n"
  "ds_read_b64_tr_b16 v[88:89], v33 offset:2048\n"
  "v_mfma_f32_16x16x32_bf16 v[224:227], v[104:107], a[124:127], v[224:227]\n"
  "ds_read_b64_tr_b16 v[90:91], v33 offset:5120\n"
  "v_mfma_f32_16x16x32_bf16 v[228:231], v[108:111], a[120:123], v[228:231]\n"
  "buffer_load_dwordx2 v[248:249], v35, %[srdB1], 0 offen offset:64\n"
  "v_mfma_f32_16x16x32_bf16 v[232:235], v[108:111], a[124:127], v[232:235]\n"
  "v_mfma_f32_16x16x32_bf16 v[236:239], v[112:115], a[120:123], v[236:239]\n"
  "v_mfma_f32_16x16x32_bf16 v[240:243], v[112:115], a[124:127], v[240:243]\n"
  "s_waitcnt lgkmcnt(0)\n"
  "s_waitcnt vmcnt(36)\n"
  "v_mfma_f32_16x16x32_bf16 v[196:199], v[68:71], a[128:131], v[196:199]\n"
  "ds_read_b64_tr_b16 v[92:93], v32 offset:6144\n"
  "ds_read_b64_tr_b16 v[94:95], v32 offset:9216\n"
  "v_cvt_pk_bf16_f32 v164, v164, v165\n"
  "v_cvt_pk_bf16_f32 v165, v166, v167\n"
  "v_cvt_pk_bf16_f32 v166, v180, v181\n"
  "v_cvt_pk_bf16_f32 v167, v182, v183\n"
  "s_add_u32 s66, s66, s68\n"
  "buffer_store_dwordx4 v[164:167], %[L16], %[srdWS], s66 offen sc1\n"
  "s_waitcnt vmcnt(36)\n"
  "v_mfma_f32_16x16x32_bf16 v[200:203], v[68:71], a[132:135], v[200:203]\n"
  "ds_read_b64_tr_b16 v[96:97], v33 offset:6144\n"
  "v_mfma_f32_16x16x32_bf16 v[204:207], v[72:75], a[128:131], v[204:207]\n"
  "ds_read_b64_tr_b16 v[98:99], v33 offset:9216\n"
  "ds_read_b64_tr_b16 v[100:101], v32 offset:7168\n"
  "v_mfma_f32_16x16x32_bf16 v[208:211], v[72:75], a[132:135], v[208:211]\n"
  "ds_read_b64_tr_b16 v[102:103], v32 offset:10240\n"
  "v_mfma_f32_16x16x32_bf16 v[212:215], v[76:79], a[128:131], v[212:215]\n"
  "ds_read_b64_tr_b16 v[104:105], v33 offset:7168\n"
  "ds_read_b64_tr_b16 v[106:107], v33 offset:10240\n"
  "v_mfma_f32_16x16x32_bf16 v[216:219], v[76:79], a[132:135], v[216:219]\n"
  "ds_read_b64_tr_b16 v[108:109], v32 offset:8192\n"
  "buffer_load_dwordx2 v[250:251], v35, %[srdB1], 0 offen offset:96\n"
  "v_mfma_f32_16x16x32_bf16 v[220:223], v[80:83], a[128:131], v[220:223]\n"
  "ds_read_b64_tr_b16 v[110:111], v32 offset:11264\n"
  "ds_read_b64_tr_b16 v[112:113], v33 offset:8192\n"
  "v_mfma_f32_16x16x32_bf16 v[224:227], v[80:83], a[132:135], v[224:227]\n"
  "ds_read_b64_tr_b16 v[114:115], v33 offset:11264\n"
  "v_mfma_f32_16x16x32_bf16 v[228:231], v[84:87], a[128:131], v[228:231]\n"
  "v_mfma_f32_16x16x32_bf16 v[232:235], v[84:87], a[132:135], v[232:235]\n"
  "v_mfma_f32_16x16x32_bf16 v[236:239], v[88:91], a[128:131], v[236:239]\n"
  "buffer_load_dwordx2 v[252:253], v35, %[srdB1], 0 offen offset:128\n"
  "v_mfma_f32_16x16x32_bf16 v[240:243], v[88:91], a[132:135], v[240:243]\n"
  "s_waitcnt lgkmcnt(0)\n"
  "s_waitcnt vmcnt(36)\n"
  "v_mfma_f32_16x16x32_bf16 v[196:199], v[92:95], a[136:139], v[196:199]\n"
  "ds_read_b64_tr_b16 v[68:69], v32 offset:12288\n"
  "ds_read_b64_tr_b16 v[70:71], v32 offset:15360\n"
  "s_waitcnt vmcnt(35)\n"
  "v_mfma_f32_16x16x32_bf16 v[200:203], v[92:95], a[140:143], v[200:203]\n"
  "ds_read_b64_tr_b16 v[72:73], v33 offset:12288\n"
  "v_mfma_f32_16x16x32_bf16 v[204:207], v[96:99], a[136:139], v[204:207]\n"
  "ds_read_b64_tr_b16 v[74:75], v33 offset:15360\n"
  "ds_read_b64_tr_b16 v[76:77], v32 offset:13312\n"
  "buffer_load_dwordx2 v[254:255], v35, %[srdB1], 0 offen offset:160\n"
  "v_mfma_f32_16x16x32_bf16 v[208:211], v[96:99], a[140:143], v[208:211]\n"
  "ds_read_b64_tr_b16 v[78:79], v32 offset:16384\n"
  "v_mfma_f32_16x16x32_bf16 v[212:215], v[100:103], a[136:139], v[212:215]\n"
  "ds_read_b64_tr_b16 v[80:81], v33 offset:13312\n"
  "ds_read_b64_tr_b16 v[82:83], v33 offset:16384\n"
  "s_add_u32 s87, %[rot], 1\n"
  "s_and_b32 s87, s87, 3\n"
  "s_lshl_b32 s87, s87, 20\n"
  "v_mfma_f32_16x16x32_bf16 v[216:219], v[100:103], a[140:143], v[216:219]\n"
  "ds_read_b64_tr_b16 v[84:85], v32 offset:14336\n"
  "s_add_u32 m0, s75, 32768\n"
  "v_mfma_f32_16x16x32_bf16 v[220:223], v[104:107], a[136:139], v[220:223]\n"
  "ds_read_b64_tr_b16 v[86:87], v32 offset:17408\n"
  "ds_read_b64_tr_b16 v[88:89], v33 offset:14336\n"
  "s_add_u32 s64, s87, 0\n"
  "v_mfma_f32_16x16x32_bf16 v[224:227], v[104:107], a[140:143], v[224:227]\n"
  "ds_read_b64_tr_b16 v[90:91], v33 offset:17408\n"
  "buffer_load_dwordx4 %[voffWq], %[srdWq], s64 offen lds\n"
  "v_mfma_f32_16x16x32_bf16 v[228:231], v[108:111], a[136:139], v[228:231]\n"
  "v_mfma_f32_16x16x32_bf16 v[232:235], v[108:111], a[140:143], v[232:235]\n"
  "v_cvt_pk_bf16_f32 v168, v168, v169\n"
  "v_cvt_pk_bf16_f32 v169, v170, v171\n"
  "v_cvt_pk_bf16_f32 v170, v184, v185\n"
  "v_cvt_pk_bf16_f32 v171, v186, v187\n"
  "v_mfma_f32_16x16x32_bf16 v[236:239], v[112:115], a[136:139], v[236:239]\n"
  "s_add_u32 s66, s66, s67\n"
  "v_mfma_f32_16x16x32_bf16 v[240:243], v[112:115], a[140:143], v[240:243]\n"
  "buffer_store_dwordx4 v[168:171], %[L16], %[srdWS], s66 offen sc1\n"
  "s_waitcnt lgkmcnt(0)\n"
  "s_waitcnt vmcnt(35)\n"
  "v_mfma_f32_16x16x32_bf16 v[196:199], v[68:71], a[144:147], v[196:199]\n"
  "ds_read_b64_tr_b16 v[92:93], v32 offset:18432\n"
  "ds_read_b64_tr_b16 v[94:95], v32 offset:21504\n"
  "s_waitcnt vmcnt(34)\n"
  "v_mfma_f32_16x16x32_bf16 v[200:203], v[68:71], a[148:151], v[200:203]\n"
  "ds_read_b64_tr_b16 v[96:97], v33 offset:18432\n"
  "v_mfma_f32_16x16x32_bf16 v[204:207], v[72:75], a[144:147], v[204:207]\n"
  "ds_read_b64_tr_b16 v[98:99], v33 offset:21504\n"
  "ds_read_b64_tr_b16 v[100:101], v32 offset:19456\n"
  "s_add_u32 m0, s75, 33792\n"
  "v_mfma_f32_16x16x32_bf16 v[208:211], v[72:75], a[148:151], v[208:211]\n"
  "ds_read_b64_tr_b16 v[102:103], v32 offset:22528\n"
  "s_add_u32 s64, s87, 131072\n"
  "v_mfma_f32_16x16x32_bf16 v[212:215], v[76:79], a[144:147], v[212:215]\n"
  "ds_read_b64_tr_b16 v[104:105], v33 offset:19456\n"
  "ds_read_b64_tr_b16 v[106:107], v33 offset:22528\n"
  "buffer_load_dwordx4 %[voffWq], %[srdWq], s64 offen lds\n"
  "v_mfma_f32_16x16x32_bf16 v[216:219], v[76:79], a[148:151], v[216:219]\n"
  "ds_read_b64_tr_b16 v[108:109], v32 offset:20480\n"
  "v_mfma_f32_16x16x32_bf16 v[220:223], v[80:83], a[144:147], v[220:223]\n"
  "ds_read_b64_tr_b16 v[110:111], v32 offset:23552\n"
  "ds_read_b64_tr_b16 v[112:113], v33 offset:20480\n"
  "s_add_u32 m0, s75, 40960\n"
  "v_mfma_f32_16x16x32_bf16 v[224:227], v[80:83], a[148:151], v[224:227]\n"
  "ds_read_b64_tr_b16 v[114:115], v33 offset:23552\n"
  "s_add_u32 s64, s87, 262144\n"
  "v_mfma_f32_16x16x32_bf16 v[228:231], v[84:87], a[144:147], v[228:231]\n"
  "buffer_load_dwordx4 %[voffWq], %[srdWq], s64 offen lds\n"
  "v_mfma_f32_16x16x32_bf16 v[232:235], v[84:87], a[148:151], v[232:235]\n"
  "v_mfma_f32_16x16x32_bf16 v[236:239], v[88:91], a[144:147], v[236:239]\n"
  "v_mfma_f32_16x16x32_bf16 v[240:243], v[88:91], a[148:151], v[240:243]\n"
  "s_add_u32 m0, s75, 41984\n"
  "s_waitcnt lgkmcnt(0)\n"
  "s_waitcnt vmcnt(35)\n"
  "v_mfma_f32_16x16x32_bf16 v[196:199], v[92:95], a[152:155], v[196:199]\n"
  "ds_read_b64_tr_b16 v[68:69], v32 offset:24576\n"
  "ds_read_b64_tr_b16 v[70:71], v32 offset:27648\n"
  "s_add_u32 s64, s87, 393216\n"
  "s_waitcnt vmcnt(33)\n"
  "v_mfma_f32_16x16x32_bf16 v[200:203], v[92:95], a[156:159], v[200:203]\n"
  "ds_read_b64_tr_b16 v[72:73], v33 offset:24576\n"
  "buffer_load_dwordx4 %[voffWq], %[srdWq], s64 offen lds\n"
  "v_mfma_f32_16x16x32_bf16 v[204:207], v[96:99], a[152:155], v[204:207]\n"
  "ds_read_b64_tr_b16 v[74:75], v33 offset:27648\n"
  "ds_read_b64_tr_b16 v[76:77], v32 offset:25600\n"
  "v_mfma_f32_16x16x32_bf16 v[208:211], v[96:99], a[156:159], v[208:211]\n"
  "ds_read_b64_tr_b16 v[78:79], v32 offset:28672\n"
  "v_mfma_f32_16x16x32_bf16 v[212:215], v[100:103], a[152:155], v[212:215]\n"
  "ds_read_b64_tr_b16 v[80:81], v33 offset:25600\n"
  "ds_read_b64_tr_b16 v[82:83], v33 offset:28672\n"
  "v_mfma_f32_16x16x32_bf16 v[216:219], v[100:103], a[156:159], v[216:219]\n"
  "ds_read_b64_tr_b16 v[84:85], v32 offset:26624\n"
  "v_mfma_f32_16x16x32_bf16 v[220:223], v[104:107], a[152:155], v[220:223]\n"
  "ds_read_b64_tr_b16 v[86:87], v32 offset:29696\n"
  "ds_read_b64_tr_b16 v[88:89], v33 offset:26624\n"
  "v_cvt_pk_bf16_f32 v172, v172, v173\n"
  "v_cvt_pk_bf16_f32 v173, v174, v175\n"
  "v_cvt_pk_bf16_f32 v174, v188, v189\n"
  "v_cvt_pk_bf16_f32 v175, v190, v191\n"
  "s_add_u32 s66, s66, s67\n"
  "buffer_store_dwordx4 v[172:175], %[L16], %[srdWS], s66 offen sc1\n"
  "v_mfma_f32_16x16x32_bf16 v[224:227], v[104:107], a[156:159], v[224:227]\n"
  "ds_read_b64_tr_b16 v[90:91], v33 offset:29696\n"
  "v_mfma_f32_16x16x32_bf16 v[228:231], v[108:111], a[152:155], v[228:231]\n"
  "s_add_u32 m0, s75, 49152\n"
  "v_mfma_f32_16x16x32_bf16 v[232:235], v[108:111], a[156:159], v[232:235]\n"
  "s_add_u32 s64, s87, 524288\n"
  "v_mfma_f32_16x16x32_bf16 v[236:239], v[112:115], a[152:155], v[236:239]\n"
  "buffer_load_dwordx4 %[voffWq], %[srdWq], s64 offen lds\n"
  "v_mfma_f32_16x16x32_bf16 v[240:243], v[112:115], a[156:159], v[240:243]\n"
  "s_waitcnt lgkmcnt(0)\n"
  "s_waitcnt vmcnt(35)\n"
  "v_mfma_f32_16x16x32_bf16 v[196:199], v[68:71], a[160:163], v[196:199]\n"
  "ds_read_b64_tr_b16 v[92:93], v32 offset:30720\n"
  "ds_read_b64_tr_b16 v[94:95], v32 offset:33792\n"
  "s_waitcnt vmcnt(33)\n"
  "v_mfma_f32_16x16x32_bf16 v[200:203], v[68:71], a[164:167], v[200:203]\n"
  "ds_read_b64_tr_b16 v[96:97], v33 offset:30720\n"
  "s_add_u32 m0, s75, 50176\n"
  "v_mfma_f32_16x16x32_bf16 v[204:207], v[72:75], a[160:163], v[204:207]\n"
  "ds_read_b64_tr_b16 v[98:99], v33 offset:33792\n"
  "ds_read_b64_tr_b16 v[100:101], v32 offset:31744\n"
  "s_add_u32 s64, s87, 655360\n"
  "v_mfma_f32_16x16x32_bf16 v[208:211], v[72:75], a[164:167], v[208:211]\n"
  "ds_read_b64_tr_b16 v[102:103], v32 offset:34816\n"
  "buffer_load_dwordx4 %[voffWq], %[srdWq], s64 offen lds\n"
  "v_mfma_f32_16x16x32_bf16 v[212:215], v[76:79], a[160:163], v[212:215]\n"
  "ds_read_b64_tr_b16 v[104:105], v33 offset:31744\n"
  "ds_read_b64_tr_b16 v[106:107], v33 offset:34816\n"
  "v_mfma_f32_16x16x32_bf16 v[216:219], v[76:79], a[164:167], v[216:219]\n"
  "ds_read_b64_tr_b16 v[108:109], v32 offset:32768\n"
  "s_add_u32 m0, s75, 57344\n"
  "v_mfma_f32_16x16x32_bf16 v[220:223], v[80:83], a[160:163], v[220:223]\n"
  "ds_read_b64_tr_b16 v[110:111], v32 offset:35840\n"
  "ds_read_b64_tr_b16 v[112:113], v33 offset:32768\n"
  "s_add_u32 s64, s87, 786432\n"
  "v_mfma_f32_16x16x32_bf16 v[224:227], v[80:83], a[164:167], v[224:227]\n"
  "ds_read_b64_tr_b16 v[114:115], v33 offset:35840\n"
  "buffer_load_dwordx4 %[voffWq], %[srdWq], s64 offen lds\n"
  "v_mfma_f32_16x16x32_bf16 v[228:231], v[84:87], a[160:163], v[228:231]\n"
  "v_mfma_f32_16x16x32_bf16 v[232:235], v[84:87], a[164:167], v[232:235]\n"
  "v_mfma_f32_16x16x32_bf16 v[236:239], v[88:91], a[160:163], v[236:239]\n"
  "s_add_u32 m0, s75, 58368\n"
  "v_mfma_f32_16x16x32_bf16 v[240:243], v[88:91], a[164:167], v[240:243]\n"
  "s_add_u32 s64, s87, 917504\n"
  "s_waitcnt lgkmcnt(0)\n"
  "s_waitcnt vmcnt(34)\n"
  "v_mfma_f32_16x16x32_bf16 v[196:199], v[92:95], a[168:171], v[196:199]\n"
  "ds_read_b64_tr_b16 v[68:69], v32 offset:36864\n"
  "ds_read_b64_tr_b16 v[70:71], v32 offset:39936\n"
  "buffer_load_dwordx4 %[voffWq], %[srdWq], s64 offen lds\n"
  "s_waitcnt vmcnt(34)\n"
  "v_mfma_f32_16x16x32_bf16 v[200:203], v[92:95], a[172:175], v[200:203]\n"
  "ds_read_b64_tr_b16 v[72:73], v33 offset:36864\n"
  "v_mfma_f32_16x16x32_bf16 v[204:207], v[96:99], a[168:171], v[204:207]\n"
  "ds_read_b64_tr_b16 v[74:75], v33 offset:39936\n"
  "ds_read_b64_tr_b16 v[76:77], v32 offset:37888\n"
  "v_mfma_f32_16x16x32_bf16 v[208:211], v[96:99], a[172:175], v[208:211]\n"
  "ds_read_b64_tr_b16 v[78:79], v32 offset:40960\n"
  "v_cvt_pk_bf16_f32 v176, v176, v177\n"
  "v_cvt_pk_bf16_f32 v177, v178, v179\n"
  "v_cvt_pk_bf16_f32 v178, v192, v193\n"
  "v_cvt_pk_bf16_f32 v179, v194, v195\n"
  "v_mfma_f32_16x16x32_bf16 v[212:215], v[100:103], a[168:171], v[212:215]\n"
  "ds_read_b64_tr_b16 v[80:81], v33 offset:37888\n"
  "ds_read_b64_tr_b16 v[82:83], v33 offset:40960\n"
  "s_add_u32 s66, s66, s67\n"
  "v_mfma_f32_16x16x32_bf16 v[216:219], v[100:103], a[172:175], v[216:219]\n"
  "ds_read_b64_tr_b16 v[84:85], v32 offset:38912\n"
  "buffer_store_dwordx4 v[176:179], %[L16], %[srdWS], s66 offen sc1\n"
  "v_mfma_f32_16x16x32_bf16 v[220:223], v[104:107], a[168:171], v[220:223]\n"
  "ds_read_b64_tr_b16 v[86:87], v32 offset:41984\n"
  "ds_read_b64_tr_b16 v[88:89], v33 offset:38912\n"
  "s_add_u32 s96, %[rot], 1\n"
  "s_and_b32 s96, s96, 3\n"
  "s_lshl_b32 s96, s96, 10\n"
  "v_mfma_f32_16x16x32_bf16 v[224:227], v[104:107], a[172:175], v[224:227]\n"
  "ds_read_b64_tr_b16 v[90:91], v33 offset:41984\n"
  "s_add_u32 s96, s96, %[oX]\n"
  "s_add_u32 s97, s96, 0x4000\n"
  "v_mfma_f32_16x16x32_bf16 v[228:231], v[108:111], a[168:171], v[228:231]\n"
  "s_add_u32 s98, s96, 0x8000\n"
  "s_add_u32 s99, s96, 0xc000\n"
  "v_mfma_f32_16x16x32_bf16 v[232:235], v[108:111], a[172:175], v[232:235]\n"
  "buffer_load_dwordx4 a[192:195], %[voffX], %[srdWS], s96 offen offset:0 sc1\n"
  "v_mfma_f32_16x16x32_bf16 v[236:239], v[112:115], a[168:171], v[236:239]\n"
  "v_mfma_f32_16x16x32_bf16 v[240:243], v[112:115], a[172:175], v[240:243]\n"
  "s_waitcnt lgkmcnt(0)\n"
  "s_waitcnt vmcnt(34)\n"
  "v_mfma_f32_16x16x32_bf16 v[196:199], v[68:71], a[176:179], v[196:199]\n"
  "ds_read_b64_tr_b16 v[92:93], v32 offset:43008\n"
  "ds_read_b64_tr_b16 v[94:95], v32 offset:46080\n"
  "s_waitcnt vmcnt(32)\n"
  "v_mfma_f32_16x16x32_bf16 v[200:203], v[68:71], a[180:183], v[200:203]\n"
  "ds_read_b64_tr_b16 v[96:97], v33 offset:43008\n"
  "v_mfma_f32_16x16x32_bf16 v[204:207], v[72:75], a[176:179], v[204:207]\n"
  "ds_read_b64_tr_b16 v[98:99], v33 offset:46080\n"
  "ds_read_b64_tr_b16 v[100:101], v32 offset:44032\n"
  "buffer_load_dwordx4 a[196:199], %[voffX], %[srdWS], s97 offen offset:0 sc1\n"
  "v_mfma_f32_16x16x32_bf16 v[208:211], v[72:75], a[180:183], v[208:211]\n"
  "ds_read_b64_tr_b16 v[102:103], v32 offset:47104\n"
  "v_mfma_f32_16x16x32_bf16 v[212:215], v[76:79], a[176:179], v[212:215]\n"
  "ds_read_b64_tr_b16 v[104:105], v33 offset:44032\n"
  "ds_read_b64_tr_b16 v[106:107], v33 offset:47104\n"
  "v_mfma_f32_16x16x32_bf16 v[216:219], v[76:79], a[180:183], v[216:219]\n"
  "ds_read_b64_tr_b16 v[108:109], v32 offset:45056\n"
  "v_mfma_f32_16x16x32_bf16 v[220:223], v[80:83], a[176:179], v[220:223]\n"
  "ds_read_b64_tr_b16 v[110:111], v32 offset:48128\n"
  "ds_read_b64_tr_b16 v[112:113], v33 offset:45056\n"
  "buffer_load_dwordx4 a[200:203], %[voffX], %[srdWS], s98 offen offset:0 sc1\n"
  "v_mfma_f32_16x16x32_bf16 v[224:227], v[80:83], a[180:183], v[224:227]\n"
  "ds_read_b64_tr_b16 v[114:115], v33 offset:48128\n"
  "v_mfma_f32_16x16x32_bf16 v[228:231], v[84:87], a[176:179], v[228:231]\n"
  "v_mfma_f32_16x16x32_bf16 v[232:235], v[84:87], a[180:183], v[232:235]\n"
  "v_mfma_f32_16x16x32_bf16 v[236:239], v[88:91], a[176:179], v[236:239]\n"
  "v_mfma_f32_16x16x32_bf16 v[240:243], v[88:91], a[180:183], v[240:243]\n"
  "buffer_load_dwordx4 a[204:207], %[voffX], %[srdWS], s99 offen offset:0 sc1\n"
  "s_waitcnt vmcnt(23) lgkmcnt(0)\n"
  "s_barrier\n"
  "v_mfma_f32_16x16x32_bf16 v[196:199], v[92:95], a[184:187], v[196:199]\n"
  "ds_read_b64_tr_b16 v[36:37], v20 offset:0\n"
  "ds_read_b64_tr_b16 v[38:39], v20 offset:4096\n"
  "v_mfma_f32_16x16x32_bf16 v[200:203], v[92:95], a[188:191], v[200:203]\n"
  "ds_read_b64_tr_b16 v[40:41], v21 offset:0\n"
  "ds_read_b64_tr_b16 v[42:43], v21 offset:4096\n"
  "v_mfma_f32_16x16x32_bf16 v[204:207], v[96:99], a[184:187], v[204:207]\n"
  "ds_read_b64_tr_b16 v[44:45], v20 offset:512\n"
  "ds_read_b64_tr_b16 v[46:47], v20 offset:4608\n"
  "v_mfma_f32_16x16x32_bf16 v[208:211], v[96:99], a[188:191], v[208:211]\n"
  "ds_read_b64_tr_b16 v[48:49], v21 offset:512\n"
  "ds_read_b64_tr_b16 v[50:51], v21 offset:4608\n"
  "v_mfma_f32_16x16x32_bf16 v[212:215], v[100:103], a[184:187], v[212:215]\n"
  "ds_read_b64_tr_b16 v[52:53], v20 offset:2048\n"
  "buffer_load_dwordx4 a[208:211], %[voffX], %[srdWS], s96 offen offset:256 sc1\n"
  "v_mfma_f32_16x16x32_bf16 v[216:219], v[100:103], a[188:191], v[216:219]\n"
  "ds_read_b64_tr_b16 v[54:55], v20 offset:6144\n"
  "ds_read_b64_tr_b16 v[56:57], v21 offset:2048\n"
  "v_mfma_f32_16x16x32_bf16 v[220:223], v[104:107], a[184:187], v[220:223]\n"
  "ds_read_b64_tr_b16 v[58:59], v21 offset:6144\n"
  "ds_read_b64_tr_b16 v[60:61], v20 offset:2560\n"
  "v_mfma_f32_16x16x32_bf16 v[224:227], v[104:107], a[188:191], v[224:227]\n"
  "ds_read_b64_tr_b16 v[62:63], v20 offset:6656\n"
  "ds_read_b64_tr_b16 v[64:65], v21 offset:2560\n"
  "v_mfma_f32_16x16x32_bf16 v[228:231], v[108:111], a[184:187], v[228:231]\n"
  "ds_read_b64_tr_b16 v[66:67], v21 offset:6656\n"
  "buffer_load_dwordx4 a[212:215], %[voffX], %[srdWS], s97 offen offset:256 sc1\n"
  "v_mfma_f32_16x16x32_bf16 v[232:235], v[108:111], a[188:191], v[232:235]\n"
  "v_mfma_f32_16x16x32_bf16 v[236:239], v[112:115], a[184:187], v[236:239]\n"
  "v_mfma_f32_16x16x32_bf16 v[240:243], v[112:115], a[188:191], v[240:243]\n"
  "s_waitcnt lgkmcnt(14)\n"
  "v_mfma_f32_16x16x32_bf16 v[68:71], v[36:39], a[0:3], 0\n"
  "v_mfma_f32_16x16x32_bf16 v[72:75], v[36:39], a[4:7], 0\n"
  "buffer_load_dwordx4 a[216:219], %[voffX], %[srdWS], s98 offen offset:256 sc1\n"
  "v_mfma_f32_16x16x32_bf16 v[76:79], v[36:39], a[8:11], 0\n"
  "v_mfma_f32_16x16x32_bf16 v[80:83], v[36:39], a[12:15], 0\n"
  "ds_read_b64_tr_b16 v[36:37], v20 offset:8192\n"
  "ds_read_b64_tr_b16 v[38:39], v20 offset:12288\n"
  "s_waitcnt lgkmcnt(14)\n"
  "v_mfma_f32_16x16x32_bf16 v[84:87], v[40:43], a[0:3], 0\n"
  "v_mov_b32 v16, 0xbdd2d3e8\n"
  "v_mfma_f32_16x16x32_bf16 v[88:91], v[40:43], a[4:7], 0\n"
  "buffer_load_dwordx4 a[220:223], %[voffX], %[srdWS], s99 offen offset:256 sc1\n"
  "v_mfma_f32_16x16x32_bf16 v[92:95], v[40:43], a[8:11], 0\n"
  "s_add_u32 s77, %[oH], 0\n"
  "s_cmp_gt_u32 %[nH], 0\n"
  "s_cselect_b32 s77, s77, 0x7f000000\n"
  "v_mfma_f32_16x16x32_bf16 v[96:99], v[40:43], a[12:15], 0\n"
  "ds_read_b64_tr_b16 v[40:41], v21 offset:8192\n"
  "ds_read_b64_tr_b16 v[42:43], v21 offset:12288\n"
  "s_waitcnt lgkmcnt(14)\n"
  "v_mfma_f32_16x16x32_bf16 v[100:103], v[44:47], a[0:3], 0\n"
  "s_waitcnt vmcnt(25)\n"
  "v_lshlrev_b32 v17, 16, v244\n"
  "v_and_b32 v18, 0xffff0000, v244\n"
  "v_mfma_f32_16x16x32_bf16 v[104:107], v[44:47], a[4:7], 0\n"
  "v_lshlrev_b32 v19, 16, v245\n"
  "v_and_b32 v24, 0xffff0000, v245\n"
  "v_mfma_f32_16x16x32_bf16 v[108:111], v[44:47], a[8:11], 0\n"
  "buffer_load_dwordx4 a[224:227], %[voffX], %[srdWS], s96 offen offset:512 sc1\n"
  "v_add_f32 v196, v196, v17\n"
  "v_mfma_f32_16x16x32_bf16 v[112:115], v[44:47], a[12:15], 0\n"
  "ds_read_b64_tr_b16 v[44:45], v20 offset:8704\n"
  "ds_read_b64_tr_b16 v[46:47], v20 offset:12800\n"
  "s_waitcnt lgkmcnt(14)\n"
  "v_mfma_f32_16x16x32_bf16 v[116:119], v[48:51], a[0:3], 0\n"
  "v_add_f32 v197, v197, v18\n"
  "v_add_f32 v198, v198, v19\n"
  "v_mfma_f32_16x16x32_bf16 v[120:123], v[48:51], a[4:7], 0\n"
  "v_add_f32 v199, v199, v24\n"
  "v_mul_f32 v30, v196, v196\n"
  "v_mfma_f32_16x16x32_bf16 v[124:127], v[48:51], a[8:11], 0\n"
  "v_mul_f32 v31, v197, v197\n"
  "v_mul_f32 v32, v198, v198\n"
  "v_mfma_f32_16x16x32_bf16 v[128:131], v[48:51], a[12:15], 0\n"
  "ds_read_b64_tr_b16 v[48:49], v21 offset:8704\n"
  "ds_read_b64_tr_b16 v[50:51], v21 offset:12800\n"
  "buffer_load_dwordx4 a[228:231], %[voffX], %[srdWS], s97 offen offset:512 sc1\n"
  "s_waitcnt lgkmcnt(14)\n"
  "v_mfma_f32_16x16x32_bf16 v[132:135], v[52:55], a[0:3], 0\n"
  "v_mul_f32 v33, v199, v199\n"
  "v_fmaak_f32 v30, v16, v30, 0xc0135761\n"
  "v_mfma_f32_16x16x32_bf16 v[136:139], v[52:55], a[4:7], 0\n"
  "v_fmaak_f32 v31, v16, v31, 0xc0135761\n"
  "v_fmaak_f32 v32, v16, v32, 0xc0135761\n"
  "v_mfma_f32_16x16x32_bf16 v[140:143], v[52:55], a[8:11], 0\n"
  "v_fmaak_f32 v33, v16, v33, 0xc0135761\n"
  "v_mul_f32 v30, v30, v196\n"
  "v_mfma_f32_16x16x32_bf16 v[144:147], v[52:55], a[12:15], 0\n"
  "ds_read_b64_tr_b16 v[52:53], v20 offset:10240\n"
  "ds_read_b64_tr_b16 v[54:55], v20 offset:14336\n"
  "buffer_load_dwordx4 a[232:235], %[voffX], %[srdWS], s98 offen offset:512 sc1\n"
  "s_waitcnt lgkmcnt(14)\n"
  "v_mfma_f32_16x16x32_bf16 v[148:151], v[56:59], a[0:3], 0\n"
  "v_mul_f32 v31, v31, v197\n"
  "v_mul_f32 v32, v32, v198\n"
  "v_mfma_f32_16x16x32_bf16 v[152:155], v[56:59], a[4:7], 0\n"
  "v_mul_f32 v33, v33, v199\n"
  "v_mfma_f32_16x16x32_bf16 v[156:159], v[56:59], a[8:11], 0\n"
  "v_exp_f32 v30, v30\n"
  "v_mfma_f32_16x16x32_bf16 v[160:163], v[56:59], a[12:15], 0\n"
  "ds_read_b64_tr_b16 v[56:57], v21 offset:10240\n"
  "ds_read_b64_tr_b16 v[58:59], v21 offset:14336\n"
  "s_waitcnt lgkmcnt(14)\n"
  "v_mfma_f32_16x16x32_bf16 v[164:167], v[60:63], a[0:3], 0\n"
  "buffer_load_dwordx4 a[236:239], %[voffX], %[srdWS], s99 offen offset:512 sc1\n"
  "v_mfma_f32_16x16x32_bf16 v[168:171], v[60:63], a[4:7], 0\n"
  "v_exp_f32 v31, v31\n"
  "v_mfma_f32_16x16x32_bf16 v[172:175], v[60:63], a[8:11], 0\n"
  "v_exp_f32 v32, v32\n"
  "v_mfma_f32_16x16x32_bf16 v[176:179], v[60:63], a[12:15], 0\n"
  "ds_read_b64_tr_b16 v[60:61], v20 offset:10752\n"
  "ds_read_b64_tr_b16 v[62:63], v20 offset:14848\n"
  "s_waitcnt lgkmcnt(14)\n"
  "v_mfma_f32_16x16x32_bf16 v[180:183], v[64:67], a[0:3], 0\n"
  "buffer_load_dwordx4 a[240:243], %[voffX], %[srdWS], s96 offen offset:768 sc1\n"
  "v_mfma_f32_16x16x32_bf16 v[184:187], v[64:67], a[4:7], 0\n"
  "v_exp_f32 v33, v33\n"
  "v_mfma_f32_16x16x32_bf16 v[188:191], v[64:67], a[8:11], 0\n"
  "v_add_f32 v30, 1.0, v30\n"
  "v_add_f32 v31, 1.0, v31\n"
  "v_mfma_f32_16x16x32_bf16 v[192:195], v[64:67], a[12:15], 0\n"
  "ds_read_b64_tr_b16 v[64:65], v21 offset:10752\n"
  "ds_read_b64_tr_b16 v[66:67], v21 offset:14848\n"
  "s_waitcnt lgkmcnt(8)\n"
  "v_mfma_f32_16x16x32_bf16 v[68:71], v[36:39], a[16:19], v[68:71]\n"
  "v_add_f32 v32, 1.0, v32\n"
  "v_add_f32 v33, 1.0, v33\n"
  "v_mfma_f32_16x16x32_bf16 v[72:75], v[36:39], a[20:23], v[72:75]\n"
  "buffer_load_dwordx4 a[244:247], %[voffX], %[srdWS], s97 offen offset:768 sc1\n"
  "v_mfma_f32_16x16x32_bf16 v[76:79], v[36:39], a[24:27], v[76:79]\n"
  "v_rcp_f32 v30, v30\n"
  "v_mfma_f32_16x16x32_bf16 v[80:83], v[36:39], a[28:31], v[80:83]\n"
  "ds_read_b64_tr_b16 v[36:37], v20 offset:16384\n"
  "ds_read_b64_tr_b16 v[38:39], v20 offset:20480\n"
  "v_mfma_f32_16x16x32_bf16 v[84:87], v[40:43], a[16:19], v[84:87]\n"
  "v_rcp_f32 v31, v31\n"
  "v_mfma_f32_16x16x32_bf16 v[88:91], v[40:43], a[20:23], v[88:91]\n"
  "v_rcp_f32 v32, v32\n"
  "v_mfma_f32_16x16x32_bf16 v[92:95], v[40:43], a[24:27], v[92:95]\n"
  "buffer_load_dwordx4 a[248:251], %[voffX], %[srdWS], s98 offen offset:768 sc1\n"
  "v_mfma_f32_16x16x32_bf16 v[96:99], v[40:43], a[28:31], v[96:99]\n"
  "ds_read_b64_tr_b16 v[40:41], v21 offset:16384\n"
  "ds_read_b64_tr_b16 v[42:43], v21 offset:20480\n"
  "v_mfma_f32_16x16x32_bf16 v[100:103], v[44:47], a[16:19], v[100:103]\n"
  "v_rcp_f32 v33, v33\n"
  "v_mfma_f32_16x16x32_bf16 v[104:107], v[44:47], a[20:23], v[104:107]\n"
  "v_mul_f32 v196, v196, v30\n"
  "v_mul_f32 v197, v197, v31\n"
  "v_mfma_f32_16x16x32_bf16 v[108:111], v[44:47], a[24:27], v[108:111]\n"
  "buffer_load_dwordx4 a[252:255], %[voffX], %[srdWS], s99 offen offset:768 sc1\n"
  "v_mul_f32 v198, v198, v32\n"
  "v_mfma_f32_16x16x32_bf16 v[112:115], v[44:47], a[28:31], v[112:115]\n"
  "ds_read_b64_tr_b16 v[44:45], v20 offset:16896\n"
  "ds_read_b64_tr_b16 v[46:47], v20 offset:20992\n"
  "v_mfma_f32_16x16x32_bf16 v[116:119], v[48:51], a[16:19], v[116:119]\n"
  "s_add_u32 s87, %[rot], 2\n"
  "s_and_b32 s87, s87, 3\n"
  "s_lshl_b32 s87, s87, 20\n"
  "v_mfma_f32_16x16x32_bf16 v[120:123], v[48:51], a[20:23], v[120:123]\n"
  "s_add_u32 m0, s75, 65536\n"
  "v_mul_f32 v199, v199, v33\n"
  "v_mfma_f32_16x16x32_bf16 v[124:127], v[48:51], a[24:27], v[124:127]\n"
  "s_add_u32 s64, s87, 0\n"
  "v_cvt_pk_bf16_f32 v196, v196, v197\n"
  "v_mfma_f32_16x16x32_bf16 v[128:131], v[48:51], a[28:31], v[128:131]\n"
  "ds_read_b64_tr_b16 v[48:49], v21 offset:16896\n"
  "ds_read_b64_tr_b16 v[50:51], v21 offset:20992\n"
  "buffer_load_dwordx4 %[voffWq], %[srdWq], s64 offen lds\n"
  "s_waitcnt lgkmcnt(8)\n"
  "v_mfma_f32_16x16x32_bf16 v[132:135], v[52:55], a[16:19], v[132:135]\n"
  "v_cvt_pk_bf16_f32 v197, v198, v199\n"
  "v_add_f32 v200, v200, v17\n"
  "v_mfma_f32_16x16x32_bf16 v[136:139], v[52:55], a[20:23], v[136:139]\n"
  "s_add_u32 m0, s75, 66560\n"
  "v_add_f32 v201, v201, v18\n"
  "v_mfma_f32_16x16x32_bf16 v[140:143], v[52:55], a[24:27], v[140:143]\n"
  "s_add_u32 s64, s87, 131072\n"
  "v_add_f32 v202, v202, v19\n"
  "v_mfma_f32_16x16x32_bf16 v[144:147], v[52:55], a[28:31], v[144:147]\n"
  "ds_read_b64_tr_b16 v[52:53], v20 offset:18432\n"
  "ds_read_b64_tr_b16 v[54:55], v20 offset:22528\n"
  "buffer_load_dwordx4 %[voffWq], %[srdWq], s64 offen lds\n"
  "v_mfma_f32_16x16x32_bf16 v[148:151], v[56:59], a[16:19], v[148:151]\n"
  "v_add_f32 v203, v203, v24\n"
  "v_mul_f32 v30, v200, v200\n"
  "v_mfma_f32_16x16x32_bf16 v[152:155], v[56:59], a[20:23], v[152:155]\n"
  "v_mul_f32 v31, v201, v201\n"
  "v_mul_f32 v32, v202, v202\n"
  "v_mfma_f32_16x16x32_bf16 v[156:159], v[56:59], a[24:27], v[156:159]\n"
  "s_add_u32 m0, s75, 73728\n"
  "v_mul_f32 v33, v203, v203\n"
  "v_mfma_f32_16x16x32_bf16 v[160:163], v[56:59], a[28:31], v[160:163]\n"
  "ds_read_b64_tr_b16 v[56:57], v21 offset:18432\n"
  "ds_read_b64_tr_b16 v[58:59], v21 offset:22528\n"
  "s_add_u32 s64, s87, 262144\n"
  "v_mfma_f32_16x16x32_bf16 v[164:167], v[60:63], a[16:19], v[164:167]\n"
  "buffer_load_dwordx4 %[voffWq], %[srdWq], s64 offen lds\n"
  "v_fmaak_f32 v30, v16, v30, 0xc0135761\n"
  "v_mfma_f32_16x16x32_bf16 v[168:171], v[60:63], a[20:23], v[168:171]\n"
  "v_fmaak_f32 v31, v16, v31, 0xc0135761\n"
  "v_fmaak_f32 v32, v16, v32, 0xc0135761\n"
  "v_mfma_f32_16x16x32_bf16 v[172:175], v[60:63], a[24:27], v[172:175]\n"
  "v_fmaak_f32 v33, v16, v33, 0xc0135761\n"
  "v_mul_f32 v30, v30, v200\n"
  "v_mfma_f32_16x16x32_bf16 v[176:179], v[60:63], a[28:31], v[176:179]\n"
  "ds_read_b64_tr_b16 v[60:61], v20 offset:18944\n"
  "ds_read_b64_tr_b16 v[62:63], v20 offset:23040\n"
  "s_add_u32 m0, s75, 74752\n"
  "v_mfma_f32_16x16x32_bf16 v[180:183], v[64:67], a[16:19], v[180:183]\n"
  "s_add_u32 s64, s87, 393216\n"
  "v_mul_f32 v31, v31, v201\n"
  "v_mfma_f32_16x16x32_bf16 v[184:187], v[64:67], a[20:23], v[184:187]\n"
  "buffer_load_dwordx4 %[voffWq], %[srdWq], s64 offen lds\n"
  "v_mul_f32 v32, v32, v202\n"
  "v_mfma_f32_16x16x32_bf16 v[188:191], v[64:67], a[24:27], v[188:191]\n"
  "v_mul_f32 v33, v33, v203\n"
  "v_mfma_f32_16x16x32_bf16 v[192:195], v[64:67], a[28:31], v[192:195]\n"
  "ds_read_b64_tr_b16 v[64:65], v21 offset:18944\n"
  "ds_read_b64_tr_b16 v[66:67], v21 offset:23040\n"
  "s_add_u32 m0, s75, 81920\n"
  "s_waitcnt lgkmcnt(8)\n"
  "v_mfma_f32_16x16x32_bf16 v[68:71], v[36:39], a[32:35], v[68:71]\n"
  "s_add_u32 s64, s87, 524288\n"
  "v_mfma_f32_16x16x32_bf16 v[72:75], v[36:39], a[36:39], v[72:75]\n"
  "buffer_load_dwordx4 %[voffWq], %[srdWq], s64 offen lds\n"
  "v_mfma_f32_16x16x32_bf16 v[76:79], v[36:39], a[40:43], v[76:79]\n"
  "v_exp_f32 v30, v30\n"
  "v_mfma_f32_16x16x32_bf16 v[80:83], v[36:39], a[44:47], v[80:83]\n"
  "ds_read_b64_tr_b16 v[36:37], v20 offset:24576\n"
  "ds_read_b64_tr_b16 v[38:39], v20 offset:28672\n"
  "v_mfma_f32_16x16x32_bf16 v[84:87], v[40:43], a[32:35], v[84:87]\n"
  "s_add_u32 m0, s75, 82944\n"
  "v_mfma_f32_16x16x32_bf16 v[88:91], v[40:43], a[36:39], v[88:91]\n"
  "s_add_u32 s64, s87, 655360\n"
  "s_add_u32 s70, %[oFl], 0x4000\n"
  "s_waitcnt vmcnt(21)\n"
  "v_mfma_f32_16x16x32_bf16 v[92:95], v[40:43], a[40:43], v[92:95]\n"
  "buffer_load_dwordx4 %[voffWq], %[srdWq], s64 offen lds\n"
  "buffer_store_dwordx2 v[26:27], off, %[srdWS], s70 sc1\n"
  "v_mfma_f32_16x16x32_bf16 v[96:99], v[40:43], a[44:47], v[96:99]\n"
  "ds_read_b64_tr_b16 v[40:41], v21 offset:24576\n"
  "ds_read_b64_tr_b16 v[42:43], v21 offset:28672\n"
  "v_mfma_f32_16x16x32_bf16 v[100:103], v[44:47], a[32:35], v[100:103]\n"
  "s_add_u32 m0, s75, 90112\n"
  "v_mfma_f32_16x16x32_bf16 v[104:107], v[44:47], a[36:39], v[104:107]\n"
  "s_add_u32 s64, s87, 786432\n"
  "v_mfma_f32_16x16x32_bf16 v[108:111], v[44:47], a[40:43], v[108:111]\n"
  "buffer_load_dwordx4 %[voffWq], %[srdWq], s64 offen lds\n"
  "v_mfma_f32_16x16x32_bf16 v[112:115], v[44:47], a[44:47], v[112:115]\n"
  "ds_read_b64_tr_b16 v[44:45], v20 offset:25088\n"
  "ds_read_b64_tr_b16 v[46:47], v20 offset:29184\n"
  "v_mfma_f32_16x16x32_bf16 v[116:119], v[48:51], a[32:35], v[116:119]\n"
  "v_exp_f32 v31, v31\n"
  "v_mfma_f32_16x16x32_bf16 v[120:123], v[48:51], a[36:39], v[120:123]\n"
  "s_add_u32 m0, s75, 91136\n"
  "v_mfma_f32_16x16x32_bf16 v[124:127], v[48:51], a[40:43], v[124:127]\n"
  "s_add_u32 s64, s87, 917504\n"
  "v_mfma_f32_16x16x32_bf16 v[128:131], v[48:51], a[44:47], v[128:131]\n"
  "ds_read_b64_tr_b16 v[48:49], v21 offset:25088\n"
  "ds_read_b64_tr_b16 v[50:51], v21 offset:29184\n"
  "buffer_load_dwordx4 %[voffWq], %[srdWq], s64 offen lds\n"
  "s_waitcnt lgkmcnt(8)\n"
  "v_mfma_f32_16x16x32_bf16 v[132:135], v[52:55], a[32:35], v[132:135]\n"
  "v_exp_f32 v32, v32\n"
  "v_mfma_f32_16x16x32_bf16 v[136:139], v[52:55], a[36:39], v[136:139]\n"
  "s_add_u32 s87, %[rot], 3\n"
  "s_and_b32 s87, s87, 3\n"
  "s_lshl_b32 s87, s87, 20\n"
  "v_mfma_f32_16x16x32_bf16 v[140:143], v[52:55], a[40:43], v[140:143]\n"
  "s_add_u32 m0, s75, 98304\n"
  "v_mfma_f32_16x16x32_bf16 v[144:147], v[52:55], a[44:47], v[144:147]\n"
  "ds_read_b64_tr_b16 v[52:53], v20 offset:26624\n"
  "ds_read_b64_tr_b16 v[54:55], v20 offset:30720\n"
  "s_add_u32 s64, s87, 0\n"
  "v_mfma_f32_16x16x32_bf16 v[148:151], v[56:59], a[32:35], v[148:151]\n"
  "buffer_load_dwordx4 %[voffWq], %[srdWq], s64 offen lds\n"
  "v_mfma_f32_16x16x32_bf16 v[152:155], v[56:59], a[36:39], v[152:155]\n"
  "v_exp_f32 v33, v33\n"
  "v_mfma_f32_16x16x32_bf16 v[156:159], v[56:59], a[40:43], v[156:159]\n"
  "s_add_u32 m0, s75, 99328\n"
  "v_add_f32 v30, 1.0, v30\n"
  "v_mfma_f32_16x16x32_bf16 v[160:163], v[56:59], a[44:47], v[160:163]\n"
  "ds_read_b64_tr_b16 v[56:57], v21 offset:26624\n"
  "ds_read_b64_tr_b16 v[58:59], v21 offset:30720\n"
  "s_add_u32 s64, s87, 131072\n"
  "v_mfma_f32_16x16x32_bf16 v[164:167], v[60:63], a[32:35], v[164:167]\n"
  "buffer_load_dwordx4 %[voffWq], %[srdWq], s64 offen lds\n"
  "v_add_f32 v31, 1.0, v31\n"
  "v_mfma_f32_16x16x32_bf16 v[168:171], v[60:63], a[36:39], v[168:171]\n"
  "v_add_f32 v32, 1.0, v32\n"
  "v_add_f32 v33, 1.0, v33\n"
  "v_mfma_f32_16x16x32_bf16 v[172:175], v[60:63], a[40:43], v[172:175]\n"
  "v_rcp_f32 v30, v30\n"
  "v_mfma_f32_16x16x32_bf16 v[176:179], v[60:63], a[44:47], v[176:179]\n"
  "ds_read_b64_tr_b16 v[60:61], v20 offset:27136\n"
  "ds_read_b64_tr_b16 v[62:63], v20 offset:31232\n"
  "s_add_u32 m0, s75, 106496\n"
  "v_mfma_f32_16x16x32_bf16 v[180:183], v[64:67], a[32:35], v[180:183]\n"
  "s_add_u32 s64, s87, 262144\n"
  "v_mfma_f32_16x16x32_bf16 v[184:187], v[64:67], a[36:39], v[184:187]\n"
  "buffer_load_dwordx4 %[voffWq], %[srdWq], s64 offen lds\n"
  "v_mfma_f32_16x16x32_bf16 v[188:191], v[64:67], a[40:43], v[188:191]\n"
  "v_rcp_f32 v31, v31\n"
  "v_mfma_f32_16x16x32_bf16 v[192:195], v[64:67], a[44:47], v[192:195]\n"
  "ds_read_b64_tr_b16 v[64:65], v21 offset:27136\n"
  "ds_read_b64_tr_b16 v[66:67], v21 offset:31232\n"
  "s_add_u32 m0, s75, 107520\n"
  "s_waitcnt lgkmcnt(8)\n"
  "s_waitcnt vmcnt(63)\n"
  "s_barrier\n"
  "v_mfma_f32_16x16x32_bf16 v[68:71], v[36:39], a[48:51], v[68:71]\n"
  "s_add_u32 s64, s87, 393216\n"
  "v_mfma_f32_16x16x32_bf16 v[72:75], v[36:39], a[52:55], v[72:75]\n"
  "buffer_load_dwordx4 %[voffWq], %[srdWq], s64 offen lds\n"
  "v_mfma_f32_16x16x32_bf16 v[76:79], v[36:39], a[56:59], v[76:79]\n"
  "v_rcp_f32 v32, v32\n"
  "v_mfma_f32_16x16x32_bf16 v[80:83], v[36:39], a[60:63], v[80:83]\n"
  "ds_read_b64_tr_b16 v[36:37], v20 offset:32768\n"
  "ds_read_b64_tr_b16 v[38:39], v20 offset:36864\n"
  "v_mfma_f32_16x16x32_bf16 v[84:87], v[40:43], a[48:51], v[84:87]\n"
  "s_add_u32 m0, s75, 114688\n"
  "v_mfma_f32_16x16x32_bf16 v[88:91], v[40:43], a[52:55], v[88:91]\n"
  "s_add_u32 s64, s87, 524288\n"
  "v_mfma_f32_16x16x32_bf16 v[92:95], v[40:43], a[56:59], v[92:95]\n"
  "buffer_load_dwordx4 %[voffWq], %[srdWq], s64 offen lds\n"
  "v_mfma_f32_16x16x32_bf16 v[96:99], v[40:43], a[60:63], v[96:99]\n"
  "ds_read_b64_tr_b16 v[40:41], v21 offset:32768\n"
  "ds_read_b64_tr_b16 v[42:43], v21 offset:36864\n"
  "v_mfma_f32_16x16x32_bf16 v[100:103], v[44:47], a[48:51], v[100:103]\n"
  "v_rcp_f32 v33, v33\n"
  "v_mfma_f32_16x16x32_bf16 v[104:107], v[44:47], a[52:55], v[104:107]\n"
  "s_add_u32 m0, s75, 115712\n"
  "v_mul_f32 v200, v200, v30\n"
  "v_mfma_f32_16x16x32_bf16 v[108:111], v[44:47], a[56:59], v[108:111]\n"
  "s_add_u32 s64, s87, 655360\n"
  "v_mul_f32 v201, v201, v31\n"
  "v_mfma_f32_16x16x32_bf16 v[112:115], v[44:47], a[60:63], v[112:115]\n"
  "ds_read_b64_tr_b16 v[44:45], v20 offset:33280\n"
  "ds_read_b64_tr_b16 v[46:47], v20 offset:37376\n"
  "buffer_load_dwordx4 %[voffWq], %[srdWq], s64 offen lds\n"
  "v_mfma_f32_16x16x32_bf16 v[116:119], v[48:51], a[48:51], v[116:119]\n"
  "v_mul_f32 v202, v202, v32\n"
  "v_mul_f32 v203, v203, v33\n"
  "v_mfma_f32_16x16x32_bf16 v[120:123], v[48:51], a[52:55], v[120:123]\n"
  "s_add_u32 m0, s75, 122880\n"
  "v_cvt_pk_bf16_f32 v200, v200, v201\n"
  "v_mfma_f32_16x16x32_bf16 v[124:127], v[48:51], a[56:59], v[124:127]\n"
  "s_add_u32 s64, s87, 786432\n"
  "v_cvt_pk_bf16_f32 v201, v202, v203\n"
  "v_mfma_f32_16x16x32_bf16 v[128:131], v[48:51], a[60:63], v[128:131]\n"
  "ds_read_b64_tr_b16 v[48:49], v21 offset:33280\n"
  "ds_read_b64_tr_b16 v[50:51], v21 offset:37376\n"
  "buffer_load_dwordx4 %[voffWq], %[srdWq], s64 offen lds\n"
  "s_waitcnt lgkmcnt(8)\n"
  "v_mfma_f32_16x16x32_bf16 v[132:135], v[52:55], a[48:51], v[132:135]\n"
  "v_lshlrev_b32 v17, 16, v246\n"
  "v_and_b32 v18, 0xffff0000, v246\n"
  "v_mfma_f32_16x16x32_bf16 v[136:139], v[52:55], a[52:55], v[136:139]\n"
  "v_lshlrev_b32 v19, 16, v247\n"
  "v_and_b32 v24, 0xffff0000, v247\n"
  "v_mfma_f32_16x16x32_bf16 v[140:143], v[52:55], a[56:59], v[140:143]\n"
  "s_add_u32 m0, s75, 123904\n"
  "v_add_f32 v204, v204, v17\n"
  "v_mfma_f32_16x16x32_bf16 v[144:147], v[52:55], a[60:63], v[144:147]\n"
  "ds_read_b64_tr_b16 v[52:53], v20 offset:34816\n"
  "ds_read_b64_tr_b16 v[54:55], v20 offset:38912\n"
  "s_add_u32 s64, s87, 917504\n"
  "v_mfma_f32_16x16x32_bf16 v[148:151], v[56:59], a[48:51], v[148:151]\n"
  "buffer_load_dwordx4 %[voffWq], %[srdWq], s64 offen lds\n"
  "v_add_f32 v205, v205, v18\n"
  "v_mfma_f32_16x16x32_bf16 v[152:155], v[56:59], a[52:55], v[152:155]\n"
  "s_add_u32 s96, %[rot], 2\n"
  "s_and_b32 s96, s96, 3\n"
  "s_lshl_b32 s96, s96, 10\n"
  "v_mfma_f32_16x16x32_bf16 v[156:159], v[56:59], a[56:59], v[156:159]\n"
  "s_add_u32 s96, s96, %[oX]\n"
  "s_add_u32 s97, s96, 0x4000\n"
  "v_mfma_f32_16x16x32_bf16 v[160:163], v[56:59], a[60:63], v[160:163]\n"
  "ds_read_b64_tr_b16 v[56:57], v21 offset:34816\n"
  "ds_read_b64_tr_b16 v[58:59], v21 offset:38912\n"
  "s_add_u32 s98, s96, 0x8000\n"
  "s_add_u32 s99, s96, 0xc000\n"
  "v_mfma_f32_16x16x32_bf16 v[164:167], v[60:63], a[48:51], v[164:167]\n"
  "buffer_load_dwordx4 a[64:67], %[voffX], %[srdWS], s96 offen offset:0 sc1\n"
  "v_add_f32 v206, v206, v19\n"
  "v_mfma_f32_16x16x32_bf16 v[168:171], v[60:63], a[52:55], v[168:171]\n"
  "v_add_f32 v207, v207, v24\n"
  "v_mul_f32 v30, v204, v204\n"
  "v_mfma_f32_16x16x32_bf16 v[172:175], v[60:63], a[56:59], v[172:175]\n"
  "v_mul_f32 v31, v205, v205\n"
  "v_mul_f32 v32, v206, v206\n"
  "v_mfma_f32_16x16x32_bf16 v[176:179], v[60:63], a[60:63], v[176:179]\n"
  "ds_read_b64_tr_b16 v[60:61], v20 offset:35328\n"
  "ds_read_b64_tr_b16 v[62:63], v20 offset:39424\n"
  "v_mfma_f32_16x16x32_bf16 v[180:183], v[64:67], a[48:51], v[180:183]\n"
  "v_mul_f32 v33, v207, v207\n"
  "v_fmaak_f32 v30, v16, v30, 0xc0135761\n"
  "v_mfma_f32_16x16x32_bf16 v[184:187], v[64:67], a[52:55], v[184:187]\n"
  "buffer_load_dwordx4 a[68:71], %[voffX], %[srdWS], s97 offen offset:0 sc1\n"
  "v_fmaak_f32 v31, v16, v31, 0xc0135761\n"
  "v_mfma_f32_16x16x32_bf16 v[188:191], v[64:67], a[56:59], v[188:191]\n"
  "v_fmaak_f32 v32, v16, v32, 0xc0135761\n"
  "v_fmaak_f32 v33, v16, v33, 0xc0135761\n"
  "v_mfma_f32_16x16x32_bf16 v[192:195], v[64:67], a[60:63], v[192:195]\n"
  "ds_read_b64_tr_b16 v[64:65], v21 offset:35328\n"
  "ds_read_b64_tr_b16 v[66:67], v21 offset:39424\n"
  "s_waitcnt lgkmcnt(8)\n"
  "s_waitcnt vmcnt(34)\n"
  "v_mfma_f32_16x16x32_bf16 v[68:71], v[36:39], a[192:195], v[68:71]\n"
  "v_mul_f32 v30, v30, v204\n"
  "v_mul_f32 v31, v31, v205\n"
  "s_waitcnt vmcnt(33)\n"
  "v_mfma_f32_16x16x32_bf16 v[72:75], v[36:39], a[196:199], v[72:75]\n"
  "v_mul_f32 v32, v32, v206\n"
  "v_mul_f32 v33, v33, v207\n"
  "s_waitcnt vmcnt(32)\n"
  "v_mfma_f32_16x16x32_bf16 v[76:79], v[36:39], a[200:203], v[76:79]\n"
  "buffer_load_dwordx4 a[72:75], %[voffX], %[srdWS], s98 offen offset:0 sc1\n"
  "s_waitcnt vmcnt(32)\n"
  "v_mfma_f32_16x16x32_bf16 v[80:83], v[36:39], a[204:207], v[80:83]\n"
  "ds_read_b64_tr_b16 v[36:37], v20 offset:40960\n"
  "ds_read_b64_tr_b16 v[38:39], v20 offset:45056\n"
  "v_mfma_f32_16x16x32_bf16 v[84:87], v[40:43], a[192:195], v[84:87]\n"
  "v_exp_f32 v30, v30\n"
  "v_mfma_f32_16x16x32_bf16 v[88:91], v[40:43], a[196:199], v[88:91]\n"
  "v_exp_f32 v31, v31\n"
  "v_mfma_f32_16x16x32_bf16 v[92:95], v[40:43], a[200:203], v[92:95]\n"
  "buffer_load_dwordx4 a[76:79], %[voffX], %[srdWS], s99 offen offset:0 sc1\n"
  "v_mfma_f32_16x16x32_bf16 v[96:99], v[40:43], a[204:207], v[96:99]\n"
  "ds_read_b64_tr_b16 v[40:41], v21 offset:40960\n"
  "ds_read_b64_tr_b16 v[42:43], v21 offset:45056\n"
  "v_mfma_f32_16x16x32_bf16 v[100:103], v[44:47], a[192:195], v[100:103]\n"
  "v_exp_f32 v32, v32\n"
  "v_mfma_f32_16x16x32_bf16 v[104:107], v[44:47], a[196:199], v[104:107]\n"
  "v_exp_f32 v33, v33\n"
  "v_mfma_f32_16x16x32_bf16 v[108:111], v[44:47], a[200:203], v[108:111]\n"
  "v_add_f32 v30, 1.0, v30\n"
  "v_add_f32 v31, 1.0, v31\n"
  "v_mfma_f32_16x16x32_bf16 v[112:115], v[44:47], a[204:207], v[112:115]\n"
  "ds_read_b64_tr_b16 v[44:45], v20 offset:41472\n"
  "ds_read_b64_tr_b16 v[46:47], v20 offset:45568\n"
  "buffer_load_dwordx4 a[80:83], %[voffX], %[srdWS], s96 offen offset:256 sc1\n"
  "v_mfma_f32_16x16x32_bf16 v[116:119], v[48:51], a[192:195], v[116:119]\n"
  "v_add_f32 v32, 1.0, v32\n"
  "v_add_f32 v33, 1.0, v33\n"
  "v_mfma_f32_16x16x32_bf16 v[120:123], v[48:51], a[196:199], v[120:123]\n"
  "v_rcp_f32 v30, v30\n"
  "v_mfma_f32_16x16x32_bf16 v[124:127], v[48:51], a[200:203], v[124:127]\n"
  "v_rcp_f32 v31, v31\n"
  "v_mfma_f32_16x16x32_bf16 v[128:131], v[48:51], a[204:207], v[128:131]\n"
  "ds_read_b64_tr_b16 v[48:49], v21 offset:41472\n"
  "ds_read_b64_tr_b16 v[50:51], v21 offset:45568\n"
  "buffer_load_dwordx4 a[84:87], %[voffX], %[srdWS], s97 offen offset:256 sc1\n"
  "s_waitcnt lgkmcnt(8)\n"
  "v_mfma_f32_16x16x32_bf16 v[132:135], v[52:55], a[192:195], v[132:135]\n"
  "v_rcp_f32 v32, v32\n"
  "v_mfma_f32_16x16x32_bf16 v[136:139], v[52:55], a[196:199], v[136:139]\n"
  "v_rcp_f32 v33, v33\n"
  "v_mfma_f32_16x16x32_bf16 v[140:143], v[52:55], a[200:203], v[140:143]\n"
  "v_mul_f32 v204, v204, v30\n"
  "v_mul_f32 v205, v205, v31\n"
  "v_mfma_f32_16x16x32_bf16 v[144:147], v[52:55], a[204:207], v[144:147]\n"
  "ds_read_b64_tr_b16 v[52:53], v20 offset:43008\n"
  "ds_read_b64_tr_b16 v[54:55], v20 offset:47104\n"
  "v_mfma_f32_16x16x32_bf16 v[148:151], v[56:59], a[192:195], v[148:151]\n"
  "buffer_load_dwordx4 a[88:91], %[voffX], %[srdWS], s98 offen offset:256 sc1\n"
  "v_mul_f32 v206, v206, v32\n"
  "v_mfma_f32_16x16x32_bf16 v[152:155], v[56:59], a[196:199], v[152:155]\n"
  "v_mul_f32 v207, v207, v33\n"
  "v_cvt_pk_bf16_f32 v198, v204, v205\n"
  "v_mfma_f32_16x16x32_bf16 v[156:159], v[56:59], a[200:203], v[156:159]\n"
  "v_cvt_pk_bf16_f32 v199, v206, v207\n"
  "buffer_store_dwordx4 v[196:199], %[L16], %[srdWS], s77 offen offset:0 sc1\n"
  "v_mfma_f32_16x16x32_bf16 v[160:163], v[56:59], a[204:207], v[160:163]\n"
  "ds_read_b64_tr_b16 v[56:57], v21 offset:43008\n"
  "ds_read_b64_tr_b16 v[58:59], v21 offset:47104\n"
  "v_mfma_f32_16x16x32_bf16 v[164:167], v[60:63], a[192:195], v[164:167]\n"
  "v_add_f32 v208, v208, v17\n"
  "v_add_f32 v209, v209, v18\n"
  "v_mfma_f32_16x16x32_bf16 v[168:171], v[60:63], a[196:199], v[168:171]\n"
  "buffer_load_dwordx4 a[92:95], %[voffX], %[srdWS], s99 offen offset:256 sc1\n"
  "v_add_f32 v210, v210, v19\n"
  "v_mfma_f32_16x16x32_bf16 v[172:175], v[60:63], a[200:203], v[172:175]\n"
  "v_add_f32 v211, v211, v24\n"
  "v_mul_f32 v30, v208, v208\n"
  "v_mfma_f32_16x16x32_bf16 v[176:179], v[60:63], a[204:207], v[176:179]\n"
  "ds_read_b64_tr_b16 v[60:61], v20 offset:43520\n"
  "ds_read_b64_tr_b16 v[62:63], v20 offset:47616\n"
  "v_mfma_f32_16x16x32_bf16 v[180:183], v[64:67], a[192:195], v[180:183]\n"
  "v_mul_f32 v31, v209, v209\n"
  "v_mul_f32 v32, v210, v210\n"
  "v_mfma_f32_16x16x32_bf16 v[184:187], v[64:67], a[196:199], v[184:187]\n"
  "buffer_load_dwordx4 a[96:99], %[voffX], %[srdWS], s96 offen offset:512 sc1\n"
  "v_mul_f32 v33, v211, v211\n"
  "v_mfma_f32_16x16x32_bf16 v[188:191], v[64:67], a[200:203], v[188:191]\n"
  "v_fmaak_f32 v30, v16, v30, 0xc0135761\n"
  "v_fmaak_f32 v31, v16, v31, 0xc0135761\n"
  "v_mfma_f32_16x16x32_bf16 v[192:195], v[64:67], a[204:207], v[192:195]\n"
  "ds_read_b64_tr_b16 v[64:65], v21 offset:43520\n"
  "ds_read_b64_tr_b16 v[66:67], v21 offset:47616\n"
  "s_waitcnt lgkmcnt(8)\n"
  "s_waitcnt vmcnt(38)\n"
  "v_mfma_f32_16x16x32_bf16 v[68:71], v[36:39], a[208:211], v[68:71]\n"
  "v_fmaak_f32 v32, v16, v32, 0xc0135761\n"
  "v_fmaak_f32 v33, v16, v33, 0xc0135761\n"
  "s_waitcnt vmcnt(37)\n"
  "v_mfma_f32_16x16x32_bf16 v[72:75], v[36:39], a[212:215], v[72:75]\n"
  "v_mul_f32 v30, v30, v208\n"
  "v_mul_f32 v31, v31, v209\n"
  "s_waitcnt vmcnt(36)\n"
  "v_mfma_f32_16x16x32_bf16 v[76:79], v[36:39], a[216:219], v[76:79]\n"
  "buffer_load_dwordx4 a[100:103], %[voffX], %[srdWS], s97 offen offset:512 sc1\n"
  "v_mul_f32 v32, v32, v210\n"
  "s_waitcnt vmcnt(36)\n"
  "v_mfma_f32_16x16x32_bf16 v[80:83], v[36:39], a[220:223], v[80:83]\n"
  "ds_read_b64_tr_b16 v[36:37], v20 offset:49152\n"
  "ds_read_b64_tr_b16 v[38:39], v20 offset:53248\n"
  "v_mfma_f32_16x16x32_bf16 v[84:87], v[40:43], a[208:211], v[84:87]\n"
  "v_mul_f32 v33, v33, v211\n"
  "v_mfma_f32_16x16x32_bf16 v[88:91], v[40:43], a[212:215], v[88:91]\n"
  "buffer_load_dwordx2 v[28:29], %[pollKV], %[srdWS], 0 offen sc1\n"
  "v_mfma_f32_16x16x32_bf16 v[92:95], v[40:43], a[216:219], v[92:95]\n"
  "buffer_load_dwordx4 a[104:107], %[voffX], %[srdWS], s98 offen offset:512 sc1\n"
  "v_mfma_f32_16x16x32_bf16 v[96:99], v[40:43], a[220:223], v[96:99]\n"
  "ds_read_b64_tr_b16 v[40:41], v21 offset:49152\n"
  "ds_read_b64_tr_b16 v[42:43], v21 offset:53248\n"
  "v_mfma_f32_16x16x32_bf16 v[100:103], v[44:47], a[208:211], v[100:103]\n"
  "v_exp_f32 v30, v30\n"
  "v_mfma_f32_16x16x32_bf16 v[104:107], v[44:47], a[212:215], v[104:107]\n"
  "v_exp_f32 v31, v31\n"
  "v_mfma_f32_16x16x32_bf16 v[108:111], v[44:47], a[216:219], v[108:111]\n"
  "v_exp_f32 v32, v32\n"
  "v_mfma_f32_16x16x32_bf16 v[112:115], v[44:47], a[220:223], v[112:115]\n"
  "ds_read_b64_tr_b16 v[44:45], v20 offset:49664\n"
  "ds_read_b64_tr_b16 v[46:47], v20 offset:53760\n"
  "buffer_load_dwordx4 a[108:111], %[voffX], %[srdWS], s99 offen offset:512 sc1\n"
  "v_mfma_f32_16x16x32_bf16 v[116:119], v[48:51], a[208:211], v[116:119]\n"
  "v_exp_f32 v33, v33\n"
  "v_mfma_f32_16x16x32_bf16 v[120:123], v[48:51], a[212:215], v[120:123]\n"
  "v_add_f32 v30, 1.0, v30\n"
  "v_add_f32 v31, 1.0, v31\n"
  "v_mfma_f32_16x16x32_bf16 v[124:127], v[48:51], a[216:219], v[124:127]\n"
  "v_add_f32 v32, 1.0, v32\n"
  "v_add_f32 v33, 1.0, v33\n"
  "v_mfma_f32_16x16x32_bf16 v[128:131], v[48:51], a[220:223], v[128:131]\n"
  "ds_read_b64_tr_b16 v[48:49], v21 offset:49664\n"
  "ds_read_b64_tr_b16 v[50:51], v21 offset:53760\n"
  "s_waitcnt lgkmcnt(8)\n"
  "v_mfma_f32_16x16x32_bf16 v[132:135], v[52:55], a[208:211], v[132:135]\n"
  "buffer_load_dwordx4 a[112:115], %[voffX], %[srdWS], s96 offen offset:768 sc1\n"
  "v_mfma_f32_16x16x32_bf16 v[136:139], v[52:55], a[212:215], v[136:139]\n"
  "v_rcp_f32 v30, v30\n"
  "v_mfma_f32_16x16x32_bf16 v[140:143], v[52:55], a[216:219], v[140:143]\n"
  "v_rcp_f32 v31, v31\n"
  "v_mfma_f32_16x16x32_bf16 v[144:147], v[52:55], a[220:223], v[144:147]\n"
  "ds_read_b64_tr_b16 v[52:53], v20 offset:51200\n"
  "ds_read_b64_tr_b16 v[54:55], v20 offset:55296\n"
  "v_mfma_f32_16x16x32_bf16 v[148:151], v[56:59], a[208:211], v[148:151]\n"
  "buffer_load_dwordx4 a[116:119], %[voffX], %[srdWS], s97 offen offset:768 sc1\n"
  "v_mfma_f32_16x16x32_bf16 v[152:155], v[56:59], a[212:215], v[152:155]\n"
  "v_rcp_f32 v32, v32\n"
  "v_mfma_f32_16x16x32_bf16 v[156:159], v[56:59], a[216:219], v[156:159]\n"
  "v_rcp_f32 v33, v33\n"
  "v_mfma_f32_16x16x32_bf16 v[160:163], v[56:59], a[220:223], v[160:163]\n"
  "ds_read_b64_tr_b16 v[56:57], v21 offset:51200\n"
  "ds_read_b64_tr_b16 v[58:59], v21 offset:55296\n"
  "v_mfma_f32_16x16x32_bf16 v[164:167], v[60:63], a[208:211], v[164:167]\n"
  "v_mul_f32 v208, v208, v30\n"
  "v_mul_f32 v209, v209, v31\n"
  "v_mfma_f32_16x16x32_bf16 v[168:171], v[60:63], a[212:215], v[168:171]\n"
  "buffer_load_dwordx4 a[120:123], %[voffX], %[srdWS], s98 offen offset:768 sc1\n"
  "v_mul_f32 v210, v210, v32\n"
  "v_mfma_f32_16x16x32_bf16 v[172:175], v[60:63], a[216:219], v[172:175]\n"
  "v_mul_f32 v211, v211, v33\n"
  "v_cvt_pk_bf16_f32 v202, v208, v209\n"
  "v_mfma_f32_16x16x32_bf16 v[176:179], v[60:63], a[220:223], v[176:179]\n"
  "ds_read_b64_tr_b16 v[60:61], v20 offset:51712\n"
  "ds_read_b64_tr_b16 v[62:63], v20 offset:55808\n"
  "v_mfma_f32_16x16x32_bf16 v[180:183], v[64:67], a[208:211], v[180:183]\n"
  "v_cvt_pk_bf16_f32 v203, v210, v211\n"
  "buffer_store_dwordx4 v[200:203], %[L16], %[srdWS], s77 offen offset:1024 sc1\n"
  "v_mfma_f32_16x16x32_bf16 v[184:187], v[64:67], a[212:215], v[184:187]\n"
  "buffer_load_dwordx4 a[124:127], %[voffX], %[srdWS], s99 offen offset:768 sc1\n"
  "v_mfma_f32_16x16x32_bf16 v[188:191], v[64:67], a[216:219], v[188:191]\n"
  "s_add_u32 s77, %[oH], 8192\n"
  "s_cmp_gt_u32 %[nH], 1\n"
  "s_cselect_b32 s77, s77, 0x7f000000\n"
  "v_mfma_f32_16x16x32_bf16 v[192:195], v[64:67], a[220:223], v[192:195]\n"
  "ds_read_b64_tr_b16 v[64:65], v21 offset:51712\n"
  "ds_read_b64_tr_b16 v[66:67], v21 offset:55808\n"
  "s_add_u32 s96, %[rot], 3\n"
  "s_and_b32 s96, s96, 3\n"
  "s_lshl_b32 s96, s96, 10\n"
  "s_waitcnt lgkmcnt(8)\n"
  "s_waitcnt vmcnt(43)\n"
  "v_mfma_f32_16x16x32_bf16 v[68:71], v[36:39], a[224:227], v[68:71]\n"
  "s_add_u32 s96, s96, %[oX]\n"
  "s_add_u32 s97, s96, 0x4000\n"
  "s_waitcnt vmcnt(42)\n"
  "v_mfma_f32_16x16x32_bf16 v[72:75], v[36:39], a[228:231], v[72:75]\n"
  "s_add_u32 s98, s96, 0x8000\n"
  "s_add_u32 s99, s96, 0xc000\n"
  "s_waitcnt vmcnt(41)\n"
  "v_mfma_f32_16x16x32_bf16 v[76:79], v[36:39], a[232:235], v[76:79]\n"
  "buffer_load_dwordx4 a[128:131], %[voffX], %[srdWS], s96 offen offset:0 sc1\n"
  "v_lshlrev_b32 v17, 16, v248\n"
  "s_waitcnt vmcnt(41)\n"
  "v_mfma_f32_16x16x32_bf16 v[80:83], v[36:39], a[236:239], v[80:83]\n"
  "ds_read_b64_tr_b16 v[36:37], v20 offset:57344\n"
  "ds_read_b64_tr_b16 v[38:39], v20 offset:61440\n"
  "v_mfma_f32_16x16x32_bf16 v[84:87], v[40:43], a[224:227], v[84:87]\n"
  "v_and_b32 v18, 0xffff0000, v248\n"
  "v_lshlrev_b32 v19, 16, v249\n"
  "v_mfma_f32_16x16x32_bf16 v[88:91], v[40:43], a[228:231], v[88:91]\n"
  "v_and_b32 v24, 0xffff0000, v249\n"
  "v_add_f32 v212, v212, v17\n"
  "v_mfma_f32_16x16x32_bf16 v[92:95], v[40:43], a[232:235], v[92:95]\n"
  "v_add_f32 v213, v213, v18\n"
  "v_add_f32 v214, v214, v19\n"
  "v_mfma_f32_16x16x32_bf16 v[96:99], v[40:43], a[236:239], v[96:99]\n"
  "ds_read_b64_tr_b16 v[40:41], v21 offset:57344\n"
  "ds_read_b64_tr_b16 v[42:43], v21 offset:61440\n"
  "buffer_load_dwordx4 a[132:135], %[voffX], %[srdWS], s97 offen offset:0 sc1\n"
  "v_mfma_f32_16x16x32_bf16 v[100:103], v[44:47], a[224:227], v[100:103]\n"
  "v_add_f32 v215, v215, v24\n"
  "v_mul_f32 v30, v212, v212\n"
  "v_mfma_f32_16x16x32_bf16 v[104:107], v[44:47], a[228:231], v[104:107]\n"
  "v_mul_f32 v31, v213, v213\n"
  "v_mul_f32 v32, v214, v214\n"
  "v_mfma_f32_16x16x32_bf16 v[108:111], v[44:47], a[232:235], v[108:111]\n"
  "v_mul_f32 v33, v215, v215\n"
  "v_fmaak_f32 v30, v16, v30, 0xc0135761\n"
  "v_mfma_f32_16x16x32_bf16 v[112:115], v[44:47], a[236:239], v[112:115]\n"
  "ds_read_b64_tr_b16 v[44:45], v20 offset:57856\n"
  "ds_read_b64_tr_b16 v[46:47], v20 offset:61952\n"
  "buffer_load_dwordx4 a[136:139], %[voffX], %[srdWS], s98 offen offset:0 sc1\n"
  "v_mfma_f32_16x16x32_bf16 v[116:119], v[48:51], a[224:227], v[116:119]\n"
  "v_fmaak_f32 v31, v16, v31, 0xc0135761\n"
  "v_fmaak_f32 v32, v16, v32, 0xc0135761\n"
  "v_mfma_f32_16x16x32_bf16 v[120:123], v[48:51], a[228:231], v[120:123]\n"
  "v_fmaak_f32 v33, v16, v33, 0xc0135761\n"
  "v_mul_f32 v30, v30, v212\n"
  "v_mfma_f32_16x16x32_bf16 v[124:127], v[48:51], a[232:235], v[124:127]\n"
  "v_mul_f32 v31, v31, v213\n"
  "v_mul_f32 v32, v32, v214\n"
  "v_mfma_f32_16x16x32_bf16 v[128:131], v[48:51], a[236:239], v[128:131]\n"
  "ds_read_b64_tr_b16 v[48:49], v21 offset:57856\n"
  "ds_read_b64_tr_b16 v[50:51], v21 offset:61952\n"
  "s_waitcnt lgkmcnt(8)\n"
  "v_mfma_f32_16x16x32_bf16 v[132:135], v[52:55], a[224:227], v[132:135]\n"
  "buffer_load_dwordx4 a[140:143], %[voffX], %[srdWS], s99 offen offset:0 sc1\n"
  "v_mul_f32 v33, v33, v215\n"
  "v_mfma_f32_16x16x32_bf16 v[136:139], v[52:55], a[228:231], v[136:139]\n"
  "v_exp_f32 v30, v30\n"
  "v_mfma_f32_16x16x32_bf16 v[140:143], v[52:55], a[232:235], v[140:143]\n"
  "v_exp_f32 v31, v31\n"
  "v_mfma_f32_16x16x32_bf16 v[144:147], v[52:55], a[236:239], v[144:147]\n"
  "ds_read_b64_tr_b16 v[52:53], v20 offset:59392\n"
  "ds_read_b64_tr_b16 v[54:55], v20 offset:63488\n"
  "v_mfma_f32_16x16x32_bf16 v[148:151], v[56:59], a[224:227], v[148:151]\n"
  "buffer_load_dwordx4 a[144:147], %[voffX], %[srdWS], s96 offen offset:256 sc1\n"
  "v_mfma_f32_16x16x32_bf16 v[152:155], v[56:59], a[228:231], v[152:155]\n"
  "v_exp_f32 v32, v32\n"
  "v_mfma_f32_16x16x32_bf16 v[156:159], v[56:59], a[232:235], v[156:159]\n"
  "v_exp_f32 v33, v33\n"
  "v_mfma_f32_16x16x32_bf16 v[160:163], v[56:59], a[236:239], v[160:163]\n"
  "ds_read_b64_tr_b16 v[56:57], v21 offset:59392\n"
  "ds_read_b64_tr_b16 v[58:59], v21 offset:63488\n"
  "v_mfma_f32_16x16x32_bf16 v[164:167], v[60:63], a[224:227], v[164:167]\n"
  "v_add_f32 v30, 1.0, v30\n"
  "v_add_f32 v31, 1.0, v31\n"
  "v_mfma_f32_16x16x32_bf16 v[168:171], v[60:63], a[228:231], v[168:171]\n"
  "buffer_load_dwordx4 a[148:151], %[voffX], %[srdWS], s97 offen offset:256 sc1\n"
  "v_add_f32 v32, 1.0, v32\n"
  "v_mfma_f32_16x16x32_bf16 v[172:175], v[60:63], a[232:235], v[172:175]\n"
  "v_add_f32 v33, 1.0, v33\n"
  "v_mfma_f32_16x16x32_bf16 v[176:179], v[60:63], a[236:239], v[176:179]\n"
  "ds_read_b64_tr_b16 v[60:61], v20 offset:59904\n"
  "ds_read_b64_tr_b16 v[62:63], v20 offset:64000\n"
  "v_mfma_f32_16x16x32_bf16 v[180:183], v[64:67], a[224:227], v[180:183]\n"
  "v_rcp_f32 v30, v30\n"
  "v_mfma_f32_16x16x32_bf16 v[184:187], v[64:67], a[228:231], v[184:187]\n"
  "v_rcp_f32 v31, v31\n"
  "v_mfma_f32_16x16x32_bf16 v[188:191], v[64:67], a[232:235], v[188:191]\n"
  "buffer_load_dwordx4 a[152:155], %[voffX], %[srdWS], s98 offen offset:256 sc1\n"
  "v_mfma_f32_16x16x32_bf16 v[192:195], v[64:67], a[236:239], v[192:195]\n"
  "ds_read_b64_tr_b16 v[64:65], v21 offset:59904\n"
  "ds_read_b64_tr_b16 v[66:67], v21 offset:64000\n"
  "s_waitcnt lgkmcnt(8)\n"
  "s_waitcnt vmcnt(26) lgkmcnt(0)\n"
  "s_barrier\n"
  "v_mfma_f32_16x16x32_bf16 v[68:71], v[36:39], a[240:243], v[68:71]\n"
  "v_rcp_f32 v32, v32\n"
  "s_waitcnt vmcnt(14)\n"
  "v_cmp_eq_u64 vcc, %[tok], v[28:29]\n"
  "s_nop 0\n"
  "s_cmp_eq_u64 vcc, -1\n"
  "s_cbranch_scc0 SPINKV_%=\n"
  "BACKKV_%=:\n"
  "v_mfma_f32_16x16x32_bf16 v[72:75], v[36:39], a[244:247], v[72:75]\n"
  "v_rcp_f32 v33, v33\n"
  "v_mfma_f32_16x16x32_bf16 v[76:79], v[36:39], a[248:251], v[76:79]\n"
  "buffer_load_dwordx4 a[156:159], %[voffX], %[srdWS], s99 offen offset:256 sc1\n"
  "v_mul_f32 v212, v212, v30\n"
  "v_mfma_f32_16x16x32_bf16 v[80:83], v[36:39], a[252:255], v[80:83]\n"
  "ds_read_b64_tr_b16 v[36:37], v22 offset:0\n"
  "ds_read_b64_tr_b16 v[38:39], v22 offset:4096\n"
  "v_mfma_f32_16x16x32_bf16 v[84:87], v[40:43], a[240:243], v[84:87]\n"
  "v_mul_f32 v213, v213, v31\n"
  "v_mul_f32 v214, v214, v32\n"
  "v_mfma_f32_16x16x32_bf16 v[88:91], v[40:43], a[244:247], v[88:91]\n"
  "v_mul_f32 v215, v215, v33\n"
  "v_cvt_pk_bf16_f32 v212, v212, v213\n"
  "v_mfma_f32_16x16x32_bf16 v[92:95], v[40:43], a[248:251], v[92:95]\n"
  "v_cvt_pk_bf16_f32 v213, v214, v215\n"
  "v_add_f32 v216, v216, v17\n"
  "v_mfma_f32_16x16x32_bf16 v[96:99], v[40:43], a[252:255], v[96:99]\n"
  "ds_read_b64_tr_b16 v[40:41], v23 offset:0\n"
  "ds_read_b64_tr_b16 v[42:43], v23 offset:4096\n"
  "buffer_load_dwordx4 a[160:163], %[voffX], %[srdWS], s96 offen offset:512 sc1\n"
  "v_mfma_f32_16x16x32_bf16 v[100:103], v[44:47], a[240:243], v[100:103]\n"
  "v_add_f32 v217, v217, v18\n"
  "v_add_f32 v218, v218, v19\n"
  "v_mfma_f32_16x16x32_bf16 v[104:107], v[44:47], a[244:247], v[104:107]\n"
  "v_add_f32 v219, v219, v24\n"
  "v_mul_f32 v30, v216, v216\n"
  "v_mfma_f32_16x16x32_bf16 v[108:111], v[44:47], a[248:251], v[108:111]\n"
  "v_mul_f32 v31, v217, v217\n"
  "v_mul_f32 v32, v218, v218\n"
  "v_mfma_f32_16x16x32_bf16 v[112:115], v[44:47], a[252:255], v[112:115]\n"
  "ds_read_b64_tr_b16 v[44:45], v22 offset:512\n"
  "ds_read_b64_tr_b16 v[46:47], v22 offset:4608\n"
  "buffer_load_dwordx4 a[164:167], %[voffX], %[srdWS], s97 offen offset:512 sc1\n"
  "v_mfma_f32_16x16x32_bf16 v[116:119], v[48:51], a[240:243], v[116:119]\n"
  "v_mul_f32 v33, v219, v219\n"
  "v_fmaak_f32 v30, v16, v30, 0xc0135761\n"
  "v_mfma_f32_16x16x32_bf16 v[120:123], v[48:51], a[244:247], v[120:123]\n"
  "v_fmaak_f32 v31, v16, v31, 0xc0135761\n"
  "v_fmaak_f32 v32, v16, v32, 0xc0135761\n"
  "v_mfma_f32_16x16x32_bf16 v[124:127], v[48:51], a[248:251], v[124:127]\n"
  "v_fmaak_f32 v33, v16, v33, 0xc0135761\n"
  "v_mul_f32 v30, v30, v216\n"
  "v_mfma_f32_16x16x32_bf16 v[128:131], v[48:51], a[252:255], v[128:131]\n"
  "ds_read_b64_tr_b16 v[48:49], v23 offset:512\n"
  "ds_read_b64_tr_b16 v[50:51], v23 offset:4608\n"
  "s_waitcnt lgkmcnt(8)\n"
  "v_mfma_f32_16x16x32_bf16 v[132:135], v[52:55], a[240:243], v[132:135]\n"
  "buffer_load_dwordx4 a[168:171], %[voffX], %[srdWS], s98 offen offset:512 sc1\n"
  "v_mul_f32 v31, v31, v217\n"
  "v_mfma_f32_16x16x32_bf16 v[136:139], v[52:55], a[244:247], v[136:139]\n"
  "v_mul_f32 v32, v32, v218\n"
  "v_mul_f32 v33, v33, v219\n"
  "v_mfma_f32_16x16x32_bf16 v[140:143], v[52:55], a[248:251], v[140:143]\n"
  "v_exp_f32 v30, v30\n"
  "v_mfma_f32_16x16x32_bf16 v[144:147], v[52:55], a[252:255], v[144:147]\n"
  "ds_read_b64_tr_b16 v[52:53], v22 offset:2048\n"
  "ds_read_b64_tr_b16 v[54:55], v22 offset:6144\n"
  "v_mfma_f32_16x16x32_bf16 v[148:151], v[56:59], a[240:243], v[148:151]\n"
  "v_exp_f32 v31, v31\n"
  "v_mfma_f32_16x16x32_bf16 v[152:155], v[56:59], a[244:247], v[152:155]\n"
  "buffer_load_dwordx4 a[172:175], %[voffX], %[srdWS], s99 offen offset:512 sc1\n"
  "v_mfma_f32_16x16x32_bf16 v[156:159], v[56:59], a[248:251], v[156:159]\n"
  "v_exp_f32 v32, v32\n"
  "v_mfma_f32_16x16x32_bf16 v[160:163], v[56:59], a[252:255], v[160:163]\n"
  "ds_read_b64_tr_b16 v[56:57], v23 offset:2048\n"
  "ds_read_b64_tr_b16 v[58:59], v23 offset:6144\n"
  "v_mfma_f32_16x16x32_bf16 v[164:167], v[60:63], a[240:243], v[164:167]\n"
  "v_exp_f32 v33, v33\n"
  "v_mfma_f32_16x16x32_bf16 v[168:171], v[60:63], a[244:247], v[168:171]\n"
  "buffer_load_dwordx4 a[176:179], %[voffX], %[srdWS], s96 offen offset:768 sc1\n"
  "v_add_f32 v30, 1.0, v30\n"
  "v_mfma_f32_16x16x32_bf16 v[172:175], v[60:63], a[248:251], v[172:175]\n"
  "v_add_f32 v31, 1.0, v31\n"
  "v_add_f32 v32, 1.0, v32\n"
  "v_mfma_f32_16x16x32_bf16 v[176:179], v[60:63], a[252:255], v[176:179]\n"
  "ds_read_b64_tr_b16 v[60:61], v22 offset:2560\n"
  "ds_read_b64_tr_b16 v[62:63], v22 offset:6656\n"
  "v_mfma_f32_16x16x32_bf16 v[180:183], v[64:67], a[240:243], v[180:183]\n"
  "v_add_f32 v33, 1.0, v33\n"
  "v_mfma_f32_16x16x32_bf16 v[184:187], v[64:67], a[244:247], v[184:187]\n"
  "v_rcp_f32 v30, v30\n"
  "v_mfma_f32_16x16x32_bf16 v[188:191], v[64:67], a[248:251], v[188:191]\n"
  "buffer_load_dwordx4 a[180:183], %[voffX], %[srdWS], s97 offen offset:768 sc1\n"
  "v_mfma_f32_16x16x32_bf16 v[192:195], v[64:67], a[252:255], v[192:195]\n"
  "ds_read_b64_tr_b16 v[64:65], v23 offset:2560\n"
  "ds_read_b64_tr_b16 v[66:67], v23 offset:6656\n"
  "s_waitcnt lgkmcnt(8)\n"
  "v_mfma_f32_16x16x32_bf16 v[68:71], v[36:39], a[64:67], v[68:71]\n"
  "v_rcp_f32 v31, v31\n"
  "v_mfma_f32_16x16x32_bf16 v[72:75], v[36:39], a[68:71], v[72:75]\n"
  "v_rcp_f32 v32, v32\n"
  "v_mfma_f32_16x16x32_bf16 v[76:79], v[36:39], a[72:75], v[76:79]\n"
  "buffer_load_dwordx4 a[184:187], %[voffX], %[srdWS], s98 offen offset:768 sc1\n"
  "v_mfma_f32_16x16x32_bf16 v[80:83], v[36:39], a[76:79], v[80:83]\n"
  "ds_read_b64_tr_b16 v[36:37], v22 offset:8192\n"
  "ds_read_b64_tr_b16 v[38:39], v22 offset:12288\n"
  "v_mfma_f32_16x16x32_bf16 v[84:87], v[40:43], a[64:67], v[84:87]\n"
  "v_rcp_f32 v33, v33\n"
  "v_mfma_f32_16x16x32_bf16 v[88:91], v[40:43], a[68:71], v[88:91]\n"
  "v_mul_f32 v216, v216, v30\n"
  "v_mul_f32 v217, v217, v31\n"
  "v_mfma_f32_16x16x32_bf16 v[92:95], v[40:43], a[72:75], v[92:95]\n"
  "v_mul_f32 v218, v218, v32\n"
  "v_mul_f32 v219, v219, v33\n"
  "v_mfma_f32_16x16x32_bf16 v[96:99], v[40:43], a[76:79], v[96:99]\n"
  "ds_read_b64_tr_b16 v[40:41], v23 offset:8192\n"
  "ds_read_b64_tr_b16 v[42:43], v23 offset:12288\n"
  "buffer_load_dwordx4 a[188:191], %[voffX], %[srdWS], s99 offen offset:768 sc1\n"
  "v_mfma_f32_16x16x32_bf16 v[100:103], v[44:47], a[64:67], v[100:103]\n"
  "v_cvt_pk_bf16_f32 v216, v216, v217\n"
  "v_cvt_pk_bf16_f32 v217, v218, v219\n"
  "v_mfma_f32_16x16x32_bf16 v[104:107], v[44:47], a[68:71], v[104:107]\n"
  "v_lshlrev_b32 v17, 16, v250\n"
  "v_and_b32 v18, 0xffff0000, v250\n"
  "v_mfma_f32_16x16x32_bf16 v[108:111], v[44:47], a[72:75], v[108:111]\n"
  "s_add_u32 m0, s76, 0\n"
  "v_lshlrev_b32 v19, 16, v251\n"
  "v_mfma_f32_16x16x32_bf16 v[112:115], v[44:47], a[76:79], v[112:115]\n"
  "ds_read_b64_tr_b16 v[44:45], v22 offset:8704\n"
  "ds_read_b64_tr_b16 v[46:47], v22 offset:12800\n"
  "s_add_u32 s64, %[oK], 0\n"
  "v_mfma_f32_16x16x32_bf16 v[116:119], v[48:51], a[64:67], v[116:119]\n"
  "buffer_load_dwordx4 %[L16], %[srdWS], s64 offen offset:0 sc1 lds\n"
  "v_and_b32 v24, 0xffff0000, v251\n"
  "v_mfma_f32_16x16x32_bf16 v[120:123], v[48:51], a[68:71], v[120:123]\n"
  "v_add_f32 v220, v220, v17\n"
  "v_add_f32 v221, v221, v18\n"
  "v_mfma_f32_16x16x32_bf16 v[124:127], v[48:51], a[72:75], v[124:127]\n"
  "v_add_f32 v222, v222, v19\n"
  "v_add_f32 v223, v223, v24\n"
  "v_mfma_f32_16x16x32_bf16 v[128:131], v[48:51], a[76:79], v[128:131]\n"
  "ds_read_b64_tr_b16 v[48:49], v23 offset:8704\n"
  "ds_read_b64_tr_b16 v[50:51], v23 offset:12800\n"
  "s_waitcnt lgkmcnt(8)\n"
  "v_mfma_f32_16x16x32_bf16 v[132:135], v[52:55], a[64:67], v[132:135]\n"
  "v_mul_f32 v30, v220, v220\n"
  "v_mul_f32 v31, v221, v221\n"
  "v_mfma_f32_16x16x32_bf16 v[136:139], v[52:55], a[68:71], v[136:139]\n"
  "v_mul_f32 v32, v222, v222\n"
  "v_mul_f32 v33, v223, v223\n"
  "v_mfma_f32_16x16x32_bf16 v[140:143], v[52:55], a[72:75], v[140:143]\n"
  "v_fmaak_f32 v30, v16, v30, 0xc0135761\n"
  "v_fmaak_f32 v31, v16, v31, 0xc0135761\n"
  "v_mfma_f32_16x16x32_bf16 v[144:147], v[52:55], a[76:79], v[144:147]\n"
  "ds_read_b64_tr_b16 v[52:53], v22 offset:10240\n"
  "ds_read_b64_tr_b16 v[54:55], v22 offset:14336\n"
  "buffer_load_dwordx4 %[L16], %[srdWS], s64 offen offset:1024 sc1 lds\n"
  "v_mfma_f32_16x16x32_bf16 v[148:151], v[56:59], a[64:67], v[148:151]\n"
  "v_fmaak_f32 v32, v16, v32, 0xc0135761\n"
  "v_fmaak_f32 v33, v16, v33, 0xc0135761\n"
  "v_mfma_f32_16x16x32_bf16 v[152:155], v[56:59], a[68:71], v[152:155]\n"
  "v_mul_f32 v30, v30, v220\n"
  "v_mul_f32 v31, v31, v221\n"
  "v_mfma_f32_16x16x32_bf16 v[156:159], v[56:59], a[72:75], v[156:159]\n"
  "v_mul_f32 v32, v32, v222\n"
  "v_mul_f32 v33, v33, v223\n"
  "v_mfma_f32_16x16x32_bf16 v[160:163], v[56:59], a[76:79], v[160:163]\n"
  "ds_read_b64_tr_b16 v[56:57], v23 offset:10240\n"
  "ds_read_b64_tr_b16 v[58:59], v23 offset:14336\n"
  "v_mfma_f32_16x16x32_bf16 v[164:167], v[60:63], a[64:67], v[164:167]\n"
  "v_exp_f32 v30, v30\n"
  "v_mfma_f32_16x16x32_bf16 v[168:171], v[60:63], a[68:71], v[168:171]\n"
  "v_exp_f32 v31, v31\n"
  "v_mfma_f32_16x16x32_bf16 v[172:175], v[60:63], a[72:75], v[172:175]\n"
  "buffer_load_dwordx4 %[L16], %[srdWS], s64 offen offset:2048 sc1 lds\n"
  "v_mfma_f32_16x16x32_bf16 v[176:179], v[60:63], a[76:79], v[176:179]\n"
  "ds_read_b64_tr_b16 v[60:61], v22 offset:10752\n"
  "ds_read_b64_tr_b16 v[62:63], v22 offset:14848\n"
  "v_mfma_f32_16x16x32_bf16 v[180:183], v[64:67], a[64:67], v[180:183]\n"
  "v_exp_f32 v32, v32\n"
  "v_mfma_f32_16x16x32_bf16 v[184:187], v[64:67], a[68:71], v[184:187]\n"
  "v_exp_f32 v33, v33\n"
  "v_mfma_f32_16x16x32_bf16 v[188:191], v[64:67], a[72:75], v[188:191]\n"
  "v_add_f32 v30, 1.0, v30\n"
  "v_add_f32 v31, 1.0, v31\n"
  "v_mfma_f32_16x16x32_bf16 v[192:195], v[64:67], a[76:79], v[192:195]\n"
  "ds_read_b64_tr_b16 v[64:65], v23 offset:10752\n"
  "ds_read_b64_tr_b16 v[66:67], v23 offset:14848\n"
  "s_waitcnt lgkmcnt(8)\n"
  "v_mfma_f32_16x16x32_bf16 v[68:71], v[36:39], a[80:83], v[68:71]\n"
  "v_add_f32 v32, 1.0, v32\n"
  "v_add_f32 v33, 1.0, v33\n"
  "v_mfma_f32_16x16x32_bf16 v[72:75], v[36:39], a[84:87], v[72:75]\n"
  "buffer_load_dwordx4 %[L16], %[srdWS], s64 offen offset:3072 sc1 lds\n"
  "v_mfma_f32_16x16x32_bf16 v[76:79], v[36:39], a[88:91], v[76:79]\n"
  "v_rcp_f32 v30, v30\n"
  "v_mfma_f32_16x16x32_bf16 v[80:83], v[36:39], a[92:95], v[80:83]\n"
  "ds_read_b64_tr_b16 v[36:37], v22 offset:16384\n"
  "ds_read_b64_tr_b16 v[38:39], v22 offset:20480\n"
  "v_mfma_f32_16x16x32_bf16 v[84:87], v[40:43], a[80:83], v[84:87]\n"
  "v_rcp_f32 v31, v31\n"
  "v_mfma_f32_16x16x32_bf16 v[88:91], v[40:43], a[84:87], v[88:91]\n"
  "v_rcp_f32 v32, v32\n"
  "v_mfma_f32_16x16x32_bf16 v[92:95], v[40:43], a[88:91], v[92:95]\n"
  "s_add_u32 m0, s76, 4096\n"
  "v_mfma_f32_16x16x32_bf16 v[96:99], v[40:43], a[92:95], v[96:99]\n"
  "ds_read_b64_tr_b16 v[40:41], v23 offset:16384\n"
  "ds_read_b64_tr_b16 v[42:43], v23 offset:20480\n"
  "s_add_u32 s64, %[oK], 4096\n"
  "v_mfma_f32_16x16x32_bf16 v[100:103], v[44:47], a[80:83], v[100:103]\n"
  "buffer_load_dwordx4 %[L16], %[srdWS], s64 offen offset:0 sc1 lds\n"
  "v_mfma_f32_16x16x32_bf16 v[104:107], v[44:47], a[84:87], v[104:107]\n"
  "v_rcp_f32 v33, v33\n"
  "v_mfma_f32_16x16x32_bf16 v[108:111], v[44:47], a[88:91], v[108:111]\n"
  "v_mul_f32 v220, v220, v30\n"
  "v_mul_f32 v221, v221, v31\n"
  "v_mfma_f32_16x16x32_bf16 v[112:115], v[44:47], a[92:95], v[112:115]\n"
  "ds_read_b64_tr_b16 v[44:45], v22 offset:16896\n"
  "ds_read_b64_tr_b16 v[46:47], v22 offset:20992\n"
  "v_mfma_f32_16x16x32_bf16 v[116:119], v[48:51], a[80:83], v[116:119]\n"
  "v_mul_f32 v222, v222, v32\n"
  "v_mul_f32 v223, v223, v33\n"
  "v_mfma_f32_16x16x32_bf16 v[120:123], v[48:51], a[84:87], v[120:123]\n"
  "v_cvt_pk_bf16_f32 v214, v220, v221\n"
  "v_cvt_pk_bf16_f32 v215, v222, v223\n"
  "v_mfma_f32_16x16x32_bf16 v[124:127], v[48:51], a[88:91], v[124:127]\n"
  "buffer_store_dwordx4 v[212:215], %[L16], %[srdWS], s77 offen offset:0 sc1\n"
  "v_add_f32 v224, v224, v17\n"
  "v_mfma_f32_16x16x32_bf16 v[128:131], v[48:51], a[92:95], v[128:131]\n"
  "ds_read_b64_tr_b16 v[48:49], v23 offset:16896\n"
  "ds_read_b64_tr_b16 v[50:51], v23 offset:20992\n"
  "buffer_load_dwordx4 %[L16], %[srdWS], s64 offen offset:1024 sc1 lds\n"
  "s_waitcnt lgkmcnt(8)\n"
  "v_mfma_f32_16x16x32_bf16 v[132:135], v[52:55], a[80:83], v[132:135]\n"
  "v_add_f32 v225, v225, v18\n"
  "v_add_f32 v226, v226, v19\n"
  "v_mfma_f32_16x16x32_bf16 v[136:139], v[52:55], a[84:87], v[136:139]\n"
  "v_add_f32 v227, v227, v24\n"
  "v_mul_f32 v30, v224, v224\n"
  "v_mfma_f32_16x16x32_bf16 v[140:143], v[52:55], a[88:91], v[140:143]\n"
  "v_mul_f32 v31, v225, v225\n"
  "v_mul_f32 v32, v226, v226\n"
  "v_mfma_f32_16x16x32_bf16 v[144:147], v[52:55], a[92:95], v[144:147]\n"
  "ds_read_b64_tr_b16 v[52:53], v22 offset:18432\n"
  "ds_read_b64_tr_b16 v[54:55], v22 offset:22528\n"
  "v_mfma_f32_16x16x32_bf16 v[148:151], v[56:59], a[80:83], v[148:151]\n"
  "v_mul_f32 v33, v227, v227\n"
  "v_fmaak_f32 v30, v16, v30, 0xc0135761\n"
  "v_mfma_f32_16x16x32_bf16 v[152:155], v[56:59], a[84:87], v[152:155]\n"
  "v_fmaak_f32 v31, v16, v31, 0xc0135761\n"
  "v_fmaak_f32 v32, v16, v32, 0xc0135761\n"
  "v_mfma_f32_16x16x32_bf16 v[156:159], v[56:59], a[88:91], v[156:159]\n"
  "buffer_load_dwordx4 %[L16], %[srdWS], s64 offen offset:2048 sc1 lds\n"
  "v_fmaak_f32 v33, v16, v33, 0xc0135761\n"
  "v_mfma_f32_16x16x32_bf16 v[160:163], v[56:59], a[92:95], v[160:163]\n"
  "ds_read_b64_tr_b16 v[56:57], v23 offset:18432\n"
  "ds_read_b64_tr_b16 v[58:59], v23 offset:22528\n"
  "v_mfma_f32_16x16x32_bf16 v[164:167], v[60:63], a[80:83], v[164:167]\n"
  "v_mul_f32 v30, v30, v224\n"
  "v_mul_f32 v31, v31, v225\n"
  "v_mfma_f32_16x16x32_bf16 v[168:171], v[60:63], a[84:87], v[168:171]\n"
  "v_mul_f32 v32, v32, v226\n"
  "v_mul_f32 v33, v33, v227\n"
  "v_mfma_f32_16x16x32_bf16 v[172:175], v[60:63], a[88:91], v[172:175]\n"
  "v_exp_f32 v30, v30\n"
  "v_mfma_f32_16x16x32_bf16 v[176:179], v[60:63], a[92:95], v[176:179]\n"
  "ds_read_b64_tr_b16 v[60:61], v22 offset:18944\n"
  "ds_read_b64_tr_b16 v[62:63], v22 offset:23040\n"
  "v_mfma_f32_16x16x32_bf16 v[180:183], v[64:67], a[80:83], v[180:183]\n"
  "v_exp_f32 v31, v31\n"
  "v_mfma_f32_16x16x32_bf16 v[184:187], v[64:67], a[84:87], v[184:187]\n"
  "buffer_load_dwordx4 %[L16], %[srdWS], s64 offen offset:3072 sc1 lds\n"
  "v_mfma_f32_16x16x32_bf16 v[188:191], v[64:67], a[88:91], v[188:191]\n"
  "v_exp_f32 v32, v32\n"
  "v_mfma_f32_16x16x32_bf16 v[192:195], v[64:67], a[92:95], v[192:195]\n"
  "ds_read_b64_tr_b16 v[64:65], v23 offset:18944\n"
  "ds_read_b64_tr_b16 v[66:67], v23 offset:23040\n"
  "s_waitcnt lgkmcnt(8)\n"
  "v_mfma_f32_16x16x32_bf16 v[68:71], v[36:39], a[96:99], v[68:71]\n"
  "v_exp_f32 v33, v33\n"
  "v_mfma_f32_16x16x32_bf16 v[72:75], v[36:39], a[100:103], v[72:75]\n"
  "v_add_f32 v30, 1.0, v30\n"
  "v_add_f32 v31, 1.0, v31\n"
  "s_waitcnt vmcnt(31)\n"
  "v_mfma_f32_16x16x32_bf16 v[76:79], v[36:39], a[104:107], v[76:79]\n"
  "s_add_u32 m0, s76, 32768\n"
  "v_add_f32 v32, 1.0, v32\n"
  "s_waitcnt vmcnt(30)\n"
  "v_mfma_f32_16x16x32_bf16 v[80:83], v[36:39], a[108:111], v[80:83]\n"
  "ds_read_b64_tr_b16 v[36:37], v22 offset:24576\n"
  "ds_read_b64_tr_b16 v[38:39], v22 offset:28672\n"
  "s_add_u32 s64, %[oK], 32768\n"
  "v_mfma_f32_16x16x32_bf16 v[84:87], v[40:43], a[96:99], v[84:87]\n"
  "buffer_load_dwordx4 %[L16], %[srdWS], s64 offen offset:0 sc1 lds\n"
  "v_add_f32 v33, 1.0, v33\n"
  "v_mfma_f32_16x16x32_bf16 v[88:91], v[40:43], a[100:103], v[88:91]\n"
  "v_rcp_f32 v30, v30\n"
  "v_mfma_f32_16x16x32_bf16 v[92:95], v[40:43], a[104:107], v[92:95]\n"
  "v_rcp_f32 v31, v31\n"
  "v_mfma_f32_16x16x32_bf16 v[96:99], v[40:43], a[108:111], v[96:99]\n"
  "ds_read_b64_tr_b16 v[40:41], v23 offset:24576\n"
  "ds_read_b64_tr_b16 v[42:43], v23 offset:28672\n"
  "v_mfma_f32_16x16x32_bf16 v[100:103], v[44:47], a[96:99], v[100:103]\n"
  "v_rcp_f32 v32, v32\n"
  "v_mfma_f32_16x16x32_bf16 v[104:107], v[44:47], a[100:103], v[104:107]\n"
  "v_rcp_f32 v33, v33\n"
  "v_mfma_f32_16x16x32_bf16 v[108:111], v[44:47], a[104:107], v[108:111]\n"
  "v_mul_f32 v224, v224, v30\n"
  "v_mul_f32 v225, v225, v31\n"
  "v_mfma_f32_16x16x32_bf16 v[112:115], v[44:47], a[108:111], v[112:115]\n"
  "ds_read_b64_tr_b16 v[44:45], v22 offset:25088\n"
  "ds_read_b64_tr_b16 v[46:47], v22 offset:29184\n"
  "buffer_load_dwordx4 %[L16], %[srdWS], s64 offen offset:1024 sc1 lds\n"
  "v_mfma_f32_16x16x32_bf16 v[116:119], v[48:51], a[96:99], v[116:119]\n"
  "v_mul_f32 v226, v226, v32\n"
  "v_mul_f32 v227, v227, v33\n"
  "v_mfma_f32_16x16x32_bf16 v[120:123], v[48:51], a[100:103], v[120:123]\n"
  "v_cvt_pk_bf16_f32 v218, v224, v225\n"
  "v_cvt_pk_bf16_f32 v219, v226, v227\n"
  "v_mfma_f32_16x16x32_bf16 v[124:127], v[48:51], a[104:107], v[124:127]\n"
  "buffer_store_dwordx4 v[216:219], %[L16], %[srdWS], s77 offen offset:1024 sc1\n"
  "v_mfma_f32_16x16x32_bf16 v[128:131], v[48:51], a[108:111], v[128:131]\n"
  "ds_read_b64_tr_b16 v[48:49], v23 offset:25088\n"
  "ds_read_b64_tr_b16 v[50:51], v23 offset:29184\n"
  "s_waitcnt lgkmcnt(8)\n"
  "v_mfma_f32_16x16x32_bf16 v[132:135], v[52:55], a[96:99], v[132:135]\n"
  "s_add_u32 s77, %[oH], 16384\n"
  "s_cmp_gt_u32 %[nH], 2\n"
  "s_cselect_b32 s77, s77, 0x7f000000\n"
  "v_mfma_f32_16x16x32_bf16 v[136:139], v[52:55], a[100:103], v[136:139]\n"
  "v_lshlrev_b32 v17, 16, v252\n"
  "v_and_b32 v18, 0xffff0000, v252\n"
  "v_mfma_f32_16x16x32_bf16 v[140:143], v[52:55], a[104:107], v[140:143]\n"
  "buffer_load_dwordx4 %[L16], %[srdWS], s64 offen offset:2048 sc1 lds\n"
  "v_lshlrev_b32 v19, 16, v253\n"
  "v_mfma_f32_16x16x32_bf16 v[144:147], v[52:55], a[108:111], v[144:147]\n"
  "ds_read_b64_tr_b16 v[52:53], v22 offset:26624\n"
  "ds_read_b64_tr_b16 v[54:55], v22 offset:30720\n"
  "v_mfma_f32_16x16x32_bf16 v[148:151], v[56:59], a[96:99], v[148:151]\n"
  "v_and_b32 v24, 0xffff0000, v253\n"
  "v_add_f32 v228, v228, v17\n"
  "v_mfma_f32_16x16x32_bf16 v[152:155], v[56:59], a[100:103], v[152:155]\n"
  "v_add_f32 v229, v229, v18\n"
  "v_add_f32 v230, v230, v19\n"
  "v_mfma_f32_16x16x32_bf16 v[156:159], v[56:59], a[104:107], v[156:159]\n"
  "v_add_f32 v231, v231, v24\n"
  "v_mul_f32 v30, v228, v228\n"
  "v_mfma_f32_16x16x32_bf16 v[160:163], v[56:59], a[108:111], v[160:163]\n"
  "ds_read_b64_tr_b16 v[56:57], v23 offset:26624\n"
  "ds_read_b64_tr_b16 v[58:59], v23 offset:30720\n"
  "v_mfma_f32_16x16x32_bf16 v[164:167], v[60:63], a[96:99], v[164:167]\n"
  "v_mul_f32 v31, v229, v229\n"
  "v_mul_f32 v32, v230, v230\n"
  "v_mfma_f32_16x16x32_bf16 v[168:171], v[60:63], a[100:103], v[168:171]\n"
  "buffer_load_dwordx4 %[L16], %[srdWS], s64 offen offset:3072 sc1 lds\n"
  "v_mul_f32 v33, v231, v231\n"
  "v_mfma_f32_16x16x32_bf16 v[172:175], v[60:63], a[104:107], v[172:175]\n"
  "v_fmaak_f32 v30, v16, v30, 0xc0135761\n"
  "v_fmaak_f32 v31, v16, v31, 0xc0135761\n"
  "v_mfma_f32_16x16x32_bf16 v[176:179], v[60:63], a[108:111], v[176:179]\n"
  "ds_read_b64_tr_b16 v[60:61], v22 offset:27136\n"
  "ds_read_b64_tr_b16 v[62:63], v22 offset:31232\n"
  "v_mfma_f32_16x16x32_bf16 v[180:183], v[64:67], a[96:99], v[180:183]\n"
  "v_fmaak_f32 v32, v16, v32, 0xc0135761\n"
  "v_fmaak_f32 v33, v16, v33, 0xc0135761\n"
  "v_mfma_f32_16x16x32_bf16 v[184:187], v[64:67], a[100:103], v[184:187]\n"
  "v_mul_f32 v30, v30, v228\n"
  "v_mul_f32 v31, v31, v229\n"
  "v_mfma_f32_16x16x32_bf16 v[188:191], v[64:67], a[104:107], v[188:191]\n"
  "s_add_u32 m0, s76, 36864\n"
  "v_mul_f32 v32, v32, v230\n"
  "v_mfma_f32_16x16x32_bf16 v[192:195], v[64:67], a[108:111], v[192:195]\n"
  "ds_read_b64_tr_b16 v[64:65], v23 offset:27136\n"
  "ds_read_b64_tr_b16 v[66:67], v23 offset:31232\n"
  "s_add_u32 s64, %[oK], 36864\n"
  "s_waitcnt lgkmcnt(8)\n"
  "s_waitcnt vmcnt(63) lgkmcnt(0)\n"
  "s_barrier\n"
  "s_waitcnt vmcnt(34)\n"
  "v_mfma_f32_16x16x32_bf16 v[68:71], v[36:39], a[112:115], v[68:71]\n"
  "buffer_load_dwordx4 %[L16], %[srdWS], s64 offen offset:0 sc1 lds\n"
  "v_mul_f32 v33, v33, v231\n"
  "s_waitcnt vmcnt(34)\n"
  "v_mfma_f32_16x16x32_bf16 v[72:75], v[36:39], a[116:119], v[72:75]\n"
  "v_exp_f32 v30, v30\n"
  "s_waitcnt vmcnt(33)\n"
  "v_mfma_f32_16x16x32_bf16 v[76:79], v[36:39], a[120:123], v[76:79]\n"
  "v_exp_f32 v31, v31\n"
  "s_waitcnt vmcnt(31)\n"
  "v_mfma_f32_16x16x32_bf16 v[80:83], v[36:39], a[124:127], v[80:83]\n"
  "ds_read_b64_tr_b16 v[36:37], v22 offset:32768\n"
  "ds_read_b64_tr_b16 v[38:39], v22 offset:36864\n"
  "v_mfma_f32_16x16x32_bf16 v[84:87], v[40:43], a[112:115], v[84:87]\n"
  "v_exp_f32 v32, v32\n"
  "v_mfma_f32_16x16x32_bf16 v[88:91], v[40:43], a[116:119], v[88:91]\n"
  "v_exp_f32 v33, v33\n"
  "v_mfma_f32_16x16x32_bf16 v[92:95], v[40:43], a[120:123], v[92:95]\n"
  "v_add_f32 v30, 1.0, v30\n"
  "v_add_f32 v31, 1.0, v31\n"
  "v_mfma_f32_16x16x32_bf16 v[96:99], v[40:43], a[124:127], v[96:99]\n"
  "ds_read_b64_tr_b16 v[40:41], v23 offset:32768\n"
  "ds_read_b64_tr_b16 v[42:43], v23 offset:36864\n"
  "buffer_load_dwordx4 %[L16], %[srdWS], s64 offen offset:1024 sc1 lds\n"
  "v_mfma_f32_16x16x32_bf16 v[100:103], v[44:47], a[112:115], v[100:103]\n"
  "v_add_f32 v32, 1.0, v32\n"
  "v_add_f32 v33, 1.0, v33\n"
  "v_mfma_f32_16x16x32_bf16 v[104:107], v[44:47], a[116:119], v[104:107]\n"
  "v_rcp_f32 v30, v30\n"
  "v_mfma_f32_16x16x32_bf16 v[108:111], v[44:47], a[120:123], v[108:111]\n"
  "v_rcp_f32 v31, v31\n"
  "v_mfma_f32_16x16x32_bf16 v[112:115], v[44:47], a[124:127], v[112:115]\n"
  "ds_read_b64_tr_b16 v[44:45], v22 offset:33280\n"
  "ds_read_b64_tr_b16 v[46:47], v22 offset:37376\n"
  "v_mfma_f32_16x16x32_bf16 v[116:119], v[48:51], a[112:115], v[116:119]\n"
  "v_rcp_f32 v32, v32\n"
  "v_mfma_f32_16x16x32_bf16 v[120:123], v[48:51], a[116:119], v[120:123]\n"
  "v_rcp_f32 v33, v33\n"
  "v_mfma_f32_16x16x32_bf16 v[124:127], v[48:51], a[120:123], v[124:127]\n"
  "buffer_load_dwordx4 %[L16], %[srdWS], s64 offen offset:2048 sc1 lds\n"
  "v_mul_f32 v228, v228, v30\n"
  "v_mfma_f32_16x16x32_bf16 v[128:131], v[48:51], a[124:127], v[128:131]\n"
  "ds_read_b64_tr_b16 v[48:49], v23 offset:33280\n"
  "ds_read_b64_tr_b16 v[50:51], v23 offset:37376\n"
  "s_waitcnt lgkmcnt(8)\n"
  "v_mfma_f32_16x16x32_bf16 v[132:135], v[52:55], a[112:115], v[132:135]\n"
  "v_mul_f32 v229, v229, v31\n"
  "v_mul_f32 v230, v230, v32\n"
  "v_mfma_f32_16x16x32_bf16 v[136:139], v[52:55], a[116:119], v[136:139]\n"
  "v_mul_f32 v231, v231, v33\n"
  "v_cvt_pk_bf16_f32 v228, v228, v229\n"
  "v_mfma_f32_16x16x32_bf16 v[140:143], v[52:55], a[120:123], v[140:143]\n"
  "v_cvt_pk_bf16_f32 v229, v230, v231\n"
  "v_add_f32 v232, v232, v17\n"
  "v_mfma_f32_16x16x32_bf16 v[144:147], v[52:55], a[124:127], v[144:147]\n"
  "ds_read_b64_tr_b16 v[52:53], v22 offset:34816\n"
  "ds_read_b64_tr_b16 v[54:55], v22 offset:38912\n"
  "v_mfma_f32_16x16x32_bf16 v[148:151], v[56:59], a[112:115], v[148:151]\n"
  "v_add_f32 v233, v233, v18\n"
  "v_add_f32 v234, v234, v19\n"
  "v_mfma_f32_16x16x32_bf16 v[152:155], v[56:59], a[116:119], v[152:155]\n"
  "buffer_load_dwordx4 %[L16], %[srdWS], s64 offen offset:3072 sc1 lds\n"
  "v_add_f32 v235, v235, v24\n"
  "v_mfma_f32_16x16x32_bf16 v[156:159], v[56:59], a[120:123], v[156:159]\n"
  "v_mul_f32 v30, v232, v232\n"
  "v_mul_f32 v31, v233, v233\n"
  "v_mfma_f32_16x16x32_bf16 v[160:163], v[56:59], a[124:127], v[160:163]\n"
  "ds_read_b64_tr_b16 v[56:57], v23 offset:34816\n"
  "ds_read_b64_tr_b16 v[58:59], v23 offset:38912\n"
  "v_mfma_f32_16x16x32_bf16 v[164:167], v[60:63], a[112:115], v[164:167]\n"
  "v_mul_f32 v32, v234, v234\n"
  "v_mul_f32 v33, v235, v235\n"
  "v_mfma_f32_16x16x32_bf16 v[168:171], v[60:63], a[116:119], v[168:171]\n"
  "v_fmaak_f32 v30, v16, v30, 0xc0135761\n"
  "v_fmaak_f32 v31, v16, v31, 0xc0135761\n"
  "v_mfma_f32_16x16x32_bf16 v[172:175], v[60:63], a[120:123], v[172:175]\n"
  "s_add_u32 m0, s76, 65536\n"
  "v_fmaak_f32 v32, v16, v32, 0xc0135761\n"
  "v_mfma_f32_16x16x32_bf16 v[176:179], v[60:63], a[124:127], v[176:179]\n"
  "ds_read_b64_tr_b16 v[60:61], v22 offset:35328\n"
  "ds_read_b64_tr_b16 v[62:63], v22 offset:39424\n"
  "s_add_u32 s64, %[oK], 16777216\n"
  "v_mfma_f32_16x16x32_bf16 v[180:183], v[64:67], a[112:115], v[180:183]\n"
  "buffer_load_dwordx4 %[L16], %[srdWS], s64 offen offset:0 sc1 lds\n"
  "v_fmaak_f32 v33, v16, v33, 0xc0135761\n"
  "v_mfma_f32_16x16x32_bf16 v[184:187], v[64:67], a[116:119], v[184:187]\n"
  "v_mul_f32 v30, v30, v232\n"
  "v_mul_f32 v31, v31, v233\n"
  "v_mfma_f32_16x16x32_bf16 v[188:191], v[64:67], a[120:123], v[188:191]\n"
  "v_mul_f32 v32, v32, v234\n"
  "v_mul_f32 v33, v33, v235\n"
  "v_mfma_f32_16x16x32_bf16 v[192:195], v[64:67], a[124:127], v[192:195]\n"
  "ds_read_b64_tr_b16 v[64:65], v23 offset:35328\n"
  "ds_read_b64_tr_b16 v[66:67], v23 offset:39424\n"
  "s_waitcnt lgkmcnt(8)\n"
  "s_waitcnt vmcnt(34)\n"
  "v_mfma_f32_16x16x32_bf16 v[68:71], v[36:39], a[128:131], v[68:71]\n"
  "v_exp_f32 v30, v30\n"
  "s_waitcnt vmcnt(33)\n"
  "v_mfma_f32_16x16x32_bf16 v[72:75], v[36:39], a[132:135], v[72:75]\n"
  "v_exp_f32 v31, v31\n"
  "s_waitcnt vmcnt(32)\n"
  "v_mfma_f32_16x16x32_bf16 v[76:79], v[36:39], a[136:139], v[76:79]\n"
  "v_exp_f32 v32, v32\n"
  "s_waitcnt vmcnt(31)\n"
  "v_mfma_f32_16x16x32_bf16 v[80:83], v[36:39], a[140:143], v[80:83]\n"
  "ds_read_b64_tr_b16 v[36:37], v22 offset:40960\n"
  "ds_read_b64_tr_b16 v[38:39], v22 offset:45056\n"
  "buffer_load_dwordx4 %[L16], %[srdWS], s64 offen offset:1024 sc1 lds\n"
  "v_mfma_f32_16x16x32_bf16 v[84:87], v[40:43], a[128:131], v[84:87]\n"
  "v_exp_f32 v33, v33\n"
  "v_mfma_f32_16x16x32_bf16 v[88:91], v[40:43], a[132:135], v[88:91]\n"
  "v_add_f32 v30, 1.0, v30\n"
  "v_add_f32 v31, 1.0, v31\n"
  "v_mfma_f32_16x16x32_bf16 v[92:95], v[40:43], a[136:139], v[92:95]\n"
  "v_add_f32 v32, 1.0, v32\n"
  "v_add_f32 v33, 1.0, v33\n"
  "v_mfma_f32_16x16x32_bf16 v[96:99], v[40:43], a[140:143], v[96:99]\n"
  "ds_read_b64_tr_b16 v[40:41], v23 offset:40960\n"
  "ds_read_b64_tr_b16 v[42:43], v23 offset:45056\n"
  "v_mfma_f32_16x16x32_bf16 v[100:103], v[44:47], a[128:131], v[100:103]\n"
  "v_rcp_f32 v30, v30\n"
  "v_mfma_f32_16x16x32_bf16 v[104:107], v[44:47], a[132:135], v[104:107]\n"
  "v_rcp_f32 v31, v31\n"
  "v_mfma_f32_16x16x32_bf16 v[108:111], v[44:47], a[136:139], v[108:111]\n"
  "buffer_load_dwordx4 %[L16], %[srdWS], s64 offen offset:2048 sc1 lds\n"
  "v_mfma_f32_16x16x32_bf16 v[112:115], v[44:47], a[140:143], v[112:115]\n"
  "ds_read_b64_tr_b16 v[44:45], v22 offset:41472\n"
  "ds_read_b64_tr_b16 v[46:47], v22 offset:45568\n"
  "v_mfma_f32_16x16x32_bf16 v[116:119], v[48:51], a[128:131], v[116:119]\n"
  "v_rcp_f32 v32, v32\n"
  "v_mfma_f32_16x16x32_bf16 v[120:123], v[48:51], a[132:135], v[120:123]\n"
  "v_rcp_f32 v33, v33\n"
  "v_mfma_f32_16x16x32_bf16 v[124:127], v[48:51], a[136:139], v[124:127]\n"
  "v_mul_f32 v232, v232, v30\n"
  "v_mul_f32 v233, v233, v31\n"
  "v_mfma_f32_16x16x32_bf16 v[128:131], v[48:51], a[140:143], v[128:131]\n"
  "ds_read_b64_tr_b16 v[48:49], v23 offset:41472\n"
  "ds_read_b64_tr_b16 v[50:51], v23 offset:45568\n"
  "s_waitcnt lgkmcnt(8)\n"
  "v_mfma_f32_16x16x32_bf16 v[132:135], v[52:55], a[128:131], v[132:135]\n"
  "v_mul_f32 v234, v234, v32\n"
  "v_mul_f32 v235, v235, v33\n"
  "v_mfma_f32_16x16x32_bf16 v[136:139], v[52:55], a[132:135], v[136:139]\n"
  "buffer_load_dwordx4 %[L16], %[srdWS], s64 offen offset:3072 sc1 lds\n"
  "v_cvt_pk_bf16_f32 v232, v232, v233\n"
  "v_mfma_f32_16x16x32_bf16 v[140:143], v[52:55], a[136:139], v[140:143]\n"
  "v_cvt_pk_bf16_f32 v233, v234, v235\n"
  "v_lshlrev_b32 v17, 16, v254\n"
  "v_mfma_f32_16x16x32_bf16 v[144:147], v[52:55], a[140:143], v[144:147]\n"
  "ds_read_b64_tr_b16 v[52:53], v22 offset:43008\n"
  "ds_read_b64_tr_b16 v[54:55], v22 offset:47104\n"
  "v_mfma_f32_16x16x32_bf16 v[148:151], v[56:59], a[128:131], v[148:151]\n"
  "v_and_b32 v18, 0xffff0000, v254\n"
  "v_lshlrev_b32 v19, 16, v255\n"
  "v_mfma_f32_16x16x32_bf16 v[152:155], v[56:59], a[132:135], v[152:155]\n"
  "v_and_b32 v24, 0xffff0000, v255\n"
  "v_add_f32 v236, v236, v17\n"
  "v_mfma_f32_16x16x32_bf16 v[156:159], v[56:59], a[136:139], v[156:159]\n"
  "s_add_u32 m0, s76, 69632\n"
  "v_add_f32 v237, v237, v18\n"
  "v_mfma_f32_16x16x32_bf16 v[160:163], v[56:59], a[140:143], v[160:163]\n"
  "ds_read_b64_tr_b16 v[56:57], v23 offset:43008\n"
  "ds_read_b64_tr_b16 v[58:59], v23 offset:47104\n"
  "s_add_u32 s64, %[oK], 16781312\n"
  "v_mfma_f32_16x16x32_bf16 v[164:167], v[60:63], a[128:131], v[164:167]\n"
  "buffer_load_dwordx4 %[L16], %[srdWS], s64 offen offset:0 sc1 lds\n"
  "v_add_f32 v238, v238, v19\n"
  "v_mfma_f32_16x16x32_bf16 v[168:171], v[60:63], a[132:135], v[168:171]\n"
  "v_add_f32 v239, v239, v24\n"
  "v_mul_f32 v30, v236, v236\n"
  "v_mfma_f32_16x16x32_bf16 v[172:175], v[60:63], a[136:139], v[172:175]\n"
  "v_mul_f32 v31, v237, v237\n"
  "v_mul_f32 v32, v238, v238\n"
  "v_mfma_f32_16x16x32_bf16 v[176:179], v[60:63], a[140:143], v[176:179]\n"
  "ds_read_b64_tr_b16 v[60:61], v22 offset:43520\n"
  "ds_read_b64_tr_b16 v[62:63], v22 offset:47616\n"
  "v_mfma_f32_16x16x32_bf16 v[180:183], v[64:67], a[128:131], v[180:183]\n"
  "v_mul_f32 v33, v239, v239\n"
  "v_fmaak_f32 v30, v16, v30, 0xc0135761\n"
  "v_mfma_f32_16x16x32_bf16 v[184:187], v[64:67], a[132:135], v[184:187]\n"
  "v_fmaak_f32 v31, v16, v31, 0xc0135761\n"
  "v_fmaak_f32 v32, v16, v32, 0xc0135761\n"
  "v_mfma_f32_16x16x32_bf16 v[188:191], v[64:67], a[136:139], v[188:191]\n"
  "v_fmaak_f32 v33, v16, v33, 0xc0135761\n"
  "v_mul_f32 v30, v30, v236\n"
  "v_mfma_f32_16x16x32_bf16 v[192:195], v[64:67], a[140:143], v[192:195]\n"
  "ds_read_b64_tr_b16 v[64:65], v23 offset:43520\n"
  "ds_read_b64_tr_b16 v[66:67], v23 offset:47616\n"
  "buffer_load_dwordx4 %[L16], %[srdWS], s64 offen offset:1024 sc1 lds\n"
  "s_waitcnt lgkmcnt(8)\n"
  "s_waitcnt vmcnt(35)\n"
  "v_mfma_f32_16x16x32_bf16 v[68:71], v[36:39], a[144:147], v[68:71]\n"
  "v_mul_f32 v31, v31, v237\n"
  "v_mul_f32 v32, v32, v238\n"
  "s_waitcnt vmcnt(34)\n"
  "v_mfma_f32_16x16x32_bf16 v[72:75], v[36:39], a[148:151], v[72:75]\n"
  "v_mul_f32 v33, v33, v239\n"
  "s_waitcnt vmcnt(33)\n"
  "v_mfma_f32_16x16x32_bf16 v[76:79], v[36:39], a[152:155], v[76:79]\n"
  "v_exp_f32 v30, v30\n"
  "s_waitcnt vmcnt(32)\n"
  "v_mfma_f32_16x16x32_bf16 v[80:83], v[36:39], a[156:159], v[80:83]\n"
  "ds_read_b64_tr_b16 v[36:37], v22 offset:49152\n"
  "ds_read_b64_tr_b16 v[38:39], v22 offset:53248\n"
  "v_mfma_f32_16x16x32_bf16 v[84:87], v[40:43], a[144:147], v[84:87]\n"
  "v_exp_f32 v31, v31\n"
  "v_mfma_f32_16x16x32_bf16 v[88:91], v[40:43], a[148:151], v[88:91]\n"
  "v_exp_f32 v32, v32\n"
  "v_mfma_f32_16x16x32_bf16 v[92:95], v[40:43], a[152:155], v[92:95]\n"
  "buffer_load_dwordx4 %[L16], %[srdWS], s64 offen offset:2048 sc1 lds\n"
  "v_mfma_f32_16x16x32_bf16 v[96:99], v[40:43], a[156:159], v[96:99]\n"
  "ds_read_b64_tr_b16 v[40:41], v23 offset:49152\n"
  "ds_read_b64_tr_b16 v[42:43], v23 offset:53248\n"
  "v_mfma_f32_16x16x32_bf16 v[100:103], v[44:47], a[144:147], v[100:103]\n"
  "v_exp_f32 v33, v33\n"
  "v_mfma_f32_16x16x32_bf16 v[104:107], v[44:47], a[148:151], v[104:107]\n"
  "v_add_f32 v30, 1.0, v30\n"
  "v_add_f32 v31, 1.0, v31\n"
  "v_mfma_f32_16x16x32_bf16 v[108:111], v[44:47], a[152:155], v[108:111]\n"
  "v_add_f32 v32, 1.0, v32\n"
  "v_add_f32 v33, 1.0, v33\n"
  "v_mfma_f32_16x16x32_bf16 v[112:115], v[44:47], a[156:159], v[112:115]\n"
  "ds_read_b64_tr_b16 v[44:45], v22 offset:49664\n"
  "ds_read_b64_tr_b16 v[46:47], v22 offset:53760\n"
  "v_mfma_f32_16x16x32_bf16 v[116:119], v[48:51], a[144:147], v[116:119]\n"
  "v_rcp_f32 v30, v30\n"
  "v_mfma_f32_16x16x32_bf16 v[120:123], v[48:51], a[148:151], v[120:123]\n"
  "buffer_load_dwordx4 %[L16], %[srdWS], s64 offen offset:3072 sc1 lds\n"
  "v_mfma_f32_16x16x32_bf16 v[124:127], v[48:51], a[152:155], v[124:127]\n"
  "v_rcp_f32 v31, v31\n"
  "v_mfma_f32_16x16x32_bf16 v[128:131], v[48:51], a[156:159], v[128:131]\n"
  "ds_read_b64_tr_b16 v[48:49], v23 offset:49664\n"
  "ds_read_b64_tr_b16 v[50:51], v23 offset:53760\n"
  "s_waitcnt lgkmcnt(8)\n"
  "v_mfma_f32_16x16x32_bf16 v[132:135], v[52:55], a[144:147], v[132:135]\n"
  "v_rcp_f32 v32, v32\n"
  "v_mfma_f32_16x16x32_bf16 v[136:139], v[52:55], a[148:151], v[136:139]\n"
  "v_rcp_f32 v33, v33\n"
  "v_mfma_f32_16x16x32_bf16 v[140:143], v[52:55], a[152:155], v[140:143]\n"
  "v_mul_f32 v236, v236, v30\n"
  "v_mul_f32 v237, v237, v31\n"
  "v_mfma_f32_16x16x32_bf16 v[144:147], v[52:55], a[156:159], v[144:147]\n"
  "ds_read_b64_tr_b16 v[52:53], v22 offset:51200\n"
  "ds_read_b64_tr_b16 v[54:55], v22 offset:55296\n"
  "v_mfma_f32_16x16x32_bf16 v[148:151], v[56:59], a[144:147], v[148:151]\n"
  "v_mul_f32 v238, v238, v32\n"
  "v_mul_f32 v239, v239, v33\n"
  "v_mfma_f32_16x16x32_bf16 v[152:155], v[56:59], a[148:151], v[152:155]\n"
  "v_cvt_pk_bf16_f32 v230, v236, v237\n"
  "v_cvt_pk_bf16_f32 v231, v238, v239\n"
  "v_mfma_f32_16x16x32_bf16 v[156:159], v[56:59], a[152:155], v[156:159]\n"
  "buffer_store_dwordx4 v[228:231], %[L16], %[srdWS], s77 offen offset:0 sc1\n"
  "v_add_f32 v240, v240, v17\n"
  "v_mfma_f32_16x16x32_bf16 v[160:163], v[56:59], a[156:159], v[160:163]\n"
  "ds_read_b64_tr_b16 v[56:57], v23 offset:51200\n"
  "ds_read_b64_tr_b16 v[58:59], v23 offset:55296\n"
  "v_mfma_f32_16x16x32_bf16 v[164:167], v[60:63], a[144:147], v[164:167]\n"
  "v_add_f32 v241, v241, v18\n"
  "v_add_f32 v242, v242, v19\n"
  "v_mfma_f32_16x16x32_bf16 v[168:171], v[60:63], a[148:151], v[168:171]\n"
  "v_add_f32 v243, v243, v24\n"
  "v_mul_f32 v30, v240, v240\n"
  "v_mfma_f32_16x16x32_bf16 v[172:175], v[60:63], a[152:155], v[172:175]\n"
  "v_mul_f32 v31, v241, v241\n"
  "v_mul_f32 v32, v242, v242\n"
  "v_mfma_f32_16x16x32_bf16 v[176:179], v[60:63], a[156:159], v[176:179]\n"
  "ds_read_b64_tr_b16 v[60:61], v22 offset:51712\n"
  "ds_read_b64_tr_b16 v[62:63], v22 offset:55808\n"
  "v_mfma_f32_16x16x32_bf16 v[180:183], v[64:67], a[144:147], v[180:183]\n"
  "v_mul_f32 v33, v243, v243\n"
  "v_fmaak_f32 v30, v16, v30, 0xc0135761\n"
  "v_mfma_f32_16x16x32_bf16 v[184:187], v[64:67], a[148:151], v[184:187]\n"
  "v_fmaak_f32 v31, v16, v31, 0xc0135761\n"
  "v_fmaak_f32 v32, v16, v32, 0xc0135761\n"
  "v_mfma_f32_16x16x32_bf16 v[188:191], v[64:67], a[152:155], v[188:191]\n"
  "v_fmaak_f32 v33, v16, v33, 0xc0135761\n"
  "v_mul_f32 v30, v30, v240\n"
  "v_mfma_f32_16x16x32_bf16 v[192:195], v[64:67], a[156:159], v[192:195]\n"
  "ds_read_b64_tr_b16 v[64:65], v23 offset:51712\n"
  "ds_read_b64_tr_b16 v[66:67], v23 offset:55808\n"
  "s_waitcnt lgkmcnt(8)\n"
  "s_waitcnt vmcnt(34)\n"
  "v_mfma_f32_16x16x32_bf16 v[68:71], v[36:39], a[160:163], v[68:71]\n"
  "v_mul_f32 v31, v31, v241\n"
  "v_mul_f32 v32, v32, v242\n"
  "s_waitcnt vmcnt(33)\n"
  "v_mfma_f32_16x16x32_bf16 v[72:75], v[36:39], a[164:167], v[72:75]\n"
  "v_mul_f32 v33, v33, v243\n"
  "s_waitcnt vmcnt(32)\n"
  "v_mfma_f32_16x16x32_bf16 v[76:79], v[36:39], a[168:171], v[76:79]\n"
  "v_exp_f32 v30, v30\n"
  "s_waitcnt vmcnt(31)\n"
  "v_mfma_f32_16x16x32_bf16 v[80:83], v[36:39], a[172:175], v[80:83]\n"
  "ds_read_b64_tr_b16 v[36:37], v22 offset:57344\n"
  "ds_read_b64_tr_b16 v[38:39], v22 offset:61440\n"
  "v_mfma_f32_16x16x32_bf16 v[84:87], v[40:43], a[160:163], v[84:87]\n"
  "v_exp_f32 v31, v31\n"
  "v_mfma_f32_16x16x32_bf16 v[88:91], v[40:43], a[164:167], v[88:91]\n"
  "v_exp_f32 v32, v32\n"
  "v_mfma_f32_16x16x32_bf16 v[92:95], v[40:43], a[168:171], v[92:95]\n"
  "v_exp_f32 v33, v33\n"
  "v_mfma_f32_16x16x32_bf16 v[96:99], v[40:43], a[172:175], v[96:99]\n"
  "ds_read_b64_tr_b16 v[40:41], v23 offset:57344\n"
  "ds_read_b64_tr_b16 v[42:43], v23 offset:61440\n"
  "v_mfma_f32_16x16x32_bf16 v[100:103], v[44:47], a[160:163], v[100:103]\n"
  "v_add_f32 v30, 1.0, v30\n"
  "v_add_f32 v31, 1.0, v31\n"
  "v_mfma_f32_16x16x32_bf16 v[104:107], v[44:47], a[164:167], v[104:107]\n"
  "v_add_f32 v32, 1.0, v32\n"
  "v_add_f32 v33, 1.0, v33\n"
  "v_mfma_f32_16x16x32_bf16 v[108:111], v[44:47], a[168:171], v[108:111]\n"
  "v_rcp_f32 v30, v30\n"
  "v_mfma_f32_16x16x32_bf16 v[112:115], v[44:47], a[172:175], v[112:115]\n"
  "ds_read_b64_tr_b16 v[44:45], v22 offset:57856\n"
  "ds_read_b64_tr_b16 v[46:47], v22 offset:61952\n"
  "v_mfma_f32_16x16x32_bf16 v[116:119], v[48:51], a[160:163], v[116:119]\n"
  "v_rcp_f32 v31, v31\n"
  "v_mfma_f32_16x16x32_bf16 v[120:123], v[48:51], a[164:167], v[120:123]\n"
  "v_rcp_f32 v32, v32\n"
  "v_mfma_f32_16x16x32_bf16 v[124:127], v[48:51], a[168:171], v[124:127]\n"
  "v_rcp_f32 v33, v33\n"
  "v_mfma_f32_16x16x32_bf16 v[128:131], v[48:51], a[172:175], v[128:131]\n"
  "ds_read_b64_tr_b16 v[48:49], v23 offset:57856\n"
  "ds_read_b64_tr_b16 v[50:51], v23 offset:61952\n"
  "s_waitcnt lgkmcnt(8)\n"
  "v_mfma_f32_16x16x32_bf16 v[132:135], v[52:55], a[160:163], v[132:135]\n"
  "v_mul_f32 v240, v240, v30\n"
  "v_mul_f32 v241, v241, v31\n"
  "v_mfma_f32_16x16x32_bf16 v[136:139], v[52:55], a[164:167], v[136:139]\n"
  "v_mul_f32 v242, v242, v32\n"
  "v_mul_f32 v243, v243, v33\n"
  "v_mfma_f32_16x16x32_bf16 v[140:143], v[52:55], a[168:171], v[140:143]\n"
  "v_cvt_pk_bf16_f32 v234, v240, v241\n"
  "v_cvt_pk_bf16_f32 v235, v242, v243\n"
  "v_mfma_f32_16x16x32_bf16 v[144:147], v[52:55], a[172:175], v[144:147]\n"
  "ds_read_b64_tr_b16 v[52:53], v22 offset:59392\n"
  "ds_read_b64_tr_b16 v[54:55], v22 offset:63488\n"
  "v_mfma_f32_16x16x32_bf16 v[148:151], v[56:59], a[160:163], v[148:151]\n"
  "buffer_store_dwordx4 v[232:235], %[L16], %[srdWS], s77 offen offset:1024 sc1\n"
  "v_mfma_f32_16x16x32_bf16 v[152:155], v[56:59], a[164:167], v[152:155]\n"
  "v_mfma_f32_16x16x32_bf16 v[156:159], v[56:59], a[168:171], v[156:159]\n"
  "v_mfma_f32_16x16x32_bf16 v[160:163], v[56:59], a[172:175], v[160:163]\n"
  "ds_read_b64_tr_b16 v[56:57], v23 offset:59392\n"
  "ds_read_b64_tr_b16 v[58:59], v23 offset:63488\n"
  "v_mfma_f32_16x16x32_bf16 v[164:167], v[60:63], a[160:163], v[164:167]\n"
  "v_mfma_f32_16x16x32_bf16 v[168:171], v[60:63], a[164:167], v[168:171]\n"
  "v_mfma_f32_16x16x32_bf16 v[172:175], v[60:63], a[168:171], v[172:175]\n"
  "v_mfma_f32_16x16x32_bf16 v[176:179], v[60:63], a[172:175], v[176:179]\n"
  "ds_read_b64_tr_b16 v[60:61], v22 offset:59904\n"
  "ds_read_b64_tr_b16 v[62:63], v22 offset:64000\n"
  "v_mfma_f32_16x16x32_bf16 v[180:183], v[64:67], a[160:163], v[180:183]\n"
  "v_mfma_f32_16x16x32_bf16 v[184:187], v[64:67], a[164:167], v[184:187]\n"
  "v_mfma_f32_16x16x32_bf16 v[188:191], v[64:67], a[168:171], v[188:191]\n"
  "v_mfma_f32_16x16x32_bf16 v[192:195], v[64:67], a[172:175], v[192:195]\n"
  "ds_read_b64_tr_b16 v[64:65], v23 offset:59904\n"
  "ds_read_b64_tr_b16 v[66:67], v23 offset:64000\n"
  "s_waitcnt lgkmcnt(8)\n"
  "s_waitcnt vmcnt(63) lgkmcnt(0)\n"
  "s_barrier\n"
  "s_waitcnt vmcnt(31)\n"
  "v_mfma_f32_16x16x32_bf16 v[68:71], v[36:39], a[176:179], v[68:71]\n"
  "s_waitcnt vmcnt(30)\n"
  "v_mfma_f32_16x16x32_bf16 v[72:75], v[36:39], a[180:183], v[72:75]\n"
  "s_waitcnt vmcnt(29)\n"
  "v_mfma_f32_16x16x32_bf16 v[76:79], v[36:39], a[184:187], v[76:79]\n"
  "s_waitcnt vmcnt(28)\n"
  "v_mfma_f32_16x16x32_bf16 v[80:83], v[36:39], a[188:191], v[80:83]\n"
  "v_mfma_f32_16x16x32_bf16 v[84:87], v[40:43], a[176:179], v[84:87]\n"
  "v_mfma_f32_16x16x32_bf16 v[88:91], v[40:43], a[180:183], v[88:91]\n"
  "v_mfma_f32_16x16x32_bf16 v[92:95], v[40:43], a[184:187], v[92:95]\n"
  "v_mfma_f32_16x16x32_bf16 v[96:99], v[40:43], a[188:191], v[96:99]\n"
  "v_mfma_f32_16x16x32_bf16 v[100:103], v[44:47], a[176:179], v[100:103]\n"
  "v_mfma_f32_16x16x32_bf16 v[104:107], v[44:47], a[180:183], v[104:107]\n"
  "v_mfma_f32_16x16x32_bf16 v[108:111], v[44:47], a[184:187], v[108:111]\n"
  "v_mfma_f32_16x16x32_bf16 v[112:115], v[44:47], a[188:191], v[112:115]\n"
  "v_mfma_f32_16x16x32_bf16 v[116:119], v[48:51], a[176:179], v[116:119]\n"
  "v_mfma_f32_16x16x32_bf16 v[120:123], v[48:51], a[180:183], v[120:123]\n"
  "v_mfma_f32_16x16x32_bf16 v[124:127], v[48:51], a[184:187], v[124:127]\n"
  "v_mfma_f32_16x16x32_bf16 v[128:131], v[48:51], a[188:191], v[128:131]\n"
  "v_mfma_f32_16x16x32_bf16 v[132:135], v[52:55], a[176:179], v[132:135]\n"
  "v_mfma_f32_16x16x32_bf16 v[136:139], v[52:55], a[180:183], v[136:139]\n"
  "v_mfma_f32_16x16x32_bf16 v[140:143], v[52:55], a[184:187], v[140:143]\n"
  "v_mfma_f32_16x16x32_bf16 v[144:147], v[52:55], a[188:191], v[144:147]\n"
  "v_mfma_f32_16x16x32_bf16 v[148:151], v[56:59], a[176:179], v[148:151]\n"
  "v_mfma_f32_16x16x32_bf16 v[152:155], v[56:59], a[180:183], v[152:155]\n"
  "v_mfma_f32_16x16x32_bf16 v[156:159], v[56:59], a[184:187], v[156:159]\n"
  "v_mfma_f32_16x16x32_bf16 v[160:163], v[56:59], a[188:191], v[160:163]\n"
  "v_mfma_f32_16x16x32_bf16 v[164:167], v[60:63], a[176:179], v[164:167]\n"
  "v_mfma_f32_16x16x32_bf16 v[168:171], v[60:63], a[180:183], v[168:171]\n"
  "v_mfma_f32_16x16x32_bf16 v[172:175], v[60:63], a[184:187], v[172:175]\n"
  "v_mfma_f32_16x16x32_bf16 v[176:179], v[60:63], a[188:191], v[176:179]\n"
  "v_mfma_f32_16x16x32_bf16 v[180:183], v[64:67], a[176:179], v[180:183]\n"
  "v_mfma_f32_16x16x32_bf16 v[184:187], v[64:67], a[180:183], v[184:187]\n"
  "v_mfma_f32_16x16x32_bf16 v[188:191], v[64:67], a[184:187], v[188:191]\n"
  "v_mfma_f32_16x16x32_bf16 v[192:195], v[64:67], a[188:191], v[192:195]\n"
  "s_mov_b32 s78, 0x3e0293ee\n"
  "s_mov_b32 s79, 0x3e0293ee\n"
  "s_lshl_b32 s64, %[wid], 14\n"
  "s_add_u32 s64, s64, 0x18000\n"
  "v_add_u32 v36, s64, %[L16]\n"
  "v_pk_mul_f32 v[68:69], v[68:69], s[78:79]\n"
  "v_pk_mul_f32 v[70:71], v[70:71], s[78:79]\n"
  "v_pk_mul_f32 v[84:85], v[84:85], s[78:79]\n"
  "v_pk_mul_f32 v[86:87], v[86:87], s[78:79]\n"
  "v_cvt_pk_bf16_f32 v68, v68, v69\n"
  "v_cvt_pk_bf16_f32 v69, v70, v71\n"
  "v_cvt_pk_bf16_f32 v70, v84, v85\n"
  "v_cvt_pk_bf16_f32 v71, v86, v87\n"
  "ds_write_b128 v36, v[68:71] offset:0\n"
  "v_pk_mul_f32 v[72:73], v[72:73], s[78:79]\n"
  "v_pk_mul_f32 v[74:75], v[74:75], s[78:79]\n"
  "v_pk_mul_f32 v[88:89], v[88:89], s[78:79]\n"
  "v_pk_mul_f32 v[90:91], v[90:91], s[78:79]\n"
  "v_cvt_pk_bf16_f32 v72, v72, v73\n"
  "v_cvt_pk_bf16_f32 v73, v74, v75\n"
  "v_cvt_pk_bf16_f32 v74, v88, v89\n"
  "v_cvt_pk_bf16_f32 v75, v90, v91\n"
  "ds_write_b128 v36, v[72:75] offset:4096\n"
  "v_pk_mul_f32 v[76:77], v[76:77], s[78:79]\n"
  "v_pk_mul_f32 v[78:79], v[78:79], s[78:79]\n"
  "v_pk_mul_f32 v[92:93], v[92:93], s[78:79]\n"
  "v_pk_mul_f32 v[94:95], v[94:95], s[78:79]\n"
  "v_cvt_pk_bf16_f32 v76, v76, v77\n"
  "v_cvt_pk_bf16_f32 v77, v78, v79\n"
  "v_cvt_pk_bf16_f32 v78, v92, v93\n"
  "v_cvt_pk_bf16_f32 v79, v94, v95\n"
  "ds_write_b128 v36, v[76:79] offset:8192\n"
  "v_pk_mul_f32 v[80:81], v[80:81], s[78:79]\n"
  "v_pk_mul_f32 v[82:83], v[82:83], s[78:79]\n"
  "v_pk_mul_f32 v[96:97], v[96:97], s[78:79]\n"
  "v_pk_mul_f32 v[98:99], v[98:99], s[78:79]\n"
  "v_cvt_pk_bf16_f32 v80, v80, v81\n"
  "v_cvt_pk_bf16_f32 v81, v82, v83\n"
  "v_cvt_pk_bf16_f32 v82, v96, v97\n"
  "v_cvt_pk_bf16_f32 v83, v98, v99\n"
  "ds_write_b128 v36, v[80:83] offset:12288\n"
  "v_pk_mul_f32 v[100:101], v[100:101], s[78:79]\n"
  "v_pk_mul_f32 v[102:103], v[102:103], s[78:79]\n"
  "v_pk_mul_f32 v[116:117], v[116:117], s[78:79]\n"
  "v_pk_mul_f32 v[118:119], v[118:119], s[78:79]\n"
  "v_cvt_pk_bf16_f32 v100, v100, v101\n"
  "v_cvt_pk_bf16_f32 v101, v102, v103\n"
  "v_cvt_pk_bf16_f32 v102, v116, v117\n"
  "v_cvt_pk_bf16_f32 v103, v118, v119\n"
  "ds_write_b128 v36, v[100:103] offset:1024\n"
  "v_pk_mul_f32 v[104:105], v[104:105], s[78:79]\n"
  "v_pk_mul_f32 v[106:107], v[106:107], s[78:79]\n"
  "v_pk_mul_f32 v[120:121], v[120:121], s[78:79]\n"
  "v_pk_mul_f32 v[122:123], v[122:123], s[78:79]\n"
  "v_cvt_pk_bf16_f32 v104, v104, v105\n"
  "v_cvt_pk_bf16_f32 v105, v106, v107\n"
  "v_cvt_pk_bf16_f32 v106, v120, v121\n"
  "v_cvt_pk_bf16_f32 v107, v122, v123\n"
  "ds_write_b128 v36, v[104:107] offset:5120\n"
  "v_pk_mul_f32 v[108:109], v[108:109], s[78:79]\n"
  "v_pk_mul_f32 v[110:111], v[110:111], s[78:79]\n"
  "v_pk_mul_f32 v[124:125], v[124:125], s[78:79]\n"
  "v_pk_mul_f32 v[126:127], v[126:127], s[78:79]\n"
  "v_cvt_pk_bf16_f32 v108, v108, v109\n"
  "v_cvt_pk_bf16_f32 v109, v110, v111\n"
  "v_cvt_pk_bf16_f32 v110, v124, v125\n"
  "v_cvt_pk_bf16_f32 v111, v126, v127\n"
  "ds_write_b128 v36, v[108:111] offset:9216\n"
  "v_pk_mul_f32 v[112:113], v[112:113], s[78:79]\n"
  "v_pk_mul_f32 v[114:115], v[114:115], s[78:79]\n"
  "v_pk_mul_f32 v[128:129], v[128:129], s[78:79]\n"
  "v_pk_mul_f32 v[130:131], v[130:131], s[78:79]\n"
  "v_cvt_pk_bf16_f32 v112, v112, v113\n"
  "v_cvt_pk_bf16_f32 v113, v114, v115\n"
  "v_cvt_pk_bf16_f32 v114, v128, v129\n"
  "v_cvt_pk_bf16_f32 v115, v130, v131\n"
  "ds_write_b128 v36, v[112:115] offset:13312\n"
  "v_pk_mul_f32 v[132:133], v[132:133], s[78:79]\n"
  "v_pk_mul_f32 v[134:135], v[134:135], s[78:79]\n"
  "v_pk_mul_f32 v[148:149], v[148:149], s[78:79]\n"
  "v_pk_mul_f32 v[150:151], v[150:151], s[78:79]\n"
  "v_cvt_pk_bf16_f32 v132, v132, v133\n"
  "v_cvt_pk_bf16_f32 v133, v134, v135\n"
  "v_cvt_pk_bf16_f32 v134, v148, v149\n"
  "v_cvt_pk_bf16_f32 v135, v150, v151\n"
  "ds_write_b128 v36, v[132:135] offset:2048\n"
  "v_pk_mul_f32 v[136:137], v[136:137], s[78:79]\n"
  "v_pk_mul_f32 v[138:139], v[138:139], s[78:79]\n"
  "v_pk_mul_f32 v[152:153], v[152:153], s[78:79]\n"
  "v_pk_mul_f32 v[154:155], v[154:155], s[78:79]\n"
  "v_cvt_pk_bf16_f32 v136, v136, v137\n"
  "v_cvt_pk_bf16_f32 v137, v138, v139\n"
  "v_cvt_pk_bf16_f32 v138, v152, v153\n"
  "v_cvt_pk_bf16_f32 v139, v154, v155\n"
  "ds_write_b128 v36, v[136:139] offset:6144\n"
  "v_pk_mul_f32 v[140:141], v[140:141], s[78:79]\n"
  "v_pk_mul_f32 v[142:143], v[142:143], s[78:79]\n"
  "v_pk_mul_f32 v[156:157], v[156:157], s[78:79]\n"
  "v_pk_mul_f32 v[158:159], v[158:159], s[78:79]\n"
  "v_cvt_pk_bf16_f32 v140, v140, v141\n"
  "v_cvt_pk_bf16_f32 v141, v142, v143\n"
  "v_cvt_pk_bf16_f32 v142, v156, v157\n"
  "v_cvt_pk_bf16_f32 v143, v158, v159\n"
  "ds_write_b128 v36, v[140:143] offset:10240\n"
  "v_pk_mul_f32 v[144:145], v[144:145], s[78:79]\n"
  "v_pk_mul_f32 v[146:147], v[146:147], s[78:79]\n"
  "v_pk_mul_f32 v[160:161], v[160:161], s[78:79]\n"
  "v_pk_mul_f32 v[162:163], v[162:163], s[78:79]\n"
  "v_cvt_pk_bf16_f32 v144, v144, v145\n"
  "v_cvt_pk_bf16_f32 v145, v146, v147\n"
  "v_cvt_pk_bf16_f32 v146, v160, v161\n"
  "v_cvt_pk_bf16_f32 v147, v162, v163\n"
  "ds_write_b128 v36, v[144:147] offset:14336\n"
  "v_pk_mul_f32 v[164:165], v[164:165], s[78:79]\n"
  "v_pk_mul_f32 v[166:167], v[166:167], s[78:79]\n"
  "v_pk_mul_f32 v[180:181], v[180:181], s[78:79]\n"
  "v_pk_mul_f32 v[182:183], v[182:183], s[78:79]\n"
  "v_cvt_pk_bf16_f32 v164, v164, v165\n"
  "v_cvt_pk_bf16_f32 v165, v166, v167\n"
  "v_cvt_pk_bf16_f32 v166, v180, v181\n"
  "v_cvt_pk_bf16_f32 v167, v182, v183\n"
  "ds_write_b128 v36, v[164:167] offset:3072\n"
  "v_pk_mul_f32 v[168:169], v[168:169], s[78:79]\n"
  "v_pk_mul_f32 v[170:171], v[170:171], s[78:79]\n"
  "v_pk_mul_f32 v[184:185], v[184:185], s[78:79]\n"
  "v_pk_mul_f32 v[186:187], v[186:187], s[78:79]\n"
  "v_cvt_pk_bf16_f32 v168, v168, v169\n"
  "v_cvt_pk_bf16_f32 v169, v170, v171\n"
  "v_cvt_pk_bf16_f32 v170, v184, v185\n"
  "v_cvt_pk_bf16_f32 v171, v186, v187\n"
  "ds_write_b128 v36, v[168:171] offset:7168\n"
  "v_pk_mul_f32 v[172:173], v[172:173], s[78:79]\n"
  "v_pk_mul_f32 v[174:175], v[174:175], s[78:79]\n"
  "v_pk_mul_f32 v[188:189], v[188:189], s[78:79]\n"
  "v_pk_mul_f32 v[190:191], v[190:191], s[78:79]\n"
  "v_cvt_pk_bf16_f32 v172, v172, v173\n"
  "v_cvt_pk_bf16_f32 v173, v174, v175\n"
  "v_cvt_pk_bf16_f32 v174, v188, v189\n"
  "v_cvt_pk_bf16_f32 v175, v190, v191\n"
  "ds_write_b128 v36, v[172:175] offset:11264\n"
  "v_pk_mul_f32 v[176:177], v[176:177], s[78:79]\n"
  "v_pk_mul_f32 v[178:179], v[178:179], s[78:79]\n"
  "v_pk_mul_f32 v[192:193], v[192:193], s[78:79]\n"
  "v_pk_mul_f32 v[194:195], v[194:195], s[78:79]\n"
  "v_cvt_pk_bf16_f32 v176, v176, v177\n"
  "v_cvt_pk_bf16_f32 v177, v178, v179\n"
  "v_cvt_pk_bf16_f32 v178, v192, v193\n"
  "v_cvt_pk_bf16_f32 v179, v194, v195\n"
  "ds_write_b128 v36, v[176:179] offset:15360\n"
  "s_waitcnt lgkmcnt(0)\n"
  "s_barrier\n"
  "s_branch FREND_%=\n"
  "SPINLNK_%=:\n"
  "s_mov_b32 s71, 0x40000\n"
  "SPINLLNK_%=:\n"
  "s_sleep 1\n"
  "buffer_load_dwordx2 v[28:29], %[pollLN], %[srdWS], 0 offen sc1\n"
  "s_waitcnt vmcnt(0)\n"
  "v_cmp_eq_u64 vcc, %[tok], v[28:29]\n"
  "s_nop 1\n"
  "s_cmp_eq_u64 vcc, -1\n"
  "s_cbranch_scc1 BACKLNK_%=\n"
  "s_sub_u32 s71, s71, 1\n"
  "s_cmp_lg_u32 s71, 0\n"
  "s_cbranch_scc1 SPINLLNK_%=\n"
  "s_branch BACKLNK_%=\n"
  "SPINLNV_%=:\n"
  "s_mov_b32 s71, 0x40000\n"
  "SPINLLNV_%=:\n"
  "s_sleep 1\n"
  "buffer_load_dwordx2 v[28:29], %[pollLN], %[srdWS], 0 offen sc1\n"
  "s_waitcnt vmcnt(0)\n"
  "v_cmp_eq_u64 vcc, %[tok], v[28:29]\n"
  "s_nop 1\n"
  "s_cmp_eq_u64 vcc, -1\n"
  "s_cbranch_scc1 BACKLNV_%=\n"
  "s_sub_u32 s71, s71, 1\n"
  "s_cmp_lg_u32 s71, 0\n"
  "s_cbranch_scc1 SPINLLNV_%=\n"
  "s_branch BACKLNV_%=\n"
  "SPINKV_%=:\n"
  "s_mov_b32 s71, 0x40000\n"
  "SPINLKV_%=:\n"
  "s_sleep 1\n"
  "buffer_load_dwordx2 v[28:29], %[pollKV], %[srdWS], 0 offen sc1\n"
  "s_waitcnt vmcnt(0)\n"
  "v_cmp_eq_u64 vcc, %[tok], v[28:29]\n"
  "s_nop 1\n"
  "s_cmp_eq_u64 vcc, -1\n"
  "s_cbranch_scc1 BACKKV_%=\n"
  "s_sub_u32 s71, s71, 1\n"
  "s_cmp_lg_u32 s71, 0\n"
  "s_cbranch_scc1 SPINLKV_%=\n"
  "s_branch BACKKV_%=\n"
  "FREND_%=:\n"
    :  : [srdWS] "s"(srdWS), [srdC] "s"(srdC), [srdW] "s"(srdW), [srdW1] "s"(srdW1), [srdB1] "s"(srdB1), [srdWq] "s"(srdWq),
       [wid] "s"(rfl(wid)), [oKVo] "s"(rfl(oKVo)), [oY] "s"(rfl(oY)), [oX] "s"(rfl(oX)), [oH] "s"(rfl(oH)), [nH] "s"(rfl(nH)), [oK] "s"(rfl(oK)), [oFl] "s"(rfl(oFl)), [tok] "s"(tok),
       [L16] "v"(l * 16), [TRB] "v"(TRB), [voffC] "v"(voffC), [voffW] "v"(voffW), [bC] "v"(bC), [voffW1] "v"(voffW1), [voffWq] "v"(voffWq),
       [pollLN] "v"(pollLN), [pollKV] "v"(pollKV), [tWq] "s"(rfl(tWq)), [rot] "s"(rfl(rot)), [rotU] "s"(rfl(rotU)), [srdX] "s"(srdX), [srdCt] "s"(srdCt), [srdW1t] "s"(srdW1t), [srdW2t] "s"(srdW2t), [voffX] "v"(voffX), [oLN] "s"(rfl(oLN))
    : "v16", "v17", "v18", "v19", "v20", "v21", "v22", "v23", "v24", "v25", "v26", "v27", "v28", "v29", "v30", "v31", "v32", "v33", "v34", "v35", "v36", "v37", "v38", "v39", "v40", "v41", "v42", "v43", "v44", "v45", "v46", "v47", "v48", "v49", "v50", "v51", "v52", "v53", "v54", "v55", "v56", "v57", "v58", "v59", "v60", "v61", "v62", "v63", "v64", "v65", "v66", "v67", "v68", "v69", "v70", "v71", "v72", "v73", "v74", "v75", "v76", "v77", "v78", "v79", "v80", "v81", "v82", "v83", "v84", "v85", "v86", "v87", "v88", "v89", "v90", "v91", "v92", "v93", "v94", "v95", "v96", "v97", "v98", "v99", "v100", "v101", "v102", "v103", "v104", "v105", "v106", "v107", "v108", "v109", "v110", "v111", "v112", "v113", "v114", "v115", "v116", "v117", "v118", "v119", "v120", "v121", "v122", "v123", "v124", "v125", "v126", "v127", "v128", "v129", "v130", "v131", "v132", "v133", "v134", "v135", "v136", "v137", "v138", "v139", "v140", "v141", "v142", "v143", "v144", "v145", "v146", "v147", "v148", "v149", "v150", "v151", "v152", "v153", "v154", "v155", "v156", "v157", "v158", "v159", "v160", "v161", "v162", "v163", "v164", "v165", "v166", "v167", "v168", "v169", "v170", "v171", "v172", "v173", "v174", "v175", "v176", "v177", "v178", "v179", "v180", "v181", "v182", "v183", "v184", "v185", "v186", "v187", "v188", "v189", "v190", "v191", "v192", "v193", "v194", "v195", "v196", "v197", "v198", "v199", "v200", "v201", "v202", "v203", "v204", "v205", "v206", "v207", "v208", "v209", "v210", "v211", "v212", "v213", "v214", "v215", "v216", "v217", "v218", "v219", "v220", "v221", "v222", "v223", "v224", "v225", "v226", "v227", "v228", "v229", "v230", "v231", "v232", "v233", "v234", "v235", "v236", "v237", "v238", "v239", "v240", "v241", "v242", "v243", "v244", "v245", "v246", "v247", "v248", "v249", "v250", "v251", "v252", "v253", "v254", "v255", "a0", "a1", "a2", "a3", "a4", "a5", "a6", "a7", "a8", "a9", "a10", "a11", "a12", "a13", "a14", "a15", "a16", "a17", "a18", "a19", "a20", "a21", "a22", "a23", "a24", "a25", "a26", "a27", "a28", "a29", "a30", "a31", "a32", "a33", "a34", "a35", "a36", "a37", "a38", "a39", "a40", "a41", "a42", "a43", "a44", "a45", "a46", "a47", "a48", "a49", "a50", "a51", "a52", "a53", "a54", "a55", "a56", "a57", "a58", "a59", "a60", "a61", "a62", "a63", "a64", "a65", "a66", "a67", "a68", "a69", "a70", "a71", "a72", "a73", "a74", "a75", "a76", "a77", "a78", "a79", "a80", "a81", "a82", "a83", "a84", "a85", "a86", "a87", "a88", "a89", "a90", "a91", "a92", "a93", "a94", "a95", "a96", "a97", "a98", "a99", "a100", "a101", "a102", "a103", "a104", "a105", "a106", "a107", "a108", "a109", "a110", "a111", "a112", "a113", "a114", "a115", "a116", "a117", "a118", "a119", "a120", "a121", "a122", "a123", "a124", "a125", "a126", "a127", "a128", "a129", "a130", "a131", "a132", "a133", "a134", "a135", "a136", "a137", "a138", "a139", "a140", "a141", "a142", "a143", "a144", "a145", "a146", "a147", "a148", "a149", "a150", "a151", "a152", "a153", "a154", "a155", "a156", "a157", "a158", "a159", "a160", "a161", "a162", "a163", "a164", "a165", "a166", "a167", "a168", "a169", "a170", "a171", "a172", "a173", "a174", "a175", "a176", "a177", "a178", "a179", "a180", "a181", "a182", "a183", "a184", "a185", "a186", "a187", "a188", "a189", "a190", "a191", "a192", "a193", "a194", "a195", "a196", "a197", "a198", "a199", "a200", "a201", "a202", "a203", "a204", "a205", "a206", "a207", "a208", "a209", "a210", "a211", "a212", "a213", "a214", "a215", "a216", "a217", "a218", "a219", "a220", "a221", "a222", "a223", "a224", "a225", "a226", "a227", "a228", "a229", "a230", "a231", "a232", "a233", "a234", "a235", "a236", "a237", "a238", "a239", "a240", "a241", "a242", "a243", "a244", "a245", "a246", "a247", "a248", "a249", "a250", "a251", "a252", "a253", "a254", "a255", "s64", "s65", "s66", "s67", "s68", "s69", "s70", "s71", "s72", "s73", "s74", "s75", "s76", "s77", "s78", "s79", "s80", "s81", "s82", "s83", "s84", "s85", "s86", "s87", "s88", "s89", "s90", "s91", "s92", "s93", "s94", "s95", "s96", "s97", "s98", "s99", "vcc", "scc", "memory");
  }
  STAMP(2);
  unsigned bad;
  const unsigned rbF = (xcd & 1) * 8 + (idx >> 2), nt = idx & 3, sp = xcd >> 1, nv = sp == 3 ? 10u : 11u;
  const __amdgpu_buffer_rsrc_t srdAH = mkrsrc(p.ws + WS_A + ((size_t)rbF * 171 + 128 + 11 * sp) * 8192, nv * 8192);
  const __amdgpu_buffer_rsrc_t srdWH = mkrsrc(p.w2 + (size_t)(11 * sp * 32) * 512 + nt * 128, (nv * 32 - 1) * 1024 + 256);
  {
    const unsigned l = lane_id(), rh = wid & 1, kp = wid >> 1;
    const int z = 4 * (int)(l >> 4) - (int)(l & 15) + 64 * ((int)kp - (int)rh);
    const unsigned oc = (unsigned)WS_A + (head * 4 + kp * 2) * 8192 + rh * 4096;
    const unsigned oCA = oc + blkA * (171 * 8192), oCB = oc + blkB * (171 * 8192), nA = blkA + 1;
    const unsigned e = l >> 5, g1 = (l >> 4) & 1, q = (l >> 2) & 3;
    const unsigned voffW2 = (16 * (wid >> 1) + 4 * g1 + q) * 1024 + (wid & 1) * 128 + (2 * e + (((l >> 1) & 1) ^ g1)) * 32 + (l & 1) * 16;
    const __amdgpu_buffer_rsrc_t srdWot = mkrsrc((const char*)p.wo + ((size_t)sp << 20) + (idx * 4 + wid) * 8192, 8192);
    const unsigned fbp = min((11 * sp) / 3 + (l >> 2), (11 * sp + nv - 1) / 3);
    const unsigned pollH = FLAG0 + 2 * 16384 + ((rbF >> 1) + 8 * ((rbF & 1) * 15 + fbp)) * 64 + (l & 3) * 8;
    asm volatile(
  "s_nop 4\n"
  "s_lshr_b32 s76, %[wid], 1\n"
  "s_and_b32 s77, %[wid], 1\n"
  "s_lshl_b32 s77, s77, 14\n"
  "s_add_u32 s77, s77, 0x18000\n"
  "v_add_u32 v20, s77, %[L16]\n"
  "ds_read_b128 a[128:131], v20 offset:0\n"
  "ds_read_b128 a[132:135], v20 offset:1024\n"
  "ds_read_b128 a[136:139], v20 offset:2048\n"
  "ds_read_b128 a[140:143], v20 offset:3072\n"
  "ds_read_b128 a[144:147], v20 offset:4096\n"
  "ds_read_b128 a[148:151], v20 offset:5120\n"
  "ds_read_b128 a[152:155], v20 offset:6144\n"
  "ds_read_b128 a[156:159], v20 offset:7168\n"
  "ds_read_b128 a[160:163], v20 offset:8192\n"
  "ds_read_b128 a[164:167], v20 offset:9216\n"
  "ds_read_b128 a[168:171], v20 offset:10240\n"
  "ds_read_b128 a[172:175], v20 offset:11264\n"
  "ds_read_b128 a[176:179], v20 offset:12288\n"
  "ds_read_b128 a[180:183], v20 offset:13312\n"
  "ds_read_b128 a[184:187], v20 offset:14336\n"
  "ds_read_b128 a[188:191], v20 offset:15360\n"
  "v_add_u32 v20, 0x8000, v20\n"
  "v_add_u32 v16, 0x1000, %[L16]\n"
  "s_lshl_b32 s77, s76, 14\n"
  "v_add_u32 v17, s77, %[L16]\n"
  "s_lshl_b32 s76, s76, 12\n"
  "s_add_u32 s76, s76, 0x10000\n"
  "v_add_u32 v19, s76, v17\n"
  "v_xor_b32 v18, 0x1000, v19\n"
  "s_add_u32 s64, %[oK], 0x8000\n"
  "s_add_u32 s65, %[oK], 0x1000000\n"
  "s_lshl_b32 s76, %[nA], 15\n"
  "s_add_u32 s69, %[oK], s76\n"
  "s_add_u32 s70, s65, s76\n"
  "s_mov_b32 s71, %[oK]\n"
  "s_mov_b32 s72, s65\n"
  "s_lshl_b32 s76, %[wid], 13\n"
  "s_mov_b32 s66, s76\n"
  "s_add_u32 s67, s76, 0x18000\n"
  "s_mov_b32 s68, %[nA]\n"
  "s_mov_b32 s73, 0\n"
  "s_mov_b32 %[bad], 0\n"
  "s_mov_b32 s74, %[oCA]\n"
  "s_add_u32 s78, %[oFl], 0x8000\n"
  "s_mov_b64 s[80:81], %[tok]\n"
  "v_mov_b32 v188, s80\n"
  "v_mov_b32 v189, s81\n"
  "v_mov_b32 v32, 0x7f800000\n"
  "v_mov_b32 v33, 0xff800000\n"
  "v_cmp_lt_i32 vcc, -48, %[z]\n"
  "s_nop 1\n"
  "v_cndmask_b32 v160, v32, v33, vcc\n"
  "v_cmp_lt_i32 vcc, -49, %[z]\n"
  "s_nop 1\n"
  "v_cndmask_b32 v161, v32, v33, vcc\n"
  "v_cmp_lt_i32 vcc, -50, %[z]\n"
  "s_nop 1\n"
  "v_cndmask_b32 v162, v32, v33, vcc\n"
  "v_cmp_lt_i32 vcc, -51, %[z]\n"
  "s_nop 1\n"
  "v_cndmask_b32 v163, v32, v33, vcc\n"
  "v_cmp_lt_i32 vcc, -32, %[z]\n"
  "s_nop 1\n"
  "v_cndmask_b32 v164, v32, v33, vcc\n"
  "v_cmp_lt_i32 vcc, -33, %[z]\n"
  "s_nop 1\n"
  "v_cndmask_b32 v165, v32, v33, vcc\n"
  "v_cmp_lt_i32 vcc, -34, %[z]\n"
  "s_nop 1\n"
  "v_cndmask_b32 v166, v32, v33, vcc\n"
  "v_cmp_lt_i32 vcc, -35, %[z]\n"
  "s_nop 1\n"
  "v_cndmask_b32 v167, v32, v33, vcc\n"
  "v_cmp_lt_i32 vcc, -16, %[z]\n"
  "s_nop 1\n"
  "v_cndmask_b32 v168, v32, v33, vcc\n"
  "v_cmp_lt_i32 vcc, -17, %[z]\n"
  "s_nop 1\n"
  "v_cndmask_b32 v169, v32, v33, vcc\n"
  "v_cmp_lt_i32 vcc, -18, %[z]\n"
  "s_nop 1\n"
  "v_cndmask_b32 v170, v32, v33, vcc\n"
  "v_cmp_lt_i32 vcc, -19, %[z]\n"
  "s_nop 1\n"
  "v_cndmask_b32 v171, v32, v33, vcc\n"
  "v_cmp_lt_i32 vcc, 0, %[z]\n"
  "s_nop 1\n"
  "v_cndmask_b32 v172, v32, v33, vcc\n"
  "v_cmp_lt_i32 vcc, -1, %[z]\n"
  "s_nop 1\n"
  "v_cndmask_b32 v173, v32, v33, vcc\n"
  "v_cmp_lt_i32 vcc, -2, %[z]\n"
  "s_nop 1\n"
  "v_cndmask_b32 v174, v32, v33, vcc\n"
  "v_cmp_lt_i32 vcc, -3, %[z]\n"
  "s_nop 1\n"
  "v_cndmask_b32 v175, v32, v33, vcc\n"
  "v_cmp_lt_i32 vcc, 16, %[z]\n"
  "s_nop 1\n"
  "v_cndmask_b32 v176, v32, v33, vcc\n"
  "v_cmp_lt_i32 vcc, 15, %[z]\n"
  "s_nop 1\n"
  "v_cndmask_b32 v177, v32, v33, vcc\n"
  "v_cmp_lt_i32 vcc, 14, %[z]\n"
  "s_nop 1\n"
  "v_cndmask_b32 v178, v32, v33, vcc\n"
  "v_cmp_lt_i32 vcc, 13, %[z]\n"
  "s_nop 1\n"
  "v_cndmask_b32 v179, v32, v33, vcc\n"
  "v_cmp_lt_i32 vcc, 32, %[z]\n"
  "s_nop 1\n"
  "v_cndmask_b32 v180, v32, v33, vcc\n"
  "v_cmp_lt_i32 vcc, 31, %[z]\n"
  "s_nop 1\n"
  "v_cndmask_b32 v181, v32, v33, vcc\n"
  "v_cmp_lt_i32 vcc, 30, %[z]\n"
  "s_nop 1\n"
  "v_cndmask_b32 v182, v32, v33, vcc\n"
  "v_cmp_lt_i32 vcc, 29, %[z]\n"
  "s_nop 1\n"
  "v_cndmask_b32 v183, v32, v33, vcc\n"
  "v_cmp_lt_i32 vcc, 48, %[z]\n"
  "s_nop 1\n"
  "v_cndmask_b32 v184, v32, v33, vcc\n"
  "v_cmp_lt_i32 vcc, 47, %[z]\n"
  "s_nop 1\n"
  "v_cndmask_b32 v185, v32, v33, vcc\n"
  "v_cmp_lt_i32 vcc, 46, %[z]\n"
  "s_nop 1\n"
  "v_cndmask_b32 v186, v32, v33, vcc\n"
  "v_cmp_lt_i32 vcc, 45, %[z]\n"
  "s_nop 1\n"
  "v_cndmask_b32 v187, v32, v33, vcc\n"
  "v_mov_b32 v23, 0xf149f2ca\n"
  "s_mov_b32 s79, 0\n"
  "s_waitcnt vmcnt(16) lgkmcnt(0)\n"
  "s_barrier\n"
  "ds_read_b128 a[192:195], v17 offset:0\n"
  "ds_read_b128 a[196:199], v17 offset:1024\n"
  "ds_read_b128 a[200:203], v17 offset:2048\n"
  "ds_read_b128 a[204:207], v17 offset:3072\n"
  "ds_read_b128 a[208:211], v17 offset:4096\n"
  "ds_read_b128 a[212:215], v17 offset:5120\n"
  "ds_read_b128 a[216:219], v17 offset:6144\n"
  "ds_read_b128 a[220:223], v17 offset:7168\n"
  "s_waitcnt lgkmcnt(7)\n"
  "v_mfma_f32_16x16x32_bf16 v[64:67], a[192:195], a[128:131], 0\n"
  "v_mfma_f32_16x16x32_bf16 v[68:71], a[192:195], a[144:147], 0\n"
  "v_mfma_f32_16x16x32_bf16 v[72:75], a[192:195], a[160:163], 0\n"
  "v_mfma_f32_16x16x32_bf16 v[76:79], a[192:195], a[176:179], 0\n"
  "s_waitcnt lgkmcnt(6)\n"
  "v_mfma_f32_16x16x32_bf16 v[64:67], a[196:199], a[132:135], v[64:67]\n"
  "v_mfma_f32_16x16x32_bf16 v[68:71], a[196:199], a[148:151], v[68:71]\n"
  "v_mfma_f32_16x16x32_bf16 v[72:75], a[196:199], a[164:167], v[72:75]\n"
  "v_mfma_f32_16x16x32_bf16 v[76:79], a[196:199], a[180:183], v[76:79]\n"
  "s_waitcnt lgkmcnt(5)\n"
  "v_mfma_f32_16x16x32_bf16 v[64:67], a[200:203], a[136:139], v[64:67]\n"
  "v_mfma_f32_16x16x32_bf16 v[68:71], a[200:203], a[152:155], v[68:71]\n"
  "v_mfma_f32_16x16x32_bf16 v[72:75], a[200:203], a[168:171], v[72:75]\n"
  "v_mfma_f32_16x16x32_bf16 v[76:79], a[200:203], a[184:187], v[76:79]\n"
  "s_waitcnt lgkmcnt(4)\n"
  "v_mfma_f32_16x16x32_bf16 v[64:67], a[204:207], a[140:143], v[64:67]\n"
  "v_mfma_f32_16x16x32_bf16 v[68:71], a[204:207], a[156:159], v[68:71]\n"
  "v_mfma_f32_16x16x32_bf16 v[72:75], a[204:207], a[172:175], v[72:75]\n"
  "v_mfma_f32_16x16x32_bf16 v[76:79], a[204:207], a[188:191], v[76:79]\n"
  "s_waitcnt lgkmcnt(3)\n"
  "v_mfma_f32_16x16x32_bf16 v[80:83], a[208:211], a[128:131], 0\n"
  "v_mfma_f32_16x16x32_bf16 v[84:87], a[208:211], a[144:147], 0\n"
  "v_mfma_f32_16x16x32_bf16 v[88:91], a[208:211], a[160:163], 0\n"
  "v_mfma_f32_16x16x32_bf16 v[92:95], a[208:211], a[176:179], 0\n"
  "s_waitcnt lgkmcnt(2)\n"
  "v_mfma_f32_16x16x32_bf16 v[80:83], a[212:215], a[132:135], v[80:83]\n"
  "v_mfma_f32_16x16x32_bf16 v[84:87], a[212:215], a[148:151], v[84:87]\n"
  "v_mfma_f32_16x16x32_bf16 v[88:91], a[212:215], a[164:167], v[88:91]\n"
  "v_mfma_f32_16x16x32_bf16 v[92:95], a[212:215], a[180:183], v[92:95]\n"
  "s_waitcnt lgkmcnt(1)\n"
  "v_mfma_f32_16x16x32_bf16 v[80:83], a[216:219], a[136:139], v[80:83]\n"
  "v_mfma_f32_16x16x32_bf16 v[84:87], a[216:219], a[152:155], v[84:87]\n"
  "v_mfma_f32_16x16x32_bf16 v[88:91], a[216:219], a[168:171], v[88:91]\n"
  "v_mfma_f32_16x16x32_bf16 v[92:95], a[216:219], a[184:187], v[92:95]\n"
  "s_waitcnt lgkmcnt(0)\n"
  "v_mfma_f32_16x16x32_bf16 v[80:83], a[220:223], a[140:143], v[80:83]\n"
  "v_mfma_f32_16x16x32_bf16 v[84:87], a[220:223], a[156:159], v[84:87]\n"
  "v_mfma_f32_16x16x32_bf16 v[88:91], a[220:223], a[172:175], v[88:91]\n"
  "v_mfma_f32_16x16x32_bf16 v[92:95], a[220:223], a[188:191], v[92:95]\n"
  "ATfirst_%=:\n"
  "ds_read_b128 a[192:195], v17 offset:8192\n"
  "ds_read_b128 a[196:199], v17 offset:9216\n"
  "ds_read_b128 a[200:203], v17 offset:10240\n"
  "ds_read_b128 a[204:207], v17 offset:11264\n"
  "ds_read_b128 a[208:211], v17 offset:12288\n"
  "ds_read_b128 a[212:215], v17 offset:13312\n"
  "ds_read_b128 a[216:219], v17 offset:14336\n"
  "ds_read_b128 a[220:223], v17 offset:15360\n"
  "v_xor_b32 v17, 0x8000, v17\n"
  "v_mov_b32 v208, 0x3f803f80\n"
  "v_mov_b32 v209, 0x3f803f80\n"
  "v_mov_b32 v210, 0x3f803f80\n"
  "v_mov_b32 v211, 0x3f803f80\n"
  "v_and_b32 v214, 0x70, %[L16]\n"
  "v_lshlrev_b32 v214, 3, v214\n"
  "v_max3_f32 v28, v64, v65, v66\n"
  "v_max3_f32 v32, v67, v80, v81\n"
  "v_max3_f32 v28, v28, v82, v83\n"
  "v_max3_f32 v29, v68, v69, v70\n"
  "v_max3_f32 v33, v71, v84, v85\n"
  "v_max3_f32 v29, v29, v86, v87\n"
  "v_max3_f32 v30, v72, v73, v74\n"
  "v_max3_f32 v34, v75, v88, v89\n"
  "v_max3_f32 v30, v30, v90, v91\n"
  "v_max3_f32 v31, v76, v77, v78\n"
  "v_max3_f32 v35, v79, v92, v93\n"
  "v_max3_f32 v31, v31, v94, v95\n"
  "v_max3_f32 v28, v28, v32, v23\n"
  "v_max3_f32 v29, v29, v33, v23\n"
  "v_max3_f32 v30, v30, v34, v23\n"
  "v_max3_f32 v31, v31, v35, v23\n"
  "v_mov_b32 v32, v28\n"
  "v_mov_b32 v33, v29\n"
  "v_mov_b32 v34, v30\n"
  "v_mov_b32 v35, v31\n"
  "v_permlane16_swap_b32 v28, v32\n"
  "v_permlane16_swap_b32 v29, v33\n"
  "v_permlane16_swap_b32 v30, v34\n"
  "v_permlane16_swap_b32 v31, v35\n"
  "v_max_f32 v28, v28, v32\n"
  "v_max_f32 v29, v29, v33\n"
  "v_max_f32 v30, v30, v34\n"
  "v_max_f32 v31, v31, v35\n"
  "v_mov_b32 v32, v28\n"
  "v_mov_b32 v33, v29\n"
  "v_mov_b32 v34, v30\n"
  "v_mov_b32 v35, v31\n"
  "v_permlane32_swap_b32 v28, v32\n"
  "v_permlane32_swap_b32 v29, v33\n"
  "v_permlane32_swap_b32 v30, v34\n"
  "v_permlane32_swap_b32 v31, v35\n"
  "v_max_f32 v28, v28, v32\n"
  "v_max_f32 v29, v29, v33\n"
  "v_max_f32 v30, v30, v34\n"
  "v_max_f32 v31, v31, v35\n"
  "v_sub_f32 v40, 0, v28\n"
  "v_sub_f32 v41, 0, v28\n"
  "v_sub_f32 v42, 0, v28\n"
  "v_sub_f32 v43, 0, v28\n"
  "v_sub_f32 v44, 0, v29\n"
  "v_sub_f32 v45, 0, v29\n"
  "v_sub_f32 v46, 0, v29\n"
  "v_sub_f32 v47, 0, v29\n"
  "v_sub_f32 v48, 0, v30\n"
  "v_sub_f32 v49, 0, v30\n"
  "v_sub_f32 v50, 0, v30\n"
  "v_sub_f32 v51, 0, v30\n"
  "v_sub_f32 v52, 0, v31\n"
  "v_sub_f32 v53, 0, v31\n"
  "v_sub_f32 v54, 0, v31\n"
  "v_sub_f32 v55, 0, v31\n"
  "s_add_u32 s64, s64, 0x8000\n"
  "s_cmp_lt_u32 s64, s69\n"
  "s_cselect_b32 s64, s64, s71\n"
  "s_add_u32 s65, s65, 0x8000\n"
  "s_cmp_lt_u32 s65, s70\n"
  "s_cselect_b32 s65, s65, s72\n"
  "v_add_f32 v64, v64, v40\n"
  "v_add_f32 v65, v65, v40\n"
  "v_add_f32 v66, v66, v40\n"
  "v_add_f32 v67, v67, v40\n"
  "v_exp_f32 v64, v64\n"
  "v_add_f32 v68, v68, v44\n"
  "v_exp_f32 v65, v65\n"
  "v_add_f32 v69, v69, v44\n"
  "v_exp_f32 v66, v66\n"
  "v_cvt_pk_bf16_f32 v128, v64, v65\n"
  "v_add_f32 v70, v70, v44\n"
  "v_exp_f32 v67, v67\n"
  "v_add_f32 v71, v71, v44\n"
  "v_exp_f32 v68, v68\n"
  "v_cvt_pk_bf16_f32 v129, v66, v67\n"
  "v_add_f32 v72, v72, v48\n"
  "v_exp_f32 v69, v69\n"
  "v_add_f32 v73, v73, v48\n"
  "v_exp_f32 v70, v70\n"
  "v_cvt_pk_bf16_f32 v132, v68, v69\n"
  "s_waitcnt vmcnt(0) lgkmcnt(0)\n"
  "s_barrier\n"
  "v_mfma_f32_16x16x32_bf16 v[96:99], a[192:195], a[128:131], v[40:43]\n"
  "ds_read_b128 a[224:227], v18 offset:0\n"
  "buffer_store_dwordx2 v[188:189], off, %[srdWS], s78 sc1\n"
  "v_mfma_f32_16x16x32_bf16 v[100:103], a[192:195], a[144:147], v[44:47]\n"
  "s_add_u32 m0, s67, 0\n"
  "v_add_f32 v74, v74, v48\n"
  "v_mfma_f32_16x16x32_bf16 v[104:107], a[192:195], a[160:163], v[48:51]\n"
  "ds_read_b128 a[228:231], v18 offset:1024\n"
  "v_mfma_f32_16x16x32_bf16 v[108:111], a[192:195], a[176:179], v[52:55]\n"
  "v_exp_f32 v71, v71\n"
  "v_mfma_f32_16x16x32_bf16 v[96:99], a[196:199], a[132:135], v[96:99]\n"
  "ds_read_b128 a[232:235], v18 offset:2048\n"
  "buffer_load_dwordx4 %[L16], %[srdWS], s65 offen offset:0 sc1 lds\n"
  "v_mfma_f32_16x16x32_bf16 v[100:103], a[196:199], a[148:151], v[100:103]\n"
  "v_add_f32 v75, v75, v48\n"
  "v_exp_f32 v72, v72\n"
  "v_mfma_f32_16x16x32_bf16 v[104:107], a[196:199], a[164:167], v[104:107]\n"
  "ds_read_b128 a[236:239], v18 offset:3072\n"
  "v_cvt_pk_bf16_f32 v133, v70, v71\n"
  "v_mfma_f32_16x16x32_bf16 v[108:111], a[196:199], a[180:183], v[108:111]\n"
  "buffer_load_dwordx4 %[L16], %[srdWS], s65 offen offset:1024 sc1 lds\n"
  "v_add_f32 v76, v76, v52\n"
  "v_mfma_f32_16x16x32_bf16 v[96:99], a[200:203], a[136:139], v[96:99]\n"
  "ds_read_b128 a[240:243], v19 offset:0\n"
  "v_mfma_f32_16x16x32_bf16 v[100:103], a[200:203], a[152:155], v[100:103]\n"
  "v_exp_f32 v73, v73\n"
  "v_mfma_f32_16x16x32_bf16 v[104:107], a[200:203], a[168:171], v[104:107]\n"
  "ds_read_b128 a[244:247], v19 offset:1024\n"
  "buffer_load_dwordx4 %[L16], %[srdWS], s65 offen offset:2048 sc1 lds\n"
  "v_mfma_f32_16x16x32_bf16 v[108:111], a[200:203], a[184:187], v[108:111]\n"
  "v_add_f32 v77, v77, v52\n"
  "v_exp_f32 v74, v74\n"
  "v_mfma_f32_16x16x32_bf16 v[96:99], a[204:207], a[140:143], v[96:99]\n"
  "ds_read_b128 a[248:251], v19 offset:2048\n"
  "v_cvt_pk_bf16_f32 v136, v72, v73\n"
  "v_mfma_f32_16x16x32_bf16 v[100:103], a[204:207], a[156:159], v[100:103]\n"
  "buffer_load_dwordx4 %[L16], %[srdWS], s65 offen offset:3072 sc1 lds\n"
  "v_add_f32 v78, v78, v52\n"
  "v_mfma_f32_16x16x32_bf16 v[104:107], a[204:207], a[172:175], v[104:107]\n"
  "ds_read_b128 a[252:255], v19 offset:3072\n"
  "v_mfma_f32_16x16x32_bf16 v[108:111], a[204:207], a[188:191], v[108:111]\n"
  "v_exp_f32 v75, v75\n"
  "v_mfma_f32_16x16x32_bf16 v[112:115], a[208:211], a[128:131], v[40:43]\n"
  "s_add_u32 m0, s67, 4096\n"
  "v_add_f32 v79, v79, v52\n"
  "v_mfma_f32_16x16x32_bf16 v[116:119], a[208:211], a[144:147], v[44:47]\n"
  "v_exp_f32 v76, v76\n"
  "v_mfma_f32_16x16x32_bf16 v[120:123], a[208:211], a[160:163], v[48:51]\n"
  "v_cvt_pk_bf16_f32 v137, v74, v75\n"
  "v_mfma_f32_16x16x32_bf16 v[124:127], a[208:211], a[176:179], v[52:55]\n"
  "buffer_load_dwordx4 v16, %[srdWS], s65 offen offset:0 sc1 lds\n"
  "v_mfma_f32_16x16x32_bf16 v[112:115], a[212:215], a[132:135], v[112:115]\n"
  "v_exp_f32 v77, v77\n"
  "v_mfma_f32_16x16x32_bf16 v[116:119], a[212:215], a[148:151], v[116:119]\n"
  "v_exp_f32 v78, v78\n"
  "v_mfma_f32_16x16x32_bf16 v[120:123], a[212:215], a[164:167], v[120:123]\n"
  "buffer_load_dwordx4 v16, %[srdWS], s65 offen offset:1024 sc1 lds\n"
  "v_cvt_pk_bf16_f32 v140, v76, v77\n"
  "v_mfma_f32_16x16x32_bf16 v[124:127], a[212:215], a[180:183], v[124:127]\n"
  "v_exp_f32 v79, v79\n"
  "v_mfma_f32_16x16x32_bf16 v[112:115], a[216:219], a[136:139], v[112:115]\n"
  "v_add_f32 v80, v80, v40\n"
  "v_add_f32 v81, v81, v40\n"
  "v_mfma_f32_16x16x32_bf16 v[116:119], a[216:219], a[152:155], v[116:119]\n"
  "buffer_load_dwordx4 v16, %[srdWS], s65 offen offset:2048 sc1 lds\n"
  "v_add_f32 v82, v82, v40\n"
  "v_mfma_f32_16x16x32_bf16 v[120:123], a[216:219], a[168:171], v[120:123]\n"
  "v_add_f32 v83, v83, v40\n"
  "v_mfma_f32_16x16x32_bf16 v[124:127], a[216:219], a[184:187], v[124:127]\n"
  "v_exp_f32 v80, v80\n"
  "v_mfma_f32_16x16x32_bf16 v[112:115], a[220:223], a[140:143], v[112:115]\n"
  "buffer_load_dwordx4 v16, %[srdWS], s65 offen offset:3072 sc1 lds\n"
  "v_cvt_pk_bf16_f32 v141, v78, v79\n"
  "v_mfma_f32_16x16x32_bf16 v[116:119], a[220:223], a[156:159], v[116:119]\n"
  "v_add_f32 v84, v84, v44\n"
  "v_mfma_f32_16x16x32_bf16 v[120:123], a[220:223], a[172:175], v[120:123]\n"
  "v_exp_f32 v81, v81\n"
  "v_add_f32 v85, v85, v44\n"
  "v_exp_f32 v82, v82\n"
  "v_cvt_pk_bf16_f32 v130, v80, v81\n"
  "v_add_f32 v86, v86, v44\n"
  "v_exp_f32 v83, v83\n"
  "v_add_f32 v87, v87, v44\n"
  "v_exp_f32 v84, v84\n"
  "v_cvt_pk_bf16_f32 v131, v82, v83\n"
  "v_add_f32 v88, v88, v48\n"
  "v_exp_f32 v85, v85\n"
  "v_add_f32 v89, v89, v48\n"
  "v_exp_f32 v86, v86\n"
  "v_cvt_pk_bf16_f32 v134, v84, v85\n"
  "v_add_f32 v90, v90, v48\n"
  "v_exp_f32 v87, v87\n"
  "v_add_f32 v91, v91, v48\n"
  "v_exp_f32 v88, v88\n"
  "v_cvt_pk_bf16_f32 v135, v86, v87\n"
  "v_add_f32 v92, v92, v52\n"
  "v_exp_f32 v89, v89\n"
  "v_add_f32 v93, v93, v52\n"
  "v_exp_f32 v90, v90\n"
  "v_cvt_pk_bf16_f32 v138, v88, v89\n"
  "v_add_f32 v94, v94, v52\n"
  "v_exp_f32 v91, v91\n"
  "v_add_f32 v95, v95, v52\n"
  "v_exp_f32 v92, v92\n"
  "v_cvt_pk_bf16_f32 v139, v90, v91\n"
  "v_exp_f32 v93, v93\n"
  "v_exp_f32 v94, v94\n"
  "v_cvt_pk_bf16_f32 v142, v92, v93\n"
  "v_exp_f32 v95, v95\n"
  "s_nop 0\n"
  "v_cvt_pk_bf16_f32 v143, v94, v95\n"
  "v_mfma_f32_16x16x32_bf16 v[124:127], a[220:223], a[188:191], v[124:127]\n"
  "s_add_u32 m0, s66, 0\n"
  "s_waitcnt lgkmcnt(0)\n"
  "v_mfma_f32_16x16x32_bf16 v[192:195], v[208:211], v[128:131], 0\n"
  "v_exp_f32 v96, v96\n"
  "v_mfma_f32_16x16x32_bf16 v[196:199], v[208:211], v[132:135], 0\n"
  "ds_read_b128 a[192:195], v17 offset:0\n"
  "v_mfma_f32_16x16x32_bf16 v[200:203], v[208:211], v[136:139], 0\n"
  "buffer_load_dwordx4 %[L16], %[srdWS], s64 offen offset:0 sc1 lds\n"
  "v_mfma_f32_16x16x32_bf16 v[204:207], v[208:211], v[140:143], 0\n"
  "v_exp_f32 v97, v97\n"
  "v_mfma_f32_16x16x32_bf16 a[0:3], a[224:227], v[128:131], 0\n"
  "ds_read_b128 a[196:199], v17 offset:1024\n"
  "v_exp_f32 v98, v98\n"
  "v_mfma_f32_16x16x32_bf16 a[4:7], a[224:227], v[132:135], 0\n"
  "buffer_load_dwordx4 %[L16], %[srdWS], s64 offen offset:1024 sc1 lds\n"
  "v_cvt_pk_bf16_f32 v144, v96, v97\n"
  "v_mfma_f32_16x16x32_bf16 a[8:11], a[224:227], v[136:139], 0\n"
  "v_exp_f32 v99, v99\n"
  "v_mfma_f32_16x16x32_bf16 a[12:15], a[224:227], v[140:143], 0\n"
  "ds_read_b128 a[200:203], v17 offset:2048\n"
  "v_exp_f32 v100, v100\n"
  "v_mfma_f32_16x16x32_bf16 a[16:19], a[228:231], v[128:131], 0\n"
  "buffer_load_dwordx4 %[L16], %[srdWS], s64 offen offset:2048 sc1 lds\n"
  "v_cvt_pk_bf16_f32 v145, v98, v99\n"
  "v_mfma_f32_16x16x32_bf16 a[20:23], a[228:231], v[132:135], 0\n"
  "v_exp_f32 v101, v101\n"
  "v_mfma_f32_16x16x32_bf16 a[24:27], a[228:231], v[136:139], 0\n"
  "ds_read_b128 a[204:207], v17 offset:3072\n"
  "v_exp_f32 v102, v102\n"
  "v_mfma_f32_16x16x32_bf16 a[28:31], a[228:231], v[140:143], 0\n"
  "buffer_load_dwordx4 %[L16], %[srdWS], s64 offen offset:3072 sc1 lds\n"
  "v_cvt_pk_bf16_f32 v148, v100, v101\n"
  "v_mfma_f32_16x16x32_bf16 a[32:35], a[232:235], v[128:131], 0\n"
  "v_exp_f32 v103, v103\n"
  "v_mfma_f32_16x16x32_bf16 a[36:39], a[232:235], v[132:135], 0\n"
  "ds_read_b128 a[208:211], v17 offset:4096\n"
  "v_exp_f32 v104, v104\n"
  "v_mfma_f32_16x16x32_bf16 a[40:43], a[232:235], v[136:139], 0\n"
  "s_add_u32 m0, s66, 4096\n"
  "v_cvt_pk_bf16_f32 v149, v102, v103\n"
  "v_mfma_f32_16x16x32_bf16 a[44:47], a[232:235], v[140:143], 0\n"
  "v_exp_f32 v105, v105\n"
  "v_mfma_f32_16x16x32_bf16 a[48:51], a[236:239], v[128:131], 0\n"
  "ds_read_b128 a[212:215], v17 offset:5120\n"
  "v_exp_f32 v106, v106\n"
  "v_mfma_f32_16x16x32_bf16 a[52:55], a[236:239], v[132:135], 0\n"
  "buffer_load_dwordx4 v16, %[srdWS], s64 offen offset:0 sc1 lds\n"
  "v_cvt_pk_bf16_f32 v152, v104, v105\n"
  "v_mfma_f32_16x16x32_bf16 a[56:59], a[236:239], v[136:139], 0\n"
  "v_exp_f32 v107, v107\n"
  "v_mfma_f32_16x16x32_bf16 a[60:63], a[236:239], v[140:143], 0\n"
  "ds_read_b128 a[216:219], v17 offset:6144\n"
  "v_exp_f32 v108, v108\n"
  "v_mfma_f32_16x16x32_bf16 a[64:67], a[240:243], v[128:131], 0\n"
  "buffer_load_dwordx4 v16, %[srdWS], s64 offen offset:1024 sc1 lds\n"
  "v_cvt_pk_bf16_f32 v153, v106, v107\n"
  "v_mfma_f32_16x16x32_bf16 a[68:71], a[240:243], v[132:135], 0\n"
  "v_exp_f32 v109, v109\n"
  "v_mfma_f32_16x16x32_bf16 a[72:75], a[240:243], v[136:139], 0\n"
  "ds_read_b128 a[220:223], v17 offset:7168\n"
  "v_exp_f32 v110, v110\n"
  "v_mfma_f32_16x16x32_bf16 a[76:79], a[240:243], v[140:143], 0\n"
  "buffer_load_dwordx4 v16, %[srdWS], s64 offen offset:2048 sc1 lds\n"
  "v_cvt_pk_bf16_f32 v156, v108, v109\n"
  "v_mfma_f32_16x16x32_bf16 a[80:83], a[244:247], v[128:131], 0\n"
  "v_exp_f32 v111, v111\n"
  "v_mfma_f32_16x16x32_bf16 a[84:87], a[244:247], v[132:135], 0\n"
  "v_exp_f32 v112, v112\n"
  "v_mfma_f32_16x16x32_bf16 a[88:91], a[244:247], v[136:139], 0\n"
  "buffer_load_dwordx4 v16, %[srdWS], s64 offen offset:3072 sc1 lds\n"
  "v_cvt_pk_bf16_f32 v157, v110, v111\n"
  "v_mfma_f32_16x16x32_bf16 a[92:95], a[244:247], v[140:143], 0\n"
  "v_exp_f32 v113, v113\n"
  "v_mfma_f32_16x16x32_bf16 a[96:99], a[248:251], v[128:131], 0\n"
  "v_exp_f32 v114, v114\n"
  "v_mfma_f32_16x16x32_bf16 a[100:103], a[248:251], v[132:135], 0\n"
  "v_cvt_pk_bf16_f32 v146, v112, v113\n"
  "v_exp_f32 v115, v115\n"
  "v_mfma_f32_16x16x32_bf16 a[104:107], a[248:251], v[136:139], 0\n"
  "buffer_load_dword v212, v214, %[srdWot], s79 offen\n"
  "v_exp_f32 v116, v116\n"
  "v_mfma_f32_16x16x32_bf16 a[108:111], a[248:251], v[140:143], 0\n"
  "s_xor_b32 s66, s66, 0x8000\n"
  "v_cvt_pk_bf16_f32 v147, v114, v115\n"
  "v_mfma_f32_16x16x32_bf16 a[112:115], a[252:255], v[128:131], 0\n"
  "s_xor_b32 s67, s67, 0x8000\n"
  "v_exp_f32 v117, v117\n"
  "v_mfma_f32_16x16x32_bf16 a[116:119], a[252:255], v[132:135], 0\n"
  "s_add_u32 s79, s79, 0x400\n"
  "v_exp_f32 v118, v118\n"
  "v_mfma_f32_16x16x32_bf16 a[120:123], a[252:255], v[136:139], 0\n"
  "s_sub_u32 s68, s68, 1\n"
  "v_cvt_pk_bf16_f32 v150, v116, v117\n"
  "v_mfma_f32_16x16x32_bf16 a[124:127], a[252:255], v[140:143], 0\n"
  "s_cmp_gt_u32 s68, 1\n"
  "v_exp_f32 v119, v119\n"
  "s_cbranch_scc0 ATlast_%=\n"
  "ATmid_%=:\n"
  "s_waitcnt lgkmcnt(0)\n"
  "v_mfma_f32_16x16x32_bf16 v[64:67], a[192:195], a[128:131], v[40:43]\n"
  "ds_read_b128 a[224:227], v18 offset:8192\n"
  "v_mfma_f32_16x16x32_bf16 v[68:71], a[192:195], a[144:147], v[44:47]\n"
  "v_exp_f32 v120, v120\n"
  "v_mfma_f32_16x16x32_bf16 v[72:75], a[192:195], a[160:163], v[48:51]\n"
  "ds_read_b128 a[228:231], v18 offset:9216\n"
  "v_cvt_pk_bf16_f32 v151, v118, v119\n"
  "v_mfma_f32_16x16x32_bf16 v[76:79], a[192:195], a[176:179], v[52:55]\n"
  "v_exp_f32 v121, v121\n"
  "v_mfma_f32_16x16x32_bf16 v[64:67], a[196:199], a[132:135], v[64:67]\n"
  "ds_read_b128 a[232:235], v18 offset:10240\n"
  "v_mfma_f32_16x16x32_bf16 v[68:71], a[196:199], a[148:151], v[68:71]\n"
  "v_exp_f32 v122, v122\n"
  "v_mfma_f32_16x16x32_bf16 v[72:75], a[196:199], a[164:167], v[72:75]\n"
  "ds_read_b128 a[236:239], v18 offset:11264\n"
  "v_cvt_pk_bf16_f32 v154, v120, v121\n"
  "v_mfma_f32_16x16x32_bf16 v[76:79], a[196:199], a[180:183], v[76:79]\n"
  "v_exp_f32 v123, v123\n"
  "v_mfma_f32_16x16x32_bf16 v[64:67], a[200:203], a[136:139], v[64:67]\n"
  "ds_read_b128 a[240:243], v19 offset:8192\n"
  "v_mfma_f32_16x16x32_bf16 v[68:71], a[200:203], a[152:155], v[68:71]\n"
  "v_exp_f32 v124, v124\n"
  "v_mfma_f32_16x16x32_bf16 v[72:75], a[200:203], a[168:171], v[72:75]\n"
  "ds_read_b128 a[244:247], v19 offset:9216\n"
  "v_cvt_pk_bf16_f32 v155, v122, v123\n"
  "v_mfma_f32_16x16x32_bf16 v[76:79], a[200:203], a[184:187], v[76:79]\n"
  "v_exp_f32 v125, v125\n"
  "v_mfma_f32_16x16x32_bf16 v[64:67], a[204:207], a[140:143], v[64:67]\n"
  "ds_read_b128 a[248:251], v19 offset:10240\n"
  "v_mfma_f32_16x16x32_bf16 v[68:71], a[204:207], a[156:159], v[68:71]\n"
  "v_exp_f32 v126, v126\n"
  "v_mfma_f32_16x16x32_bf16 v[72:75], a[204:207], a[172:175], v[72:75]\n"
  "ds_read_b128 a[252:255], v19 offset:11264\n"
  "v_cvt_pk_bf16_f32 v158, v124, v125\n"
  "v_mfma_f32_16x16x32_bf16 v[76:79], a[204:207], a[188:191], v[76:79]\n"
  "v_exp_f32 v127, v127\n"
  "v_mfma_f32_16x16x32_bf16 v[80:83], a[208:211], a[128:131], v[40:43]\n"
  "v_cvt_pk_bf16_f32 v159, v126, v127\n"
  "v_mfma_f32_16x16x32_bf16 v[84:87], a[208:211], a[144:147], v[44:47]\n"
  "v_xor_b32 v18, 0x8000, v18\n"
  "v_mfma_f32_16x16x32_bf16 v[88:91], a[208:211], a[160:163], v[48:51]\n"
  "v_xor_b32 v19, 0x8000, v19\n"
  "v_mfma_f32_16x16x32_bf16 v[92:95], a[208:211], a[176:179], v[52:55]\n"
  "v_exp_f32 v64, v64\n"
  "v_mfma_f32_16x16x32_bf16 v[80:83], a[212:215], a[132:135], v[80:83]\n"
  "v_exp_f32 v65, v65\n"
  "v_mfma_f32_16x16x32_bf16 v[84:87], a[212:215], a[148:151], v[84:87]\n"
  "v_exp_f32 v66, v66\n"
  "v_mfma_f32_16x16x32_bf16 v[88:91], a[212:215], a[164:167], v[88:91]\n"
  "v_cvt_pk_bf16_f32 v128, v64, v65\n"
  "v_mfma_f32_16x16x32_bf16 v[92:95], a[212:215], a[180:183], v[92:95]\n"
  "v_exp_f32 v67, v67\n"
  "v_mfma_f32_16x16x32_bf16 v[80:83], a[216:219], a[136:139], v[80:83]\n"
  "v_exp_f32 v68, v68\n"
  "v_mfma_f32_16x16x32_bf16 v[84:87], a[216:219], a[152:155], v[84:87]\n"
  "v_mfma_f32_16x16x32_bf16 v[88:91], a[216:219], a[168:171], v[88:91]\n"
  "v_cvt_pk_bf16_f32 v129, v66, v67\n"
  "v_mfma_f32_16x16x32_bf16 v[92:95], a[216:219], a[184:187], v[92:95]\n"
  "v_exp_f32 v69, v69\n"
  "v_mfma_f32_16x16x32_bf16 v[80:83], a[220:223], a[140:143], v[80:83]\n"
  "v_exp_f32 v70, v70\n"
  "v_mfma_f32_16x16x32_bf16 v[84:87], a[220:223], a[156:159], v[84:87]\n"
  "v_mfma_f32_16x16x32_bf16 v[88:91], a[220:223], a[172:175], v[88:91]\n"
  "v_cvt_pk_bf16_f32 v132, v68, v69\n"
  "v_mfma_f32_16x16x32_bf16 v[92:95], a[220:223], a[188:191], v[92:95]\n"
  "v_exp_f32 v71, v71\n"
  "s_waitcnt lgkmcnt(0)\n"
  "v_mfma_f32_16x16x32_bf16 v[192:195], v[208:211], v[144:147], v[192:195]\n"
  "v_mfma_f32_16x16x32_bf16 v[196:199], v[208:211], v[148:151], v[196:199]\n"
  "ds_read_b128 a[192:195], v17 offset:8192\n"
  "v_mfma_f32_16x16x32_bf16 v[200:203], v[208:211], v[152:155], v[200:203]\n"
  "v_exp_f32 v72, v72\n"
  "v_mfma_f32_16x16x32_bf16 v[204:207], v[208:211], v[156:159], v[204:207]\n"
  "v_cvt_pk_bf16_f32 v133, v70, v71\n"
  "v_mfma_f32_16x16x32_bf16 a[0:3], a[224:227], v[144:147], a[0:3]\n"
  "ds_read_b128 a[196:199], v17 offset:9216\n"
  "v_mfma_f32_16x16x32_bf16 a[4:7], a[224:227], v[148:151], a[4:7]\n"
  "v_exp_f32 v73, v73\n"
  "v_mfma_f32_16x16x32_bf16 a[8:11], a[224:227], v[152:155], a[8:11]\n"
  "v_exp_f32 v74, v74\n"
  "v_mfma_f32_16x16x32_bf16 a[12:15], a[224:227], v[156:159], a[12:15]\n"
  "ds_read_b128 a[200:203], v17 offset:10240\n"
  "v_cvt_pk_bf16_f32 v136, v72, v73\n"
  "v_mfma_f32_16x16x32_bf16 a[16:19], a[228:231], v[144:147], a[16:19]\n"
  "v_exp_f32 v75, v75\n"
  "v_mfma_f32_16x16x32_bf16 a[20:23], a[228:231], v[148:151], a[20:23]\n"
  "v_exp_f32 v76, v76\n"
  "v_mfma_f32_16x16x32_bf16 a[24:27], a[228:231], v[152:155], a[24:27]\n"
  "ds_read_b128 a[204:207], v17 offset:11264\n"
  "v_mfma_f32_16x16x32_bf16 a[28:31], a[228:231], v[156:159], a[28:31]\n"
  "v_cvt_pk_bf16_f32 v137, v74, v75\n"
  "v_mfma_f32_16x16x32_bf16 a[32:35], a[232:235], v[144:147], a[32:35]\n"
  "v_exp_f32 v77, v77\n"
  "v_mfma_f32_16x16x32_bf16 a[36:39], a[232:235], v[148:151], a[36:39]\n"
  "ds_read_b128 a[208:211], v17 offset:12288\n"
  "v_mfma_f32_16x16x32_bf16 a[40:43], a[232:235], v[152:155], a[40:43]\n"
  "v_exp_f32 v78, v78\n"
  "v_mfma_f32_16x16x32_bf16 a[44:47], a[232:235], v[156:159], a[44:47]\n"
  "v_cvt_pk_bf16_f32 v140, v76, v77\n"
  "v_mfma_f32_16x16x32_bf16 a[48:51], a[236:239], v[144:147], a[48:51]\n"
  "ds_read_b128 a[212:215], v17 offset:13312\n"
  "v_mfma_f32_16x16x32_bf16 a[52:55], a[236:239], v[148:151], a[52:55]\n"
  "v_exp_f32 v79, v79\n"
  "v_mfma_f32_16x16x32_bf16 a[56:59], a[236:239], v[152:155], a[56:59]\n"
  "v_exp_f32 v80, v80\n"
  "v_mfma_f32_16x16x32_bf16 a[60:63], a[236:239], v[156:159], a[60:63]\n"
  "ds_read_b128 a[216:219], v17 offset:14336\n"
  "v_cvt_pk_bf16_f32 v141, v78, v79\n"
  "v_mfma_f32_16x16x32_bf16 a[64:67], a[240:243], v[144:147], a[64:67]\n"
  "v_exp_f32 v81, v81\n"
  "v_mfma_f32_16x16x32_bf16 a[68:71], a[240:243], v[148:151], a[68:71]\n"
  "v_mfma_f32_16x16x32_bf16 a[72:75], a[240:243], v[152:155], a[72:75]\n"
  "ds_read_b128 a[220:223], v17 offset:15360\n"
  "v_mfma_f32_16x16x32_bf16 a[76:79], a[240:243], v[156:159], a[76:79]\n"
  "v_exp_f32 v82, v82\n"
  "v_mfma_f32_16x16x32_bf16 a[80:83], a[244:247], v[144:147], a[80:83]\n"
  "v_cvt_pk_bf16_f32 v130, v80, v81\n"
  "v_mfma_f32_16x16x32_bf16 a[84:87], a[244:247], v[148:151], a[84:87]\n"
  "v_exp_f32 v83, v83\n"
  "v_mfma_f32_16x16x32_bf16 a[88:91], a[244:247], v[152:155], a[88:91]\n"
  "v_exp_f32 v84, v84\n"
  "v_mfma_f32_16x16x32_bf16 a[92:95], a[244:247], v[156:159], a[92:95]\n"
  "v_mfma_f32_16x16x32_bf16 a[96:99], a[248:251], v[144:147], a[96:99]\n"
  "v_xor_b32 v17, 0x8000, v17\n"
  "v_cvt_pk_bf16_f32 v131, v82, v83\n"
  "v_mfma_f32_16x16x32_bf16 a[100:103], a[248:251], v[148:151], a[100:103]\n"
  "v_exp_f32 v85, v85\n"
  "v_mfma_f32_16x16x32_bf16 a[104:107], a[248:251], v[152:155], a[104:107]\n"
  "s_add_u32 s64, s64, 0x8000\n"
  "s_cmp_lt_u32 s64, s69\n"
  "s_cselect_b32 s64, s64, s71\n"
  "v_mfma_f32_16x16x32_bf16 a[108:111], a[248:251], v[156:159], a[108:111]\n"
  "v_exp_f32 v86, v86\n"
  "v_mfma_f32_16x16x32_bf16 a[112:115], a[252:255], v[144:147], a[112:115]\n"
  "s_add_u32 s65, s65, 0x8000\n"
  "s_cmp_lt_u32 s65, s70\n"
  "s_cselect_b32 s65, s65, s72\n"
  "v_mfma_f32_16x16x32_bf16 a[116:119], a[252:255], v[148:151], a[116:119]\n"
  "v_cvt_pk_bf16_f32 v134, v84, v85\n"
  "v_mfma_f32_16x16x32_bf16 a[120:123], a[252:255], v[152:155], a[120:123]\n"
  "v_exp_f32 v87, v87\n"
  "v_mfma_f32_16x16x32_bf16 a[124:127], a[252:255], v[156:159], a[124:127]\n"
  "v_exp_f32 v88, v88\n"
  "s_waitcnt vmcnt(1) lgkmcnt(0)\n"
  "s_barrier\n"
  "v_mfma_f32_16x16x32_bf16 v[96:99], a[192:195], a[128:131], v[40:43]\n"
  "ds_read_b128 a[224:227], v18 offset:0\n"
  "v_cvt_pk_bf16_f32 v135, v86, v87\n"
  "v_mfma_f32_16x16x32_bf16 v[100:103], a[192:195], a[144:147], v[44:47]\n"
  "s_add_u32 m0, s67, 0\n"
  "v_mfma_f32_16x16x32_bf16 v[104:107], a[192:195], a[160:163], v[48:51]\n"
  "ds_read_b128 a[228:231], v18 offset:1024\n"
  "v_mfma_f32_16x16x32_bf16 v[108:111], a[192:195], a[176:179], v[52:55]\n"
  "v_exp_f32 v89, v89\n"
  "v_mfma_f32_16x16x32_bf16 v[96:99], a[196:199], a[132:135], v[96:99]\n"
  "ds_read_b128 a[232:235], v18 offset:2048\n"
  "buffer_load_dwordx4 %[L16], %[srdWS], s65 offen offset:0 sc1 lds\n"
  "v_mfma_f32_16x16x32_bf16 v[100:103], a[196:199], a[148:151], v[100:103]\n"
  "v_exp_f32 v90, v90\n"
  "v_mfma_f32_16x16x32_bf16 v[104:107], a[196:199], a[164:167], v[104:107]\n"
  "ds_read_b128 a[236:239], v18 offset:3072\n"
  "v_cvt_pk_bf16_f32 v138, v88, v89\n"
  "v_mfma_f32_16x16x32_bf16 v[108:111], a[196:199], a[180:183], v[108:111]\n"
  "buffer_load_dwordx4 %[L16], %[srdWS], s65 offen offset:1024 sc1 lds\n"
  "v_exp_f32 v91, v91\n"
  "v_mfma_f32_16x16x32_bf16 v[96:99], a[200:203], a[136:139], v[96:99]\n"
  "ds_read_b128 a[240:243], v19 offset:0\n"
  "v_mfma_f32_16x16x32_bf16 v[100:103], a[200:203], a[152:155], v[100:103]\n"
  "v_exp_f32 v92, v92\n"
  "v_mfma_f32_16x16x32_bf16 v[104:107], a[200:203], a[168:171], v[104:107]\n"
  "ds_read_b128 a[244:247], v19 offset:1024\n"
  "buffer_load_dwordx4 %[L16], %[srdWS], s65 offen offset:2048 sc1 lds\n"
  "v_mfma_f32_16x16x32_bf16 v[108:111], a[200:203], a[184:187], v[108:111]\n"
  "v_cvt_pk_bf16_f32 v139, v90, v91\n"
  "v_exp_f32 v93, v93\n"
  "v_mfma_f32_16x16x32_bf16 v[96:99], a[204:207], a[140:143], v[96:99]\n"
  "ds_read_b128 a[248:251], v19 offset:2048\n"
  "v_exp_f32 v94, v94\n"
  "v_mfma_f32_16x16x32_bf16 v[100:103], a[204:207], a[156:159], v[100:103]\n"
  "buffer_load_dwordx4 %[L16], %[srdWS], s65 offen offset:3072 sc1 lds\n"
  "v_cvt_pk_bf16_f32 v142, v92, v93\n"
  "v_mfma_f32_16x16x32_bf16 v[104:107], a[204:207], a[172:175], v[104:107]\n"
  "ds_read_b128 a[252:255], v19 offset:3072\n"
  "v_mfma_f32_16x16x32_bf16 v[108:111], a[204:207], a[188:191], v[108:111]\n"
  "v_exp_f32 v95, v95\n"
  "v_mfma_f32_16x16x32_bf16 v[112:115], a[208:211], a[128:131], v[40:43]\n"
  "s_add_u32 m0, s67, 4096\n"
  "v_cvt_pk_bf16_f32 v143, v94, v95\n"
  "v_mfma_f32_16x16x32_bf16 v[116:119], a[208:211], a[144:147], v[44:47]\n"
  "v_exp_f32 v96, v96\n"
  "v_mfma_f32_16x16x32_bf16 v[120:123], a[208:211], a[160:163], v[48:51]\n"
  "v_exp_f32 v97, v97\n"
  "v_mfma_f32_16x16x32_bf16 v[124:127], a[208:211], a[176:179], v[52:55]\n"
  "buffer_load_dwordx4 v16, %[srdWS], s65 offen offset:0 sc1 lds\n"
  "v_mfma_f32_16x16x32_bf16 v[112:115], a[212:215], a[132:135], v[112:115]\n"
  "v_exp_f32 v98, v98\n"
  "v_mfma_f32_16x16x32_bf16 v[116:119], a[212:215], a[148:151], v[116:119]\n"
  "v_mfma_f32_16x16x32_bf16 v[120:123], a[212:215], a[164:167], v[120:123]\n"
  "buffer_load_dwordx4 v16, %[srdWS], s65 offen offset:1024 sc1 lds\n"
  "v_cvt_pk_bf16_f32 v144, v96, v97\n"
  "v_mfma_f32_16x16x32_bf16 v[124:127], a[212:215], a[180:183], v[124:127]\n"
  "v_exp_f32 v99, v99\n"
  "v_mfma_f32_16x16x32_bf16 v[112:115], a[216:219], a[136:139], v[112:115]\n"
  "v_exp_f32 v100, v100\n"
  "v_mfma_f32_16x16x32_bf16 v[116:119], a[216:219], a[152:155], v[116:119]\n"
  "buffer_load_dwordx4 v16, %[srdWS], s65 offen offset:2048 sc1 lds\n"
  "v_mfma_f32_16x16x32_bf16 v[120:123], a[216:219], a[168:171], v[120:123]\n"
  "v_cvt_pk_bf16_f32 v145, v98, v99\n"
  "v_mfma_f32_16x16x32_bf16 v[124:127], a[216:219], a[184:187], v[124:127]\n"
  "v_exp_f32 v101, v101\n"
  "v_mfma_f32_16x16x32_bf16 v[112:115], a[220:223], a[140:143], v[112:115]\n"
  "buffer_load_dwordx4 v16, %[srdWS], s65 offen offset:3072 sc1 lds\n"
  "v_mfma_f32_16x16x32_bf16 v[116:119], a[220:223], a[156:159], v[116:119]\n"
  "v_exp_f32 v102, v102\n"
  "v_mfma_f32_16x16x32_bf16 v[120:123], a[220:223], a[172:175], v[120:123]\n"
  "v_cvt_pk_bf16_f32 v148, v100, v101\n"
  "v_mfma_f32_16x16x32_bf16 v[124:127], a[220:223], a[188:191], v[124:127]\n"
  "s_add_u32 m0, s66, 0\n"
  "s_waitcnt lgkmcnt(0)\n"
  "v_mfma_f32_16x16x32_bf16 v[192:195], v[208:211], v[128:131], v[192:195]\n"
  "v_exp_f32 v103, v103\n"
  "v_mfma_f32_16x16x32_bf16 v[196:199], v[208:211], v[132:135], v[196:199]\n"
  "ds_read_b128 a[192:195], v17 offset:0\n"
  "v_mfma_f32_16x16x32_bf16 v[200:203], v[208:211], v[136:139], v[200:203]\n"
  "buffer_load_dwordx4 %[L16], %[srdWS], s64 offen offset:0 sc1 lds\n"
  "v_mfma_f32_16x16x32_bf16 v[204:207], v[208:211], v[140:143], v[204:207]\n"
  "v_exp_f32 v104, v104\n"
  "v_mfma_f32_16x16x32_bf16 a[0:3], a[224:227], v[128:131], a[0:3]\n"
  "ds_read_b128 a[196:199], v17 offset:1024\n"
  "v_cvt_pk_bf16_f32 v149, v102, v103\n"
  "v_mfma_f32_16x16x32_bf16 a[4:7], a[224:227], v[132:135], a[4:7]\n"
  "buffer_load_dwordx4 %[L16], %[srdWS], s64 offen offset:1024 sc1 lds\n"
  "v_mfma_f32_16x16x32_bf16 a[8:11], a[224:227], v[136:139], a[8:11]\n"
  "v_exp_f32 v105, v105\n"
  "v_mfma_f32_16x16x32_bf16 a[12:15], a[224:227], v[140:143], a[12:15]\n"
  "ds_read_b128 a[200:203], v17 offset:2048\n"
  "v_exp_f32 v106, v106\n"
  "v_mfma_f32_16x16x32_bf16 a[16:19], a[228:231], v[128:131], a[16:19]\n"
  "buffer_load_dwordx4 %[L16], %[srdWS], s64 offen offset:2048 sc1 lds\n"
  "v_cvt_pk_bf16_f32 v152, v104, v105\n"
  "v_mfma_f32_16x16x32_bf16 a[20:23], a[228:231], v[132:135], a[20:23]\n"
  "v_exp_f32 v107, v107\n"
  "v_mfma_f32_16x16x32_bf16 a[24:27], a[228:231], v[136:139], a[24:27]\n"
  "ds_read_b128 a[204:207], v17 offset:3072\n"
  "v_exp_f32 v108, v108\n"
  "v_mfma_f32_16x16x32_bf16 a[28:31], a[228:231], v[140:143], a[28:31]\n"
  "buffer_load_dwordx4 %[L16], %[srdWS], s64 offen offset:3072 sc1 lds\n"
  "v_cvt_pk_bf16_f32 v153, v106, v107\n"
  "v_mfma_f32_16x16x32_bf16 a[32:35], a[232:235], v[128:131], a[32:35]\n"
  "v_exp_f32 v109, v109\n"
  "v_mfma_f32_16x16x32_bf16 a[36:39], a[232:235], v[132:135], a[36:39]\n"
  "ds_read_b128 a[208:211], v17 offset:4096\n"
  "v_mfma_f32_16x16x32_bf16 a[40:43], a[232:235], v[136:139], a[40:43]\n"
  "s_add_u32 m0, s66, 4096\n"
  "v_mfma_f32_16x16x32_bf16 a[44:47], a[232:235], v[140:143], a[44:47]\n"
  "v_exp_f32 v110, v110\n"
  "v_mfma_f32_16x16x32_bf16 a[48:51], a[236:239], v[128:131], a[48:51]\n"
  "ds_read_b128 a[212:215], v17 offset:5120\n"
  "v_cvt_pk_bf16_f32 v156, v108, v109\n"
  "v_mfma_f32_16x16x32_bf16 a[52:55], a[236:239], v[132:135], a[52:55]\n"
  "buffer_load_dwordx4 v16, %[srdWS], s64 offen offset:0 sc1 lds\n"
  "v_mfma_f32_16x16x32_bf16 a[56:59], a[236:239], v[136:139], a[56:59]\n"
  "v_exp_f32 v111, v111\n"
  "v_mfma_f32_16x16x32_bf16 a[60:63], a[236:239], v[140:143], a[60:63]\n"
  "ds_read_b128 a[216:219], v17 offset:6144\n"
  "v_exp_f32 v112, v112\n"
  "v_mfma_f32_16x16x32_bf16 a[64:67], a[240:243], v[128:131], a[64:67]\n"
  "buffer_load_dwordx4 v16, %[srdWS], s64 offen offset:1024 sc1 lds\n"
  "v_cvt_pk_bf16_f32 v157, v110, v111\n"
  "v_mfma_f32_16x16x32_bf16 a[68:71], a[240:243], v[132:135], a[68:71]\n"
  "v_exp_f32 v113, v113\n"
  "v_mfma_f32_16x16x32_bf16 a[72:75], a[240:243], v[136:139], a[72:75]\n"
  "ds_read_b128 a[220:223], v17 offset:7168\n"
  "v_mfma_f32_16x16x32_bf16 a[76:79], a[240:243], v[140:143], a[76:79]\n"
  "buffer_load_dwordx4 v16, %[srdWS], s64 offen offset:2048 sc1 lds\n"
  "v_mfma_f32_16x16x32_bf16 a[80:83], a[244:247], v[128:131], a[80:83]\n"
  "v_exp_f32 v114, v114\n"
  "v_mfma_f32_16x16x32_bf16 a[84:87], a[244:247], v[132:135], a[84:87]\n"
  "v_cvt_pk_bf16_f32 v146, v112, v113\n"
  "v_mfma_f32_16x16x32_bf16 a[88:91], a[244:247], v[136:139], a[88:91]\n"
  "buffer_load_dwordx4 v16, %[srdWS], s64 offen offset:3072 sc1 lds\n"
  "v_mfma_f32_16x16x32_bf16 a[92:95], a[244:247], v[140:143], a[92:95]\n"
  "v_exp_f32 v115, v115\n"
  "v_mfma_f32_16x16x32_bf16 a[96:99], a[248:251], v[128:131], a[96:99]\n"
  "v_exp_f32 v116, v116\n"
  "v_mfma_f32_16x16x32_bf16 a[100:103], a[248:251], v[132:135], a[100:103]\n"
  "v_cvt_pk_bf16_f32 v147, v114, v115\n"
  "v_exp_f32 v117, v117\n"
  "v_mfma_f32_16x16x32_bf16 a[104:107], a[248:251], v[136:139], a[104:107]\n"
  "buffer_load_dword v212, v214, %[srdWot], s79 offen\n"
  "v_exp_f32 v118, v118\n"
  "v_mfma_f32_16x16x32_bf16 a[108:111], a[248:251], v[140:143], a[108:111]\n"
  "s_xor_b32 s66, s66, 0x8000\n"
  "v_mfma_f32_16x16x32_bf16 a[112:115], a[252:255], v[128:131], a[112:115]\n"
  "s_xor_b32 s67, s67, 0x8000\n"
  "v_cvt_pk_bf16_f32 v150, v116, v117\n"
  "v_mfma_f32_16x16x32_bf16 a[116:119], a[252:255], v[132:135], a[116:119]\n"
  "s_add_u32 s79, s79, 0x400\n"
  "v_exp_f32 v119, v119\n"
  "v_mfma_f32_16x16x32_bf16 a[120:123], a[252:255], v[136:139], a[120:123]\n"
  "s_sub_u32 s68, s68, 1\n"
  "v_mfma_f32_16x16x32_bf16 a[124:127], a[252:255], v[140:143], a[124:127]\n"
  "s_cmp_gt_u32 s68, 1\n"
  "s_cbranch_scc1 ATmid_%=\n"
  "ATlast_%=:\n"
  "s_waitcnt lgkmcnt(0)\n"
  "v_mfma_f32_16x16x32_bf16 v[64:67], a[192:195], a[128:131], v[40:43]\n"
  "ds_read_b128 a[224:227], v18 offset:8192\n"
  "v_mfma_f32_16x16x32_bf16 v[68:71], a[192:195], a[144:147], v[44:47]\n"
  "v_exp_f32 v120, v120\n"
  "v_mfma_f32_16x16x32_bf16 v[72:75], a[192:195], a[160:163], v[48:51]\n"
  "ds_read_b128 a[228:231], v18 offset:9216\n"
  "v_cvt_pk_bf16_f32 v151, v118, v119\n"
  "v_mfma_f32_16x16x32_bf16 v[76:79], a[192:195], a[176:179], v[52:55]\n"
  "v_exp_f32 v121, v121\n"
  "v_mfma_f32_16x16x32_bf16 v[64:67], a[196:199], a[132:135], v[64:67]\n"
  "ds_read_b128 a[232:235], v18 offset:10240\n"
  "v_mfma_f32_16x16x32_bf16 v[68:71], a[196:199], a[148:151], v[68:71]\n"
  "v_exp_f32 v122, v122\n"
  "v_mfma_f32_16x16x32_bf16 v[72:75], a[196:199], a[164:167], v[72:75]\n"
  "ds_read_b128 a[236:239], v18 offset:11264\n"
  "v_cvt_pk_bf16_f32 v154, v120, v121\n"
  "v_mfma_f32_16x16x32_bf16 v[76:79], a[196:199], a[180:183], v[76:79]\n"
  "v_exp_f32 v123, v123\n"
  "v_mfma_f32_16x16x32_bf16 v[64:67], a[200:203], a[136:139], v[64:67]\n"
  "ds_read_b128 a[240:243], v19 offset:8192\n"
  "v_mfma_f32_16x16x32_bf16 v[68:71], a[200:203], a[152:155], v[68:71]\n"
  "v_exp_f32 v124, v124\n"
  "v_mfma_f32_16x16x32_bf16 v[72:75], a[200:203], a[168:171], v[72:75]\n"
  "ds_read_b128 a[244:247], v19 offset:9216\n"
  "v_cvt_pk_bf16_f32 v155, v122, v123\n"
  "v_mfma_f32_16x16x32_bf16 v[76:79], a[200:203], a[184:187], v[76:79]\n"
  "v_exp_f32 v125, v125\n"
  "v_mfma_f32_16x16x32_bf16 v[64:67], a[204:207], a[140:143], v[64:67]\n"
  "ds_read_b128 a[248:251], v19 offset:10240\n"
  "v_mfma_f32_16x16x32_bf16 v[68:71], a[204:207], a[156:159], v[68:71]\n"
  "v_exp_f32 v126, v126\n"
  "v_mfma_f32_16x16x32_bf16 v[72:75], a[204:207], a[172:175], v[72:75]\n"
  "ds_read_b128 a[252:255], v19 offset:11264\n"
  "v_cvt_pk_bf16_f32 v158, v124, v125\n"
  "v_mfma_f32_16x16x32_bf16 v[76:79], a[204:207], a[188:191], v[76:79]\n"
  "v_exp_f32 v127, v127\n"
  "v_mfma_f32_16x16x32_bf16 v[80:83], a[208:211], a[128:131], v[40:43]\n"
  "v_cvt_pk_bf16_f32 v159, v126, v127\n"
  "v_mfma_f32_16x16x32_bf16 v[84:87], a[208:211], a[144:147], v[44:47]\n"
  "v_xor_b32 v18, 0x8000, v18\n"
  "v_min_f32 v64, v64, v172\n"
  "v_mfma_f32_16x16x32_bf16 v[88:91], a[208:211], a[160:163], v[48:51]\n"
  "v_xor_b32 v19, 0x8000, v19\n"
  "v_min_f32 v65, v65, v173\n"
  "v_mfma_f32_16x16x32_bf16 v[92:95], a[208:211], a[176:179], v[52:55]\n"
  "v_min_f32 v66, v66, v174\n"
  "v_min_f32 v67, v67, v175\n"
  "v_mfma_f32_16x16x32_bf16 v[80:83], a[212:215], a[132:135], v[80:83]\n"
  "v_exp_f32 v64, v64\n"
  "v_mfma_f32_16x16x32_bf16 v[84:87], a[212:215], a[148:151], v[84:87]\n"
  "v_min_f32 v68, v68, v176\n"
  "v_mfma_f32_16x16x32_bf16 v[88:91], a[212:215], a[164:167], v[88:91]\n"
  "v_exp_f32 v65, v65\n"
  "v_mfma_f32_16x16x32_bf16 v[92:95], a[212:215], a[180:183], v[92:95]\n"
  "v_min_f32 v69, v69, v177\n"
  "v_mfma_f32_16x16x32_bf16 v[80:83], a[216:219], a[136:139], v[80:83]\n"
  "v_exp_f32 v66, v66\n"
  "v_mfma_f32_16x16x32_bf16 v[84:87], a[216:219], a[152:155], v[84:87]\n"
  "v_cvt_pk_bf16_f32 v128, v64, v65\n"
  "v_min_f32 v70, v70, v178\n"
  "v_mfma_f32_16x16x32_bf16 v[88:91], a[216:219], a[168:171], v[88:91]\n"
  "v_exp_f32 v67, v67\n"
  "v_mfma_f32_16x16x32_bf16 v[92:95], a[216:219], a[184:187], v[92:95]\n"
  "v_min_f32 v71, v71, v179\n"
  "v_mfma_f32_16x16x32_bf16 v[80:83], a[220:223], a[140:143], v[80:83]\n"
  "v_exp_f32 v68, v68\n"
  "v_mfma_f32_16x16x32_bf16 v[84:87], a[220:223], a[156:159], v[84:87]\n"
  "v_cvt_pk_bf16_f32 v129, v66, v67\n"
  "v_min_f32 v72, v72, v180\n"
  "v_mfma_f32_16x16x32_bf16 v[88:91], a[220:223], a[172:175], v[88:91]\n"
  "v_exp_f32 v69, v69\n"
  "v_mfma_f32_16x16x32_bf16 v[92:95], a[220:223], a[188:191], v[92:95]\n"
  "v_min_f32 v73, v73, v181\n"
  "s_waitcnt lgkmcnt(0)\n"
  "v_mfma_f32_16x16x32_bf16 v[192:195], v[208:211], v[144:147], v[192:195]\n"
  "v_exp_f32 v70, v70\n"
  "v_mfma_f32_16x16x32_bf16 v[196:199], v[208:211], v[148:151], v[196:199]\n"
  "ds_read_b128 a[192:195], v17 offset:8192\n"
  "v_cvt_pk_bf16_f32 v132, v68, v69\n"
  "v_mfma_f32_16x16x32_bf16 v[200:203], v[208:211], v[152:155], v[200:203]\n"
  "v_min_f32 v74, v74, v182\n"
  "v_mfma_f32_16x16x32_bf16 v[204:207], v[208:211], v[156:159], v[204:207]\n"
  "v_exp_f32 v71, v71\n"
  "v_mfma_f32_16x16x32_bf16 a[0:3], a[224:227], v[144:147], a[0:3]\n"
  "ds_read_b128 a[196:199], v17 offset:9216\n"
  "v_min_f32 v75, v75, v183\n"
  "v_mfma_f32_16x16x32_bf16 a[4:7], a[224:227], v[148:151], a[4:7]\n"
  "v_exp_f32 v72, v72\n"
  "v_mfma_f32_16x16x32_bf16 a[8:11], a[224:227], v[152:155], a[8:11]\n"
  "v_cvt_pk_bf16_f32 v133, v70, v71\n"
  "v_min_f32 v76, v76, v184\n"
  "v_mfma_f32_16x16x32_bf16 a[12:15], a[224:227], v[156:159], a[12:15]\n"
  "ds_read_b128 a[200:203], v17 offset:10240\n"
  "v_mfma_f32_16x16x32_bf16 a[16:19], a[228:231], v[144:147], a[16:19]\n"
  "v_exp_f32 v73, v73\n"
  "v_mfma_f32_16x16x32_bf16 a[20:23], a[228:231], v[148:151], a[20:23]\n"
  "v_min_f32 v77, v77, v185\n"
  "v_mfma_f32_16x16x32_bf16 a[24:27], a[228:231], v[152:155], a[24:27]\n"
  "ds_read_b128 a[204:207], v17 offset:11264\n"
  "v_mfma_f32_16x16x32_bf16 a[28:31], a[228:231], v[156:159], a[28:31]\n"
  "v_exp_f32 v74, v74\n"
  "v_mfma_f32_16x16x32_bf16 a[32:35], a[232:235], v[144:147], a[32:35]\n"
  "v_cvt_pk_bf16_f32 v136, v72, v73\n"
  "v_min_f32 v78, v78, v186\n"
  "v_mfma_f32_16x16x32_bf16 a[36:39], a[232:235], v[148:151], a[36:39]\n"
  "ds_read_b128 a[208:211], v17 offset:12288\n"
  "v_mfma_f32_16x16x32_bf16 a[40:43], a[232:235], v[152:155], a[40:43]\n"
  "v_exp_f32 v75, v75\n"
  "v_mfma_f32_16x16x32_bf16 a[44:47], a[232:235], v[156:159], a[44:47]\n"
  "v_min_f32 v79, v79, v187\n"
  "v_mfma_f32_16x16x32_bf16 a[48:51], a[236:239], v[144:147], a[48:51]\n"
  "ds_read_b128 a[212:215], v17 offset:13312\n"
  "v_mfma_f32_16x16x32_bf16 a[52:55], a[236:239], v[148:151], a[52:55]\n"
  "v_exp_f32 v76, v76\n"
  "v_mfma_f32_16x16x32_bf16 a[56:59], a[236:239], v[152:155], a[56:59]\n"
  "v_cvt_pk_bf16_f32 v137, v74, v75\n"
  "v_mfma_f32_16x16x32_bf16 a[60:63], a[236:239], v[156:159], a[60:63]\n"
  "ds_read_b128 a[216:219], v17 offset:14336\n"
  "v_mfma_f32_16x16x32_bf16 a[64:67], a[240:243], v[144:147], a[64:67]\n"
  "v_exp_f32 v77, v77\n"
  "v_mfma_f32_16x16x32_bf16 a[68:71], a[240:243], v[148:151], a[68:71]\n"
  "v_exp_f32 v78, v78\n"
  "v_mfma_f32_16x16x32_bf16 a[72:75], a[240:243], v[152:155], a[72:75]\n"
  "ds_read_b128 a[220:223], v17 offset:15360\n"
  "v_cvt_pk_bf16_f32 v140, v76, v77\n"
  "v_mfma_f32_16x16x32_bf16 a[76:79], a[240:243], v[156:159], a[76:79]\n"
  "v_exp_f32 v79, v79\n"
  "v_mfma_f32_16x16x32_bf16 a[80:83], a[244:247], v[144:147], a[80:83]\n"
  "v_min_f32 v80, v80, v168\n"
  "v_min_f32 v81, v81, v169\n"
  "v_mfma_f32_16x16x32_bf16 a[84:87], a[244:247], v[148:151], a[84:87]\n"
  "v_min_f32 v82, v82, v170\n"
  "v_min_f32 v83, v83, v171\n"
  "v_mfma_f32_16x16x32_bf16 a[88:91], a[244:247], v[152:155], a[88:91]\n"
  "v_exp_f32 v80, v80\n"
  "v_mfma_f32_16x16x32_bf16 a[92:95], a[244:247], v[156:159], a[92:95]\n"
  "v_cvt_pk_bf16_f32 v141, v78, v79\n"
  "v_min_f32 v84, v84, v172\n"
  "v_mfma_f32_16x16x32_bf16 a[96:99], a[248:251], v[144:147], a[96:99]\n"
  "v_xor_b32 v17, 0x8000, v17\n"
  "v_mfma_f32_16x16x32_bf16 a[100:103], a[248:251], v[148:151], a[100:103]\n"
  "v_exp_f32 v81, v81\n"
  "v_mfma_f32_16x16x32_bf16 a[104:107], a[248:251], v[152:155], a[104:107]\n"
  "s_add_u32 s64, s64, 0x8000\n"
  "s_cmp_lt_u32 s64, s69\n"
  "s_cselect_b32 s64, s64, s71\n"
  "v_mfma_f32_16x16x32_bf16 a[108:111], a[248:251], v[156:159], a[108:111]\n"
  "v_min_f32 v85, v85, v173\n"
  "v_mfma_f32_16x16x32_bf16 a[112:115], a[252:255], v[144:147], a[112:115]\n"
  "s_add_u32 s65, s65, 0x8000\n"
  "s_cmp_lt_u32 s65, s70\n"
  "s_cselect_b32 s65, s65, s72\n"
  "v_mfma_f32_16x16x32_bf16 a[116:119], a[252:255], v[148:151], a[116:119]\n"
  "v_exp_f32 v82, v82\n"
  "v_mfma_f32_16x16x32_bf16 a[120:123], a[252:255], v[152:155], a[120:123]\n"
  "v_cvt_pk_bf16_f32 v130, v80, v81\n"
  "v_min_f32 v86, v86, v174\n"
  "v_mfma_f32_16x16x32_bf16 a[124:127], a[252:255], v[156:159], a[124:127]\n"
  "v_exp_f32 v83, v83\n"
  "s_waitcnt vmcnt(1) lgkmcnt(0)\n"
  "s_barrier\n"
  "v_mfma_f32_16x16x32_bf16 v[96:99], a[192:195], a[128:131], v[40:43]\n"
  "ds_read_b128 a[224:227], v18 offset:0\n"
  "v_min_f32 v87, v87, v175\n"
  "v_mfma_f32_16x16x32_bf16 v[100:103], a[192:195], a[144:147], v[44:47]\n"
  "s_add_u32 m0, s67, 0\n"
  "v_exp_f32 v84, v84\n"
  "v_mfma_f32_16x16x32_bf16 v[104:107], a[192:195], a[160:163], v[48:51]\n"
  "ds_read_b128 a[228:231], v18 offset:1024\n"
  "v_cvt_pk_bf16_f32 v131, v82, v83\n"
  "v_mfma_f32_16x16x32_bf16 v[108:111], a[192:195], a[176:179], v[52:55]\n"
  "v_min_f32 v88, v88, v176\n"
  "v_mfma_f32_16x16x32_bf16 v[96:99], a[196:199], a[132:135], v[96:99]\n"
  "ds_read_b128 a[232:235], v18 offset:2048\n"
  "buffer_load_dwordx4 %[L16], %[srdWS], s65 offen offset:0 sc1 lds\n"
  "v_mfma_f32_16x16x32_bf16 v[100:103], a[196:199], a[148:151], v[100:103]\n"
  "v_exp_f32 v85, v85\n"
  "v_mfma_f32_16x16x32_bf16 v[104:107], a[196:199], a[164:167], v[104:107]\n"
  "ds_read_b128 a[236:239], v18 offset:3072\n"
  "v_min_f32 v89, v89, v177\n"
  "v_mfma_f32_16x16x32_bf16 v[108:111], a[196:199], a[180:183], v[108:111]\n"
  "buffer_load_dwordx4 %[L16], %[srdWS], s65 offen offset:1024 sc1 lds\n"
  "v_exp_f32 v86, v86\n"
  "v_mfma_f32_16x16x32_bf16 v[96:99], a[200:203], a[136:139], v[96:99]\n"
  "ds_read_b128 a[240:243], v19 offset:0\n"
  "v_cvt_pk_bf16_f32 v134, v84, v85\n"
  "v_mfma_f32_16x16x32_bf16 v[100:103], a[200:203], a[152:155], v[100:103]\n"
  "v_min_f32 v90, v90, v178\n"
  "v_mfma_f32_16x16x32_bf16 v[104:107], a[200:203], a[168:171], v[104:107]\n"
  "ds_read_b128 a[244:247], v19 offset:1024\n"
  "buffer_load_dwordx4 %[L16], %[srdWS], s65 offen offset:2048 sc1 lds\n"
  "v_mfma_f32_16x16x32_bf16 v[108:111], a[200:203], a[184:187], v[108:111]\n"
  "v_exp_f32 v87, v87\n"
  "v_mfma_f32_16x16x32_bf16 v[96:99], a[204:207], a[140:143], v[96:99]\n"
  "ds_read_b128 a[248:251], v19 offset:2048\n"
  "v_min_f32 v91, v91, v179\n"
  "v_mfma_f32_16x16x32_bf16 v[100:103], a[204:207], a[156:159], v[100:103]\n"
  "buffer_load_dwordx4 %[L16], %[srdWS], s65 offen offset:3072 sc1 lds\n"
  "v_exp_f32 v88, v88\n"
  "v_mfma_f32_16x16x32_bf16 v[104:107], a[204:207], a[172:175], v[104:107]\n"
  "ds_read_b128 a[252:255], v19 offset:3072\n"
  "v_cvt_pk_bf16_f32 v135, v86, v87\n"
  "v_mfma_f32_16x16x32_bf16 v[108:111], a[204:207], a[188:191], v[108:111]\n"
  "v_min_f32 v92, v92, v180\n"
  "v_exp_f32 v89, v89\n"
  "v_mfma_f32_16x16x32_bf16 v[112:115], a[208:211], a[128:131], v[40:43]\n"
  "s_add_u32 m0, s67, 4096\n"
  "v_min_f32 v93, v93, v181\n"
  "v_exp_f32 v90, v90\n"
  "v_cvt_pk_bf16_f32 v138, v88, v89\n"
  "v_min_f32 v94, v94, v182\n"
  "v_exp_f32 v91, v91\n"
  "v_min_f32 v95, v95, v183\n"
  "v_exp_f32 v92, v92\n"
  "v_cvt_pk_bf16_f32 v139, v90, v91\n"
  "v_exp_f32 v93, v93\n"
  "v_exp_f32 v94, v94\n"
  "v_cvt_pk_bf16_f32 v142, v92, v93\n"
  "v_exp_f32 v95, v95\n"
  "s_nop 0\n"
  "v_cvt_pk_bf16_f32 v143, v94, v95\n"
  "v_mfma_f32_16x16x32_bf16 v[116:119], a[208:211], a[144:147], v[44:47]\n"
  "v_min_f32 v96, v96, v164\n"
  "v_min_f32 v97, v97, v165\n"
  "v_mfma_f32_16x16x32_bf16 v[120:123], a[208:211], a[160:163], v[48:51]\n"
  "v_min_f32 v98, v98, v166\n"
  "v_min_f32 v99, v99, v167\n"
  "v_mfma_f32_16x16x32_bf16 v[124:127], a[208:211], a[176:179], v[52:55]\n"
  "buffer_load_dwordx4 v16, %[srdWS], s65 offen offset:0 sc1 lds\n"
  "v_mfma_f32_16x16x32_bf16 v[112:115], a[212:215], a[132:135], v[112:115]\n"
  "v_exp_f32 v96, v96\n"
  "v_mfma_f32_16x16x32_bf16 v[116:119], a[212:215], a[148:151], v[116:119]\n"
  "v_min_f32 v100, v100, v168\n"
  "v_mfma_f32_16x16x32_bf16 v[120:123], a[212:215], a[164:167], v[120:123]\n"
  "buffer_load_dwordx4 v16, %[srdWS], s65 offen offset:1024 sc1 lds\n"
  "v_mfma_f32_16x16x32_bf16 v[124:127], a[212:215], a[180:183], v[124:127]\n"
  "v_exp_f32 v97, v97\n"
  "v_mfma_f32_16x16x32_bf16 v[112:115], a[216:219], a[136:139], v[112:115]\n"
  "v_min_f32 v101, v101, v169\n"
  "v_mfma_f32_16x16x32_bf16 v[116:119], a[216:219], a[152:155], v[116:119]\n"
  "buffer_load_dwordx4 v16, %[srdWS], s65 offen offset:2048 sc1 lds\n"
  "v_mfma_f32_16x16x32_bf16 v[120:123], a[216:219], a[168:171], v[120:123]\n"
  "v_exp_f32 v98, v98\n"
  "v_mfma_f32_16x16x32_bf16 v[124:127], a[216:219], a[184:187], v[124:127]\n"
  "v_cvt_pk_bf16_f32 v144, v96, v97\n"
  "v_min_f32 v102, v102, v170\n"
  "v_mfma_f32_16x16x32_bf16 v[112:115], a[220:223], a[140:143], v[112:115]\n"
  "buffer_load_dwordx4 v16, %[srdWS], s65 offen offset:3072 sc1 lds\n"
  "v_mfma_f32_16x16x32_bf16 v[116:119], a[220:223], a[156:159], v[116:119]\n"
  "v_exp_f32 v99, v99\n"
  "v_mfma_f32_16x16x32_bf16 v[120:123], a[220:223], a[172:175], v[120:123]\n"
  "v_min_f32 v103, v103, v171\n"
  "v_mfma_f32_16x16x32_bf16 v[124:127], a[220:223], a[188:191], v[124:127]\n"
  "s_add_u32 m0, s66, 0\n"
  "s_waitcnt lgkmcnt(0)\n"
  "v_mfma_f32_16x16x32_bf16 v[192:195], v[208:211], v[128:131], v[192:195]\n"
  "v_exp_f32 v100, v100\n"
  "v_mfma_f32_16x16x32_bf16 v[196:199], v[208:211], v[132:135], v[196:199]\n"
  "ds_read_b128 a[192:195], v17 offset:0\n"
  "v_cvt_pk_bf16_f32 v145, v98, v99\n"
  "v_mfma_f32_16x16x32_bf16 v[200:203], v[208:211], v[136:139], v[200:203]\n"
  "ds_read_b128 a[128:131], v20 offset:0\n"
  "buffer_load_dwordx4 %[L16], %[srdWS], s64 offen offset:0 sc1 lds\n"
  "v_mfma_f32_16x16x32_bf16 v[204:207], v[208:211], v[140:143], v[204:207]\n"
  "ds_read_b128 a[132:135], v20 offset:1024\n"
  "v_min_f32 v104, v104, v172\n"
  "v_mfma_f32_16x16x32_bf16 a[0:3], a[224:227], v[128:131], a[0:3]\n"
  "ds_read_b128 a[196:199], v17 offset:1024\n"
  "v_exp_f32 v101, v101\n"
  "v_mfma_f32_16x16x32_bf16 a[4:7], a[224:227], v[132:135], a[4:7]\n"
  "ds_read_b128 a[136:139], v20 offset:2048\n"
  "buffer_load_dwordx4 %[L16], %[srdWS], s64 offen offset:1024 sc1 lds\n"
  "v_mfma_f32_16x16x32_bf16 a[8:11], a[224:227], v[136:139], a[8:11]\n"
  "v_min_f32 v105, v105, v173\n"
  "v_exp_f32 v102, v102\n"
  "v_mfma_f32_16x16x32_bf16 a[12:15], a[224:227], v[140:143], a[12:15]\n"
  "ds_read_b128 a[200:203], v17 offset:2048\n"
  "ds_read_b128 a[140:143], v20 offset:3072\n"
  "v_mfma_f32_16x16x32_bf16 a[16:19], a[228:231], v[128:131], a[16:19]\n"
  "buffer_load_dwordx4 %[L16], %[srdWS], s64 offen offset:2048 sc1 lds\n"
  "v_cvt_pk_bf16_f32 v148, v100, v101\n"
  "v_mfma_f32_16x16x32_bf16 a[20:23], a[228:231], v[132:135], a[20:23]\n"
  "ds_read_b128 a[144:147], v20 offset:4096\n"
  "v_min_f32 v106, v106, v174\n"
  "v_mfma_f32_16x16x32_bf16 a[24:27], a[228:231], v[136:139], a[24:27]\n"
  "ds_read_b128 a[204:207], v17 offset:3072\n"
  "v_exp_f32 v103, v103\n"
  "v_mfma_f32_16x16x32_bf16 a[28:31], a[228:231], v[140:143], a[28:31]\n"
  "ds_read_b128 a[148:151], v20 offset:5120\n"
  "buffer_load_dwordx4 %[L16], %[srdWS], s64 offen offset:3072 sc1 lds\n"
  "v_mfma_f32_16x16x32_bf16 a[32:35], a[232:235], v[128:131], a[32:35]\n"
  "v_min_f32 v107, v107, v175\n"
  "v_exp_f32 v104, v104\n"
  "v_mfma_f32_16x16x32_bf16 a[36:39], a[232:235], v[132:135], a[36:39]\n"
  "ds_read_b128 a[208:211], v17 offset:4096\n"
  "ds_read_b128 a[152:155], v20 offset:6144\n"
  "v_mfma_f32_16x16x32_bf16 a[40:43], a[232:235], v[136:139], a[40:43]\n"
  "s_add_u32 m0, s66, 4096\n"
  "v_cvt_pk_bf16_f32 v149, v102, v103\n"
  "v_mfma_f32_16x16x32_bf16 a[44:47], a[232:235], v[140:143], a[44:47]\n"
  "ds_read_b128 a[156:159], v20 offset:7168\n"
  "v_min_f32 v108, v108, v176\n"
  "v_mfma_f32_16x16x32_bf16 a[48:51], a[236:239], v[128:131], a[48:51]\n"
  "ds_read_b128 a[212:215], v17 offset:5120\n"
  "v_exp_f32 v105, v105\n"
  "v_mfma_f32_16x16x32_bf16 a[52:55], a[236:239], v[132:135], a[52:55]\n"
  "ds_read_b128 a[160:163], v20 offset:8192\n"
  "buffer_load_dwordx4 v16, %[srdWS], s64 offen offset:0 sc1 lds\n"
  "v_mfma_f32_16x16x32_bf16 a[56:59], a[236:239], v[136:139], a[56:59]\n"
  "ds_read_b128 a[164:167], v20 offset:9216\n"
  "v_min_f32 v109, v109, v177\n"
  "v_mfma_f32_16x16x32_bf16 a[60:63], a[236:239], v[140:143], a[60:63]\n"
  "ds_read_b128 a[216:219], v17 offset:6144\n"
  "v_exp_f32 v106, v106\n"
  "v_mfma_f32_16x16x32_bf16 a[64:67], a[240:243], v[128:131], a[64:67]\n"
  "ds_read_b128 a[168:171], v20 offset:10240\n"
  "buffer_load_dwordx4 v16, %[srdWS], s64 offen offset:1024 sc1 lds\n"
  "v_mfma_f32_16x16x32_bf16 a[68:71], a[240:243], v[132:135], a[68:71]\n"
  "v_cvt_pk_bf16_f32 v152, v104, v105\n"
  "v_min_f32 v110, v110, v178\n"
  "v_mfma_f32_16x16x32_bf16 a[72:75], a[240:243], v[136:139], a[72:75]\n"
  "ds_read_b128 a[220:223], v17 offset:7168\n"
  "ds_read_b128 a[172:175], v20 offset:11264\n"
  "v_mfma_f32_16x16x32_bf16 a[76:79], a[240:243], v[140:143], a[76:79]\n"
  "buffer_load_dwordx4 v16, %[srdWS], s64 offen offset:2048 sc1 lds\n"
  "v_exp_f32 v107, v107\n"
  "v_mfma_f32_16x16x32_bf16 a[80:83], a[244:247], v[128:131], a[80:83]\n"
  "ds_read_b128 a[176:179], v20 offset:12288\n"
  "v_min_f32 v111, v111, v179\n"
  "v_mfma_f32_16x16x32_bf16 a[84:87], a[244:247], v[132:135], a[84:87]\n"
  "v_exp_f32 v108, v108\n"
  "v_mfma_f32_16x16x32_bf16 a[88:91], a[244:247], v[136:139], a[88:91]\n"
  "ds_read_b128 a[180:183], v20 offset:13312\n"
  "buffer_load_dwordx4 v16, %[srdWS], s64 offen offset:3072 sc1 lds\n"
  "v_mfma_f32_16x16x32_bf16 a[92:95], a[244:247], v[140:143], a[92:95]\n"
  "v_cvt_pk_bf16_f32 v153, v106, v107\n"
  "v_mfma_f32_16x16x32_bf16 a[96:99], a[248:251], v[128:131], a[96:99]\n"
  "ds_read_b128 a[184:187], v20 offset:14336\n"
  "v_mfma_f32_16x16x32_bf16 a[100:103], a[248:251], v[132:135], a[100:103]\n"
  "v_exp_f32 v109, v109\n"
  "v_mfma_f32_16x16x32_bf16 a[104:107], a[248:251], v[136:139], a[104:107]\n"
  "buffer_load_dword v212, v214, %[srdWot], s79 offen\n"
  "ds_read_b128 a[188:191], v20 offset:15360\n"
  "v_mfma_f32_16x16x32_bf16 a[108:111], a[248:251], v[140:143], a[108:111]\n"
  "s_xor_b32 s66, s66, 0x8000\n"
  "v_exp_f32 v110, v110\n"
  "v_mfma_f32_16x16x32_bf16 a[112:115], a[252:255], v[128:131], a[112:115]\n"
  "s_xor_b32 s67, s67, 0x8000\n"
  "v_cvt_pk_bf16_f32 v156, v108, v109\n"
  "v_mfma_f32_16x16x32_bf16 a[116:119], a[252:255], v[132:135], a[116:119]\n"
  "s_add_u32 s79, s79, 0x400\n"
  "v_mfma_f32_16x16x32_bf16 a[120:123], a[252:255], v[136:139], a[120:123]\n"
  "v_exp_f32 v111, v111\n"
  "v_mfma_f32_16x16x32_bf16 a[124:127], a[252:255], v[140:143], a[124:127]\n"
  "v_min_f32 v112, v112, v160\n"
  "v_min_f32 v113, v113, v161\n"
  "v_min_f32 v114, v114, v162\n"
  "v_min_f32 v115, v115, v163\n"
  "v_exp_f32 v112, v112\n"
  "v_cvt_pk_bf16_f32 v157, v110, v111\n"
  "v_min_f32 v116, v116, v164\n"
  "v_exp_f32 v113, v113\n"
  "v_min_f32 v117, v117, v165\n"
  "v_exp_f32 v114, v114\n"
  "v_cvt_pk_bf16_f32 v146, v112, v113\n"
  "v_min_f32 v118, v118, v166\n"
  "v_exp_f32 v115, v115\n"
  "v_min_f32 v119, v119, v167\n"
  "v_exp_f32 v116, v116\n"
  "v_cvt_pk_bf16_f32 v147, v114, v115\n"
  "v_min_f32 v120, v120, v168\n"
  "v_exp_f32 v117, v117\n"
  "v_min_f32 v121, v121, v169\n"
  "v_exp_f32 v118, v118\n"
  "v_cvt_pk_bf16_f32 v150, v116, v117\n"
  "v_min_f32 v122, v122, v170\n"
  "v_exp_f32 v119, v119\n"
  "v_min_f32 v123, v123, v171\n"
  "v_exp_f32 v120, v120\n"
  "v_cvt_pk_bf16_f32 v151, v118, v119\n"
  "v_min_f32 v124, v124, v172\n"
  "v_exp_f32 v121, v121\n"
  "v_min_f32 v125, v125, v173\n"
  "s_branch ATtail_%=\n"
  "ATonly_%=:\n"
  "ds_read_b128 a[192:195], v17 offset:8192\n"
  "ds_read_b128 a[196:199], v17 offset:9216\n"
  "ds_read_b128 a[200:203], v17 offset:10240\n"
  "ds_read_b128 a[204:207], v17 offset:11264\n"
  "ds_read_b128 a[208:211], v17 offset:12288\n"
  "ds_read_b128 a[212:215], v17 offset:13312\n"
  "ds_read_b128 a[216:219], v17 offset:14336\n"
  "ds_read_b128 a[220:223], v17 offset:15360\n"
  "v_xor_b32 v17, 0x8000, v17\n"
  "v_mov_b32 v208, 0x3f803f80\n"
  "v_mov_b32 v209, 0x3f803f80\n"
  "v_mov_b32 v210, 0x3f803f80\n"
  "v_mov_b32 v211, 0x3f803f80\n"
  "v_and_b32 v214, 0x70, %[L16]\n"
  "v_lshlrev_b32 v214, 3, v214\n"
  "v_min_f32 v64, v64, v172\n"
  "v_min_f32 v65, v65, v173\n"
  "v_min_f32 v66, v66, v174\n"
  "v_min_f32 v67, v67, v175\n"
  "v_min_f32 v68, v68, v176\n"
  "v_min_f32 v69, v69, v177\n"
  "v_min_f32 v70, v70, v178\n"
  "v_min_f32 v71, v71, v179\n"
  "v_min_f32 v72, v72, v180\n"
  "v_min_f32 v73, v73, v181\n"
  "v_min_f32 v74, v74, v182\n"
  "v_min_f32 v75, v75, v183\n"
  "v_min_f32 v76, v76, v184\n"
  "v_min_f32 v77, v77, v185\n"
  "v_min_f32 v78, v78, v186\n"
  "v_min_f32 v79, v79, v187\n"
  "v_min_f32 v80, v80, v168\n"
  "v_min_f32 v81, v81, v169\n"
  "v_min_f32 v82, v82, v170\n"
  "v_min_f32 v83, v83, v171\n"
  "v_min_f32 v84, v84, v172\n"
  "v_min_f32 v85, v85, v173\n"
  "v_min_f32 v86, v86, v174\n"
  "v_min_f32 v87, v87, v175\n"
  "v_min_f32 v88, v88, v176\n"
  "v_min_f32 v89, v89, v177\n"
  "v_min_f32 v90, v90, v178\n"
  "v_min_f32 v91, v91, v179\n"
  "v_min_f32 v92, v92, v180\n"
  "v_min_f32 v93, v93, v181\n"
  "v_min_f32 v94, v94, v182\n"
  "v_min_f32 v95, v95, v183\n"
  "v_max3_f32 v28, v64, v65, v66\n"
  "v_max3_f32 v32, v67, v80, v81\n"
  "v_max3_f32 v28, v28, v82, v83\n"
  "v_max3_f32 v29, v68, v69, v70\n"
  "v_max3_f32 v33, v71, v84, v85\n"
  "v_max3_f32 v29, v29, v86, v87\n"
  "v_max3_f32 v30, v72, v73, v74\n"
  "v_max3_f32 v34, v75, v88, v89\n"
  "v_max3_f32 v30, v30, v90, v91\n"
  "v_max3_f32 v31, v76, v77, v78\n"
  "v_max3_f32 v35, v79, v92, v93\n"
  "v_max3_f32 v31, v31, v94, v95\n"
  "v_max3_f32 v28, v28, v32, v23\n"
  "v_max3_f32 v29, v29, v33, v23\n"
  "v_max3_f32 v30, v30, v34, v23\n"
  "v_max3_f32 v31, v31, v35, v23\n"
  "v_mov_b32 v32, v28\n"
  "v_mov_b32 v33, v29\n"
  "v_mov_b32 v34, v30\n"
  "v_mov_b32 v35, v31\n"
  "v_permlane16_swap_b32 v28, v32\n"
  "v_permlane16_swap_b32 v29, v33\n"
  "v_permlane16_swap_b32 v30, v34\n"
  "v_permlane16_swap_b32 v31, v35\n"
  "v_max_f32 v28, v28, v32\n"
  "v_max_f32 v29, v29, v33\n"
  "v_max_f32 v30, v30, v34\n"
  "v_max_f32 v31, v31, v35\n"
  "v_mov_b32 v32, v28\n"
  "v_mov_b32 v33, v29\n"
  "v_mov_b32 v34, v30\n"
  "v_mov_b32 v35, v31\n"
  "v_permlane32_swap_b32 v28, v32\n"
  "v_permlane32_swap_b32 v29, v33\n"
  "v_permlane32_swap_b32 v30, v34\n"
  "v_permlane32_swap_b32 v31, v35\n"
  "v_max_f32 v28, v28, v32\n"
  "v_max_f32 v29, v29, v33\n"
  "v_max_f32 v30, v30, v34\n"
  "v_max_f32 v31, v31, v35\n"
  "v_sub_f32 v40, 0, v28\n"
  "v_sub_f32 v41, 0, v28\n"
  "v_sub_f32 v42, 0, v28\n"
  "v_sub_f32 v43, 0, v28\n"
  "v_sub_f32 v44, 0, v29\n"
  "v_sub_f32 v45, 0, v29\n"
  "v_sub_f32 v46, 0, v29\n"
  "v_sub_f32 v47, 0, v29\n"
  "v_sub_f32 v48, 0, v30\n"
  "v_sub_f32 v49, 0, v30\n"
  "v_sub_f32 v50, 0, v30\n"
  "v_sub_f32 v51, 0, v30\n"
  "v_sub_f32 v52, 0, v31\n"
  "v_sub_f32 v53, 0, v31\n"
  "v_sub_f32 v54, 0, v31\n"
  "v_sub_f32 v55, 0, v31\n"
  "v_add_f32 v64, v64, v40\n"
  "v_add_f32 v65, v65, v40\n"
  "v_add_f32 v66, v66, v40\n"
  "v_add_f32 v67, v67, v40\n"
  "v_exp_f32 v64, v64\n"
  "v_add_f32 v68, v68, v44\n"
  "v_exp_f32 v65, v65\n"
  "v_add_f32 v69, v69, v44\n"
  "v_exp_f32 v66, v66\n"
  "v_cvt_pk_bf16_f32 v128, v64, v65\n"
  "v_add_f32 v70, v70, v44\n"
  "v_exp_f32 v67, v67\n"
  "v_add_f32 v71, v71, v44\n"
  "v_exp_f32 v68, v68\n"
  "v_cvt_pk_bf16_f32 v129, v66, v67\n"
  "v_add_f32 v72, v72, v48\n"
  "v_exp_f32 v69, v69\n"
  "v_add_f32 v73, v73, v48\n"
  "v_exp_f32 v70, v70\n"
  "v_cvt_pk_bf16_f32 v132, v68, v69\n"
  "s_waitcnt vmcnt(0) lgkmcnt(0)\n"
  "s_barrier\n"
  "v_mfma_f32_16x16x32_bf16 v[96:99], a[192:195], a[128:131], v[40:43]\n"
  "ds_read_b128 a[224:227], v18 offset:0\n"
  "v_add_f32 v74, v74, v48\n"
  "v_mfma_f32_16x16x32_bf16 v[100:103], a[192:195], a[144:147], v[44:47]\n"
  "v_exp_f32 v71, v71\n"
  "v_mfma_f32_16x16x32_bf16 v[104:107], a[192:195], a[160:163], v[48:51]\n"
  "ds_read_b128 a[228:231], v18 offset:1024\n"
  "v_add_f32 v75, v75, v48\n"
  "v_mfma_f32_16x16x32_bf16 v[108:111], a[192:195], a[176:179], v[52:55]\n"
  "v_exp_f32 v72, v72\n"
  "v_mfma_f32_16x16x32_bf16 v[96:99], a[196:199], a[132:135], v[96:99]\n"
  "ds_read_b128 a[232:235], v18 offset:2048\n"
  "v_cvt_pk_bf16_f32 v133, v70, v71\n"
  "v_mfma_f32_16x16x32_bf16 v[100:103], a[196:199], a[148:151], v[100:103]\n"
  "v_add_f32 v76, v76, v52\n"
  "v_mfma_f32_16x16x32_bf16 v[104:107], a[196:199], a[164:167], v[104:107]\n"
  "ds_read_b128 a[236:239], v18 offset:3072\n"
  "v_mfma_f32_16x16x32_bf16 v[108:111], a[196:199], a[180:183], v[108:111]\n"
  "v_exp_f32 v73, v73\n"
  "v_mfma_f32_16x16x32_bf16 v[96:99], a[200:203], a[136:139], v[96:99]\n"
  "ds_read_b128 a[240:243], v19 offset:0\n"
  "v_add_f32 v77, v77, v52\n"
  "v_mfma_f32_16x16x32_bf16 v[100:103], a[200:203], a[152:155], v[100:103]\n"
  "v_exp_f32 v74, v74\n"
  "v_mfma_f32_16x16x32_bf16 v[104:107], a[200:203], a[168:171], v[104:107]\n"
  "ds_read_b128 a[244:247], v19 offset:1024\n"
  "v_cvt_pk_bf16_f32 v136, v72, v73\n"
  "v_mfma_f32_16x16x32_bf16 v[108:111], a[200:203], a[184:187], v[108:111]\n"
  "v_add_f32 v78, v78, v52\n"
  "v_mfma_f32_16x16x32_bf16 v[96:99], a[204:207], a[140:143], v[96:99]\n"
  "ds_read_b128 a[248:251], v19 offset:2048\n"
  "v_mfma_f32_16x16x32_bf16 v[100:103], a[204:207], a[156:159], v[100:103]\n"
  "v_exp_f32 v75, v75\n"
  "v_mfma_f32_16x16x32_bf16 v[104:107], a[204:207], a[172:175], v[104:107]\n"
  "ds_read_b128 a[252:255], v19 offset:3072\n"
  "v_add_f32 v79, v79, v52\n"
  "v_mfma_f32_16x16x32_bf16 v[108:111], a[204:207], a[188:191], v[108:111]\n"
  "v_exp_f32 v76, v76\n"
  "v_mfma_f32_16x16x32_bf16 v[112:115], a[208:211], a[128:131], v[40:43]\n"
  "v_cvt_pk_bf16_f32 v137, v74, v75\n"
  "v_mfma_f32_16x16x32_bf16 v[116:119], a[208:211], a[144:147], v[44:47]\n"
  "v_exp_f32 v77, v77\n"
  "v_mfma_f32_16x16x32_bf16 v[120:123], a[208:211], a[160:163], v[48:51]\n"
  "v_exp_f32 v78, v78\n"
  "v_mfma_f32_16x16x32_bf16 v[124:127], a[208:211], a[176:179], v[52:55]\n"
  "v_cvt_pk_bf16_f32 v140, v76, v77\n"
  "v_mfma_f32_16x16x32_bf16 v[112:115], a[212:215], a[132:135], v[112:115]\n"
  "v_exp_f32 v79, v79\n"
  "v_mfma_f32_16x16x32_bf16 v[116:119], a[212:215], a[148:151], v[116:119]\n"
  "v_add_f32 v80, v80, v40\n"
  "v_add_f32 v81, v81, v40\n"
  "v_mfma_f32_16x16x32_bf16 v[120:123], a[212:215], a[164:167], v[120:123]\n"
  "v_add_f32 v82, v82, v40\n"
  "v_add_f32 v83, v83, v40\n"
  "v_mfma_f32_16x16x32_bf16 v[124:127], a[212:215], a[180:183], v[124:127]\n"
  "v_exp_f32 v80, v80\n"
  "v_mfma_f32_16x16x32_bf16 v[112:115], a[216:219], a[136:139], v[112:115]\n"
  "v_cvt_pk_bf16_f32 v141, v78, v79\n"
  "v_add_f32 v84, v84, v44\n"
  "v_mfma_f32_16x16x32_bf16 v[116:119], a[216:219], a[152:155], v[116:119]\n"
  "v_exp_f32 v81, v81\n"
  "v_mfma_f32_16x16x32_bf16 v[120:123], a[216:219], a[168:171], v[120:123]\n"
  "v_add_f32 v85, v85, v44\n"
  "v_mfma_f32_16x16x32_bf16 v[124:127], a[216:219], a[184:187], v[124:127]\n"
  "v_exp_f32 v82, v82\n"
  "v_mfma_f32_16x16x32_bf16 v[112:115], a[220:223], a[140:143], v[112:115]\n"
  "v_cvt_pk_bf16_f32 v130, v80, v81\n"
  "v_add_f32 v86, v86, v44\n"
  "v_mfma_f32_16x16x32_bf16 v[116:119], a[220:223], a[156:159], v[116:119]\n"
  "v_exp_f32 v83, v83\n"
  "v_mfma_f32_16x16x32_bf16 v[120:123], a[220:223], a[172:175], v[120:123]\n"
  "v_add_f32 v87, v87, v44\n"
  "v_exp_f32 v84, v84\n"
  "v_cvt_pk_bf16_f32 v131, v82, v83\n"
  "v_add_f32 v88, v88, v48\n"
  "v_exp_f32 v85, v85\n"
  "v_add_f32 v89, v89, v48\n"
  "v_exp_f32 v86, v86\n"
  "v_cvt_pk_bf16_f32 v134, v84, v85\n"
  "v_add_f32 v90, v90, v48\n"
  "v_exp_f32 v87, v87\n"
  "v_add_f32 v91, v91, v48\n"
  "v_exp_f32 v88, v88\n"
  "v_cvt_pk_bf16_f32 v135, v86, v87\n"
  "v_add_f32 v92, v92, v52\n"
  "v_exp_f32 v89, v89\n"
  "v_add_f32 v93, v93, v52\n"
  "v_exp_f32 v90, v90\n"
  "v_cvt_pk_bf16_f32 v138, v88, v89\n"
  "v_add_f32 v94, v94, v52\n"
  "v_exp_f32 v91, v91\n"
  "v_add_f32 v95, v95, v52\n"
  "v_exp_f32 v92, v92\n"
  "v_cvt_pk_bf16_f32 v139, v90, v91\n"
  "v_exp_f32 v93, v93\n"
  "v_exp_f32 v94, v94\n"
  "v_cvt_pk_bf16_f32 v142, v92, v93\n"
  "v_exp_f32 v95, v95\n"
  "s_nop 0\n"
  "v_cvt_pk_bf16_f32 v143, v94, v95\n"
  "v_mfma_f32_16x16x32_bf16 v[124:127], a[220:223], a[188:191], v[124:127]\n"
  "s_waitcnt lgkmcnt(0)\n"
  "v_mfma_f32_16x16x32_bf16 v[192:195], v[208:211], v[128:131], 0\n"
  "v_min_f32 v96, v96, v164\n"
  "v_min_f32 v97, v97, v165\n"
  "v_mfma_f32_16x16x32_bf16 v[196:199], v[208:211], v[132:135], 0\n"
  "v_min_f32 v98, v98, v166\n"
  "v_min_f32 v99, v99, v167\n"
  "v_mfma_f32_16x16x32_bf16 v[200:203], v[208:211], v[136:139], 0\n"
  "v_exp_f32 v96, v96\n"
  "v_mfma_f32_16x16x32_bf16 v[204:207], v[208:211], v[140:143], 0\n"
  "v_min_f32 v100, v100, v168\n"
  "v_mfma_f32_16x16x32_bf16 a[0:3], a[224:227], v[128:131], 0\n"
  "v_exp_f32 v97, v97\n"
  "v_mfma_f32_16x16x32_bf16 a[4:7], a[224:227], v[132:135], 0\n"
  "v_min_f32 v101, v101, v169\n"
  "v_mfma_f32_16x16x32_bf16 a[8:11], a[224:227], v[136:139], 0\n"
  "v_exp_f32 v98, v98\n"
  "v_mfma_f32_16x16x32_bf16 a[12:15], a[224:227], v[140:143], 0\n"
  "v_cvt_pk_bf16_f32 v144, v96, v97\n"
  "v_min_f32 v102, v102, v170\n"
  "v_mfma_f32_16x16x32_bf16 a[16:19], a[228:231], v[128:131], 0\n"
  "v_exp_f32 v99, v99\n"
  "v_mfma_f32_16x16x32_bf16 a[20:23], a[228:231], v[132:135], 0\n"
  "v_min_f32 v103, v103, v171\n"
  "v_mfma_f32_16x16x32_bf16 a[24:27], a[228:231], v[136:139], 0\n"
  "v_exp_f32 v100, v100\n"
  "v_mfma_f32_16x16x32_bf16 a[28:31], a[228:231], v[140:143], 0\n"
  "v_cvt_pk_bf16_f32 v145, v98, v99\n"
  "v_min_f32 v104, v104, v172\n"
  "v_mfma_f32_16x16x32_bf16 a[32:35], a[232:235], v[128:131], 0\n"
  "v_exp_f32 v101, v101\n"
  "v_mfma_f32_16x16x32_bf16 a[36:39], a[232:235], v[132:135], 0\n"
  "v_min_f32 v105, v105, v173\n"
  "v_mfma_f32_16x16x32_bf16 a[40:43], a[232:235], v[136:139], 0\n"
  "v_exp_f32 v102, v102\n"
  "v_mfma_f32_16x16x32_bf16 a[44:47], a[232:235], v[140:143], 0\n"
  "v_cvt_pk_bf16_f32 v148, v100, v101\n"
  "v_min_f32 v106, v106, v174\n"
  "v_mfma_f32_16x16x32_bf16 a[48:51], a[236:239], v[128:131], 0\n"
  "v_exp_f32 v103, v103\n"
  "v_mfma_f32_16x16x32_bf16 a[52:55], a[236:239], v[132:135], 0\n"
  "v_min_f32 v107, v107, v175\n"
  "v_mfma_f32_16x16x32_bf16 a[56:59], a[236:239], v[136:139], 0\n"
  "v_exp_f32 v104, v104\n"
  "v_mfma_f32_16x16x32_bf16 a[60:63], a[236:239], v[140:143], 0\n"
  "v_cvt_pk_bf16_f32 v149, v102, v103\n"
  "v_min_f32 v108, v108, v176\n"
  "v_mfma_f32_16x16x32_bf16 a[64:67], a[240:243], v[128:131], 0\n"
  "v_exp_f32 v105, v105\n"
  "v_mfma_f32_16x16x32_bf16 a[68:71], a[240:243], v[132:135], 0\n"
  "v_min_f32 v109, v109, v177\n"
  "v_mfma_f32_16x16x32_bf16 a[72:75], a[240:243], v[136:139], 0\n"
  "v_exp_f32 v106, v106\n"
  "v_mfma_f32_16x16x32_bf16 a[76:79], a[240:243], v[140:143], 0\n"
  "v_cvt_pk_bf16_f32 v152, v104, v105\n"
  "v_min_f32 v110, v110, v178\n"
  "v_mfma_f32_16x16x32_bf16 a[80:83], a[244:247], v[128:131], 0\n"
  "v_exp_f32 v107, v107\n"
  "v_mfma_f32_16x16x32_bf16 a[84:87], a[244:247], v[132:135], 0\n"
  "v_min_f32 v111, v111, v179\n"
  "v_mfma_f32_16x16x32_bf16 a[88:91], a[244:247], v[136:139], 0\n"
  "v_exp_f32 v108, v108\n"
  "v_mfma_f32_16x16x32_bf16 a[92:95], a[244:247], v[140:143], 0\n"
  "v_cvt_pk_bf16_f32 v153, v106, v107\n"
  "v_mfma_f32_16x16x32_bf16 a[96:99], a[248:251], v[128:131], 0\n"
  "v_exp_f32 v109, v109\n"
  "v_mfma_f32_16x16x32_bf16 a[100:103], a[248:251], v[132:135], 0\n"
  "v_exp_f32 v110, v110\n"
  "v_mfma_f32_16x16x32_bf16 a[104:107], a[248:251], v[136:139], 0\n"
  "v_cvt_pk_bf16_f32 v156, v108, v109\n"
  "v_mfma_f32_16x16x32_bf16 a[108:111], a[248:251], v[140:143], 0\n"
  "v_exp_f32 v111, v111\n"
  "v_mfma_f32_16x16x32_bf16 a[112:115], a[252:255], v[128:131], 0\n"
  "v_min_f32 v112, v112, v160\n"
  "v_min_f32 v113, v113, v161\n"
  "v_mfma_f32_16x16x32_bf16 a[116:119], a[252:255], v[132:135], 0\n"
  "v_min_f32 v114, v114, v162\n"
  "v_min_f32 v115, v115, v163\n"
  "v_mfma_f32_16x16x32_bf16 a[120:123], a[252:255], v[136:139], 0\n"
  "v_exp_f32 v112, v112\n"
  "v_mfma_f32_16x16x32_bf16 a[124:127], a[252:255], v[140:143], 0\n"
  "v_cvt_pk_bf16_f32 v157, v110, v111\n"
  "v_min_f32 v116, v116, v164\n"
  "v_exp_f32 v113, v113\n"
  "v_min_f32 v117, v117, v165\n"
  "v_exp_f32 v114, v114\n"
  "v_cvt_pk_bf16_f32 v146, v112, v113\n"
  "v_min_f32 v118, v118, v166\n"
  "v_exp_f32 v115, v115\n"
  "v_min_f32 v119, v119, v167\n"
  "v_exp_f32 v116, v116\n"
  "v_cvt_pk_bf16_f32 v147, v114, v115\n"
  "v_min_f32 v120, v120, v168\n"
  "v_exp_f32 v117, v117\n"
  "v_min_f32 v121, v121, v169\n"
  "v_exp_f32 v118, v118\n"
  "v_cvt_pk_bf16_f32 v150, v116, v117\n"
  "v_min_f32 v122, v122, v170\n"
  "v_exp_f32 v119, v119\n"
  "v_min_f32 v123, v123, v171\n"
  "v_exp_f32 v120, v120\n"
  "v_cvt_pk_bf16_f32 v151, v118, v119\n"
  "v_min_f32 v124, v124, v172\n"
  "v_exp_f32 v121, v121\n"
  "v_min_f32 v125, v125, v173\n"
  "ATtail_%=:\n"
  "ds_read_b128 a[224:227], v18 offset:8192\n"
  "ds_read_b128 a[228:231], v18 offset:9216\n"
  "ds_read_b128 a[232:235], v18 offset:10240\n"
  "ds_read_b128 a[236:239], v18 offset:11264\n"
  "ds_read_b128 a[240:243], v19 offset:8192\n"
  "ds_read_b128 a[244:247], v19 offset:9216\n"
  "ds_read_b128 a[248:251], v19 offset:10240\n"
  "ds_read_b128 a[252:255], v19 offset:11264\n"
  "buffer_load_dwordx2 v[190:191], %[pollH], %[srdWS], 0 offen sc1\n"
  "v_exp_f32 v122, v122\n"
  "v_cvt_pk_bf16_f32 v154, v120, v121\n"
  "v_min_f32 v126, v126, v174\n"
  "v_exp_f32 v123, v123\n"
  "v_min_f32 v127, v127, v175\n"
  "v_exp_f32 v124, v124\n"
  "v_cvt_pk_bf16_f32 v155, v122, v123\n"
  "v_exp_f32 v125, v125\n"
  "v_exp_f32 v126, v126\n"
  "v_cvt_pk_bf16_f32 v158, v124, v125\n"
  "v_exp_f32 v127, v127\n"
  "s_nop 0\n"
  "v_cvt_pk_bf16_f32 v159, v126, v127\n"
  "v_and_b32 v21, 0x18000, v18\n"
  "v_add_u32 v21, v21, %[L16]\n"
  "s_and_b32 s77, %[wid], 1\n"
  "s_lshl_b32 s77, s77, 14\n"
  "v_add_u32 v21, s77, v21\n"
  "s_cmp_lt_u32 %[wid], 2\n"
  "s_cselect_b64 vcc, -1, 0\n"
  "v_cndmask_b32 v22, v21, v20, vcc\n"
  "v_cndmask_b32 v21, v20, v21, vcc\n"
  "s_waitcnt lgkmcnt(0)\n"
  "s_barrier\n"
  "v_mfma_f32_16x16x32_bf16 v[192:195], v[208:211], v[144:147], v[192:195]\n"
  "s_lshr_b32 s77, s77, 3\n"
  "v_add_u32 v36, s77, v17\n"
  "v_mfma_f32_16x16x32_bf16 v[196:199], v[208:211], v[148:151], v[196:199]\n"
  "v_xor_b32 v37, 0x4000, v36\n"
  "ds_write_b128 v36, v[28:31] offset:1024\n"
  "v_mfma_f32_16x16x32_bf16 v[200:203], v[208:211], v[152:155], v[200:203]\n"
  "v_mfma_f32_16x16x32_bf16 v[204:207], v[208:211], v[156:159], v[204:207]\n"
  "v_mfma_f32_16x16x32_bf16 a[0:3], a[224:227], v[144:147], a[0:3]\n"
  "v_mfma_f32_16x16x32_bf16 a[4:7], a[224:227], v[148:151], a[4:7]\n"
  "v_mfma_f32_16x16x32_bf16 a[8:11], a[224:227], v[152:155], a[8:11]\n"
  "v_mfma_f32_16x16x32_bf16 a[12:15], a[224:227], v[156:159], a[12:15]\n"
  "v_mfma_f32_16x16x32_bf16 a[16:19], a[228:231], v[144:147], a[16:19]\n"
  "v_mov_b32 v24, v192\n"
  "v_mov_b32 v25, v196\n"
  "v_mfma_f32_16x16x32_bf16 a[20:23], a[228:231], v[148:151], a[20:23]\n"
  "v_mov_b32 v26, v200\n"
  "v_mfma_f32_16x16x32_bf16 a[24:27], a[228:231], v[152:155], a[24:27]\n"
  "v_mov_b32 v27, v204\n"
  "v_mfma_f32_16x16x32_bf16 a[28:31], a[228:231], v[156:159], a[28:31]\n"
  "ds_write_b128 v36, v[24:27]\n"
  "v_mfma_f32_16x16x32_bf16 a[32:35], a[232:235], v[144:147], a[32:35]\n"
  "ds_write_b128 v21, a[0:3] offset:0\n"
  "v_mfma_f32_16x16x32_bf16 a[36:39], a[232:235], v[148:151], a[36:39]\n"
  "ds_write_b128 v21, a[4:7] offset:1024\n"
  "v_mfma_f32_16x16x32_bf16 a[40:43], a[232:235], v[152:155], a[40:43]\n"
  "v_mfma_f32_16x16x32_bf16 a[44:47], a[232:235], v[156:159], a[44:47]\n"
  "ds_write_b128 v21, a[8:11] offset:2048\n"
  "v_mfma_f32_16x16x32_bf16 a[48:51], a[236:239], v[144:147], a[48:51]\n"
  "ds_write_b128 v21, a[12:15] offset:3072\n"
  "v_mfma_f32_16x16x32_bf16 a[52:55], a[236:239], v[148:151], a[52:55]\n"
  "v_mfma_f32_16x16x32_bf16 a[56:59], a[236:239], v[152:155], a[56:59]\n"
  "ds_write_b128 v21, a[16:19] offset:4096\n"
  "v_mfma_f32_16x16x32_bf16 a[60:63], a[236:239], v[156:159], a[60:63]\n"
  "ds_write_b128 v21, a[20:23] offset:5120\n"
  "v_mfma_f32_16x16x32_bf16 a[64:67], a[240:243], v[144:147], a[64:67]\n"
  "v_mfma_f32_16x16x32_bf16 a[68:71], a[240:243], v[148:151], a[68:71]\n"
  "ds_write_b128 v21, a[24:27] offset:6144\n"
  "v_mfma_f32_16x16x32_bf16 a[72:75], a[240:243], v[152:155], a[72:75]\n"
  "ds_write_b128 v21, a[28:31] offset:7168\n"
  "v_mfma_f32_16x16x32_bf16 a[76:79], a[240:243], v[156:159], a[76:79]\n"
  "v_mfma_f32_16x16x32_bf16 a[80:83], a[244:247], v[144:147], a[80:83]\n"
  "ds_write_b128 v21, a[32:35] offset:8192\n"
  "v_mfma_f32_16x16x32_bf16 a[84:87], a[244:247], v[148:151], a[84:87]\n"
  "ds_write_b128 v21, a[36:39] offset:9216\n"
  "v_mfma_f32_16x16x32_bf16 a[88:91], a[244:247], v[152:155], a[88:91]\n"
  "v_mfma_f32_16x16x32_bf16 a[92:95], a[244:247], v[156:159], a[92:95]\n"
  "ds_write_b128 v21, a[40:43] offset:10240\n"
  "v_mfma_f32_16x16x32_bf16 a[96:99], a[248:251], v[144:147], a[96:99]\n"
  "ds_write_b128 v21, a[44:47] offset:11264\n"
  "v_mfma_f32_16x16x32_bf16 a[100:103], a[248:251], v[148:151], a[100:103]\n"
  "v_mfma_f32_16x16x32_bf16 a[104:107], a[248:251], v[152:155], a[104:107]\n"
  "ds_write_b128 v21, a[48:51] offset:12288\n"
  "v_mfma_f32_16x16x32_bf16 a[108:111], a[248:251], v[156:159], a[108:111]\n"
  "ds_write_b128 v21, a[52:55] offset:13312\n"
  "v_mfma_f32_16x16x32_bf16 a[112:115], a[252:255], v[144:147], a[112:115]\n"
  "v_mfma_f32_16x16x32_bf16 a[116:119], a[252:255], v[148:151], a[116:119]\n"
  "ds_write_b128 v21, a[56:59] offset:14336\n"
  "v_mfma_f32_16x16x32_bf16 a[120:123], a[252:255], v[152:155], a[120:123]\n"
  "ds_write_b128 v21, a[60:63] offset:15360\n"
  "v_mfma_f32_16x16x32_bf16 a[124:127], a[252:255], v[156:159], a[124:127]\n"
  "v_xor_b32 v18, 0x8000, v18\n"
  "v_xor_b32 v19, 0x8000, v19\n"
  "v_accvgpr_read_b32 v96, a64\n"
  "v_accvgpr_read_b32 v97, a65\n"
  "v_accvgpr_read_b32 v98, a66\n"
  "v_accvgpr_read_b32 v99, a67\n"
  "v_accvgpr_read_b32 v100, a68\n"
  "v_accvgpr_read_b32 v101, a69\n"
  "v_accvgpr_read_b32 v102, a70\n"
  "v_accvgpr_read_b32 v103, a71\n"
  "v_accvgpr_read_b32 v104, a72\n"
  "v_accvgpr_read_b32 v105, a73\n"
  "v_accvgpr_read_b32 v106, a74\n"
  "v_accvgpr_read_b32 v107, a75\n"
  "v_accvgpr_read_b32 v108, a76\n"
  "v_accvgpr_read_b32 v109, a77\n"
  "v_accvgpr_read_b32 v110, a78\n"
  "v_accvgpr_read_b32 v111, a79\n"
  "s_waitcnt vmcnt(0) lgkmcnt(0)\n"
  "s_barrier\n"
  "ds_read_b128 v[56:59], v37\n"
  "ds_read_b128 v[60:63], v37 offset:1024\n"
  "ds_read_b128 v[192:195], v22 offset:0\n"
  "ds_read_b128 v[196:199], v22 offset:1024\n"
  "ds_read_b128 v[200:203], v22 offset:2048\n"
  "ds_read_b128 v[204:207], v22 offset:3072\n"
  "ds_read_b128 v[208:211], v22 offset:4096\n"
  "ds_read_b128 v[212:215], v22 offset:5120\n"
  "ds_read_b128 v[216:219], v22 offset:6144\n"
  "ds_read_b128 v[220:223], v22 offset:7168\n"
  "ds_read_b128 v[224:227], v22 offset:8192\n"
  "ds_read_b128 v[228:231], v22 offset:9216\n"
  "ds_read_b128 v[232:235], v22 offset:10240\n"
  "ds_read_b128 v[236:239], v22 offset:11264\n"
  "ds_read_b128 v[240:243], v22 offset:12288\n"
  "ds_read_b128 v[244:247], v22 offset:13312\n"
  "ds_read_b128 v[248:251], v22 offset:14336\n"
  "ds_read_b128 v[252:255], v22 offset:15360\n"
  "s_cmp_eq_u32 s73, 0\n"
  "s_cbranch_scc1 NOPF_%=\n"
  "v_cmp_eq_u64 vcc, %[tok], v[190:191]\n"
  "s_nop 0\n"
  "s_cmp_eq_u64 vcc, -1\n"
  "s_cbranch_scc0 SPINH_%=\n"
  "BACKH_%=:\n"
  "s_lshl_b32 s76, %[wid], 12\n"
  "v_add_u32 v190, s76, %[L16]\n"
  "v_add_u32 v191, 0x2000, %[voffW2]\n"
  "s_add_u32 m0, s76, 0\n"
  "s_mov_b32 s77, 0\n"
  "buffer_load_dwordx4 v190, %[srdAH], s77 offen offset:0 sc1 lds\n"
  "buffer_load_dwordx4 v190, %[srdAH], s77 offen offset:1024 sc1 lds\n"
  "buffer_load_dwordx4 v190, %[srdAH], s77 offen offset:2048 sc1 lds\n"
  "buffer_load_dwordx4 v190, %[srdAH], s77 offen offset:3072 sc1 lds\n"
  "s_lshr_b32 s77, s76, 1\n"
  "s_add_u32 m0, s77, 16384\n"
  "s_mov_b32 s77, 0\n"
  "buffer_load_dwordx4 %[voffW2], %[srdWH], s77 offen lds\n"
  "s_lshr_b32 s77, s76, 1\n"
  "s_add_u32 m0, s77, 17408\n"
  "s_mov_b32 s77, 0\n"
  "buffer_load_dwordx4 v191, %[srdWH], s77 offen lds\n"
  "s_lshr_b32 s77, s76, 1\n"
  "s_add_u32 m0, s77, 24576\n"
  "s_mov_b32 s77, 32768\n"
  "buffer_load_dwordx4 %[voffW2], %[srdWH], s77 offen lds\n"
  "s_lshr_b32 s77, s76, 1\n"
  "s_add_u32 m0, s77, 25600\n"
  "s_mov_b32 s77, 32768\n"
  "buffer_load_dwordx4 v191, %[srdWH], s77 offen lds\n"
  "s_add_u32 m0, s76, 98304\n"
  "s_mov_b32 s77, 16384\n"
  "buffer_load_dwordx4 v190, %[srdAH], s77 offen offset:0 sc1 lds\n"
  "buffer_load_dwordx4 v190, %[srdAH], s77 offen offset:1024 sc1 lds\n"
  "buffer_load_dwordx4 v190, %[srdAH], s77 offen offset:2048 sc1 lds\n"
  "buffer_load_dwordx4 v190, %[srdAH], s77 offen offset:3072 sc1 lds\n"
  "s_lshr_b32 s77, s76, 1\n"
  "s_add_u32 m0, s77, 114688\n"
  "s_mov_b32 s77, 65536\n"
  "buffer_load_dwordx4 %[voffW2], %[srdWH], s77 offen lds\n"
  "s_lshr_b32 s77, s76, 1\n"
  "s_add_u32 m0, s77, 115712\n"
  "s_mov_b32 s77, 65536\n"
  "buffer_load_dwordx4 v191, %[srdWH], s77 offen lds\n"
  "s_lshr_b32 s77, s76, 1\n"
  "s_add_u32 m0, s77, 122880\n"
  "s_mov_b32 s77, 98304\n"
  "buffer_load_dwordx4 %[voffW2], %[srdWH], s77 offen lds\n"
  "s_lshr_b32 s77, s76, 1\n"
  "s_add_u32 m0, s77, 123904\n"
  "s_mov_b32 s77, 98304\n"
  "buffer_load_dwordx4 v191, %[srdWH], s77 offen lds\n"
  "NOPF_%=:\n"
  "v_mfma_f32_16x16x32_bf16 v[64:67], a[192:195], a[128:131], 0\n"
  "v_accvgpr_read_b32 v112, a80\n"
  "v_accvgpr_read_b32 v113, a81\n"
  "v_accvgpr_read_b32 v114, a82\n"
  "v_accvgpr_read_b32 v115, a83\n"
  "v_accvgpr_read_b32 v116, a84\n"
  "v_accvgpr_read_b32 v117, a85\n"
  "v_accvgpr_read_b32 v118, a86\n"
  "v_accvgpr_read_b32 v119, a87\n"
  "v_accvgpr_read_b32 v120, a88\n"
  "v_mfma_f32_16x16x32_bf16 v[68:71], a[192:195], a[144:147], 0\n"
  "v_accvgpr_read_b32 v121, a89\n"
  "v_accvgpr_read_b32 v122, a90\n"
  "v_accvgpr_read_b32 v123, a91\n"
  "v_accvgpr_read_b32 v124, a92\n"
  "v_accvgpr_read_b32 v125, a93\n"
  "v_accvgpr_read_b32 v126, a94\n"
  "v_accvgpr_read_b32 v127, a95\n"
  "v_accvgpr_read_b32 v128, a96\n"
  "v_accvgpr_read_b32 v129, a97\n"
  "v_mfma_f32_16x16x32_bf16 v[72:75], a[192:195], a[160:163], 0\n"
  "v_accvgpr_read_b32 v130, a98\n"
  "v_accvgpr_read_b32 v131, a99\n"
  "v_accvgpr_read_b32 v132, a100\n"
  "v_accvgpr_read_b32 v133, a101\n"
  "v_accvgpr_read_b32 v134, a102\n"
  "v_accvgpr_read_b32 v135, a103\n"
  "v_accvgpr_read_b32 v136, a104\n"
  "v_accvgpr_read_b32 v137, a105\n"
  "v_accvgpr_read_b32 v138, a106\n"
  "v_mfma_f32_16x16x32_bf16 v[76:79], a[192:195], a[176:179], 0\n"
  "v_accvgpr_read_b32 v139, a107\n"
  "v_accvgpr_read_b32 v140, a108\n"
  "v_accvgpr_read_b32 v141, a109\n"
  "v_accvgpr_read_b32 v142, a110\n"
  "v_accvgpr_read_b32 v143, a111\n"
  "v_accvgpr_read_b32 v144, a112\n"
  "v_accvgpr_read_b32 v145, a113\n"
  "v_accvgpr_read_b32 v146, a114\n"
  "v_accvgpr_read_b32 v147, a115\n"
  "v_mfma_f32_16x16x32_bf16 v[64:67], a[196:199], a[132:135], v[64:67]\n"
  "v_accvgpr_read_b32 v148, a116\n"
  "v_accvgpr_read_b32 v149, a117\n"
  "v_accvgpr_read_b32 v150, a118\n"
  "v_accvgpr_read_b32 v151, a119\n"
  "v_accvgpr_read_b32 v152, a120\n"
  "v_accvgpr_read_b32 v153, a121\n"
  "v_accvgpr_read_b32 v154, a122\n"
  "v_accvgpr_read_b32 v155, a123\n"
  "v_accvgpr_read_b32 v156, a124\n"
  "v_mfma_f32_16x16x32_bf16 v[68:71], a[196:199], a[148:151], v[68:71]\n"
  "v_accvgpr_read_b32 v157, a125\n"
  "v_accvgpr_read_b32 v158, a126\n"
  "v_accvgpr_read_b32 v159, a127\n"
  "v_add_f32 v38, v24, v25\n"
  "v_add_f32 v39, v26, v27\n"
  "s_waitcnt lgkmcnt(15)\n"
  "v_add_f32 v38, v38, v56\n"
  "v_add_f32 v39, v39, v57\n"
  "v_add_f32 v38, v38, v58\n"
  "v_add_f32 v39, v39, v59\n"
  "v_mfma_f32_16x16x32_bf16 v[72:75], a[196:199], a[164:167], v[72:75]\n"
  "v_add_f32 v38, v38, v39\n"
  "v_mul_f32 v38, 0x4d800000, v38\n"
  "v_mul_f32 v38, 0, v38\n"
  "v_mov_b32 v39, 0\n"
  "v_max_f32 v24, 0x0da24260, v24\n"
  "v_max_f32 v56, 0x0da24260, v56\n"
  "v_max_f32 v25, 0x0da24260, v25\n"
  "v_max_f32 v57, 0x0da24260, v57\n"
  "v_max_f32 v26, 0x0da24260, v26\n"
  "v_max_f32 v58, 0x0da24260, v58\n"
  "v_mfma_f32_16x16x32_bf16 v[76:79], a[196:199], a[180:183], v[76:79]\n"
  "v_max_f32 v27, 0x0da24260, v27\n"
  "v_max_f32 v59, 0x0da24260, v59\n"
  "v_log_f32 v40, v24\n"
  "v_log_f32 v48, v56\n"
  "v_log_f32 v42, v25\n"
  "v_mfma_f32_16x16x32_bf16 v[64:67], a[200:203], a[136:139], v[64:67]\n"
  "v_log_f32 v50, v57\n"
  "v_log_f32 v44, v26\n"
  "v_log_f32 v52, v58\n"
  "v_log_f32 v46, v27\n"
  "v_log_f32 v54, v59\n"
  "v_mfma_f32_16x16x32_bf16 v[68:71], a[200:203], a[152:155], v[68:71]\n"
  "v_add_f32 v40, v40, v28\n"
  "v_add_f32 v48, v48, v60\n"
  "v_add_f32 v42, v42, v29\n"
  "v_add_f32 v50, v50, v61\n"
  "v_add_f32 v44, v44, v30\n"
  "v_add_f32 v52, v52, v62\n"
  "v_add_f32 v46, v46, v31\n"
  "v_add_f32 v54, v54, v63\n"
  "v_max_f32 v32, v40, v48\n"
  "v_mfma_f32_16x16x32_bf16 v[72:75], a[200:203], a[168:171], v[72:75]\n"
  "v_max_f32 v33, v42, v50\n"
  "v_max_f32 v34, v44, v52\n"
  "v_max_f32 v35, v46, v54\n"
  "v_sub_f32 v40, v28, v32\n"
  "v_sub_f32 v48, v60, v32\n"
  "v_sub_f32 v42, v29, v33\n"
  "v_sub_f32 v50, v61, v33\n"
  "v_sub_f32 v44, v30, v34\n"
  "v_sub_f32 v52, v62, v34\n"
  "v_mfma_f32_16x16x32_bf16 v[76:79], a[200:203], a[184:187], v[76:79]\n"
  "v_sub_f32 v46, v31, v35\n"
  "v_sub_f32 v54, v63, v35\n"
  "v_exp_f32 v40, v40\n"
  "v_exp_f32 v48, v48\n"
  "v_exp_f32 v42, v42\n"
  "v_mfma_f32_16x16x32_bf16 v[64:67], a[204:207], a[140:143], v[64:67]\n"
  "v_exp_f32 v50, v50\n"
  "v_exp_f32 v44, v44\n"
  "v_exp_f32 v52, v52\n"
  "v_exp_f32 v46, v46\n"
  "v_exp_f32 v54, v54\n"
  "v_mfma_f32_16x16x32_bf16 v[68:71], a[204:207], a[156:159], v[68:71]\n"
  "v_mul_f32 v32, v24, v40\n"
  "v_mul_f32 v33, v25, v42\n"
  "v_mul_f32 v34, v26, v44\n"
  "v_mul_f32 v35, v27, v46\n"
  "v_fmac_f32 v32, v56, v48\n"
  "v_fmac_f32 v33, v57, v50\n"
  "v_fmac_f32 v34, v58, v52\n"
  "v_fmac_f32 v35, v59, v54\n"
  "v_rcp_f32 v32, v32\n"
  "v_mfma_f32_16x16x32_bf16 v[72:75], a[204:207], a[172:175], v[72:75]\n"
  "v_rcp_f32 v33, v33\n"
  "v_rcp_f32 v34, v34\n"
  "v_rcp_f32 v35, v35\n"
  "v_mul_f32 v40, v40, v32\n"
  "v_mul_f32 v48, v48, v32\n"
  "v_mul_f32 v42, v42, v33\n"
  "v_mfma_f32_16x16x32_bf16 v[76:79], a[204:207], a[188:191], v[76:79]\n"
  "v_mul_f32 v50, v50, v33\n"
  "v_mul_f32 v44, v44, v34\n"
  "v_mul_f32 v52, v52, v34\n"
  "v_mul_f32 v46, v46, v35\n"
  "v_mul_f32 v54, v54, v35\n"
  "v_mov_b32 v41, v40\n"
  "v_mov_b32 v49, v48\n"
  "v_mov_b32 v43, v42\n"
  "v_mov_b32 v51, v50\n"
  "v_mfma_f32_16x16x32_bf16 v[80:83], a[208:211], a[128:131], 0\n"
  "v_mov_b32 v45, v44\n"
  "v_mov_b32 v53, v52\n"
  "v_mov_b32 v47, v46\n"
  "v_mov_b32 v55, v54\n"
  "s_add_u32 s75, s74, 0x2000\n"
  "v_pk_mul_f32 v[96:97], v[96:97], v[40:41]\n"
  "v_pk_mul_f32 v[98:99], v[98:99], v[40:41]\n"
  "v_pk_mul_f32 v[112:113], v[112:113], v[40:41]\n"
  "v_pk_mul_f32 v[114:115], v[114:115], v[40:41]\n"
  "v_mfma_f32_16x16x32_bf16 v[84:87], a[208:211], a[144:147], 0\n"
  "v_pk_fma_f32 v[96:97], v[192:193], v[48:49], v[96:97]\n"
  "v_pk_fma_f32 v[98:99], v[194:195], v[48:49], v[98:99]\n"
  "s_waitcnt lgkmcnt(11)\n"
  "v_pk_fma_f32 v[112:113], v[208:209], v[48:49], v[112:113]\n"
  "v_pk_fma_f32 v[114:115], v[210:211], v[48:49], v[114:115]\n"
  "v_pk_fma_f32 v[38:39], v[96:97], 0, v[38:39]\n"
  "v_pk_fma_f32 v[38:39], v[98:99], 0, v[38:39]\n"
  "v_pk_fma_f32 v[38:39], v[112:113], 0, v[38:39]\n"
  "v_pk_fma_f32 v[38:39], v[114:115], 0, v[38:39]\n"
  "v_cvt_pk_bf16_f32 v192, v96, v97\n"
  "v_mfma_f32_16x16x32_bf16 v[88:91], a[208:211], a[160:163], 0\n"
  "v_cvt_pk_bf16_f32 v193, v98, v99\n"
  "v_cvt_pk_bf16_f32 v194, v112, v113\n"
  "v_cvt_pk_bf16_f32 v195, v114, v115\n"
  "buffer_store_dwordx4 v[192:195], %[L16], %[srdWS], s74 offen offset:0 sc1\n"
  "v_pk_mul_f32 v[100:101], v[100:101], v[42:43]\n"
  "v_pk_mul_f32 v[102:103], v[102:103], v[42:43]\n"
  "v_pk_mul_f32 v[116:117], v[116:117], v[42:43]\n"
  "v_pk_mul_f32 v[118:119], v[118:119], v[42:43]\n"
  "v_pk_fma_f32 v[100:101], v[196:197], v[50:51], v[100:101]\n"
  "v_mfma_f32_16x16x32_bf16 v[92:95], a[208:211], a[176:179], 0\n"
  "v_pk_fma_f32 v[102:103], v[198:199], v[50:51], v[102:103]\n"
  "s_waitcnt lgkmcnt(10)\n"
  "v_pk_fma_f32 v[116:117], v[212:213], v[50:51], v[116:117]\n"
  "v_pk_fma_f32 v[118:119], v[214:215], v[50:51], v[118:119]\n"
  "v_pk_fma_f32 v[38:39], v[100:101], 0, v[38:39]\n"
  "v_pk_fma_f32 v[38:39], v[102:103], 0, v[38:39]\n"
  "v_pk_fma_f32 v[38:39], v[116:117], 0, v[38:39]\n"
  "v_pk_fma_f32 v[38:39], v[118:119], 0, v[38:39]\n"
  "v_cvt_pk_bf16_f32 v196, v100, v101\n"
  "v_cvt_pk_bf16_f32 v197, v102, v103\n"
  "v_mfma_f32_16x16x32_bf16 v[80:83], a[212:215], a[132:135], v[80:83]\n"
  "v_cvt_pk_bf16_f32 v198, v116, v117\n"
  "v_cvt_pk_bf16_f32 v199, v118, v119\n"
  "buffer_store_dwordx4 v[196:199], %[L16], %[srdWS], s74 offen offset:1024 sc1\n"
  "v_pk_mul_f32 v[104:105], v[104:105], v[44:45]\n"
  "v_pk_mul_f32 v[106:107], v[106:107], v[44:45]\n"
  "v_pk_mul_f32 v[120:121], v[120:121], v[44:45]\n"
  "v_pk_mul_f32 v[122:123], v[122:123], v[44:45]\n"
  "v_pk_fma_f32 v[104:105], v[200:201], v[52:53], v[104:105]\n"
  "v_mfma_f32_16x16x32_bf16 v[84:87], a[212:215], a[148:151], v[84:87]\n"
  "v_pk_fma_f32 v[106:107], v[202:203], v[52:53], v[106:107]\n"
  "s_waitcnt lgkmcnt(9)\n"
  "v_pk_fma_f32 v[120:121], v[216:217], v[52:53], v[120:121]\n"
  "v_pk_fma_f32 v[122:123], v[218:219], v[52:53], v[122:123]\n"
  "v_pk_fma_f32 v[38:39], v[104:105], 0, v[38:39]\n"
  "v_pk_fma_f32 v[38:39], v[106:107], 0, v[38:39]\n"
  "v_pk_fma_f32 v[38:39], v[120:121], 0, v[38:39]\n"
  "v_pk_fma_f32 v[38:39], v[122:123], 0, v[38:39]\n"
  "v_cvt_pk_bf16_f32 v200, v104, v105\n"
  "v_cvt_pk_bf16_f32 v201, v106, v107\n"
  "v_mfma_f32_16x16x32_bf16 v[88:91], a[212:215], a[164:167], v[88:91]\n"
  "v_cvt_pk_bf16_f32 v202, v120, v121\n"
  "v_cvt_pk_bf16_f32 v203, v122, v123\n"
  "buffer_store_dwordx4 v[200:203], %[L16], %[srdWS], s74 offen offset:2048 sc1\n"
  "v_pk_mul_f32 v[108:109], v[108:109], v[46:47]\n"
  "v_pk_mul_f32 v[110:111], v[110:111], v[46:47]\n"
  "v_pk_mul_f32 v[124:125], v[124:125], v[46:47]\n"
  "v_pk_mul_f32 v[126:127], v[126:127], v[46:47]\n"
  "v_pk_fma_f32 v[108:109], v[204:205], v[54:55], v[108:109]\n"
  "v_pk_fma_f32 v[110:111], v[206:207], v[54:55], v[110:111]\n"
  "v_mfma_f32_16x16x32_bf16 v[92:95], a[212:215], a[180:183], v[92:95]\n"
  "s_waitcnt lgkmcnt(8)\n"
  "v_pk_fma_f32 v[124:125], v[220:221], v[54:55], v[124:125]\n"
  "v_pk_fma_f32 v[126:127], v[222:223], v[54:55], v[126:127]\n"
  "v_pk_fma_f32 v[38:39], v[108:109], 0, v[38:39]\n"
  "v_pk_fma_f32 v[38:39], v[110:111], 0, v[38:39]\n"
  "v_pk_fma_f32 v[38:39], v[124:125], 0, v[38:39]\n"
  "v_pk_fma_f32 v[38:39], v[126:127], 0, v[38:39]\n"
  "v_cvt_pk_bf16_f32 v204, v108, v109\n"
  "v_cvt_pk_bf16_f32 v205, v110, v111\n"
  "v_cvt_pk_bf16_f32 v206, v124, v125\n"
  "v_mfma_f32_16x16x32_bf16 v[80:83], a[216:219], a[136:139], v[80:83]\n"
  "v_cvt_pk_bf16_f32 v207, v126, v127\n"
  "buffer_store_dwordx4 v[204:207], %[L16], %[srdWS], s74 offen offset:3072 sc1\n"
  "v_pk_mul_f32 v[128:129], v[128:129], v[40:41]\n"
  "v_pk_mul_f32 v[130:131], v[130:131], v[40:41]\n"
  "v_pk_mul_f32 v[144:145], v[144:145], v[40:41]\n"
  "v_pk_mul_f32 v[146:147], v[146:147], v[40:41]\n"
  "s_waitcnt lgkmcnt(7)\n"
  "v_pk_fma_f32 v[128:129], v[224:225], v[48:49], v[128:129]\n"
  "v_pk_fma_f32 v[130:131], v[226:227], v[48:49], v[130:131]\n"
  "s_waitcnt lgkmcnt(3)\n"
  "v_pk_fma_f32 v[144:145], v[240:241], v[48:49], v[144:145]\n"
  "v_mfma_f32_16x16x32_bf16 v[84:87], a[216:219], a[152:155], v[84:87]\n"
  "v_pk_fma_f32 v[146:147], v[242:243], v[48:49], v[146:147]\n"
  "v_pk_fma_f32 v[38:39], v[128:129], 0, v[38:39]\n"
  "v_pk_fma_f32 v[38:39], v[130:131], 0, v[38:39]\n"
  "v_pk_fma_f32 v[38:39], v[144:145], 0, v[38:39]\n"
  "v_pk_fma_f32 v[38:39], v[146:147], 0, v[38:39]\n"
  "v_cvt_pk_bf16_f32 v224, v128, v129\n"
  "v_cvt_pk_bf16_f32 v225, v130, v131\n"
  "v_cvt_pk_bf16_f32 v226, v144, v145\n"
  "v_cvt_pk_bf16_f32 v227, v146, v147\n"
  "v_mfma_f32_16x16x32_bf16 v[88:91], a[216:219], a[168:171], v[88:91]\n"
  "buffer_store_dwordx4 v[224:227], %[L16], %[srdWS], s75 offen offset:0 sc1\n"
  "v_pk_mul_f32 v[132:133], v[132:133], v[42:43]\n"
  "v_pk_mul_f32 v[134:135], v[134:135], v[42:43]\n"
  "v_pk_mul_f32 v[148:149], v[148:149], v[42:43]\n"
  "v_pk_mul_f32 v[150:151], v[150:151], v[42:43]\n"
  "v_pk_fma_f32 v[132:133], v[228:229], v[50:51], v[132:133]\n"
  "v_pk_fma_f32 v[134:135], v[230:231], v[50:51], v[134:135]\n"
  "s_waitcnt lgkmcnt(2)\n"
  "v_pk_fma_f32 v[148:149], v[244:245], v[50:51], v[148:149]\n"
  "v_pk_fma_f32 v[150:151], v[246:247], v[50:51], v[150:151]\n"
  "v_mfma_f32_16x16x32_bf16 v[92:95], a[216:219], a[184:187], v[92:95]\n"
  "v_pk_fma_f32 v[38:39], v[132:133], 0, v[38:39]\n"
  "v_pk_fma_f32 v[38:39], v[134:135], 0, v[38:39]\n"
  "v_pk_fma_f32 v[38:39], v[148:149], 0, v[38:39]\n"
  "v_pk_fma_f32 v[38:39], v[150:151], 0, v[38:39]\n"
  "v_cvt_pk_bf16_f32 v228, v132, v133\n"
  "v_cvt_pk_bf16_f32 v229, v134, v135\n"
  "v_cvt_pk_bf16_f32 v230, v148, v149\n"
  "v_cvt_pk_bf16_f32 v231, v150, v151\n"
  "buffer_store_dwordx4 v[228:231], %[L16], %[srdWS], s75 offen offset:1024 sc1\n"
  "v_mfma_f32_16x16x32_bf16 v[80:83], a[220:223], a[140:143], v[80:83]\n"
  "v_pk_mul_f32 v[136:137], v[136:137], v[44:45]\n"
  "v_pk_mul_f32 v[138:139], v[138:139], v[44:45]\n"
  "v_pk_mul_f32 v[152:153], v[152:153], v[44:45]\n"
  "v_pk_mul_f32 v[154:155], v[154:155], v[44:45]\n"
  "v_pk_fma_f32 v[136:137], v[232:233], v[52:53], v[136:137]\n"
  "v_pk_fma_f32 v[138:139], v[234:235], v[52:53], v[138:139]\n"
  "s_waitcnt lgkmcnt(1)\n"
  "v_pk_fma_f32 v[152:153], v[248:249], v[52:53], v[152:153]\n"
  "v_pk_fma_f32 v[154:155], v[250:251], v[52:53], v[154:155]\n"
  "v_pk_fma_f32 v[38:39], v[136:137], 0, v[38:39]\n"
  "v_mfma_f32_16x16x32_bf16 v[84:87], a[220:223], a[156:159], v[84:87]\n"
  "v_pk_fma_f32 v[38:39], v[138:139], 0, v[38:39]\n"
  "v_pk_fma_f32 v[38:39], v[152:153], 0, v[38:39]\n"
  "v_pk_fma_f32 v[38:39], v[154:155], 0, v[38:39]\n"
  "v_cvt_pk_bf16_f32 v232, v136, v137\n"
  "v_cvt_pk_bf16_f32 v233, v138, v139\n"
  "v_cvt_pk_bf16_f32 v234, v152, v153\n"
  "v_cvt_pk_bf16_f32 v235, v154, v155\n"
  "buffer_store_dwordx4 v[232:235], %[L16], %[srdWS], s75 offen offset:2048 sc1\n"
  "v_mfma_f32_16x16x32_bf16 v[88:91], a[220:223], a[172:175], v[88:91]\n"
  "v_pk_mul_f32 v[140:141], v[140:141], v[46:47]\n"
  "v_pk_mul_f32 v[142:143], v[142:143], v[46:47]\n"
  "v_pk_mul_f32 v[156:157], v[156:157], v[46:47]\n"
  "v_pk_mul_f32 v[158:159], v[158:159], v[46:47]\n"
  "v_pk_fma_f32 v[140:141], v[236:237], v[54:55], v[140:141]\n"
  "v_pk_fma_f32 v[142:143], v[238:239], v[54:55], v[142:143]\n"
  "s_waitcnt lgkmcnt(0)\n"
  "v_pk_fma_f32 v[156:157], v[252:253], v[54:55], v[156:157]\n"
  "v_pk_fma_f32 v[158:159], v[254:255], v[54:55], v[158:159]\n"
  "v_pk_fma_f32 v[38:39], v[140:141], 0, v[38:39]\n"
  "v_mfma_f32_16x16x32_bf16 v[92:95], a[220:223], a[188:191], v[92:95]\n"
  "v_pk_fma_f32 v[38:39], v[142:143], 0, v[38:39]\n"
  "v_pk_fma_f32 v[38:39], v[156:157], 0, v[38:39]\n"
  "v_pk_fma_f32 v[38:39], v[158:159], 0, v[38:39]\n"
  "v_cvt_pk_bf16_f32 v236, v140, v141\n"
  "v_cvt_pk_bf16_f32 v237, v142, v143\n"
  "v_cvt_pk_bf16_f32 v238, v156, v157\n"
  "v_cvt_pk_bf16_f32 v239, v158, v159\n"
  "buffer_store_dwordx4 v[236:239], %[L16], %[srdWS], s75 offen offset:3072 sc1\n"
  "v_cmp_u_f32 vcc, v38, v39\n"
  "s_nop 0\n"
  "s_cmp_lg_u64 vcc, 0\n"
  "s_cselect_b32 s76, 1, 0\n"
  "s_lshl_b32 s76, s76, s73\n"
  "s_or_b32 %[bad], %[bad], s76\n"
  "s_cmp_lg_u32 s73, 0\n"
  "s_cbranch_scc1 ATend_%=\n"
  "s_mov_b32 s73, 1\n"
  "s_sub_u32 s68, 17, %[nA]\n"
  "s_mov_b32 s74, %[oCB]\n"
  "s_lshl_b32 s76, s68, 15\n"
  "s_add_u32 s69, %[oK], s76\n"
  "s_add_u32 s70, s69, 0x1000000\n"
  "s_mov_b32 s71, 0x7f000000\n"
  "s_mov_b32 s72, 0x7f000000\n"
  "s_cmp_eq_u32 s68, 1\n"
  "s_cbranch_scc1 ATonly_%=\n"
  "s_branch ATfirst_%=\n"
  "SPINH_%=:\n"
  "s_mov_b32 s76, 0x40000\n"
  "SPINLH_%=:\n"
  "s_sleep 1\n"
  "buffer_load_dwordx2 v[190:191], %[pollH], %[srdWS], 0 offen sc1\n"
  "s_waitcnt vmcnt(0)\n"
  "v_cmp_eq_u64 vcc, %[tok], v[190:191]\n"
  "s_nop 1\n"
  "s_cmp_eq_u64 vcc, -1\n"
  "s_cbranch_scc1 BACKH_%=\n"
  "s_sub_u32 s76, s76, 1\n"
  "s_cmp_lg_u32 s76, 0\n"
  "s_cbranch_scc1 SPINLH_%=\n"
  "s_branch BACKH_%=\n"
  "ATend_%=:\n"
  "s_waitcnt lgkmcnt(0)\n"
  "s_barrier\n"
    : [bad] "=&s"(bad)
    : [srdWS] "s"(srdWS), [wid] "s"(rfl(wid)), [oK] "s"(rfl(oK)), [nA] "s"(rfl(nA)), [oCA] "s"(rfl(oCA)), [oCB] "s"(rfl(oCB)), [oFl] "s"(rfl(oFl)), [tok] "s"(tok),
      [L16] "v"(l * 16), [z] "v"(z), [srdAH] "s"(srdAH), [srdWH] "s"(srdWH), [voffW2] "v"(voffW2), [pollH] "v"(pollH), [srdWot] "s"(srdWot)
    : "v16", "v17", "v18", "v19", "v20", "v21", "v22", "v23", "v24", "v25", "v26", "v27", "v28", "v29", "v30", "v31", "v32", "v33", "v34", "v35", "v36", "v37", "v38", "v39", "v40", "v41", "v42", "v43", "v44", "v45", "v46", "v47", "v48", "v49", "v50", "v51", "v52", "v53", "v54", "v55", "v56", "v57", "v58", "v59", "v60", "v61", "v62", "v63", "v64", "v65", "v66", "v67", "v68", "v69", "v70", "v71", "v72", "v73", "v74", "v75", "v76", "v77", "v78", "v79", "v80", "v81", "v82", "v83", "v84", "v85", "v86", "v87", "v88", "v89", "v90", "v91", "v92", "v93", "v94", "v95", "v96", "v97", "v98", "v99", "v100", "v101", "v102", "v103", "v104", "v105", "v106", "v107", "v108", "v109", "v110", "v111", "v112", "v113", "v114", "v115", "v116", "v117", "v118", "v119", "v120", "v121", "v122", "v123", "v124", "v125", "v126", "v127", "v128", "v129", "v130", "v131", "v132", "v133", "v134", "v135", "v136", "v137", "v138", "v139", "v140", "v141", "v142", "v143", "v144", "v145", "v146", "v147", "v148", "v149", "v150", "v151", "v152", "v153", "v154", "v155", "v156", "v157", "v158", "v159", "v160", "v161", "v162", "v163", "v164", "v165", "v166", "v167", "v168", "v169", "v170", "v171", "v172", "v173", "v174", "v175", "v176", "v177", "v178", "v179", "v180", "v181", "v182", "v183", "v184", "v185", "v186", "v187", "v188", "v189", "v190", "v191", "v192", "v193", "v194", "v195", "v196", "v197", "v198", "v199", "v200", "v201", "v202", "v203", "v204", "v205", "v206", "v207", "v208", "v209", "v210", "v211", "v212", "v213", "v214", "v215", "v216", "v217", "v218", "v219", "v220", "v221", "v222", "v223", "v224", "v225", "v226", "v227", "v228", "v229", "v230", "v231", "v232", "v233", "v234", "v235", "v236", "v237", "v238", "v239", "v240", "v241", "v242", "v243", "v244", "v245", "v246", "v247", "v248", "v249", "v250", "v251", "v252", "v253", "v254", "v255", "a0", "a1", "a2", "a3", "a4", "a5", "a6", "a7", "a8", "a9", "a10", "a11", "a12", "a13", "a14", "a15", "a16", "a17", "a18", "a19", "a20", "a21", "a22", "a23", "a24", "a25", "a26", "a27", "a28", "a29", "a30", "a31", "a32", "a33", "a34", "a35", "a36", "a37", "a38", "a39", "a40", "a41", "a42", "a43", "a44", "a45", "a46", "a47", "a48", "a49", "a50", "a51", "a52", "a53", "a54", "a55", "a56", "a57", "a58", "a59", "a60", "a61", "a62", "a63", "a64", "a65", "a66", "a67", "a68", "a69", "a70", "a71", "a72", "a73", "a74", "a75", "a76", "a77", "a78", "a79", "a80", "a81", "a82", "a83", "a84", "a85", "a86", "a87", "a88", "a89", "a90", "a91", "a92", "a93", "a94", "a95", "a96", "a97", "a98", "a99", "a100", "a101", "a102", "a103", "a104", "a105", "a106", "a107", "a108", "a109", "a110", "a111", "a112", "a113", "a114", "a115", "a116", "a117", "a118", "a119", "a120", "a121", "a122", "a123", "a124", "a125", "a126", "a127", "a128", "a129", "a130", "a131", "a132", "a133", "a134", "a135", "a136", "a137", "a138", "a139", "a140", "a141", "a142", "a143", "a144", "a145", "a146", "a147", "a148", "a149", "a150", "a151", "a152", "a153", "a154", "a155", "a156", "a157", "a158", "a159", "a160", "a161", "a162", "a163", "a164", "a165", "a166", "a167", "a168", "a169", "a170", "a171", "a172", "a173", "a174", "a175", "a176", "a177", "a178", "a179", "a180", "a181", "a182", "a183", "a184", "a185", "a186", "a187", "a188", "a189", "a190", "a191", "a192", "a193", "a194", "a195", "a196", "a197", "a198", "a199", "a200", "a201", "a202", "a203", "a204", "a205", "a206", "a207", "a208", "a209", "a210", "a211", "a212", "a213", "a214", "a215", "a216", "a217", "a218", "a219", "a220", "a221", "a222", "a223", "a224", "a225", "a226", "a227", "a228", "a229", "a230", "a231", "a232", "a233", "a234", "a235", "a236", "a237", "a238", "a239", "a240", "a241", "a242", "a243", "a244", "a245", "a246", "a247", "a248", "a249", "a250", "a251", "a252", "a253", "a254", "a255", "s64", "s65", "s66", "s67", "s68", "s69", "s70", "s71", "s72", "s73", "s74", "s75", "s76", "s77", "s78", "s79", "s80", "s81", "s82", "s83", "s84", "s85", "s86", "s87", "s88", "s89", "s90", "s91", "vcc", "scc", "memory");
  }
  STAMP(3);
  if (__builtin_expect(bad != 0, 0)) {
#pragma unroll 1
    for (unsigned b = 0; b < 2; ++b)
      if ((bad >> b) & 1) slow_rows(p.wq, p.ws, head, b ? blkB : blkA, wid);
  }
  const unsigned wm = wid & 1, wn = wid >> 1;
  {
    const unsigned l = lane_id();
    const __amdgpu_buffer_rsrc_t srdAC = mkrsrc(p.ws + WS_A + ((size_t)rbF * 171 + 32 * sp) * 8192, 0x40000);
    const __amdgpu_buffer_rsrc_t srdWC = mkrsrc(p.wo + (size_t)(1024 * sp) * 512 + nt * 128, 1023 * 1024 + 256);
    const unsigned row0 = rbF * 128 + (4 * wm + sp) * 16, col0 = nt * 128 + wn * 64;
    const __amdgpu_buffer_rsrc_t srdXr = mkrsrc(p.x + row0 * 512 + col0, 15 * 1024 + 128), srdO = mkrsrc(p.out + row0 * 512 + col0, 15 * 1024 + 128), srdB2 = mkrsrc(p.b2 + col0, 128);
    const unsigned voffO = (l & 15) * 1024 + (l >> 4) * 8, oP = (unsigned)WS_P + (rbF * 4 + nt) * 98304 + wid * 2048;
    const unsigned pollP = FLAG0 + 4 * 16384 + (bid ^ (((l & 3) % 3 + 1) << 1)) * 64 + wid * 8;
    const unsigned e = l >> 5, g1 = (l >> 4) & 1, q = (l >> 2) & 3;
    const unsigned voffW2 = (16 * (wid >> 1) + 4 * g1 + q) * 1024 + (wid & 1) * 128 + (2 * e + (((l >> 1) & 1) ^ g1)) * 32 + (l & 1) * 16;
    const unsigned bW = wn * 2048 + e * 1024 + g1 * 256 + q * 64 + g1 * 32 + (l & 3) * 8;
    const unsigned bA0 = (4 * wm + (sp & 3)) * 1024 + l * 16, bA1 = (4 * wm + ((sp + 1) & 3)) * 1024 + l * 16,
                   bA2 = (4 * wm + ((sp + 2) & 3)) * 1024 + l * 16, bA3 = (4 * wm + ((sp + 3) & 3)) * 1024 + l * 16;
    const unsigned hp = 8 * sp + ((l >> 2) & 7), jp = rbF < 8 ? rbF : 15 - rbF;
    const unsigned pollC = FLAG0 + 3 * 16384 + ((hp >> 2) + 8 * ((hp & 3) * 8 + jp)) * 64 + (l & 3) * 8;
    asm volatile(
  "s_nop 4\n"
  "s_lshl_b32 s71, %[wid], 12\n"
  "s_lshl_b32 s70, %[wid], 11\n"
  "v_add_u32 v16, s71, %[L16]\n"
  "v_mov_b32 v17, %[voffW2]\n"
  "v_add_u32 v18, 0x2000, %[voffW2]\n"
  "s_add_u32 s80, s70, 16384\n"
  "s_add_u32 s81, s70, 17408\n"
  "s_add_u32 s82, s70, 24576\n"
  "s_add_u32 s83, s70, 25600\n"
  "s_mov_b32 s64, 0x8000\n"
  "s_mov_b32 s65, 0x20000\n"
  "s_mov_b32 s68, 0x28000\n"
  "s_mov_b32 s66, 0x8000\n"
  "v_mov_b32 v20, %[bA0]\n"
  "v_mov_b32 v26, %[bA0]\n"
  "v_mov_b32 v21, %[bA1]\n"
  "v_mov_b32 v27, %[bA1]\n"
  "v_mov_b32 v22, %[bA2]\n"
  "v_mov_b32 v28, %[bA2]\n"
  "v_mov_b32 v23, %[bA3]\n"
  "v_mov_b32 v29, %[bA3]\n"
  "v_mov_b32 v24, %[bW]\n"
  "v_xor_b32 v25, 32, %[bW]\n"
  "v_mov_b32 v30, v24\n"
  "v_mov_b32 v31, v25\n"
  "s_mov_b32 s67, 0\n"
  "s_waitcnt vmcnt(16)\n"
  "s_barrier\n"
  "ds_read_b128 a[16:19], v26 offset:0\n"
  "ds_read_b128 a[20:23], v27 offset:0\n"
  "ds_read_b128 a[24:27], v28 offset:0\n"
  "ds_read_b128 a[28:31], v29 offset:0\n"
  "ds_read_b64_tr_b16 a[0:1], v30 offset:16384\n"
  "ds_read_b64_tr_b16 a[2:3], v30 offset:20480\n"
  "ds_read_b64_tr_b16 a[4:5], v31 offset:16384\n"
  "ds_read_b64_tr_b16 a[6:7], v31 offset:20480\n"
  "ds_read_b64_tr_b16 a[8:9], v30 offset:16896\n"
  "ds_read_b64_tr_b16 a[10:11], v30 offset:20992\n"
  "ds_read_b64_tr_b16 a[12:13], v31 offset:16896\n"
  "ds_read_b64_tr_b16 a[14:15], v31 offset:20992\n"
  "ds_read_b128 a[48:51], v26 offset:8192\n"
  "ds_read_b128 a[52:55], v27 offset:8192\n"
  "ds_read_b128 a[56:59], v28 offset:8192\n"
  "ds_read_b128 a[60:63], v29 offset:8192\n"
  "ds_read_b64_tr_b16 a[32:33], v30 offset:24576\n"
  "ds_read_b64_tr_b16 a[34:35], v30 offset:28672\n"
  "ds_read_b64_tr_b16 a[36:37], v31 offset:24576\n"
  "ds_read_b64_tr_b16 a[38:39], v31 offset:28672\n"
  "ds_read_b64_tr_b16 a[40:41], v30 offset:25088\n"
  "ds_read_b64_tr_b16 a[42:43], v30 offset:29184\n"
  "ds_read_b64_tr_b16 a[44:45], v31 offset:25088\n"
  "ds_read_b64_tr_b16 a[46:47], v31 offset:29184\n"
  "s_add_u32 s67, s67, 0x18000\n"
  "s_sub_u32 s72, s67, 0x28000\n"
  "s_cselect_b32 s67, s67, s72\n"
  "v_add_u32 v26, s67, v20\n"
  "v_add_u32 v27, s67, v21\n"
  "v_add_u32 v28, s67, v22\n"
  "v_add_u32 v29, s67, v23\n"
  "v_add_u32 v30, s67, v24\n"
  "v_add_u32 v31, s67, v25\n"
  "s_add_u32 m0, s66, s80\n"
  "s_nop 0\n"
  "buffer_load_dwordx4 v17, %[srdWH], s65 offen lds\n"
  "s_add_u32 m0, s66, s81\n"
  "s_nop 0\n"
  "buffer_load_dwordx4 v18, %[srdWH], s65 offen lds\n"
  "s_add_u32 m0, s66, s82\n"
  "s_nop 0\n"
  "buffer_load_dwordx4 v17, %[srdWH], s68 offen lds\n"
  "s_add_u32 m0, s66, s83\n"
  "s_nop 0\n"
  "buffer_load_dwordx4 v18, %[srdWH], s68 offen lds\n"
  "s_add_u32 m0, s66, s71\n"
  "s_nop 0\n"
  "buffer_load_dwordx4 v16, %[srdAH], s64 offen offset:0 sc1 lds\n"
  "s_add_u32 s65, s65, 0x10000\n"
  "buffer_load_dwordx4 v16, %[srdAH], s64 offen offset:1024 sc1 lds\n"
  "s_add_u32 s68, s65, 0x8000\n"
  "buffer_load_dwordx4 v16, %[srdAH], s64 offen offset:2048 sc1 lds\n"
  "s_add_u32 s66, s66, 0x18000\n"
  "s_sub_u32 s72, s66, 0x28000\n"
  "s_cselect_b32 s66, s66, s72\n"
  "buffer_load_dwordx4 v16, %[srdAH], s64 offen offset:3072 sc1 lds\n"
  "s_add_u32 s64, s64, 0x4000\n"
  "s_mov_b32 s69, 4\n"
  "s_mov_b64 s[84:85], %[tok]\n"
  "v_mov_b32 v34, s84\n"
  "v_mov_b32 v35, s85\n"
  "s_add_u32 s70, %[oFl], 0xc000\n"
  "s_waitcnt vmcnt(8)\n"
  "s_waitcnt lgkmcnt(12)\n"
  "s_barrier\n"
  "v_mfma_f32_16x16x32_bf16 v[128:131], a[0:3], a[16:19], 0\n"
  "ds_read_b128 a[80:83], v26 offset:0\n"
  "buffer_store_dwordx2 v[34:35], off, %[srdWS], s70 sc1\n"
  "s_add_u32 m0, s66, s80\n"
  "v_mfma_f32_16x16x32_bf16 v[144:147], a[0:3], a[20:23], 0\n"
  "ds_read_b128 a[84:87], v27 offset:0\n"
  "buffer_load_dwordx4 v17, %[srdWH], s65 offen lds\n"
  "v_mfma_f32_16x16x32_bf16 v[160:163], a[0:3], a[24:27], 0\n"
  "ds_read_b128 a[88:91], v28 offset:0\n"
  "s_add_u32 m0, s66, s81\n"
  "v_mfma_f32_16x16x32_bf16 v[176:179], a[0:3], a[28:31], 0\n"
  "ds_read_b128 a[92:95], v29 offset:0\n"
  "buffer_load_dwordx4 v18, %[srdWH], s65 offen lds\n"
  "v_mfma_f32_16x16x32_bf16 v[132:135], a[4:7], a[16:19], 0\n"
  "ds_read_b64_tr_b16 a[64:65], v30 offset:16384\n"
  "s_add_u32 m0, s66, s82\n"
  "v_mfma_f32_16x16x32_bf16 v[148:151], a[4:7], a[20:23], 0\n"
  "ds_read_b64_tr_b16 a[66:67], v30 offset:20480\n"
  "buffer_load_dwordx4 v17, %[srdWH], s68 offen lds\n"
  "v_mfma_f32_16x16x32_bf16 v[164:167], a[4:7], a[24:27], 0\n"
  "ds_read_b64_tr_b16 a[68:69], v31 offset:16384\n"
  "s_add_u32 m0, s66, s83\n"
  "s_nop 0\n"
  "buffer_load_dwordx4 v18, %[srdWH], s68 offen lds\n"
  "v_mfma_f32_16x16x32_bf16 v[180:183], a[4:7], a[28:31], 0\n"
  "ds_read_b64_tr_b16 a[70:71], v31 offset:20480\n"
  "s_add_u32 m0, s66, s71\n"
  "v_mfma_f32_16x16x32_bf16 v[136:139], a[8:11], a[16:19], 0\n"
  "ds_read_b64_tr_b16 a[72:73], v30 offset:16896\n"
  "buffer_load_dwordx4 v16, %[srdAH], s64 offen offset:0 sc1 lds\n"
  "v_mfma_f32_16x16x32_bf16 v[152:155], a[8:11], a[20:23], 0\n"
  "ds_read_b64_tr_b16 a[74:75], v30 offset:20992\n"
  "s_add_u32 s65, s65, 0x10000\n"
  "v_mfma_f32_16x16x32_bf16 v[168:171], a[8:11], a[24:27], 0\n"
  "ds_read_b64_tr_b16 a[76:77], v31 offset:16896\n"
  "buffer_load_dwordx4 v16, %[srdAH], s64 offen offset:1024 sc1 lds\n"
  "v_mfma_f32_16x16x32_bf16 v[184:187], a[8:11], a[28:31], 0\n"
  "ds_read_b64_tr_b16 a[78:79], v31 offset:20992\n"
  "s_add_u32 s68, s65, 0x8000\n"
  "v_mfma_f32_16x16x32_bf16 v[140:143], a[12:15], a[16:19], 0\n"
  "ds_read_b128 a[112:115], v26 offset:8192\n"
  "buffer_load_dwordx4 v16, %[srdAH], s64 offen offset:2048 sc1 lds\n"
  "s_add_u32 s66, s66, 0x18000\n"
  "s_sub_u32 s72, s66, 0x28000\n"
  "s_cselect_b32 s66, s66, s72\n"
  "v_mfma_f32_16x16x32_bf16 v[156:159], a[12:15], a[20:23], 0\n"
  "ds_read_b128 a[116:119], v27 offset:8192\n"
  "buffer_load_dwordx4 v16, %[srdAH], s64 offen offset:3072 sc1 lds\n"
  "v_mfma_f32_16x16x32_bf16 v[172:175], a[12:15], a[24:27], 0\n"
  "ds_read_b128 a[120:123], v28 offset:8192\n"
  "s_add_u32 s64, s64, 0x4000\n"
  "v_mfma_f32_16x16x32_bf16 v[188:191], a[12:15], a[28:31], 0\n"
  "ds_read_b128 a[124:127], v29 offset:8192\n"
  "s_add_u32 m0, s66, s80\n"
  "s_waitcnt lgkmcnt(15)\n"
  "v_mfma_f32_16x16x32_bf16 v[128:131], a[32:35], a[48:51], v[128:131]\n"
  "ds_read_b64_tr_b16 a[96:97], v30 offset:24576\n"
  "buffer_load_dwordx4 v17, %[srdWH], s65 offen lds\n"
  "v_mfma_f32_16x16x32_bf16 v[144:147], a[32:35], a[52:55], v[144:147]\n"
  "ds_read_b64_tr_b16 a[98:99], v30 offset:28672\n"
  "s_add_u32 m0, s66, s81\n"
  "v_mfma_f32_16x16x32_bf16 v[160:163], a[32:35], a[56:59], v[160:163]\n"
  "ds_read_b64_tr_b16 a[100:101], v31 offset:24576\n"
  "buffer_load_dwordx4 v18, %[srdWH], s65 offen lds\n"
  "s_add_u32 m0, s66, s82\n"
  "v_mfma_f32_16x16x32_bf16 v[176:179], a[32:35], a[60:63], v[176:179]\n"
  "ds_read_b64_tr_b16 a[102:103], v31 offset:28672\n"
  "buffer_load_dwordx4 v17, %[srdWH], s68 offen lds\n"
  "v_mfma_f32_16x16x32_bf16 v[132:135], a[36:39], a[48:51], v[132:135]\n"
  "ds_read_b64_tr_b16 a[104:105], v30 offset:25088\n"
  "s_add_u32 m0, s66, s83\n"
  "v_mfma_f32_16x16x32_bf16 v[148:151], a[36:39], a[52:55], v[148:151]\n"
  "ds_read_b64_tr_b16 a[106:107], v30 offset:29184\n"
  "buffer_load_dwordx4 v18, %[srdWH], s68 offen lds\n"
  "v_mfma_f32_16x16x32_bf16 v[164:167], a[36:39], a[56:59], v[164:167]\n"
  "ds_read_b64_tr_b16 a[108:109], v31 offset:25088\n"
  "s_add_u32 m0, s66, s71\n"
  "v_mfma_f32_16x16x32_bf16 v[180:183], a[36:39], a[60:63], v[180:183]\n"
  "ds_read_b64_tr_b16 a[110:111], v31 offset:29184\n"
  "buffer_load_dwordx4 v16, %[srdAH], s64 offen offset:0 sc1 lds\n"
  "v_mfma_f32_16x16x32_bf16 v[136:139], a[40:43], a[48:51], v[136:139]\n"
  "s_add_u32 s67, s67, 0x18000\n"
  "s_sub_u32 s72, s67, 0x28000\n"
  "s_cselect_b32 s67, s67, s72\n"
  "s_add_u32 s65, s65, 0x10000\n"
  "buffer_load_dwordx4 v16, %[srdAH], s64 offen offset:1024 sc1 lds\n"
  "v_mfma_f32_16x16x32_bf16 v[152:155], a[40:43], a[52:55], v[152:155]\n"
  "s_add_u32 s68, s65, 0x8000\n"
  "v_mfma_f32_16x16x32_bf16 v[168:171], a[40:43], a[56:59], v[168:171]\n"
  "v_add_u32 v26, s67, v20\n"
  "buffer_load_dwordx4 v16, %[srdAH], s64 offen offset:2048 sc1 lds\n"
  "v_mfma_f32_16x16x32_bf16 v[184:187], a[40:43], a[60:63], v[184:187]\n"
  "v_add_u32 v27, s67, v21\n"
  "s_add_u32 s66, s66, 0x18000\n"
  "s_sub_u32 s72, s66, 0x28000\n"
  "s_cselect_b32 s66, s66, s72\n"
  "v_mfma_f32_16x16x32_bf16 v[140:143], a[44:47], a[48:51], v[140:143]\n"
  "v_add_u32 v28, s67, v22\n"
  "buffer_load_dwordx4 v16, %[srdAH], s64 offen offset:3072 sc1 lds\n"
  "v_mfma_f32_16x16x32_bf16 v[156:159], a[44:47], a[52:55], v[156:159]\n"
  "v_add_u32 v29, s67, v23\n"
  "s_add_u32 s64, s64, 0x4000\n"
  "v_mfma_f32_16x16x32_bf16 v[172:175], a[44:47], a[56:59], v[172:175]\n"
  "v_add_u32 v30, s67, v24\n"
  "v_mfma_f32_16x16x32_bf16 v[188:191], a[44:47], a[60:63], v[188:191]\n"
  "v_add_u32 v31, s67, v25\n"
  "s_waitcnt vmcnt(17)\n"
  "s_waitcnt lgkmcnt(12)\n"
  "s_barrier\n"
  "v_mfma_f32_16x16x32_bf16 v[128:131], a[64:67], a[80:83], v[128:131]\n"
  "ds_read_b128 a[16:19], v26 offset:0\n"
  "s_add_u32 m0, s66, s80\n"
  "v_mfma_f32_16x16x32_bf16 v[144:147], a[64:67], a[84:87], v[144:147]\n"
  "ds_read_b128 a[20:23], v27 offset:0\n"
  "buffer_load_dwordx4 v17, %[srdWH], s65 offen lds\n"
  "v_mfma_f32_16x16x32_bf16 v[160:163], a[64:67], a[88:91], v[160:163]\n"
  "ds_read_b128 a[24:27], v28 offset:0\n"
  "v_mfma_f32_16x16x32_bf16 v[176:179], a[64:67], a[92:95], v[176:179]\n"
  "ds_read_b128 a[28:31], v29 offset:0\n"
  "s_add_u32 m0, s66, s81\n"
  "v_mfma_f32_16x16x32_bf16 v[132:135], a[68:71], a[80:83], v[132:135]\n"
  "ds_read_b64_tr_b16 a[0:1], v30 offset:16384\n"
  "v_mfma_f32_16x16x32_bf16 v[148:151], a[68:71], a[84:87], v[148:151]\n"
  "ds_read_b64_tr_b16 a[2:3], v30 offset:20480\n"
  "buffer_load_dwordx4 v18, %[srdWH], s65 offen lds\n"
  "v_mfma_f32_16x16x32_bf16 v[164:167], a[68:71], a[88:91], v[164:167]\n"
  "ds_read_b64_tr_b16 a[4:5], v31 offset:16384\n"
  "v_mfma_f32_16x16x32_bf16 v[180:183], a[68:71], a[92:95], v[180:183]\n"
  "ds_read_b64_tr_b16 a[6:7], v31 offset:20480\n"
  "s_add_u32 m0, s66, s82\n"
  "v_mfma_f32_16x16x32_bf16 v[136:139], a[72:75], a[80:83], v[136:139]\n"
  "ds_read_b64_tr_b16 a[8:9], v30 offset:16896\n"
  "buffer_load_dwordx4 v17, %[srdWH], s68 offen lds\n"
  "v_mfma_f32_16x16x32_bf16 v[152:155], a[72:75], a[84:87], v[152:155]\n"
  "ds_read_b64_tr_b16 a[10:11], v30 offset:20992\n"
  "v_mfma_f32_16x16x32_bf16 v[168:171], a[72:75], a[88:91], v[168:171]\n"
  "ds_read_b64_tr_b16 a[12:13], v31 offset:16896\n"
  "s_add_u32 m0, s66, s83\n"
  "v_mfma_f32_16x16x32_bf16 v[184:187], a[72:75], a[92:95], v[184:187]\n"
  "ds_read_b64_tr_b16 a[14:15], v31 offset:20992\n"
  "v_mfma_f32_16x16x32_bf16 v[140:143], a[76:79], a[80:83], v[140:143]\n"
  "ds_read_b128 a[48:51], v26 offset:8192\n"
  "buffer_load_dwordx4 v18, %[srdWH], s68 offen lds\n"
  "v_mfma_f32_16x16x32_bf16 v[156:159], a[76:79], a[84:87], v[156:159]\n"
  "ds_read_b128 a[52:55], v27 offset:8192\n"
  "v_mfma_f32_16x16x32_bf16 v[172:175], a[76:79], a[88:91], v[172:175]\n"
  "ds_read_b128 a[56:59], v28 offset:8192\n"
  "s_add_u32 m0, s66, s71\n"
  "v_mfma_f32_16x16x32_bf16 v[188:191], a[76:79], a[92:95], v[188:191]\n"
  "ds_read_b128 a[60:63], v29 offset:8192\n"
  "buffer_load_dwordx4 v16, %[srdAH], s64 offen offset:0 sc1 lds\n"
  "s_waitcnt lgkmcnt(15)\n"
  "v_mfma_f32_16x16x32_bf16 v[128:131], a[96:99], a[112:115], v[128:131]\n"
  "ds_read_b64_tr_b16 a[32:33], v30 offset:24576\n"
  "v_mfma_f32_16x16x32_bf16 v[144:147], a[96:99], a[116:119], v[144:147]\n"
  "ds_read_b64_tr_b16 a[34:35], v30 offset:28672\n"
  "s_add_u32 s65, s65, 0x10000\n"
  "v_mfma_f32_16x16x32_bf16 v[160:163], a[96:99], a[120:123], v[160:163]\n"
  "ds_read_b64_tr_b16 a[36:37], v31 offset:24576\n"
  "v_mfma_f32_16x16x32_bf16 v[176:179], a[96:99], a[124:127], v[176:179]\n"
  "ds_read_b64_tr_b16 a[38:39], v31 offset:28672\n"
  "buffer_load_dwordx4 v16, %[srdAH], s64 offen offset:1024 sc1 lds\n"
  "v_mfma_f32_16x16x32_bf16 v[132:135], a[100:103], a[112:115], v[132:135]\n"
  "ds_read_b64_tr_b16 a[40:41], v30 offset:25088\n"
  "v_mfma_f32_16x16x32_bf16 v[148:151], a[100:103], a[116:119], v[148:151]\n"
  "ds_read_b64_tr_b16 a[42:43], v30 offset:29184\n"
  "s_add_u32 s68, s65, 0x8000\n"
  "v_mfma_f32_16x16x32_bf16 v[164:167], a[100:103], a[120:123], v[164:167]\n"
  "ds_read_b64_tr_b16 a[44:45], v31 offset:25088\n"
  "buffer_load_dwordx4 v16, %[srdAH], s64 offen offset:2048 sc1 lds\n"
  "v_mfma_f32_16x16x32_bf16 v[180:183], a[100:103], a[124:127], v[180:183]\n"
  "ds_read_b64_tr_b16 a[46:47], v31 offset:29184\n"
  "v_mfma_f32_16x16x32_bf16 v[136:139], a[104:107], a[112:115], v[136:139]\n"
  "s_add_u32 s67, s67, 0x18000\n"
  "s_sub_u32 s72, s67, 0x28000\n"
  "s_cselect_b32 s67, s67, s72\n"
  "s_add_u32 s66, s66, 0x18000\n"
  "s_sub_u32 s72, s66, 0x28000\n"
  "s_cselect_b32 s66, s66, s72\n"
  "v_mfma_f32_16x16x32_bf16 v[152:155], a[104:107], a[116:119], v[152:155]\n"
  "v_mfma_f32_16x16x32_bf16 v[168:171], a[104:107], a[120:123], v[168:171]\n"
  "v_add_u32 v26, s67, v20\n"
  "buffer_load_dwordx4 v16, %[srdAH], s64 offen offset:3072 sc1 lds\n"
  "v_mfma_f32_16x16x32_bf16 v[184:187], a[104:107], a[124:127], v[184:187]\n"
  "v_add_u32 v27, s67, v21\n"
  "v_mfma_f32_16x16x32_bf16 v[140:143], a[108:111], a[112:115], v[140:143]\n"
  "v_add_u32 v28, s67, v22\n"
  "s_add_u32 s64, s64, 0x4000\n"
  "v_mfma_f32_16x16x32_bf16 v[156:159], a[108:111], a[116:119], v[156:159]\n"
  "v_add_u32 v29, s67, v23\n"
  "v_mfma_f32_16x16x32_bf16 v[172:175], a[108:111], a[120:123], v[172:175]\n"
  "v_add_u32 v30, s67, v24\n"
  "v_mfma_f32_16x16x32_bf16 v[188:191], a[108:111], a[124:127], v[188:191]\n"
  "v_add_u32 v31, s67, v25\n"
  "s_waitcnt vmcnt(16)\n"
  "s_waitcnt lgkmcnt(12)\n"
  "s_barrier\n"
  "v_mfma_f32_16x16x32_bf16 v[128:131], a[0:3], a[16:19], v[128:131]\n"
  "ds_read_b128 a[80:83], v26 offset:0\n"
  "buffer_load_dwordx2 v[32:33], %[pollC], %[srdWS], 0 offen sc1\n"
  "v_mfma_f32_16x16x32_bf16 v[144:147], a[0:3], a[20:23], v[144:147]\n"
  "ds_read_b128 a[84:87], v27 offset:0\n"
  "v_mfma_f32_16x16x32_bf16 v[160:163], a[0:3], a[24:27], v[160:163]\n"
  "ds_read_b128 a[88:91], v28 offset:0\n"
  "v_mfma_f32_16x16x32_bf16 v[176:179], a[0:3], a[28:31], v[176:179]\n"
  "ds_read_b128 a[92:95], v29 offset:0\n"
  "v_mfma_f32_16x16x32_bf16 v[132:135], a[4:7], a[16:19], v[132:135]\n"
  "ds_read_b64_tr_b16 a[64:65], v30 offset:16384\n"
  "v_mfma_f32_16x16x32_bf16 v[148:151], a[4:7], a[20:23], v[148:151]\n"
  "ds_read_b64_tr_b16 a[66:67], v30 offset:20480\n"
  "v_mfma_f32_16x16x32_bf16 v[164:167], a[4:7], a[24:27], v[164:167]\n"
  "ds_read_b64_tr_b16 a[68:69], v31 offset:16384\n"
  "v_mfma_f32_16x16x32_bf16 v[180:183], a[4:7], a[28:31], v[180:183]\n"
  "ds_read_b64_tr_b16 a[70:71], v31 offset:20480\n"
  "s_mov_b32 s64, 0\n"
  "v_mfma_f32_16x16x32_bf16 v[136:139], a[8:11], a[16:19], v[136:139]\n"
  "ds_read_b64_tr_b16 a[72:73], v30 offset:16896\n"
  "v_mfma_f32_16x16x32_bf16 v[152:155], a[8:11], a[20:23], v[152:155]\n"
  "ds_read_b64_tr_b16 a[74:75], v30 offset:20992\n"
  "v_mfma_f32_16x16x32_bf16 v[168:171], a[8:11], a[24:27], v[168:171]\n"
  "ds_read_b64_tr_b16 a[76:77], v31 offset:16896\n"
  "v_mfma_f32_16x16x32_bf16 v[184:187], a[8:11], a[28:31], v[184:187]\n"
  "ds_read_b64_tr_b16 a[78:79], v31 offset:20992\n"
  "v_mfma_f32_16x16x32_bf16 v[140:143], a[12:15], a[16:19], v[140:143]\n"
  "ds_read_b128 a[112:115], v26 offset:8192\n"
  "v_mfma_f32_16x16x32_bf16 v[156:159], a[12:15], a[20:23], v[156:159]\n"
  "ds_read_b128 a[116:119], v27 offset:8192\n"
  "v_mfma_f32_16x16x32_bf16 v[172:175], a[12:15], a[24:27], v[172:175]\n"
  "ds_read_b128 a[120:123], v28 offset:8192\n"
  "v_mfma_f32_16x16x32_bf16 v[188:191], a[12:15], a[28:31], v[188:191]\n"
  "ds_read_b128 a[124:127], v29 offset:8192\n"
  "s_mov_b32 s65, 0\n"
  "s_waitcnt lgkmcnt(15)\n"
  "v_mfma_f32_16x16x32_bf16 v[128:131], a[32:35], a[48:51], v[128:131]\n"
  "ds_read_b64_tr_b16 a[96:97], v30 offset:24576\n"
  "v_mfma_f32_16x16x32_bf16 v[144:147], a[32:35], a[52:55], v[144:147]\n"
  "ds_read_b64_tr_b16 a[98:99], v30 offset:28672\n"
  "v_mfma_f32_16x16x32_bf16 v[160:163], a[32:35], a[56:59], v[160:163]\n"
  "ds_read_b64_tr_b16 a[100:101], v31 offset:24576\n"
  "v_mfma_f32_16x16x32_bf16 v[176:179], a[32:35], a[60:63], v[176:179]\n"
  "ds_read_b64_tr_b16 a[102:103], v31 offset:28672\n"
  "v_mfma_f32_16x16x32_bf16 v[132:135], a[36:39], a[48:51], v[132:135]\n"
  "ds_read_b64_tr_b16 a[104:105], v30 offset:25088\n"
  "v_mfma_f32_16x16x32_bf16 v[148:151], a[36:39], a[52:55], v[148:151]\n"
  "ds_read_b64_tr_b16 a[106:107], v30 offset:29184\n"
  "v_mfma_f32_16x16x32_bf16 v[164:167], a[36:39], a[56:59], v[164:167]\n"
  "ds_read_b64_tr_b16 a[108:109], v31 offset:25088\n"
  "s_mov_b32 s68, 0x8000\n"
  "v_mfma_f32_16x16x32_bf16 v[180:183], a[36:39], a[60:63], v[180:183]\n"
  "ds_read_b64_tr_b16 a[110:111], v31 offset:29184\n"
  "v_mfma_f32_16x16x32_bf16 v[136:139], a[40:43], a[48:51], v[136:139]\n"
  "s_add_u32 s67, s67, 0x18000\n"
  "s_sub_u32 s72, s67, 0x28000\n"
  "s_cselect_b32 s67, s67, s72\n"
  "v_mfma_f32_16x16x32_bf16 v[152:155], a[40:43], a[52:55], v[152:155]\n"
  "v_mfma_f32_16x16x32_bf16 v[168:171], a[40:43], a[56:59], v[168:171]\n"
  "v_add_u32 v26, s67, v20\n"
  "v_mfma_f32_16x16x32_bf16 v[184:187], a[40:43], a[60:63], v[184:187]\n"
  "v_add_u32 v27, s67, v21\n"
  "v_mfma_f32_16x16x32_bf16 v[140:143], a[44:47], a[48:51], v[140:143]\n"
  "v_add_u32 v28, s67, v22\n"
  "v_mfma_f32_16x16x32_bf16 v[156:159], a[44:47], a[52:55], v[156:159]\n"
  "v_add_u32 v29, s67, v23\n"
  "v_mfma_f32_16x16x32_bf16 v[172:175], a[44:47], a[56:59], v[172:175]\n"
  "v_add_u32 v30, s67, v24\n"
  "v_mfma_f32_16x16x32_bf16 v[188:191], a[44:47], a[60:63], v[188:191]\n"
  "v_add_u32 v31, s67, v25\n"
  "s_waitcnt vmcnt(0)\n"
  "s_waitcnt lgkmcnt(12)\n"
  "s_barrier\n"
  "v_cmp_eq_u64 vcc, %[tok], v[32:33]\n"
  "s_nop 0\n"
  "s_cmp_eq_u64 vcc, -1\n"
  "s_cbranch_scc0 SPINC_%=\n"
  "BACKC_%=:\n"
  "v_mfma_f32_16x16x32_bf16 v[128:131], a[64:67], a[80:83], v[128:131]\n"
  "ds_read_b128 a[16:19], v26 offset:0\n"
  "s_add_u32 m0, s66, s80\n"
  "s_nop 0\n"
  "buffer_load_dwordx4 v17, %[srdWC], s65 offen lds\n"
  "v_mfma_f32_16x16x32_bf16 v[144:147], a[64:67], a[84:87], v[144:147]\n"
  "ds_read_b128 a[20:23], v27 offset:0\n"
  "s_add_u32 m0, s66, s81\n"
  "v_mfma_f32_16x16x32_bf16 v[160:163], a[64:67], a[88:91], v[160:163]\n"
  "ds_read_b128 a[24:27], v28 offset:0\n"
  "buffer_load_dwordx4 v18, %[srdWC], s65 offen lds\n"
  "v_mfma_f32_16x16x32_bf16 v[176:179], a[64:67], a[92:95], v[176:179]\n"
  "ds_read_b128 a[28:31], v29 offset:0\n"
  "s_add_u32 m0, s66, s82\n"
  "v_mfma_f32_16x16x32_bf16 v[132:135], a[68:71], a[80:83], v[132:135]\n"
  "ds_read_b64_tr_b16 a[0:1], v30 offset:16384\n"
  "buffer_load_dwordx4 v17, %[srdWC], s68 offen lds\n"
  "v_mfma_f32_16x16x32_bf16 v[148:151], a[68:71], a[84:87], v[148:151]\n"
  "ds_read_b64_tr_b16 a[2:3], v30 offset:20480\n"
  "s_add_u32 m0, s66, s83\n"
  "v_mfma_f32_16x16x32_bf16 v[164:167], a[68:71], a[88:91], v[164:167]\n"
  "ds_read_b64_tr_b16 a[4:5], v31 offset:16384\n"
  "buffer_load_dwordx4 v18, %[srdWC], s68 offen lds\n"
  "v_mfma_f32_16x16x32_bf16 v[180:183], a[68:71], a[92:95], v[180:183]\n"
  "ds_read_b64_tr_b16 a[6:7], v31 offset:20480\n"
  "s_add_u32 m0, s66, s71\n"
  "s_nop 0\n"
  "buffer_load_dwordx4 v16, %[srdAC], s64 offen offset:0 sc1 lds\n"
  "v_mfma_f32_16x16x32_bf16 v[136:139], a[72:75], a[80:83], v[136:139]\n"
  "ds_read_b64_tr_b16 a[8:9], v30 offset:16896\n"
  "s_add_u32 s65, s65, 0x10000\n"
  "v_mfma_f32_16x16x32_bf16 v[152:155], a[72:75], a[84:87], v[152:155]\n"
  "ds_read_b64_tr_b16 a[10:11], v30 offset:20992\n"
  "buffer_load_dwordx4 v16, %[srdAC], s64 offen offset:1024 sc1 lds\n"
  "v_mfma_f32_16x16x32_bf16 v[168:171], a[72:75], a[88:91], v[168:171]\n"
  "ds_read_b64_tr_b16 a[12:13], v31 offset:16896\n"
  "s_add_u32 s68, s65, 0x8000\n"
  "v_mfma_f32_16x16x32_bf16 v[184:187], a[72:75], a[92:95], v[184:187]\n"
  "ds_read_b64_tr_b16 a[14:15], v31 offset:20992\n"
  "buffer_load_dwordx4 v16, %[srdAC], s64 offen offset:2048 sc1 lds\n"
  "v_mfma_f32_16x16x32_bf16 v[140:143], a[76:79], a[80:83], v[140:143]\n"
  "ds_read_b128 a[48:51], v26 offset:8192\n"
  "s_add_u32 s66, s66, 0x18000\n"
  "s_sub_u32 s72, s66, 0x28000\n"
  "s_cselect_b32 s66, s66, s72\n"
  "v_mfma_f32_16x16x32_bf16 v[156:159], a[76:79], a[84:87], v[156:159]\n"
  "ds_read_b128 a[52:55], v27 offset:8192\n"
  "buffer_load_dwordx4 v16, %[srdAC], s64 offen offset:3072 sc1 lds\n"
  "v_mfma_f32_16x16x32_bf16 v[172:175], a[76:79], a[88:91], v[172:175]\n"
  "ds_read_b128 a[56:59], v28 offset:8192\n"
  "s_add_u32 s64, s64, 0x4000\n"
  "v_mfma_f32_16x16x32_bf16 v[188:191], a[76:79], a[92:95], v[188:191]\n"
  "ds_read_b128 a[60:63], v29 offset:8192\n"
  "s_add_u32 m0, s66, s80\n"
  "s_nop 0\n"
  "buffer_load_dwordx4 v17, %[srdWC], s65 offen lds\n"
  "s_waitcnt lgkmcnt(15)\n"
  "v_mfma_f32_16x16x32_bf16 v[128:131], a[96:99], a[112:115], v[128:131]\n"
  "ds_read_b64_tr_b16 a[32:33], v30 offset:24576\n"
  "s_add_u32 m0, s66, s81\n"
  "v_mfma_f32_16x16x32_bf16 v[144:147], a[96:99], a[116:119], v[144:147]\n"
  "ds_read_b64_tr_b16 a[34:35], v30 offset:28672\n"
  "buffer_load_dwordx4 v18, %[srdWC], s65 offen lds\n"
  "v_mfma_f32_16x16x32_bf16 v[160:163], a[96:99], a[120:123], v[160:163]\n"
  "ds_read_b64_tr_b16 a[36:37], v31 offset:24576\n"
  "s_add_u32 m0, s66, s82\n"
  "v_mfma_f32_16x16x32_bf16 v[176:179], a[96:99], a[124:127], v[176:179]\n"
  "ds_read_b64_tr_b16 a[38:39], v31 offset:28672\n"
  "buffer_load_dwordx4 v17, %[srdWC], s68 offen lds\n"
  "v_mfma_f32_16x16x32_bf16 v[132:135], a[100:103], a[112:115], v[132:135]\n"
  "ds_read_b64_tr_b16 a[40:41], v30 offset:25088\n"
  "s_add_u32 m0, s66, s83\n"
  "v_mfma_f32_16x16x32_bf16 v[148:151], a[100:103], a[116:119], v[148:151]\n"
  "ds_read_b64_tr_b16 a[42:43], v30 offset:29184\n"
  "buffer_load_dwordx4 v18, %[srdWC], s68 offen lds\n"
  "v_mfma_f32_16x16x32_bf16 v[164:167], a[100:103], a[120:123], v[164:167]\n"
  "ds_read_b64_tr_b16 a[44:45], v31 offset:25088\n"
  "s_add_u32 m0, s66, s71\n"
  "s_nop 0\n"
  "buffer_load_dwordx4 v16, %[srdAC], s64 offen offset:0 sc1 lds\n"
  "v_mfma_f32_16x16x32_bf16 v[180:183], a[100:103], a[124:127], v[180:183]\n"
  "ds_read_b64_tr_b16 a[46:47], v31 offset:29184\n"
  "s_add_u32 s65, s65, 0x10000\n"
  "v_mfma_f32_16x16x32_bf16 v[136:139], a[104:107], a[112:115], v[136:139]\n"
  "s_add_u32 s67, s67, 0x18000\n"
  "s_sub_u32 s72, s67, 0x28000\n"
  "s_cselect_b32 s67, s67, s72\n"
  "buffer_load_dwordx4 v16, %[srdAC], s64 offen offset:1024 sc1 lds\n"
  "v_mfma_f32_16x16x32_bf16 v[152:155], a[104:107], a[116:119], v[152:155]\n"
  "s_add_u32 s68, s65, 0x8000\n"
  "v_mfma_f32_16x16x32_bf16 v[168:171], a[104:107], a[120:123], v[168:171]\n"
  "v_add_u32 v26, s67, v20\n"
  "buffer_load_dwordx4 v16, %[srdAC], s64 offen offset:2048 sc1 lds\n"
  "v_mfma_f32_16x16x32_bf16 v[184:187], a[104:107], a[124:127], v[184:187]\n"
  "v_add_u32 v27, s67, v21\n"
  "s_add_u32 s66, s66, 0x18000\n"
  "s_sub_u32 s72, s66, 0x28000\n"
  "s_cselect_b32 s66, s66, s72\n"
  "v_mfma_f32_16x16x32_bf16 v[140:143], a[108:111], a[112:115], v[140:143]\n"
  "v_add_u32 v28, s67, v22\n"
  "buffer_load_dwordx4 v16, %[srdAC], s64 offen offset:3072 sc1 lds\n"
  "v_mfma_f32_16x16x32_bf16 v[156:159], a[108:111], a[116:119], v[156:159]\n"
  "v_add_u32 v29, s67, v23\n"
  "s_add_u32 s64, s64, 0x4000\n"
  "v_mfma_f32_16x16x32_bf16 v[172:175], a[108:111], a[120:123], v[172:175]\n"
  "v_add_u32 v30, s67, v24\n"
  "v_mfma_f32_16x16x32_bf16 v[188:191], a[108:111], a[124:127], v[188:191]\n"
  "v_add_u32 v31, s67, v25\n"
  "FNLOOP_%=:\n"
  "s_waitcnt vmcnt(16) lgkmcnt(12)\n"
  "s_barrier\n"
  "v_mfma_f32_16x16x32_bf16 v[128:131], a[0:3], a[16:19], v[128:131]\n"
  "ds_read_b128 a[80:83], v26 offset:0\n"
  "s_add_u32 m0, s66, s80\n"
  "v_mfma_f32_16x16x32_bf16 v[144:147], a[0:3], a[20:23], v[144:147]\n"
  "ds_read_b128 a[84:87], v27 offset:0\n"
  "buffer_load_dwordx4 v17, %[srdWC], s65 offen lds\n"
  "v_mfma_f32_16x16x32_bf16 v[160:163], a[0:3], a[24:27], v[160:163]\n"
  "ds_read_b128 a[88:91], v28 offset:0\n"
  "v_mfma_f32_16x16x32_bf16 v[176:179], a[0:3], a[28:31], v[176:179]\n"
  "ds_read_b128 a[92:95], v29 offset:0\n"
  "s_add_u32 m0, s66, s81\n"
  "v_mfma_f32_16x16x32_bf16 v[132:135], a[4:7], a[16:19], v[132:135]\n"
  "ds_read_b64_tr_b16 a[64:65], v30 offset:16384\n"
  "v_mfma_f32_16x16x32_bf16 v[148:151], a[4:7], a[20:23], v[148:151]\n"
  "ds_read_b64_tr_b16 a[66:67], v30 offset:20480\n"
  "buffer_load_dwordx4 v18, %[srdWC], s65 offen lds\n"
  "v_mfma_f32_16x16x32_bf16 v[164:167], a[4:7], a[24:27], v[164:167]\n"
  "ds_read_b64_tr_b16 a[68:69], v31 offset:16384\n"
  "v_mfma_f32_16x16x32_bf16 v[180:183], a[4:7], a[28:31], v[180:183]\n"
  "ds_read_b64_tr_b16 a[70:71], v31 offset:20480\n"
  "s_add_u32 m0, s66, s82\n"
  "v_mfma_f32_16x16x32_bf16 v[136:139], a[8:11], a[16:19], v[136:139]\n"
  "ds_read_b64_tr_b16 a[72:73], v30 offset:16896\n"
  "buffer_load_dwordx4 v17, %[srdWC], s68 offen lds\n"
  "v_mfma_f32_16x16x32_bf16 v[152:155], a[8:11], a[20:23], v[152:155]\n"
  "ds_read_b64_tr_b16 a[74:75], v30 offset:20992\n"
  "v_mfma_f32_16x16x32_bf16 v[168:171], a[8:11], a[24:27], v[168:171]\n"
  "ds_read_b64_tr_b16 a[76:77], v31 offset:16896\n"
  "s_add_u32 m0, s66, s83\n"
  "v_mfma_f32_16x16x32_bf16 v[184:187], a[8:11], a[28:31], v[184:187]\n"
  "ds_read_b64_tr_b16 a[78:79], v31 offset:20992\n"
  "v_mfma_f32_16x16x32_bf16 v[140:143], a[12:15], a[16:19], v[140:143]\n"
  "ds_read_b128 a[112:115], v26 offset:8192\n"
  "buffer_load_dwordx4 v18, %[srdWC], s68 offen lds\n"
  "v_mfma_f32_16x16x32_bf16 v[156:159], a[12:15], a[20:23], v[156:159]\n"
  "ds_read_b128 a[116:119], v27 offset:8192\n"
  "v_mfma_f32_16x16x32_bf16 v[172:175], a[12:15], a[24:27], v[172:175]\n"
  "ds_read_b128 a[120:123], v28 offset:8192\n"
  "s_add_u32 m0, s66, s71\n"
  "v_mfma_f32_16x16x32_bf16 v[188:191], a[12:15], a[28:31], v[188:191]\n"
  "ds_read_b128 a[124:127], v29 offset:8192\n"
  "buffer_load_dwordx4 v16, %[srdAC], s64 offen offset:0 sc1 lds\n"
  "s_waitcnt lgkmcnt(15)\n"
  "v_mfma_f32_16x16x32_bf16 v[128:131], a[32:35], a[48:51], v[128:131]\n"
  "ds_read_b64_tr_b16 a[96:97], v30 offset:24576\n"
  "v_mfma_f32_16x16x32_bf16 v[144:147], a[32:35], a[52:55], v[144:147]\n"
  "ds_read_b64_tr_b16 a[98:99], v30 offset:28672\n"
  "s_add_u32 s65, s65, 0x10000\n"
  "v_mfma_f32_16x16x32_bf16 v[160:163], a[32:35], a[56:59], v[160:163]\n"
  "ds_read_b64_tr_b16 a[100:101], v31 offset:24576\n"
  "v_mfma_f32_16x16x32_bf16 v[176:179], a[32:35], a[60:63], v[176:179]\n"
  "ds_read_b64_tr_b16 a[102:103], v31 offset:28672\n"
  "buffer_load_dwordx4 v16, %[srdAC], s64 offen offset:1024 sc1 lds\n"
  "v_mfma_f32_16x16x32_bf16 v[132:135], a[36:39], a[48:51], v[132:135]\n"
  "ds_read_b64_tr_b16 a[104:105], v30 offset:25088\n"
  "v_mfma_f32_16x16x32_bf16 v[148:151], a[36:39], a[52:55], v[148:151]\n"
  "ds_read_b64_tr_b16 a[106:107], v30 offset:29184\n"
  "s_add_u32 s68, s65, 0x8000\n"
  "v_mfma_f32_16x16x32_bf16 v[164:167], a[36:39], a[56:59], v[164:167]\n"
  "ds_read_b64_tr_b16 a[108:109], v31 offset:25088\n"
  "buffer_load_dwordx4 v16, %[srdAC], s64 offen offset:2048 sc1 lds\n"
  "v_mfma_f32_16x16x32_bf16 v[180:183], a[36:39], a[60:63], v[180:183]\n"
  "ds_read_b64_tr_b16 a[110:111], v31 offset:29184\n"
  "v_mfma_f32_16x16x32_bf16 v[136:139], a[40:43], a[48:51], v[136:139]\n"
  "s_add_u32 s67, s67, 0x18000\n"
  "s_sub_u32 s72, s67, 0x28000\n"
  "s_cselect_b32 s67, s67, s72\n"
  "s_add_u32 s66, s66, 0x18000\n"
  "s_sub_u32 s72, s66, 0x28000\n"
  "s_cselect_b32 s66, s66, s72\n"
  "v_mfma_f32_16x16x32_bf16 v[152:155], a[40:43], a[52:55], v[152:155]\n"
  "v_mfma_f32_16x16x32_bf16 v[168:171], a[40:43], a[56:59], v[168:171]\n"
  "v_add_u32 v26, s67, v20\n"
  "buffer_load_dwordx4 v16, %[srdAC], s64 offen offset:3072 sc1 lds\n"
  "v_mfma_f32_16x16x32_bf16 v[184:187], a[40:43], a[60:63], v[184:187]\n"
  "v_add_u32 v27, s67, v21\n"
  "v_mfma_f32_16x16x32_bf16 v[140:143], a[44:47], a[48:51], v[140:143]\n"
  "v_add_u32 v28, s67, v22\n"
  "s_add_u32 s64, s64, 0x4000\n"
  "v_mfma_f32_16x16x32_bf16 v[156:159], a[44:47], a[52:55], v[156:159]\n"
  "v_add_u32 v29, s67, v23\n"
  "v_mfma_f32_16x16x32_bf16 v[172:175], a[44:47], a[56:59], v[172:175]\n"
  "v_add_u32 v30, s67, v24\n"
  "v_mfma_f32_16x16x32_bf16 v[188:191], a[44:47], a[60:63], v[188:191]\n"
  "v_add_u32 v31, s67, v25\n"
  "s_waitcnt vmcnt(16) lgkmcnt(12)\n"
  "s_barrier\n"
  "v_mfma_f32_16x16x32_bf16 v[128:131], a[64:67], a[80:83], v[128:131]\n"
  "ds_read_b128 a[16:19], v26 offset:0\n"
  "s_add_u32 m0, s66, s80\n"
  "v_mfma_f32_16x16x32_bf16 v[144:147], a[64:67], a[84:87], v[144:147]\n"
  "ds_read_b128 a[20:23], v27 offset:0\n"
  "buffer_load_dwordx4 v17, %[srdWC], s65 offen lds\n"
  "v_mfma_f32_16x16x32_bf16 v[160:163], a[64:67], a[88:91], v[160:163]\n"
  "ds_read_b128 a[24:27], v28 offset:0\n"
  "v_mfma_f32_16x16x32_bf16 v[176:179], a[64:67], a[92:95], v[176:179]\n"
  "ds_read_b128 a[28:31], v29 offset:0\n"
  "s_add_u32 m0, s66, s81\n"
  "v_mfma_f32_16x16x32_bf16 v[132:135], a[68:71], a[80:83], v[132:135]\n"
  "ds_read_b64_tr_b16 a[0:1], v30 offset:16384\n"
  "v_mfma_f32_16x16x32_bf16 v[148:151], a[68:71], a[84:87], v[148:151]\n"
  "ds_read_b64_tr_b16 a[2:3], v30 offset:20480\n"
  "buffer_load_dwordx4 v18, %[srdWC], s65 offen lds\n"
  "v_mfma_f32_16x16x32_bf16 v[164:167], a[68:71], a[88:91], v[164:167]\n"
  "ds_read_b64_tr_b16 a[4:5], v31 offset:16384\n"
  "v_mfma_f32_16x16x32_bf16 v[180:183], a[68:71], a[92:95], v[180:183]\n"
  "ds_read_b64_tr_b16 a[6:7], v31 offset:20480\n"
  "s_add_u32 m0, s66, s82\n"
  "v_mfma_f32_16x16x32_bf16 v[136:139], a[72:75], a[80:83], v[136:139]\n"
  "ds_read_b64_tr_b16 a[8:9], v30 offset:16896\n"
  "buffer_load_dwordx4 v17, %[srdWC], s68 offen lds\n"
  "v_mfma_f32_16x16x32_bf16 v[152:155], a[72:75], a[84:87], v[152:155]\n"
  "ds_read_b64_tr_b16 a[10:11], v30 offset:20992\n"
  "v_mfma_f32_16x16x32_bf16 v[168:171], a[72:75], a[88:91], v[168:171]\n"
  "ds_read_b64_tr_b16 a[12:13], v31 offset:16896\n"
  "s_add_u32 m0, s66, s83\n"
  "v_mfma_f32_16x16x32_bf16 v[184:187], a[72:75], a[92:95], v[184:187]\n"
  "ds_read_b64_tr_b16 a[14:15], v31 offset:20992\n"
  "v_mfma_f32_16x16x32_bf16 v[140:143], a[76:79], a[80:83], v[140:143]\n"
  "ds_read_b128 a[48:51], v26 offset:8192\n"
  "buffer_load_dwordx4 v18, %[srdWC], s68 offen lds\n"
  "v_mfma_f32_16x16x32_bf16 v[156:159], a[76:79], a[84:87], v[156:159]\n"
  "ds_read_b128 a[52:55], v27 offset:8192\n"
  "v_mfma_f32_16x16x32_bf16 v[172:175], a[76:79], a[88:91], v[172:175]\n"
  "ds_read_b128 a[56:59], v28 offset:8192\n"
  "s_add_u32 m0, s66, s71\n"
  "v_mfma_f32_16x16x32_bf16 v[188:191], a[76:79], a[92:95], v[188:191]\n"
  "ds_read_b128 a[60:63], v29 offset:8192\n"
  "buffer_load_dwordx4 v16, %[srdAC], s64 offen offset:0 sc1 lds\n"
  "s_waitcnt lgkmcnt(15)\n"
  "v_mfma_f32_16x16x32_bf16 v[128:131], a[96:99], a[112:115], v[128:131]\n"
  "ds_read_b64_tr_b16 a[32:33], v30 offset:24576\n"
  "v_mfma_f32_16x16x32_bf16 v[144:147], a[96:99], a[116:119], v[144:147]\n"
  "ds_read_b64_tr_b16 a[34:35], v30 offset:28672\n"
  "s_add_u32 s65, s65, 0x10000\n"
  "v_mfma_f32_16x16x32_bf16 v[160:163], a[96:99], a[120:123], v[160:163]\n"
  "ds_read_b64_tr_b16 a[36:37], v31 offset:24576\n"
  "v_mfma_f32_16x16x32_bf16 v[176:179], a[96:99], a[124:127], v[176:179]\n"
  "ds_read_b64_tr_b16 a[38:39], v31 offset:28672\n"
  "buffer_load_dwordx4 v16, %[srdAC], s64 offen offset:1024 sc1 lds\n"
  "v_mfma_f32_16x16x32_bf16 v[132:135], a[100:103], a[112:115], v[132:135]\n"
  "ds_read_b64_tr_b16 a[40:41], v30 offset:25088\n"
  "v_mfma_f32_16x16x32_bf16 v[148:151], a[100:103], a[116:119], v[148:151]\n"
  "ds_read_b64_tr_b16 a[42:43], v30 offset:29184\n"
  "s_add_u32 s68, s65, 0x8000\n"
  "v_mfma_f32_16x16x32_bf16 v[164:167], a[100:103], a[120:123], v[164:167]\n"
  "ds_read_b64_tr_b16 a[44:45], v31 offset:25088\n"
  "buffer_load_dwordx4 v16, %[srdAC], s64 offen offset:2048 sc1 lds\n"
  "v_mfma_f32_16x16x32_bf16 v[180:183], a[100:103], a[124:127], v[180:183]\n"
  "ds_read_b64_tr_b16 a[46:47], v31 offset:29184\n"
  "v_mfma_f32_16x16x32_bf16 v[136:139], a[104:107], a[112:115], v[136:139]\n"
  "s_add_u32 s67, s67, 0x18000\n"
  "s_sub_u32 s72, s67, 0x28000\n"
  "s_cselect_b32 s67, s67, s72\n"
  "s_add_u32 s66, s66, 0x18000\n"
  "s_sub_u32 s72, s66, 0x28000\n"
  "s_cselect_b32 s66, s66, s72\n"
  "v_mfma_f32_16x16x32_bf16 v[152:155], a[104:107], a[116:119], v[152:155]\n"
  "v_mfma_f32_16x16x32_bf16 v[168:171], a[104:107], a[120:123], v[168:171]\n"
  "v_add_u32 v26, s67, v20\n"
  "buffer_load_dwordx4 v16, %[srdAC], s64 offen offset:3072 sc1 lds\n"
  "v_mfma_f32_16x16x32_bf16 v[184:187], a[104:107], a[124:127], v[184:187]\n"
  "v_add_u32 v27, s67, v21\n"
  "v_mfma_f32_16x16x32_bf16 v[140:143], a[108:111], a[112:115], v[140:143]\n"
  "v_add_u32 v28, s67, v22\n"
  "s_add_u32 s64, s64, 0x4000\n"
  "v_mfma_f32_16x16x32_bf16 v[156:159], a[108:111], a[116:119], v[156:159]\n"
  "v_add_u32 v29, s67, v23\n"
  "v_mfma_f32_16x16x32_bf16 v[172:175], a[108:111], a[120:123], v[172:175]\n"
  "v_add_u32 v30, s67, v24\n"
  "s_add_u32 s69, s69, 2\n"
  "v_mfma_f32_16x16x32_bf16 v[188:191], a[108:111], a[124:127], v[188:191]\n"
  "v_add_u32 v31, s67, v25\n"
  "s_cmp_lt_u32 s69, 22\n"
  "s_cbranch_scc1 FNLOOP_%=\n"
  "s_add_u32 s70, %[sp], 1\n"
  "s_and_b32 s70, s70, 3\n"
  "s_mul_i32 s70, s70, 24576\n"
  "s_add_u32 s70, s70, %[oP]\n"
  "s_add_u32 s70, s70, 0\n"
  "v_cvt_pk_bf16_f32 v144, v144, v145\n"
  "v_cvt_pk_bf16_f32 v145, v146, v147\n"
  "v_cvt_pk_bf16_f32 v146, v148, v149\n"
  "v_cvt_pk_bf16_f32 v147, v150, v151\n"
  "buffer_store_dwordx4 v[144:147], %[L16], %[srdWS], s70 offen offset:0 sc1\n"
  "v_cvt_pk_bf16_f32 v152, v152, v153\n"
  "v_cvt_pk_bf16_f32 v153, v154, v155\n"
  "v_cvt_pk_bf16_f32 v154, v156, v157\n"
  "v_cvt_pk_bf16_f32 v155, v158, v159\n"
  "buffer_store_dwordx4 v[152:155], %[L16], %[srdWS], s70 offen offset:1024 sc1\n"
  "s_add_u32 s70, %[sp], 2\n"
  "s_and_b32 s70, s70, 3\n"
  "s_mul_i32 s70, s70, 24576\n"
  "s_add_u32 s70, s70, %[oP]\n"
  "s_add_u32 s70, s70, 8192\n"
  "v_cvt_pk_bf16_f32 v160, v160, v161\n"
  "v_cvt_pk_bf16_f32 v161, v162, v163\n"
  "v_cvt_pk_bf16_f32 v162, v164, v165\n"
  "v_cvt_pk_bf16_f32 v163, v166, v167\n"
  "buffer_store_dwordx4 v[160:163], %[L16], %[srdWS], s70 offen offset:0 sc1\n"
  "v_cvt_pk_bf16_f32 v168, v168, v169\n"
  "v_cvt_pk_bf16_f32 v169, v170, v171\n"
  "v_cvt_pk_bf16_f32 v170, v172, v173\n"
  "v_cvt_pk_bf16_f32 v171, v174, v175\n"
  "buffer_store_dwordx4 v[168:171], %[L16], %[srdWS], s70 offen offset:1024 sc1\n"
  "s_add_u32 s70, %[sp], 3\n"
  "s_and_b32 s70, s70, 3\n"
  "s_mul_i32 s70, s70, 24576\n"
  "s_add_u32 s70, s70, %[oP]\n"
  "s_add_u32 s70, s70, 16384\n"
  "v_cvt_pk_bf16_f32 v176, v176, v177\n"
  "v_cvt_pk_bf16_f32 v177, v178, v179\n"
  "v_cvt_pk_bf16_f32 v178, v180, v181\n"
  "v_cvt_pk_bf16_f32 v179, v182, v183\n"
  "buffer_store_dwordx4 v[176:179], %[L16], %[srdWS], s70 offen offset:0 sc1\n"
  "v_cvt_pk_bf16_f32 v184, v184, v185\n"
  "v_cvt_pk_bf16_f32 v185, v186, v187\n"
  "v_cvt_pk_bf16_f32 v186, v188, v189\n"
  "v_cvt_pk_bf16_f32 v187, v190, v191\n"
  "buffer_store_dwordx4 v[184:187], %[L16], %[srdWS], s70 offen offset:1024 sc1\n"
  "buffer_load_dwordx2 v[192:193], %[voffO], %[srdXr], 0 offen offset:0\n"
  "buffer_load_dwordx2 v[194:195], %[voffO], %[srdXr], 0 offen offset:32\n"
  "buffer_load_dwordx2 v[196:197], %[voffO], %[srdXr], 0 offen offset:64\n"
  "buffer_load_dwordx2 v[198:199], %[voffO], %[srdXr], 0 offen offset:96\n"
  "v_and_b32 v38, 0x3ff, %[voffO]\n"
  "buffer_load_dwordx2 v[200:201], v38, %[srdB2], 0 offen offset:0\n"
  "buffer_load_dwordx2 v[202:203], v38, %[srdB2], 0 offen offset:32\n"
  "buffer_load_dwordx2 v[204:205], v38, %[srdB2], 0 offen offset:64\n"
  "buffer_load_dwordx2 v[206:207], v38, %[srdB2], 0 offen offset:96\n"
  "s_add_u32 s70, %[oFl], 0x10000\n"
  "s_waitcnt vmcnt(8)\n"
  "buffer_store_dwordx2 v[34:35], off, %[srdWS], s70 sc1\n"
  "buffer_load_dwordx2 v[32:33], %[pollP], %[srdWS], 0 offen sc1\n"
  "s_waitcnt vmcnt(9)\n"
  "v_lshlrev_b32 v36, 16, v192\n"
  "v_and_b32 v37, 0xffff0000, v192\n"
  "v_add_f32 v128, v128, v36\n"
  "v_add_f32 v129, v129, v37\n"
  "v_lshlrev_b32 v36, 16, v193\n"
  "v_and_b32 v37, 0xffff0000, v193\n"
  "v_add_f32 v130, v130, v36\n"
  "v_add_f32 v131, v131, v37\n"
  "s_waitcnt vmcnt(5)\n"
  "v_lshlrev_b32 v36, 16, v200\n"
  "v_and_b32 v37, 0xffff0000, v200\n"
  "v_add_f32 v128, v128, v36\n"
  "v_add_f32 v129, v129, v37\n"
  "v_lshlrev_b32 v36, 16, v201\n"
  "v_and_b32 v37, 0xffff0000, v201\n"
  "v_add_f32 v130, v130, v36\n"
  "v_add_f32 v131, v131, v37\n"
  "v_lshlrev_b32 v36, 16, v194\n"
  "v_and_b32 v37, 0xffff0000, v194\n"
  "v_add_f32 v132, v132, v36\n"
  "v_add_f32 v133, v133, v37\n"
  "v_lshlrev_b32 v36, 16, v195\n"
  "v_and_b32 v37, 0xffff0000, v195\n"
  "v_add_f32 v134, v134, v36\n"
  "v_add_f32 v135, v135, v37\n"
  "s_waitcnt vmcnt(4)\n"
  "v_lshlrev_b32 v36, 16, v202\n"
  "v_and_b32 v37, 0xffff0000, v202\n"
  "v_add_f32 v132, v132, v36\n"
  "v_add_f32 v133, v133, v37\n"
  "v_lshlrev_b32 v36, 16, v203\n"
  "v_and_b32 v37, 0xffff0000, v203\n"
  "v_add_f32 v134, v134, v36\n"
  "v_add_f32 v135, v135, v37\n"
  "v_lshlrev_b32 v36, 16, v196\n"
  "v_and_b32 v37, 0xffff0000, v196\n"
  "v_add_f32 v136, v136, v36\n"
  "v_add_f32 v137, v137, v37\n"
  "v_lshlrev_b32 v36, 16, v197\n"
  "v_and_b32 v37, 0xffff0000, v197\n"
  "v_add_f32 v138, v138, v36\n"
  "v_add_f32 v139, v139, v37\n"
  "s_waitcnt vmcnt(3)\n"
  "v_lshlrev_b32 v36, 16, v204\n"
  "v_and_b32 v37, 0xffff0000, v204\n"
  "v_add_f32 v136, v136, v36\n"
  "v_add_f32 v137, v137, v37\n"
  "v_lshlrev_b32 v36, 16, v205\n"
  "v_and_b32 v37, 0xffff0000, v205\n"
  "v_add_f32 v138, v138, v36\n"
  "v_add_f32 v139, v139, v37\n"
  "v_lshlrev_b32 v36, 16, v198\n"
  "v_and_b32 v37, 0xffff0000, v198\n"
  "v_add_f32 v140, v140, v36\n"
  "v_add_f32 v141, v141, v37\n"
  "v_lshlrev_b32 v36, 16, v199\n"
  "v_and_b32 v37, 0xffff0000, v199\n"
  "v_add_f32 v142, v142, v36\n"
  "v_add_f32 v143, v143, v37\n"
  "s_waitcnt vmcnt(2)\n"
  "v_lshlrev_b32 v36, 16, v206\n"
  "v_and_b32 v37, 0xffff0000, v206\n"
  "v_add_f32 v140, v140, v36\n"
  "v_add_f32 v141, v141, v37\n"
  "v_lshlrev_b32 v36, 16, v207\n"
  "v_and_b32 v37, 0xffff0000, v207\n"
  "v_add_f32 v142, v142, v36\n"
  "v_add_f32 v143, v143, v37\n"
  "s_mul_i32 s72, %[sp], 24576\n"
  "s_add_u32 s72, s72, %[oP]\n"
  "s_waitcnt vmcnt(0)\n"
  "v_cmp_eq_u64 vcc, %[tok], v[32:33]\n"
  "s_nop 0\n"
  "s_cmp_eq_u64 vcc, -1\n"
  "s_cbranch_scc0 SPINP_%=\n"
  "BACKP_%=:\n"
  "buffer_load_dwordx4 v[208:211], %[L16], %[srdWS], s72 offen offset:0 sc1\n"
  "buffer_load_dwordx4 v[212:215], %[L16], %[srdWS], s72 offen offset:1024 sc1\n"
  "s_add_u32 s72, s72, 0x2000\n"
  "buffer_load_dwordx4 v[216:219], %[L16], %[srdWS], s72 offen offset:0 sc1\n"
  "buffer_load_dwordx4 v[220:223], %[L16], %[srdWS], s72 offen offset:1024 sc1\n"
  "s_add_u32 s72, s72, 0x2000\n"
  "buffer_load_dwordx4 v[224:227], %[L16], %[srdWS], s72 offen offset:0 sc1\n"
  "buffer_load_dwordx4 v[228:231], %[L16], %[srdWS], s72 offen offset:1024 sc1\n"
  "s_waitcnt vmcnt(5)\n"
  "v_lshlrev_b32 v36, 16, v208\n"
  "v_and_b32 v37, 0xffff0000, v208\n"
  "v_add_f32 v128, v128, v36\n"
  "v_add_f32 v129, v129, v37\n"
  "v_lshlrev_b32 v36, 16, v209\n"
  "v_and_b32 v37, 0xffff0000, v209\n"
  "v_add_f32 v130, v130, v36\n"
  "v_add_f32 v131, v131, v37\n"
  "v_lshlrev_b32 v36, 16, v210\n"
  "v_and_b32 v37, 0xffff0000, v210\n"
  "v_add_f32 v132, v132, v36\n"
  "v_add_f32 v133, v133, v37\n"
  "v_lshlrev_b32 v36, 16, v211\n"
  "v_and_b32 v37, 0xffff0000, v211\n"
  "v_add_f32 v134, v134, v36\n"
  "v_add_f32 v135, v135, v37\n"
  "s_waitcnt vmcnt(4)\n"
  "v_lshlrev_b32 v36, 16, v212\n"
  "v_and_b32 v37, 0xffff0000, v212\n"
  "v_add_f32 v136, v136, v36\n"
  "v_add_f32 v137, v137, v37\n"
  "v_lshlrev_b32 v36, 16, v213\n"
  "v_and_b32 v37, 0xffff0000, v213\n"
  "v_add_f32 v138, v138, v36\n"
  "v_add_f32 v139, v139, v37\n"
  "v_lshlrev_b32 v36, 16, v214\n"
  "v_and_b32 v37, 0xffff0000, v214\n"
  "v_add_f32 v140, v140, v36\n"
  "v_add_f32 v141, v141, v37\n"
  "v_lshlrev_b32 v36, 16, v215\n"
  "v_and_b32 v37, 0xffff0000, v215\n"
  "v_add_f32 v142, v142, v36\n"
  "v_add_f32 v143, v143, v37\n"
  "s_waitcnt vmcnt(3)\n"
  "v_lshlrev_b32 v36, 16, v216\n"
  "v_and_b32 v37, 0xffff0000, v216\n"
  "v_add_f32 v128, v128, v36\n"
  "v_add_f32 v129, v129, v37\n"
  "v_lshlrev_b32 v36, 16, v217\n"
  "v_and_b32 v37, 0xffff0000, v217\n"
  "v_add_f32 v130, v130, v36\n"
  "v_add_f32 v131, v131, v37\n"
  "v_lshlrev_b32 v36, 16, v218\n"
  "v_and_b32 v37, 0xffff0000, v218\n"
  "v_add_f32 v132, v132, v36\n"
  "v_add_f32 v133, v133, v37\n"
  "v_lshlrev_b32 v36, 16, v219\n"
  "v_and_b32 v37, 0xffff0000, v219\n"
  "v_add_f32 v134, v134, v36\n"
  "v_add_f32 v135, v135, v37\n"
  "s_waitcnt vmcnt(2)\n"
  "v_lshlrev_b32 v36, 16, v220\n"
  "v_and_b32 v37, 0xffff0000, v220\n"
  "v_add_f32 v136, v136, v36\n"
  "v_add_f32 v137, v137, v37\n"
  "v_lshlrev_b32 v36, 16, v221\n"
  "v_and_b32 v37, 0xffff0000, v221\n"
  "v_add_f32 v138, v138, v36\n"
  "v_add_f32 v139, v139, v37\n"
  "v_lshlrev_b32 v36, 16, v222\n"
  "v_and_b32 v37, 0xffff0000, v222\n"
  "v_add_f32 v140, v140, v36\n"
  "v_add_f32 v141, v141, v37\n"
  "v_lshlrev_b32 v36, 16, v223\n"
  "v_and_b32 v37, 0xffff0000, v223\n"
  "v_add_f32 v142, v142, v36\n"
  "v_add_f32 v143, v143, v37\n"
  "s_waitcnt vmcnt(1)\n"
  "v_lshlrev_b32 v36, 16, v224\n"
  "v_and_b32 v37, 0xffff0000, v224\n"
  "v_add_f32 v128, v128, v36\n"
  "v_add_f32 v129, v129, v37\n"
  "v_lshlrev_b32 v36, 16, v225\n"
  "v_and_b32 v37, 0xffff0000, v225\n"
  "v_add_f32 v130, v130, v36\n"
  "v_add_f32 v131, v131, v37\n"
  "v_cvt_pk_bf16_f32 v128, v128, v129\n"
  "v_cvt_pk_bf16_f32 v129, v130, v131\n"
  "buffer_store_dwordx2 v[128:129], %[voffO], %[srdO], 0 offen offset:0\n"
  "v_lshlrev_b32 v36, 16, v226\n"
  "v_and_b32 v37, 0xffff0000, v226\n"
  "v_add_f32 v132, v132, v36\n"
  "v_add_f32 v133, v133, v37\n"
  "v_lshlrev_b32 v36, 16, v227\n"
  "v_and_b32 v37, 0xffff0000, v227\n"
  "v_add_f32 v134, v134, v36\n"
  "v_add_f32 v135, v135, v37\n"
  "v_cvt_pk_bf16_f32 v132, v132, v133\n"
  "v_cvt_pk_bf16_f32 v133, v134, v135\n"
  "buffer_store_dwordx2 v[132:133], %[voffO], %[srdO], 0 offen offset:32\n"
  "s_waitcnt vmcnt(2)\n"
  "v_lshlrev_b32 v36, 16, v228\n"
  "v_and_b32 v37, 0xffff0000, v228\n"
  "v_add_f32 v136, v136, v36\n"
  "v_add_f32 v137, v137, v37\n"
  "v_lshlrev_b32 v36, 16, v229\n"
  "v_and_b32 v37, 0xffff0000, v229\n"
  "v_add_f32 v138, v138, v36\n"
  "v_add_f32 v139, v139, v37\n"
  "v_cvt_pk_bf16_f32 v136, v136, v137\n"
  "v_cvt_pk_bf16_f32 v137, v138, v139\n"
  "buffer_store_dwordx2 v[136:137], %[voffO], %[srdO], 0 offen offset:64\n"
  "v_lshlrev_b32 v36, 16, v230\n"
  "v_and_b32 v37, 0xffff0000, v230\n"
  "v_add_f32 v140, v140, v36\n"
  "v_add_f32 v141, v141, v37\n"
  "v_lshlrev_b32 v36, 16, v231\n"
  "v_and_b32 v37, 0xffff0000, v231\n"
  "v_add_f32 v142, v142, v36\n"
  "v_add_f32 v143, v143, v37\n"
  "v_cvt_pk_bf16_f32 v140, v140, v141\n"
  "v_cvt_pk_bf16_f32 v141, v142, v143\n"
  "buffer_store_dwordx2 v[140:141], %[voffO], %[srdO], 0 offen offset:96\n"
  "s_branch FNEND_%=\n"
  "SPINC_%=:\n"
  "s_mov_b32 s70, 0x40000\n"
  "SPINLC_%=:\n"
  "s_sleep 1\n"
  "buffer_load_dwordx2 v[32:33], %[pollC], %[srdWS], 0 offen sc1\n"
  "s_waitcnt vmcnt(0)\n"
  "v_cmp_eq_u64 vcc, %[tok], v[32:33]\n"
  "s_nop 1\n"
  "s_cmp_eq_u64 vcc, -1\n"
  "s_cbranch_scc1 BACKC_%=\n"
  "s_sub_u32 s70, s70, 1\n"
  "s_cmp_lg_u32 s70, 0\n"
  "s_cbranch_scc1 SPINLC_%=\n"
  "s_branch BACKC_%=\n"
  "SPINP_%=:\n"
  "s_mov_b32 s70, 0x40000\n"
  "SPINLP_%=:\n"
  "s_sleep 1\n"
  "buffer_load_dwordx2 v[32:33], %[pollP], %[srdWS], 0 offen sc1\n"
  "s_waitcnt vmcnt(0)\n"
  "v_cmp_eq_u64 vcc, %[tok], v[32:33]\n"
  "s_nop 1\n"
  "s_cmp_eq_u64 vcc, -1\n"
  "s_cbranch_scc1 BACKP_%=\n"
  "s_sub_u32 s70, s70, 1\n"
  "s_cmp_lg_u32 s70, 0\n"
  "s_cbranch_scc1 SPINLP_%=\n"
  "s_branch BACKP_%=\n"
  "FNEND_%=:\n"
  "KCODE_END:\n"
    :
    : [srdWS] "s"(srdWS), [srdAH] "s"(srdAH), [srdWH] "s"(srdWH), [srdAC] "s"(srdAC), [srdWC] "s"(srdWC), [srdXr] "s"(srdXr), [srdO] "s"(srdO), [srdB2] "s"(srdB2), [oP] "s"(rfl(oP)), [sp] "s"(rfl(sp)), [wid] "s"(rfl(wid)), [oFl] "s"(rfl(oFl)), [tok] "s"(tok),
      [L16] "v"(l * 16), [voffW2] "v"(voffW2), [bW] "v"(bW), [bA0] "v"(bA0), [bA1] "v"(bA1), [bA2] "v"(bA2), [bA3] "v"(bA3), [pollC] "v"(pollC), [pollP] "v"(pollP), [voffO] "v"(voffO)
    : "v16", "v17", "v18", "v19", "v20", "v21", "v22", "v23", "v24", "v25", "v26", "v27", "v28", "v29", "v30", "v31", "v32", "v33", "v34", "v35", "v36", "v37", "v38", "v39", "v40", "v41", "v42", "v43", "v44", "v45", "v46", "v47", "v48", "v49", "v50", "v51", "v52", "v53", "v54", "v55", "v56", "v57", "v58", "v59", "v60", "v61", "v62", "v63", "v64", "v65", "v66", "v67", "v68", "v69", "v70", "v71", "v72", "v73", "v74", "v75", "v76", "v77", "v78", "v79", "v80", "v81", "v82", "v83", "v84", "v85", "v86", "v87", "v88", "v89", "v90", "v91", "v92", "v93", "v94", "v95", "v96", "v97", "v98", "v99", "v100", "v101", "v102", "v103", "v104", "v105", "v106", "v107", "v108", "v109", "v110", "v111", "v112", "v113", "v114", "v115", "v116", "v117", "v118", "v119", "v120", "v121", "v122", "v123", "v124", "v125", "v126", "v127", "v128", "v129", "v130", "v131", "v132", "v133", "v134", "v135", "v136", "v137", "v138", "v139", "v140", "v141", "v142", "v143", "v144", "v145", "v146", "v147", "v148", "v149", "v150", "v151", "v152", "v153", "v154", "v155", "v156", "v157", "v158", "v159", "v160", "v161", "v162", "v163", "v164", "v165", "v166", "v167", "v168", "v169", "v170", "v171", "v172", "v173", "v174", "v175", "v176", "v177", "v178", "v179", "v180", "v181", "v182", "v183", "v184", "v185", "v186", "v187", "v188", "v189", "v190", "v191", "v192", "v193", "v194", "v195", "v196", "v197", "v198", "v199", "v200", "v201", "v202", "v203", "v204", "v205", "v206", "v207", "v208", "v209", "v210", "v211", "v212", "v213", "v214", "v215", "v216", "v217", "v218", "v219", "v220", "v221", "v222", "v223", "v224", "v225", "v226", "v227", "v228", "v229", "v230", "v231", "v232", "v233", "v234", "v235", "a0", "a1", "a2", "a3", "a4", "a5", "a6", "a7", "a8", "a9", "a10", "a11", "a12", "a13", "a14", "a15", "a16", "a17", "a18", "a19", "a20", "a21", "a22", "a23", "a24", "a25", "a26", "a27", "a28", "a29", "a30", "a31", "a32", "a33", "a34", "a35", "a36", "a37", "a38", "a39", "a40", "a41", "a42", "a43", "a44", "a45", "a46", "a47", "a48", "a49", "a50", "a51", "a52", "a53", "a54", "a55", "a56", "a57", "a58", "a59", "a60", "a61", "a62", "a63", "a64", "a65", "a66", "a67", "a68", "a69", "a70", "a71", "a72", "a73", "a74", "a75", "a76", "a77", "a78", "a79", "a80", "a81", "a82", "a83", "a84", "a85", "a86", "a87", "a88", "a89", "a90", "a91", "a92", "a93", "a94", "a95", "a96", "a97", "a98", "a99", "a100", "a101", "a102", "a103", "a104", "a105", "a106", "a107", "a108", "a109", "a110", "a111", "a112", "a113", "a114", "a115", "a116", "a117", "a118", "a119", "a120", "a121", "a122", "a123", "a124", "a125", "a126", "a127", "s64", "s65", "s66", "s67", "s68", "s69", "s70", "s71", "s72", "s73", "s74", "s75", "s76", "s77", "s78", "s79", "s80", "s81", "s82", "s83", "s84", "s85", "s86", "s87", "s88", "s89", "s90", "s91", "s92", "s93", "s94", "s95", "vcc", "scc", "memory");
  }
  if (bid == 0 && wid == 0 && lane_id() == 0) __hip_atomic_store((gu64*)p.ws, tok, __ATOMIC_RELAXED, __HIP_MEMORY_SCOPE_AGENT);
  STAMP(4); STAMP(5);
  STAMP(6);
}

extern "C" void kernel_launch(void* const* d_in, const int* in_sizes, int n_in, void* d_out, int out_size, void* d_ws, size_t ws_size, hipStream_t stream) {
  Params p;
  p.x = (const u16*)d_in[0]; p.ckv = (const u16*)d_in[1]; p.wq = (const u16*)d_in[2]; p.wuk = (const u16*)d_in[3]; p.wuv = (const u16*)d_in[4];
  p.wo = (const u16*)d_in[5]; p.w1 = (const u16*)d_in[7]; p.b1 = (const u16*)d_in[8]; p.w2 = (const u16*)d_in[9]; p.b2 = (const u16*)d_in[10];
  p.out = (u16*)d_out; p.ws = (char*)d_ws;
  k_main<<<256, 256, 0, stream>>>(p);
}
```
